```python
import torch

def _softmax(x, axis=-1):
    return torch.softmax(x, dim=axis)
def _gelu(x):
    return torch.nn.functional.gelu(x, approximate="tanh")
def _silu(x):
    return torch.nn.functional.silu(x)
def _sigmoid(x):
    return torch.sigmoid(x)
def _layernorm(x, axis=-1, eps=1e-5):
    m = x.mean(dim=axis, keepdim=True)
    v = x.var(dim=axis, keepdim=True, unbiased=False)
    return (x - m) / torch.sqrt(v + eps)
def _scatter_add(tgt, idx, src, axis=0):
    return tgt.clone().index_add_(axis, idx, src)
def _segment_sum(src, seg, n):
    out = torch.zeros((n,) + src.shape[1:], dtype=src.dtype)
    return out.index_add_(0, seg, src)
def _pad(x, pw):
    flat = tuple(v for pair in reversed(pw) for v in pair)
    return torch.nn.functional.pad(x, flat)

kernel_name = 'concat_heads__mha_block_cached__mha_block_cached__near_ridge'

def setup_inputs():

    _w = (lambda a: (lambda t: t.to(torch.bfloat16) if t.is_floating_point() else t)(torch.as_tensor(a)))
    def _A_setup():
        B, Sq, D = 8, 128, 640
        H, Sk, Dh = 32, 1024, 192
        return {
            "x": _w((lambda *s: torch.randn(*s, dtype=torch.bfloat16) * 0.8)(B, Sq, D)),
            "k_cache": _w((lambda *s: torch.randn(*s, dtype=torch.bfloat16) * 0.8)(B, H, Sk, Dh)),
            "v_cache": _w((lambda *s: torch.randn(*s, dtype=torch.bfloat16) * 0.05)(B, H, Sk, Dh)),
            "wq": _w((lambda *s: torch.randn(*s, dtype=torch.bfloat16) * 0.8)(D, H * Dh) * (D ** -0.5)),
            "wo": _w((lambda *s: torch.randn(*s, dtype=torch.bfloat16) * 0.05)(H * Dh, D) * ((H * Dh) ** -0.5)),
        }
    _A_in = _A_setup()

    def _B_setup():
        B, Sq, D = 8, 128, 640
        H, Sk, Dh = 32, 1024, 192
        return {
            "x": _w((lambda *s: torch.randn(*s, dtype=torch.bfloat16) * 0.8)(B, Sq, D)),
            "k_cache": _w((lambda *s: torch.randn(*s, dtype=torch.bfloat16) * 0.8)(B, H, Sk, Dh)),
            "v_cache": _w((lambda *s: torch.randn(*s, dtype=torch.bfloat16) * 0.05)(B, H, Sk, Dh)),
            "wq": _w((lambda *s: torch.randn(*s, dtype=torch.bfloat16) * 0.8)(D, H * Dh) * (D ** -0.5)),
            "wo": _w((lambda *s: torch.randn(*s, dtype=torch.bfloat16) * 0.05)(H * Dh, D) * ((H * Dh) ** -0.5)),
        }
    _B_in = _B_setup()

    return {
        "A_x": _A_in["x"],
        "A_k_cache": _A_in["k_cache"],
        "A_v_cache": _A_in["v_cache"],
        "A_wq": _A_in["wq"],
        "A_wo": _A_in["wo"],
        "B_x": _B_in["x"],
        "B_k_cache": _B_in["k_cache"],
        "B_v_cache": _B_in["v_cache"],
        "B_wq": _B_in["wq"],
        "B_wo": _B_in["wo"],
    }

def reference(A_x, A_k_cache, A_v_cache, A_wq, A_wo, B_x, B_k_cache, B_v_cache, B_wq, B_wo):
    def _A_ref(x, k_cache, v_cache, wq, wo):
        B, Sq, D = x.shape
        H, Dh = 32, 192
        q = (lambda x, *d: x.permute(*d))((x @ wq).reshape(B, Sq, H, Dh), 0, 2, 1, 3)
        scores = torch.einsum("bhqd,bhkd->bhqk", q, k_cache) * (Dh ** -0.5)
        p = _softmax(scores, axis=-1)
        o = torch.einsum("bhqk,bhkd->bhqd", p, v_cache)
        return (lambda x, *d: x.permute(*d))(o, 0, 2, 1, 3).reshape(B, Sq, H * Dh) @ wo

    def _B_ref(x, k_cache, v_cache, wq, wo):
        B, Sq, D = x.shape
        H, Dh = 32, 192
        q = (lambda x, *d: x.permute(*d))((x @ wq).reshape(B, Sq, H, Dh), 0, 2, 1, 3)
        scores = torch.einsum("bhqd,bhkd->bhqk", q, k_cache) * (Dh ** -0.5)
        p = _softmax(scores, axis=-1)
        o = torch.einsum("bhqk,bhkd->bhqd", p, v_cache)
        return (lambda x, *d: x.permute(*d))(o, 0, 2, 1, 3).reshape(B, Sq, H * Dh) @ wo

    a = _A_ref(A_x, A_k_cache, A_v_cache, A_wq, A_wo)
    b = _B_ref(B_x, B_k_cache, B_v_cache, B_wq, B_wo)
    return torch.concatenate([a, b], axis=-1)
```

```cpp
#include <hip/hip_runtime.h>
#include <hip/hip_bf16.h>
typedef unsigned short u16;
__device__ __forceinline__ float b2f(u16 v){ return __uint_as_float(((unsigned)v)<<16); }
__device__ __forceinline__ u16 f2b(float x){ unsigned u=__float_as_uint(x); u += 0x7fffu + ((u>>16)&1u); return (u16)(u>>16); }
__global__ void qproj(const u16* x, const u16* wq, u16* q){
  int n = blockIdx.x*256+threadIdx.x; int m = blockIdx.y;
  __shared__ float xs[640];
  for(int i=threadIdx.x;i<640;i+=256) xs[i]=b2f(x[m*640+i]);
  __syncthreads();
  float acc=0;
  for(int c=0;c<640;c++) acc += xs[c]*b2f(wq[(size_t)c*6144+n]);
  q[(size_t)m*6144+n]=f2b(acc);
}
__global__ void attn(const u16* q, const u16* k, const u16* v, u16* o){
  int bh = blockIdx.x; int b=bh>>5, h=bh&31; int q0 = blockIdx.y*8;
  __shared__ float qs[8][192]; __shared__ float s[8][1024]; __shared__ float red[8];
  for(int i=threadIdx.x;i<8*192;i+=256){int r=i/192,d=i%192; qs[r][d]=b2f(q[(size_t)(b*128+q0+r)*6144+h*192+d]);}
  __syncthreads();
  const u16* kb = k + (size_t)bh*1024*192; const u16* vb = v + (size_t)bh*1024*192;
  for(int j=threadIdx.x;j<1024;j+=256){
    float a[8]={0,0,0,0,0,0,0,0};
    for(int d=0;d<192;d++){ float kv=b2f(kb[j*192+d]);
      #pragma unroll
      for(int r=0;r<8;r++) a[r]+=qs[r][d]*kv; }
    #pragma unroll
    for(int r=0;r<8;r++) s[r][j]=a[r]*0.07216878364870322f;
  }
  __syncthreads();
  int r=threadIdx.x>>5, l=threadIdx.x&31;
  float m=-1e30f; for(int j=l;j<1024;j+=32) m=fmaxf(m,s[r][j]);
  for(int o2=16;o2;o2>>=1) m=fmaxf(m,__shfl_xor(m,o2,64));
  float sum=0; for(int j=l;j<1024;j+=32){ float e=__expf(s[r][j]-m); s[r][j]=e; sum+=e; }
  for(int o2=16;o2;o2>>=1) sum+=__shfl_xor(sum,o2,64);
  if(l==0) red[r]=1.f/sum;
  __syncthreads();
  if(threadIdx.x<192){ int d=threadIdx.x; float a[8]={0,0,0,0,0,0,0,0};
    for(int j=0;j<1024;j++){ float vv=b2f(vb[j*192+d]);
      #pragma unroll
      for(int rr=0;rr<8;rr++) a[rr]+=s[rr][j]*vv; }
    #pragma unroll
    for(int rr=0;rr<8;rr++) o[(size_t)(b*128+q0+rr)*6144+h*192+d]=f2b(a[rr]*red[rr]);
  }
}
__global__ void oproj(const u16* o, const u16* wo, u16* out, int coloff){
  int n = blockIdx.x*128+threadIdx.x; int m0 = blockIdx.y*4;
  float acc[4]={0,0,0,0};
  for(int c=0;c<6144;c++){ float w=b2f(wo[(size_t)c*640+n]);
    #pragma unroll
    for(int r=0;r<4;r++) acc[r]+= b2f(o[(size_t)(m0+r)*6144+c])*w; }
  #pragma unroll
  for(int r=0;r<4;r++) out[(size_t)(m0+r)*1280+coloff+n]=f2b(acc[r]);
}
extern "C" void kernel_launch(void* const* d_in, const int* in_sizes, int n_in, void* d_out, int out_size, void* d_ws, size_t ws_size, hipStream_t stream){
  u16* q=(u16*)d_ws; u16* o=q+(size_t)1024*6144;
  for(int br=0;br<2;br++){
    const u16* x=(const u16*)d_in[5*br+0]; const u16* k=(const u16*)d_in[5*br+1]; const u16* v=(const u16*)d_in[5*br+2];
    const u16* wq=(const u16*)d_in[5*br+3]; const u16* wo=(const u16*)d_in[5*br+4];
    qproj<<<dim3(24,1024),256,0,stream>>>(x,wq,q);
    attn<<<dim3(256,16),256,0,stream>>>(q,k,v,o);
    oproj<<<dim3(5,256),128,0,stream>>>(o,wo,(u16*)d_out,640*br);
  }
}
```

```cpp
#include <hip/hip_runtime.h>

__global__ __launch_bounds__(512, 2) void mha_pair_fused(const void* xA, const void* kA, const void* vA, const void* wqA,
                                                         const void* woA, const void* xB, const void* kB, const void* vB,
                                                         const void* wqB, const void* woB, void* out, void* ws) {
  __shared__ __attribute__((aligned(1024))) char lds[163840];
  const unsigned lb = (unsigned)(size_t)lds;
  asm volatile(
R"(Lcode0:
s_mov_b32 s67, %1
s_bitcmp1_b32 s67, 2
s_cselect_b32 s70, 40, 0
s_mov_b64 s[68:69], %0
s_add_u32 s68, s68, s70
s_addc_u32 s69, s69, 0
s_load_dwordx2 s[36:37], s[68:69], 0
s_load_dwordx2 s[28:29], s[68:69], 24
s_load_dwordx2 s[20:21], s[68:69], 8
s_load_dwordx2 s[24:25], s[68:69], 16
s_load_dwordx2 s[32:33], s[68:69], 32
s_load_dwordx2 s[44:45], %0, 80
s_load_dwordx2 s[40:41], %0, 88
v_lshrrev_b32 v2, 6, %2
v_and_b32 v1, 63, %2
)"
R"(v_readfirstlane_b32 s59, v2
s_and_b32 s61, s67, 7
s_lshr_b32 s66, s67, 3
s_lshr_b32 s65, s61, 2
s_and_b32 s64, s61, 3
s_and_b32 s62, s66, 7
s_lshr_b32 s63, s66, 3
s_and_b32 s60, s59, 3
s_mul_i32 s56, s60, 3072
s_add_u32 s56, s56, %3
s_lshl_b32 s84, s60, 12
s_add_u32 s84, s84, %3
s_cmp_ge_u32 s59, 4
s_cbranch_scc1 Lteam_B
s_waitcnt lgkmcnt(0)
s_memrealtime s[88:89]
)"
R"(s_memtime s[74:75]
s_and_b32 s29, s29, 0xffff
s_mov_b32 s30, 7864320
s_mov_b32 s31, 0x20000
s_and_b32 s37, s37, 0xffff
s_mov_b32 s38, 163840
s_mov_b32 s39, 0x20000
s_mul_i32 s68, s62, 163840
s_add_u32 s36, s36, s68
s_addc_u32 s37, s37, 0
s_and_b32 s21, s21, 0xffff
s_mov_b32 s22, 100663296
s_mov_b32 s23, 0x20000
s_and_b32 s25, s25, 0xffff
s_mov_b32 s26, 100663296
s_mov_b32 s27, 0x20000
)"
R"(s_and_b32 s33, s33, 0xffff
s_mov_b32 s34, 7864320
s_mov_b32 s35, 0x20000
s_and_b32 s41, s41, 0xffff
s_mov_b32 s42, 35848192
s_mov_b32 s43, 0x20000
s_and_b32 s45, s45, 0xffff
s_mov_b32 s46, 2621440
s_mov_b32 s47, 0x20000
s_mov_b32 s57, 0x3dd53b95
s_waitcnt lgkmcnt(0)
s_mov_b32 s75, s88
s_add_u32 s94, s88, 3870
s_mov_b32 s95, s94
s_cmp_eq_u32 s60, 0
s_cbranch_scc0 Lcpf0
)"
R"(s_getpc_b64 s[68:69]
Lcpc0:
s_sub_u32 s68, s68, Lcpc0-Lcode0
s_subb_u32 s69, s69, 0
s_and_b32 s69, s69, 0xffff
s_mov_b32 s70, Lcode1-Lcode0
s_mov_b32 s71, 0x20000
s_lshl_b32 s72, s66, 13
v_lshlrev_b32 v255, 7, v1
buffer_load_dword v255, v255, s[68:71], s72 offen
Lcpf0:
v_lshrrev_b32 v2, 4, v1
v_bfe_u32 v3, v1, 2, 2
v_lshl_add_u32 v4, v2, 3, v3
v_mul_u32_u24 v4, 384, v4
v_lshrrev_b32 v5, 1, v3
)"
R"(v_and_b32 v6, 1, v2
v_lshl_add_u32 v5, v6, 1, v5
v_lshl_add_u32 v4, v5, 5, v4
v_and_b32 v5, 3, v1
v_lshl_add_u32 v4, v5, 3, v4
v_add_u32 v238, 98304, v4
v_xor_b32 v239, 32, v238
v_xor_b32 v240, 64, v238
v_xor_b32 v241, 96, v238
v_and_b32 v2, 15, v1
v_lshrrev_b32 v3, 4, v1
v_lshrrev_b32 v4, 1, v2
v_xor_b32 v4, v3, v4
v_mul_u32_u24 v2, 128, v2
v_lshl_add_u32 v176, v4, 4, v2
s_lshl_b32 s68, s60, 12
)"
R"(s_add_u32 s68, s68, 65536
v_add_u32 v176, s68, v176
v_xor_b32 v177, 64, v176
v_and_b32 v2, 15, v1
v_lshrrev_b32 v3, 4, v1
v_lshrrev_b32 v4, 1, v2
v_xor_b32 v4, v3, v4
v_mul_u32_u24 v2, 384, v2
v_lshl_add_u32 v232, v4, 4, v2
v_xor_b32 v233, 64, v232
v_lshrrev_b32 v2, 4, v1
v_bfe_u32 v3, v1, 2, 2
v_lshl_add_u32 v4, v2, 2, v3
v_mul_u32_u24 v4, 384, v4
v_lshrrev_b32 v5, 1, v3
v_and_b32 v6, 1, v2
)"
R"(v_lshl_add_u32 v5, v6, 1, v5
v_lshl_add_u32 v4, v5, 5, v4
v_and_b32 v5, 3, v1
v_lshl_add_u32 v4, v5, 3, v4
v_add_u32 v234, 49152, v4
v_xor_b32 v235, 32, v234
v_xor_b32 v236, 64, v234
v_xor_b32 v237, 96, v234
v_add_u32 v3, 0, v1
v_mul_u32_u24 v2, 0xaab, v3
v_lshrrev_b32 v2, 16, v2
v_mul_u32_u24 v6, 24, v2
v_sub_u32 v3, v3, v6
s_lshl_b32 s68, s60, 3
v_add_u32 v4, s68, v2
v_and_b32 v6, 6, v2
)"
R"(v_xor_b32 v5, v3, v6
v_mul_u32_u24 v6, 384, v4
v_lshl_add_u32 v6, v5, 4, v6
v_mov_b32 v251, v6
v_mul_u32_u24 v6, 1280, v4
v_lshl_add_u32 v6, v5, 4, v6
v_mov_b32 v7, v6
v_cmp_gt_u32 vcc, 20, v5
v_mov_b32 v8, 0x80000000
s_nop 0
v_cndmask_b32 v244, v8, v7, vcc
v_bfe_u32 v7, v4, 1, 3
v_xor_b32 v7, v3, v7
v_mul_u32_u24 v6, 384, v4
v_lshl_add_u32 v6, v7, 4, v6
v_mov_b32 v248, v6
)"
R"(v_add_u32 v3, 64, v1
v_mul_u32_u24 v2, 0xaab, v3
v_lshrrev_b32 v2, 16, v2
v_mul_u32_u24 v6, 24, v2
v_sub_u32 v3, v3, v6
s_lshl_b32 s68, s60, 3
v_add_u32 v4, s68, v2
v_and_b32 v6, 6, v2
v_xor_b32 v5, v3, v6
v_mul_u32_u24 v6, 384, v4
v_lshl_add_u32 v6, v5, 4, v6
v_subrev_u32 v252, 1024, v6
v_mul_u32_u24 v6, 1280, v4
v_lshl_add_u32 v6, v5, 4, v6
v_subrev_u32 v7, 1024, v6
v_cmp_gt_u32 vcc, 20, v5
)"
R"(v_mov_b32 v8, 0x80000000
s_nop 0
v_cndmask_b32 v245, v8, v7, vcc
v_bfe_u32 v7, v4, 1, 3
v_xor_b32 v7, v3, v7
v_mul_u32_u24 v6, 384, v4
v_lshl_add_u32 v6, v7, 4, v6
v_subrev_u32 v249, 1024, v6
v_add_u32 v3, 128, v1
v_mul_u32_u24 v2, 0xaab, v3
v_lshrrev_b32 v2, 16, v2
v_mul_u32_u24 v6, 24, v2
v_sub_u32 v3, v3, v6
s_lshl_b32 s68, s60, 3
v_add_u32 v4, s68, v2
v_and_b32 v6, 6, v2
)"
R"(v_xor_b32 v5, v3, v6
v_mul_u32_u24 v6, 384, v4
v_lshl_add_u32 v6, v5, 4, v6
v_subrev_u32 v253, 2048, v6
v_mul_u32_u24 v6, 1280, v4
v_lshl_add_u32 v6, v5, 4, v6
v_subrev_u32 v7, 2048, v6
v_cmp_gt_u32 vcc, 20, v5
v_mov_b32 v8, 0x80000000
s_nop 0
v_cndmask_b32 v246, v8, v7, vcc
v_bfe_u32 v7, v4, 1, 3
v_xor_b32 v7, v3, v7
v_mul_u32_u24 v6, 384, v4
v_lshl_add_u32 v6, v7, 4, v6
v_subrev_u32 v250, 2048, v6
)"
R"(v_lshlrev_b32 v247, 4, v1
s_lshl_b32 s68, s62, 5
s_lshl_b32 s69, s64, 3
s_add_u32 s68, s68, s69
s_add_u32 s68, s68, s63
s_add_u32 s68, s68, 4
s_mul_i32 s68, s68, 393216
s_add_u32 s48, s68, 49152
s_add_u32 s49, s68, 36864
s_mul_i32 s68, s64, 1966080
s_mul_i32 s69, s63, 320
s_add_u32 s52, s68, s69
s_mov_b32 s90, s63
s_cmp_ge_u32 0, s63
s_cselect_b32 s91, 1, 0
s_cmp_ge_u32 1, s63
)"
R"(s_cselect_b32 s92, 2, 1
s_cmp_ge_u32 2, s63
s_cselect_b32 s93, 3, 2
s_mov_b32 s96, s63
s_add_u32 s97, s63, 1
s_and_b32 s97, s97, 3
s_add_u32 s98, s63, 2
s_and_b32 s98, s98, 3
s_add_u32 s99, s63, 3
s_and_b32 s99, s99, 3
s_lshl_b32 s68, s62, 3
s_add_u32 s68, s68, s61
s_lshl_b32 s68, s68, 2
s_add_u32 s68, s68, s60
s_mul_i32 s68, s68, 49152
s_add_u32 s54, s68, 0
)"
R"(s_mul_i32 s68, s63, 12288
s_add_u32 s83, s54, s68
s_setprio 2
s_memtime s[76:77]
s_memrealtime s[88:89]
s_waitcnt lgkmcnt(0)
s_sub_u32 s76, s76, s74
s_sub_u32 s88, s88, s75
s_mul_i32 s88, s88, 19
s_cmp_gt_u32 s76, s88
s_cselect_b32 s73, -1, 0
s_cmp_eq_u32 s60, 0
s_cselect_b32 s73, s73, 0
s_barrier
ds_read_b128 v[96:99], v176 offset:0
ds_read_b128 v[100:103], v176 offset:2048
)"
R"(ds_read_b128 v[104:107], v177 offset:0
ds_read_b128 v[108:111], v177 offset:2048
ds_read_b64_tr_b16 v[184:185], v238 offset:0
ds_read_b64_tr_b16 v[186:187], v238 offset:1536
ds_read_b64_tr_b16 v[188:189], v239 offset:0
ds_read_b64_tr_b16 v[190:191], v239 offset:1536
ds_read_b64_tr_b16 v[192:193], v240 offset:0
ds_read_b64_tr_b16 v[194:195], v240 offset:1536
ds_read_b64_tr_b16 v[196:197], v241 offset:0
ds_read_b64_tr_b16 v[198:199], v241 offset:1536
ds_read_b64_tr_b16 v[200:201], v238 offset:128
ds_read_b64_tr_b16 v[202:203], v238 offset:1664
s_waitcnt lgkmcnt(12)
s_waitcnt lgkmcnt(8)
v_mfma_f32_16x16x32_bf16 v[0:3], v[184:187], v[96:99], 0
ds_read_b64_tr_b16 v[204:205], v239 offset:128
)"
R"(ds_read_b64_tr_b16 v[206:207], v239 offset:1664
v_mfma_f32_16x16x32_bf16 v[4:7], v[184:187], v[100:103], 0
s_waitcnt lgkmcnt(8)
v_mfma_f32_16x16x32_bf16 v[12:15], v[188:191], v[100:103], 0
ds_read_b64_tr_b16 v[208:209], v240 offset:128
ds_read_b64_tr_b16 v[210:211], v240 offset:1664
v_mfma_f32_16x16x32_bf16 v[8:11], v[188:191], v[96:99], 0
s_waitcnt lgkmcnt(8)
v_mfma_f32_16x16x32_bf16 v[16:19], v[192:195], v[96:99], 0
ds_read_b64_tr_b16 v[212:213], v241 offset:128
ds_read_b64_tr_b16 v[214:215], v241 offset:1664
v_mfma_f32_16x16x32_bf16 v[20:23], v[192:195], v[100:103], 0
s_waitcnt lgkmcnt(8)
v_mfma_f32_16x16x32_bf16 v[28:31], v[196:199], v[100:103], 0
ds_read_b64_tr_b16 v[216:217], v238 offset:256
ds_read_b64_tr_b16 v[218:219], v238 offset:1792
)"
R"(v_mfma_f32_16x16x32_bf16 v[24:27], v[196:199], v[96:99], 0
s_waitcnt lgkmcnt(8)
v_mfma_f32_16x16x32_bf16 v[32:35], v[200:203], v[96:99], 0
ds_read_b64_tr_b16 v[220:221], v239 offset:256
ds_read_b64_tr_b16 v[222:223], v239 offset:1792
v_mfma_f32_16x16x32_bf16 v[36:39], v[200:203], v[100:103], 0
s_waitcnt lgkmcnt(8)
v_mfma_f32_16x16x32_bf16 v[44:47], v[204:207], v[100:103], 0
ds_read_b64_tr_b16 v[224:225], v240 offset:256
ds_read_b64_tr_b16 v[226:227], v240 offset:1792
v_mfma_f32_16x16x32_bf16 v[40:43], v[204:207], v[96:99], 0
s_waitcnt lgkmcnt(8)
v_mfma_f32_16x16x32_bf16 v[48:51], v[208:211], v[96:99], 0
ds_read_b64_tr_b16 v[228:229], v241 offset:256
ds_read_b64_tr_b16 v[230:231], v241 offset:1792
v_mfma_f32_16x16x32_bf16 v[52:55], v[208:211], v[100:103], 0
)"
R"(s_waitcnt lgkmcnt(8)
v_mfma_f32_16x16x32_bf16 v[60:63], v[212:215], v[100:103], 0
ds_read_b64_tr_b16 v[184:185], v238 offset:12288
ds_read_b64_tr_b16 v[186:187], v238 offset:13824
v_mfma_f32_16x16x32_bf16 v[56:59], v[212:215], v[96:99], 0
s_waitcnt lgkmcnt(8)
v_mfma_f32_16x16x32_bf16 v[64:67], v[216:219], v[96:99], 0
ds_read_b64_tr_b16 v[188:189], v239 offset:12288
ds_read_b64_tr_b16 v[190:191], v239 offset:13824
v_mfma_f32_16x16x32_bf16 v[68:71], v[216:219], v[100:103], 0
s_waitcnt lgkmcnt(8)
v_mfma_f32_16x16x32_bf16 v[76:79], v[220:223], v[100:103], 0
ds_read_b64_tr_b16 v[192:193], v240 offset:12288
ds_read_b64_tr_b16 v[194:195], v240 offset:13824
v_mfma_f32_16x16x32_bf16 v[72:75], v[220:223], v[96:99], 0
s_waitcnt lgkmcnt(8)
)"
R"(v_mfma_f32_16x16x32_bf16 v[80:83], v[224:227], v[96:99], 0
ds_read_b64_tr_b16 v[196:197], v241 offset:12288
ds_read_b64_tr_b16 v[198:199], v241 offset:13824
v_mfma_f32_16x16x32_bf16 v[84:87], v[224:227], v[100:103], 0
s_waitcnt lgkmcnt(8)
v_mfma_f32_16x16x32_bf16 v[92:95], v[228:231], v[100:103], 0
ds_read_b64_tr_b16 v[200:201], v238 offset:12416
ds_read_b64_tr_b16 v[202:203], v238 offset:13952
v_mfma_f32_16x16x32_bf16 v[88:91], v[228:231], v[96:99], 0
s_barrier
s_waitcnt lgkmcnt(8)
v_mfma_f32_16x16x32_bf16 v[0:3], v[184:187], v[104:107], v[0:3]
ds_read_b64_tr_b16 v[204:205], v239 offset:12416
ds_read_b64_tr_b16 v[206:207], v239 offset:13952
v_mfma_f32_16x16x32_bf16 v[4:7], v[184:187], v[108:111], v[4:7]
s_waitcnt lgkmcnt(8)
)"
R"(ds_read_b128 v[112:115], v176 offset:16384
v_mfma_f32_16x16x32_bf16 v[12:15], v[188:191], v[108:111], v[12:15]
ds_read_b64_tr_b16 v[208:209], v240 offset:12416
ds_read_b64_tr_b16 v[210:211], v240 offset:13952
v_mfma_f32_16x16x32_bf16 v[8:11], v[188:191], v[104:107], v[8:11]
s_waitcnt lgkmcnt(9)
ds_read_b128 v[116:119], v176 offset:18432
v_mfma_f32_16x16x32_bf16 v[16:19], v[192:195], v[104:107], v[16:19]
ds_read_b64_tr_b16 v[212:213], v241 offset:12416
ds_read_b64_tr_b16 v[214:215], v241 offset:13952
v_mfma_f32_16x16x32_bf16 v[20:23], v[192:195], v[108:111], v[20:23]
s_waitcnt lgkmcnt(10)
ds_read_b128 v[120:123], v177 offset:16384
v_mfma_f32_16x16x32_bf16 v[28:31], v[196:199], v[108:111], v[28:31]
ds_read_b64_tr_b16 v[216:217], v238 offset:12544
ds_read_b64_tr_b16 v[218:219], v238 offset:14080
)"
R"(v_mfma_f32_16x16x32_bf16 v[24:27], v[196:199], v[104:107], v[24:27]
s_waitcnt lgkmcnt(11)
ds_read_b128 v[124:127], v177 offset:18432
v_mfma_f32_16x16x32_bf16 v[32:35], v[200:203], v[104:107], v[32:35]
ds_read_b64_tr_b16 v[220:221], v239 offset:12544
ds_read_b64_tr_b16 v[222:223], v239 offset:14080
v_mfma_f32_16x16x32_bf16 v[36:39], v[200:203], v[108:111], v[36:39]
s_waitcnt lgkmcnt(12)
v_mfma_f32_16x16x32_bf16 v[44:47], v[204:207], v[108:111], v[44:47]
ds_read_b64_tr_b16 v[224:225], v240 offset:12544
ds_read_b64_tr_b16 v[226:227], v240 offset:14080
v_mfma_f32_16x16x32_bf16 v[40:43], v[204:207], v[104:107], v[40:43]
s_waitcnt lgkmcnt(11)
v_mfma_f32_16x16x32_bf16 v[48:51], v[208:211], v[104:107], v[48:51]
ds_read_b64_tr_b16 v[228:229], v241 offset:12544
ds_read_b64_tr_b16 v[230:231], v241 offset:14080
)"
R"(v_mfma_f32_16x16x32_bf16 v[52:55], v[208:211], v[108:111], v[52:55]
s_waitcnt lgkmcnt(10)
v_mfma_f32_16x16x32_bf16 v[60:63], v[212:215], v[108:111], v[60:63]
ds_read_b64_tr_b16 v[184:185], v238 offset:24576
ds_read_b64_tr_b16 v[186:187], v238 offset:26112
v_mfma_f32_16x16x32_bf16 v[56:59], v[212:215], v[104:107], v[56:59]
s_waitcnt lgkmcnt(9)
v_mfma_f32_16x16x32_bf16 v[64:67], v[216:219], v[104:107], v[64:67]
ds_read_b64_tr_b16 v[188:189], v239 offset:24576
ds_read_b64_tr_b16 v[190:191], v239 offset:26112
v_mfma_f32_16x16x32_bf16 v[68:71], v[216:219], v[108:111], v[68:71]
s_waitcnt lgkmcnt(8)
v_mfma_f32_16x16x32_bf16 v[76:79], v[220:223], v[108:111], v[76:79]
ds_read_b64_tr_b16 v[192:193], v240 offset:24576
ds_read_b64_tr_b16 v[194:195], v240 offset:26112
v_mfma_f32_16x16x32_bf16 v[72:75], v[220:223], v[104:107], v[72:75]
)"
R"(s_waitcnt lgkmcnt(8)
v_mfma_f32_16x16x32_bf16 v[80:83], v[224:227], v[104:107], v[80:83]
ds_read_b64_tr_b16 v[196:197], v241 offset:24576
ds_read_b64_tr_b16 v[198:199], v241 offset:26112
v_mfma_f32_16x16x32_bf16 v[84:87], v[224:227], v[108:111], v[84:87]
s_waitcnt lgkmcnt(8)
v_mfma_f32_16x16x32_bf16 v[92:95], v[228:231], v[108:111], v[92:95]
ds_read_b64_tr_b16 v[200:201], v238 offset:24704
ds_read_b64_tr_b16 v[202:203], v238 offset:26240
v_mfma_f32_16x16x32_bf16 v[88:91], v[228:231], v[104:107], v[88:91]
s_barrier
s_waitcnt lgkmcnt(8)
v_mfma_f32_16x16x32_bf16 v[0:3], v[184:187], v[112:115], v[0:3]
ds_read_b64_tr_b16 v[204:205], v239 offset:24704
ds_read_b64_tr_b16 v[206:207], v239 offset:26240
v_mfma_f32_16x16x32_bf16 v[4:7], v[184:187], v[116:119], v[4:7]
)"
R"(s_waitcnt lgkmcnt(8)
v_mfma_f32_16x16x32_bf16 v[12:15], v[188:191], v[116:119], v[12:15]
ds_read_b64_tr_b16 v[208:209], v240 offset:24704
ds_read_b64_tr_b16 v[210:211], v240 offset:26240
v_mfma_f32_16x16x32_bf16 v[8:11], v[188:191], v[112:115], v[8:11]
s_waitcnt lgkmcnt(8)
v_mfma_f32_16x16x32_bf16 v[16:19], v[192:195], v[112:115], v[16:19]
ds_read_b64_tr_b16 v[212:213], v241 offset:24704
ds_read_b64_tr_b16 v[214:215], v241 offset:26240
v_mfma_f32_16x16x32_bf16 v[20:23], v[192:195], v[116:119], v[20:23]
s_waitcnt lgkmcnt(8)
v_mfma_f32_16x16x32_bf16 v[28:31], v[196:199], v[116:119], v[28:31]
ds_read_b64_tr_b16 v[216:217], v238 offset:24832
ds_read_b64_tr_b16 v[218:219], v238 offset:26368
v_mfma_f32_16x16x32_bf16 v[24:27], v[196:199], v[112:115], v[24:27]
s_waitcnt lgkmcnt(8)
)"
R"(v_mfma_f32_16x16x32_bf16 v[32:35], v[200:203], v[112:115], v[32:35]
ds_read_b64_tr_b16 v[220:221], v239 offset:24832
ds_read_b64_tr_b16 v[222:223], v239 offset:26368
v_mfma_f32_16x16x32_bf16 v[36:39], v[200:203], v[116:119], v[36:39]
s_waitcnt lgkmcnt(8)
v_mfma_f32_16x16x32_bf16 v[44:47], v[204:207], v[116:119], v[44:47]
ds_read_b64_tr_b16 v[224:225], v240 offset:24832
ds_read_b64_tr_b16 v[226:227], v240 offset:26368
v_mfma_f32_16x16x32_bf16 v[40:43], v[204:207], v[112:115], v[40:43]
s_waitcnt lgkmcnt(8)
v_mfma_f32_16x16x32_bf16 v[48:51], v[208:211], v[112:115], v[48:51]
ds_read_b64_tr_b16 v[228:229], v241 offset:24832
ds_read_b64_tr_b16 v[230:231], v241 offset:26368
v_mfma_f32_16x16x32_bf16 v[52:55], v[208:211], v[116:119], v[52:55]
s_waitcnt lgkmcnt(8)
v_mfma_f32_16x16x32_bf16 v[60:63], v[212:215], v[116:119], v[60:63]
)"
R"(ds_read_b64_tr_b16 v[184:185], v238 offset:36864
ds_read_b64_tr_b16 v[186:187], v238 offset:38400
v_mfma_f32_16x16x32_bf16 v[56:59], v[212:215], v[112:115], v[56:59]
s_waitcnt lgkmcnt(8)
v_mfma_f32_16x16x32_bf16 v[64:67], v[216:219], v[112:115], v[64:67]
ds_read_b64_tr_b16 v[188:189], v239 offset:36864
ds_read_b64_tr_b16 v[190:191], v239 offset:38400
v_mfma_f32_16x16x32_bf16 v[68:71], v[216:219], v[116:119], v[68:71]
s_waitcnt lgkmcnt(8)
v_mfma_f32_16x16x32_bf16 v[76:79], v[220:223], v[116:119], v[76:79]
ds_read_b64_tr_b16 v[192:193], v240 offset:36864
ds_read_b64_tr_b16 v[194:195], v240 offset:38400
v_mfma_f32_16x16x32_bf16 v[72:75], v[220:223], v[112:115], v[72:75]
s_waitcnt lgkmcnt(8)
v_mfma_f32_16x16x32_bf16 v[80:83], v[224:227], v[112:115], v[80:83]
ds_read_b64_tr_b16 v[196:197], v241 offset:36864
)"
R"(ds_read_b64_tr_b16 v[198:199], v241 offset:38400
v_mfma_f32_16x16x32_bf16 v[84:87], v[224:227], v[116:119], v[84:87]
s_waitcnt lgkmcnt(8)
v_mfma_f32_16x16x32_bf16 v[92:95], v[228:231], v[116:119], v[92:95]
ds_read_b64_tr_b16 v[200:201], v238 offset:36992
ds_read_b64_tr_b16 v[202:203], v238 offset:38528
v_mfma_f32_16x16x32_bf16 v[88:91], v[228:231], v[112:115], v[88:91]
s_barrier
s_waitcnt lgkmcnt(8)
v_mfma_f32_16x16x32_bf16 v[0:3], v[184:187], v[120:123], v[0:3]
ds_read_b64_tr_b16 v[204:205], v239 offset:36992
ds_read_b64_tr_b16 v[206:207], v239 offset:38528
v_mfma_f32_16x16x32_bf16 v[4:7], v[184:187], v[124:127], v[4:7]
s_waitcnt lgkmcnt(8)
ds_read_b128 v[128:131], v176 offset:0
v_mfma_f32_16x16x32_bf16 v[12:15], v[188:191], v[124:127], v[12:15]
)"
R"(ds_read_b64_tr_b16 v[208:209], v240 offset:36992
ds_read_b64_tr_b16 v[210:211], v240 offset:38528
v_mfma_f32_16x16x32_bf16 v[8:11], v[188:191], v[120:123], v[8:11]
s_waitcnt lgkmcnt(9)
ds_read_b128 v[132:135], v176 offset:2048
v_mfma_f32_16x16x32_bf16 v[16:19], v[192:195], v[120:123], v[16:19]
ds_read_b64_tr_b16 v[212:213], v241 offset:36992
ds_read_b64_tr_b16 v[214:215], v241 offset:38528
v_mfma_f32_16x16x32_bf16 v[20:23], v[192:195], v[124:127], v[20:23]
s_waitcnt lgkmcnt(10)
ds_read_b128 v[136:139], v177 offset:0
v_mfma_f32_16x16x32_bf16 v[28:31], v[196:199], v[124:127], v[28:31]
ds_read_b64_tr_b16 v[216:217], v238 offset:37120
ds_read_b64_tr_b16 v[218:219], v238 offset:38656
v_mfma_f32_16x16x32_bf16 v[24:27], v[196:199], v[120:123], v[24:27]
s_waitcnt lgkmcnt(11)
)"
R"(ds_read_b128 v[140:143], v177 offset:2048
v_mfma_f32_16x16x32_bf16 v[32:35], v[200:203], v[120:123], v[32:35]
ds_read_b64_tr_b16 v[220:221], v239 offset:37120
ds_read_b64_tr_b16 v[222:223], v239 offset:38656
v_mfma_f32_16x16x32_bf16 v[36:39], v[200:203], v[124:127], v[36:39]
s_waitcnt lgkmcnt(12)
v_mfma_f32_16x16x32_bf16 v[44:47], v[204:207], v[124:127], v[44:47]
ds_read_b64_tr_b16 v[224:225], v240 offset:37120
ds_read_b64_tr_b16 v[226:227], v240 offset:38656
v_mfma_f32_16x16x32_bf16 v[40:43], v[204:207], v[120:123], v[40:43]
s_waitcnt lgkmcnt(11)
v_mfma_f32_16x16x32_bf16 v[48:51], v[208:211], v[120:123], v[48:51]
ds_read_b64_tr_b16 v[228:229], v241 offset:37120
ds_read_b64_tr_b16 v[230:231], v241 offset:38656
v_mfma_f32_16x16x32_bf16 v[52:55], v[208:211], v[124:127], v[52:55]
s_waitcnt lgkmcnt(10)
)"
R"(v_mfma_f32_16x16x32_bf16 v[60:63], v[212:215], v[124:127], v[60:63]
ds_read_b64_tr_b16 v[184:185], v238 offset:49152
ds_read_b64_tr_b16 v[186:187], v238 offset:50688
v_mfma_f32_16x16x32_bf16 v[56:59], v[212:215], v[120:123], v[56:59]
s_waitcnt lgkmcnt(9)
v_mfma_f32_16x16x32_bf16 v[64:67], v[216:219], v[120:123], v[64:67]
ds_read_b64_tr_b16 v[188:189], v239 offset:49152
ds_read_b64_tr_b16 v[190:191], v239 offset:50688
v_mfma_f32_16x16x32_bf16 v[68:71], v[216:219], v[124:127], v[68:71]
s_waitcnt lgkmcnt(8)
v_mfma_f32_16x16x32_bf16 v[76:79], v[220:223], v[124:127], v[76:79]
ds_read_b64_tr_b16 v[192:193], v240 offset:49152
ds_read_b64_tr_b16 v[194:195], v240 offset:50688
v_mfma_f32_16x16x32_bf16 v[72:75], v[220:223], v[120:123], v[72:75]
s_waitcnt lgkmcnt(8)
v_mfma_f32_16x16x32_bf16 v[80:83], v[224:227], v[120:123], v[80:83]
)"
R"(ds_read_b64_tr_b16 v[196:197], v241 offset:49152
ds_read_b64_tr_b16 v[198:199], v241 offset:50688
v_mfma_f32_16x16x32_bf16 v[84:87], v[224:227], v[124:127], v[84:87]
s_waitcnt lgkmcnt(8)
v_mfma_f32_16x16x32_bf16 v[92:95], v[228:231], v[124:127], v[92:95]
ds_read_b64_tr_b16 v[200:201], v238 offset:49280
ds_read_b64_tr_b16 v[202:203], v238 offset:50816
v_mfma_f32_16x16x32_bf16 v[88:91], v[228:231], v[120:123], v[88:91]
s_barrier
s_waitcnt lgkmcnt(8)
v_mfma_f32_16x16x32_bf16 v[0:3], v[184:187], v[128:131], v[0:3]
ds_read_b64_tr_b16 v[204:205], v239 offset:49280
ds_read_b64_tr_b16 v[206:207], v239 offset:50816
v_mfma_f32_16x16x32_bf16 v[4:7], v[184:187], v[132:135], v[4:7]
s_waitcnt lgkmcnt(8)
v_mfma_f32_16x16x32_bf16 v[12:15], v[188:191], v[132:135], v[12:15]
)"
R"(ds_read_b64_tr_b16 v[208:209], v240 offset:49280
ds_read_b64_tr_b16 v[210:211], v240 offset:50816
v_mfma_f32_16x16x32_bf16 v[8:11], v[188:191], v[128:131], v[8:11]
s_waitcnt lgkmcnt(8)
v_mfma_f32_16x16x32_bf16 v[16:19], v[192:195], v[128:131], v[16:19]
ds_read_b64_tr_b16 v[212:213], v241 offset:49280
ds_read_b64_tr_b16 v[214:215], v241 offset:50816
v_mfma_f32_16x16x32_bf16 v[20:23], v[192:195], v[132:135], v[20:23]
s_waitcnt lgkmcnt(8)
v_mfma_f32_16x16x32_bf16 v[28:31], v[196:199], v[132:135], v[28:31]
ds_read_b64_tr_b16 v[216:217], v238 offset:49408
ds_read_b64_tr_b16 v[218:219], v238 offset:50944
v_mfma_f32_16x16x32_bf16 v[24:27], v[196:199], v[128:131], v[24:27]
s_waitcnt lgkmcnt(8)
v_mfma_f32_16x16x32_bf16 v[32:35], v[200:203], v[128:131], v[32:35]
ds_read_b64_tr_b16 v[220:221], v239 offset:49408
)"
R"(ds_read_b64_tr_b16 v[222:223], v239 offset:50944
v_mfma_f32_16x16x32_bf16 v[36:39], v[200:203], v[132:135], v[36:39]
s_waitcnt lgkmcnt(8)
v_mfma_f32_16x16x32_bf16 v[44:47], v[204:207], v[132:135], v[44:47]
ds_read_b64_tr_b16 v[224:225], v240 offset:49408
ds_read_b64_tr_b16 v[226:227], v240 offset:50944
v_mfma_f32_16x16x32_bf16 v[40:43], v[204:207], v[128:131], v[40:43]
s_waitcnt lgkmcnt(8)
v_mfma_f32_16x16x32_bf16 v[48:51], v[208:211], v[128:131], v[48:51]
ds_read_b64_tr_b16 v[228:229], v241 offset:49408
ds_read_b64_tr_b16 v[230:231], v241 offset:50944
v_mfma_f32_16x16x32_bf16 v[52:55], v[208:211], v[132:135], v[52:55]
s_waitcnt lgkmcnt(8)
v_mfma_f32_16x16x32_bf16 v[60:63], v[212:215], v[132:135], v[60:63]
ds_read_b64_tr_b16 v[184:185], v238 offset:0
ds_read_b64_tr_b16 v[186:187], v238 offset:1536
)"
R"(v_mfma_f32_16x16x32_bf16 v[56:59], v[212:215], v[128:131], v[56:59]
s_waitcnt lgkmcnt(8)
v_mfma_f32_16x16x32_bf16 v[64:67], v[216:219], v[128:131], v[64:67]
ds_read_b64_tr_b16 v[188:189], v239 offset:0
ds_read_b64_tr_b16 v[190:191], v239 offset:1536
v_mfma_f32_16x16x32_bf16 v[68:71], v[216:219], v[132:135], v[68:71]
s_waitcnt lgkmcnt(8)
v_mfma_f32_16x16x32_bf16 v[76:79], v[220:223], v[132:135], v[76:79]
ds_read_b64_tr_b16 v[192:193], v240 offset:0
ds_read_b64_tr_b16 v[194:195], v240 offset:1536
v_mfma_f32_16x16x32_bf16 v[72:75], v[220:223], v[128:131], v[72:75]
s_waitcnt lgkmcnt(8)
v_mfma_f32_16x16x32_bf16 v[80:83], v[224:227], v[128:131], v[80:83]
ds_read_b64_tr_b16 v[196:197], v241 offset:0
ds_read_b64_tr_b16 v[198:199], v241 offset:1536
v_mfma_f32_16x16x32_bf16 v[84:87], v[224:227], v[132:135], v[84:87]
)"
R"(s_waitcnt lgkmcnt(8)
v_mfma_f32_16x16x32_bf16 v[92:95], v[228:231], v[132:135], v[92:95]
ds_read_b64_tr_b16 v[200:201], v238 offset:128
ds_read_b64_tr_b16 v[202:203], v238 offset:1664
v_mfma_f32_16x16x32_bf16 v[88:91], v[228:231], v[128:131], v[88:91]
s_barrier
s_waitcnt lgkmcnt(8)
v_mfma_f32_16x16x32_bf16 v[0:3], v[184:187], v[136:139], v[0:3]
ds_read_b64_tr_b16 v[204:205], v239 offset:128
ds_read_b64_tr_b16 v[206:207], v239 offset:1664
v_mfma_f32_16x16x32_bf16 v[4:7], v[184:187], v[140:143], v[4:7]
s_waitcnt lgkmcnt(8)
ds_read_b128 v[144:147], v176 offset:16384
v_mfma_f32_16x16x32_bf16 v[12:15], v[188:191], v[140:143], v[12:15]
ds_read_b64_tr_b16 v[208:209], v240 offset:128
ds_read_b64_tr_b16 v[210:211], v240 offset:1664
)"
R"(v_mfma_f32_16x16x32_bf16 v[8:11], v[188:191], v[136:139], v[8:11]
s_waitcnt lgkmcnt(9)
ds_read_b128 v[148:151], v176 offset:18432
v_mfma_f32_16x16x32_bf16 v[16:19], v[192:195], v[136:139], v[16:19]
ds_read_b64_tr_b16 v[212:213], v241 offset:128
ds_read_b64_tr_b16 v[214:215], v241 offset:1664
v_mfma_f32_16x16x32_bf16 v[20:23], v[192:195], v[140:143], v[20:23]
s_waitcnt lgkmcnt(10)
ds_read_b128 v[152:155], v177 offset:16384
v_mfma_f32_16x16x32_bf16 v[28:31], v[196:199], v[140:143], v[28:31]
ds_read_b64_tr_b16 v[216:217], v238 offset:256
ds_read_b64_tr_b16 v[218:219], v238 offset:1792
v_mfma_f32_16x16x32_bf16 v[24:27], v[196:199], v[136:139], v[24:27]
s_waitcnt lgkmcnt(11)
ds_read_b128 v[156:159], v177 offset:18432
v_mfma_f32_16x16x32_bf16 v[32:35], v[200:203], v[136:139], v[32:35]
)"
R"(ds_read_b64_tr_b16 v[220:221], v239 offset:256
ds_read_b64_tr_b16 v[222:223], v239 offset:1792
v_mfma_f32_16x16x32_bf16 v[36:39], v[200:203], v[140:143], v[36:39]
s_waitcnt lgkmcnt(12)
v_mfma_f32_16x16x32_bf16 v[44:47], v[204:207], v[140:143], v[44:47]
ds_read_b64_tr_b16 v[224:225], v240 offset:256
ds_read_b64_tr_b16 v[226:227], v240 offset:1792
v_mfma_f32_16x16x32_bf16 v[40:43], v[204:207], v[136:139], v[40:43]
s_waitcnt lgkmcnt(11)
v_mfma_f32_16x16x32_bf16 v[48:51], v[208:211], v[136:139], v[48:51]
ds_read_b64_tr_b16 v[228:229], v241 offset:256
ds_read_b64_tr_b16 v[230:231], v241 offset:1792
v_mfma_f32_16x16x32_bf16 v[52:55], v[208:211], v[140:143], v[52:55]
s_waitcnt lgkmcnt(10)
v_mfma_f32_16x16x32_bf16 v[60:63], v[212:215], v[140:143], v[60:63]
ds_read_b64_tr_b16 v[184:185], v238 offset:12288
)"
R"(ds_read_b64_tr_b16 v[186:187], v238 offset:13824
v_mfma_f32_16x16x32_bf16 v[56:59], v[212:215], v[136:139], v[56:59]
s_waitcnt lgkmcnt(9)
v_mfma_f32_16x16x32_bf16 v[64:67], v[216:219], v[136:139], v[64:67]
ds_read_b64_tr_b16 v[188:189], v239 offset:12288
ds_read_b64_tr_b16 v[190:191], v239 offset:13824
v_mfma_f32_16x16x32_bf16 v[68:71], v[216:219], v[140:143], v[68:71]
s_waitcnt lgkmcnt(8)
v_mfma_f32_16x16x32_bf16 v[76:79], v[220:223], v[140:143], v[76:79]
ds_read_b64_tr_b16 v[192:193], v240 offset:12288
ds_read_b64_tr_b16 v[194:195], v240 offset:13824
v_mfma_f32_16x16x32_bf16 v[72:75], v[220:223], v[136:139], v[72:75]
s_waitcnt lgkmcnt(8)
v_mfma_f32_16x16x32_bf16 v[80:83], v[224:227], v[136:139], v[80:83]
ds_read_b64_tr_b16 v[196:197], v241 offset:12288
ds_read_b64_tr_b16 v[198:199], v241 offset:13824
)"
R"(v_mfma_f32_16x16x32_bf16 v[84:87], v[224:227], v[140:143], v[84:87]
s_waitcnt lgkmcnt(8)
v_mfma_f32_16x16x32_bf16 v[92:95], v[228:231], v[140:143], v[92:95]
ds_read_b64_tr_b16 v[200:201], v238 offset:12416
ds_read_b64_tr_b16 v[202:203], v238 offset:13952
v_mfma_f32_16x16x32_bf16 v[88:91], v[228:231], v[136:139], v[88:91]
s_barrier
s_waitcnt lgkmcnt(8)
v_mfma_f32_16x16x32_bf16 v[0:3], v[184:187], v[144:147], v[0:3]
ds_read_b64_tr_b16 v[204:205], v239 offset:12416
ds_read_b64_tr_b16 v[206:207], v239 offset:13952
v_mfma_f32_16x16x32_bf16 v[4:7], v[184:187], v[148:151], v[4:7]
s_waitcnt lgkmcnt(8)
v_mfma_f32_16x16x32_bf16 v[12:15], v[188:191], v[148:151], v[12:15]
ds_read_b64_tr_b16 v[208:209], v240 offset:12416
ds_read_b64_tr_b16 v[210:211], v240 offset:13952
)"
R"(v_mfma_f32_16x16x32_bf16 v[8:11], v[188:191], v[144:147], v[8:11]
s_waitcnt lgkmcnt(8)
v_mfma_f32_16x16x32_bf16 v[16:19], v[192:195], v[144:147], v[16:19]
ds_read_b64_tr_b16 v[212:213], v241 offset:12416
ds_read_b64_tr_b16 v[214:215], v241 offset:13952
v_mfma_f32_16x16x32_bf16 v[20:23], v[192:195], v[148:151], v[20:23]
s_waitcnt lgkmcnt(8)
v_mfma_f32_16x16x32_bf16 v[28:31], v[196:199], v[148:151], v[28:31]
ds_read_b64_tr_b16 v[216:217], v238 offset:12544
ds_read_b64_tr_b16 v[218:219], v238 offset:14080
v_mfma_f32_16x16x32_bf16 v[24:27], v[196:199], v[144:147], v[24:27]
s_waitcnt lgkmcnt(8)
v_mfma_f32_16x16x32_bf16 v[32:35], v[200:203], v[144:147], v[32:35]
ds_read_b64_tr_b16 v[220:221], v239 offset:12544
ds_read_b64_tr_b16 v[222:223], v239 offset:14080
v_mfma_f32_16x16x32_bf16 v[36:39], v[200:203], v[148:151], v[36:39]
)"
R"(s_waitcnt lgkmcnt(8)
v_mfma_f32_16x16x32_bf16 v[44:47], v[204:207], v[148:151], v[44:47]
ds_read_b64_tr_b16 v[224:225], v240 offset:12544
ds_read_b64_tr_b16 v[226:227], v240 offset:14080
v_mfma_f32_16x16x32_bf16 v[40:43], v[204:207], v[144:147], v[40:43]
s_waitcnt lgkmcnt(8)
v_mfma_f32_16x16x32_bf16 v[48:51], v[208:211], v[144:147], v[48:51]
ds_read_b64_tr_b16 v[228:229], v241 offset:12544
ds_read_b64_tr_b16 v[230:231], v241 offset:14080
v_mfma_f32_16x16x32_bf16 v[52:55], v[208:211], v[148:151], v[52:55]
s_waitcnt lgkmcnt(8)
v_mfma_f32_16x16x32_bf16 v[60:63], v[212:215], v[148:151], v[60:63]
ds_read_b64_tr_b16 v[184:185], v238 offset:24576
ds_read_b64_tr_b16 v[186:187], v238 offset:26112
v_mfma_f32_16x16x32_bf16 v[56:59], v[212:215], v[144:147], v[56:59]
s_waitcnt lgkmcnt(8)
)"
R"(v_mfma_f32_16x16x32_bf16 v[64:67], v[216:219], v[144:147], v[64:67]
ds_read_b64_tr_b16 v[188:189], v239 offset:24576
ds_read_b64_tr_b16 v[190:191], v239 offset:26112
v_mfma_f32_16x16x32_bf16 v[68:71], v[216:219], v[148:151], v[68:71]
s_waitcnt lgkmcnt(8)
v_mfma_f32_16x16x32_bf16 v[76:79], v[220:223], v[148:151], v[76:79]
ds_read_b64_tr_b16 v[192:193], v240 offset:24576
ds_read_b64_tr_b16 v[194:195], v240 offset:26112
v_mfma_f32_16x16x32_bf16 v[72:75], v[220:223], v[144:147], v[72:75]
s_waitcnt lgkmcnt(8)
v_mfma_f32_16x16x32_bf16 v[80:83], v[224:227], v[144:147], v[80:83]
ds_read_b64_tr_b16 v[196:197], v241 offset:24576
ds_read_b64_tr_b16 v[198:199], v241 offset:26112
v_mfma_f32_16x16x32_bf16 v[84:87], v[224:227], v[148:151], v[84:87]
s_waitcnt lgkmcnt(8)
v_mfma_f32_16x16x32_bf16 v[92:95], v[228:231], v[148:151], v[92:95]
)"
R"(ds_read_b64_tr_b16 v[200:201], v238 offset:24704
ds_read_b64_tr_b16 v[202:203], v238 offset:26240
v_mfma_f32_16x16x32_bf16 v[88:91], v[228:231], v[144:147], v[88:91]
s_barrier
s_waitcnt lgkmcnt(8)
v_mfma_f32_16x16x32_bf16 v[0:3], v[184:187], v[152:155], v[0:3]
ds_read_b64_tr_b16 v[204:205], v239 offset:24704
ds_read_b64_tr_b16 v[206:207], v239 offset:26240
v_mfma_f32_16x16x32_bf16 v[4:7], v[184:187], v[156:159], v[4:7]
s_waitcnt lgkmcnt(8)
ds_read_b128 v[160:163], v176 offset:0
v_mfma_f32_16x16x32_bf16 v[12:15], v[188:191], v[156:159], v[12:15]
ds_read_b64_tr_b16 v[208:209], v240 offset:24704
ds_read_b64_tr_b16 v[210:211], v240 offset:26240
v_mfma_f32_16x16x32_bf16 v[8:11], v[188:191], v[152:155], v[8:11]
s_waitcnt lgkmcnt(9)
)"
R"(ds_read_b128 v[164:167], v176 offset:2048
v_mfma_f32_16x16x32_bf16 v[16:19], v[192:195], v[152:155], v[16:19]
ds_read_b64_tr_b16 v[212:213], v241 offset:24704
ds_read_b64_tr_b16 v[214:215], v241 offset:26240
v_mfma_f32_16x16x32_bf16 v[20:23], v[192:195], v[156:159], v[20:23]
s_waitcnt lgkmcnt(10)
ds_read_b128 v[168:171], v177 offset:0
v_mfma_f32_16x16x32_bf16 v[28:31], v[196:199], v[156:159], v[28:31]
ds_read_b64_tr_b16 v[216:217], v238 offset:24832
ds_read_b64_tr_b16 v[218:219], v238 offset:26368
v_mfma_f32_16x16x32_bf16 v[24:27], v[196:199], v[152:155], v[24:27]
s_waitcnt lgkmcnt(11)
ds_read_b128 v[172:175], v177 offset:2048
v_mfma_f32_16x16x32_bf16 v[32:35], v[200:203], v[152:155], v[32:35]
ds_read_b64_tr_b16 v[220:221], v239 offset:24832
ds_read_b64_tr_b16 v[222:223], v239 offset:26368
)"
R"(v_mfma_f32_16x16x32_bf16 v[36:39], v[200:203], v[156:159], v[36:39]
s_waitcnt lgkmcnt(12)
v_mfma_f32_16x16x32_bf16 v[44:47], v[204:207], v[156:159], v[44:47]
ds_read_b64_tr_b16 v[224:225], v240 offset:24832
ds_read_b64_tr_b16 v[226:227], v240 offset:26368
v_mfma_f32_16x16x32_bf16 v[40:43], v[204:207], v[152:155], v[40:43]
s_waitcnt lgkmcnt(11)
v_mfma_f32_16x16x32_bf16 v[48:51], v[208:211], v[152:155], v[48:51]
ds_read_b64_tr_b16 v[228:229], v241 offset:24832
ds_read_b64_tr_b16 v[230:231], v241 offset:26368
v_mfma_f32_16x16x32_bf16 v[52:55], v[208:211], v[156:159], v[52:55]
s_waitcnt lgkmcnt(10)
v_mfma_f32_16x16x32_bf16 v[60:63], v[212:215], v[156:159], v[60:63]
ds_read_b64_tr_b16 v[184:185], v238 offset:36864
ds_read_b64_tr_b16 v[186:187], v238 offset:38400
v_mfma_f32_16x16x32_bf16 v[56:59], v[212:215], v[152:155], v[56:59]
)"
R"(s_waitcnt lgkmcnt(9)
v_mfma_f32_16x16x32_bf16 v[64:67], v[216:219], v[152:155], v[64:67]
ds_read_b64_tr_b16 v[188:189], v239 offset:36864
ds_read_b64_tr_b16 v[190:191], v239 offset:38400
v_mfma_f32_16x16x32_bf16 v[68:71], v[216:219], v[156:159], v[68:71]
s_waitcnt lgkmcnt(8)
v_mfma_f32_16x16x32_bf16 v[76:79], v[220:223], v[156:159], v[76:79]
ds_read_b64_tr_b16 v[192:193], v240 offset:36864
ds_read_b64_tr_b16 v[194:195], v240 offset:38400
v_mfma_f32_16x16x32_bf16 v[72:75], v[220:223], v[152:155], v[72:75]
s_waitcnt lgkmcnt(8)
v_mfma_f32_16x16x32_bf16 v[80:83], v[224:227], v[152:155], v[80:83]
ds_read_b64_tr_b16 v[196:197], v241 offset:36864
ds_read_b64_tr_b16 v[198:199], v241 offset:38400
v_mfma_f32_16x16x32_bf16 v[84:87], v[224:227], v[156:159], v[84:87]
s_waitcnt lgkmcnt(8)
)"
R"(v_mfma_f32_16x16x32_bf16 v[92:95], v[228:231], v[156:159], v[92:95]
ds_read_b64_tr_b16 v[200:201], v238 offset:36992
ds_read_b64_tr_b16 v[202:203], v238 offset:38528
v_mfma_f32_16x16x32_bf16 v[88:91], v[228:231], v[152:155], v[88:91]
s_barrier
s_waitcnt lgkmcnt(8)
v_mfma_f32_16x16x32_bf16 v[0:3], v[184:187], v[160:163], v[0:3]
ds_read_b64_tr_b16 v[204:205], v239 offset:36992
ds_read_b64_tr_b16 v[206:207], v239 offset:38528
v_mfma_f32_16x16x32_bf16 v[4:7], v[184:187], v[164:167], v[4:7]
s_waitcnt lgkmcnt(8)
ds_read_b128 v[96:99], v176 offset:16384
v_mfma_f32_16x16x32_bf16 v[12:15], v[188:191], v[164:167], v[12:15]
ds_read_b64_tr_b16 v[208:209], v240 offset:36992
ds_read_b64_tr_b16 v[210:211], v240 offset:38528
v_mfma_f32_16x16x32_bf16 v[8:11], v[188:191], v[160:163], v[8:11]
)"
R"(s_waitcnt lgkmcnt(9)
ds_read_b128 v[100:103], v176 offset:18432
v_mfma_f32_16x16x32_bf16 v[16:19], v[192:195], v[160:163], v[16:19]
ds_read_b64_tr_b16 v[212:213], v241 offset:36992
ds_read_b64_tr_b16 v[214:215], v241 offset:38528
v_mfma_f32_16x16x32_bf16 v[20:23], v[192:195], v[164:167], v[20:23]
s_waitcnt lgkmcnt(10)
ds_read_b128 v[104:107], v177 offset:16384
v_mfma_f32_16x16x32_bf16 v[28:31], v[196:199], v[164:167], v[28:31]
ds_read_b64_tr_b16 v[216:217], v238 offset:37120
ds_read_b64_tr_b16 v[218:219], v238 offset:38656
v_mfma_f32_16x16x32_bf16 v[24:27], v[196:199], v[160:163], v[24:27]
s_waitcnt lgkmcnt(11)
ds_read_b128 v[108:111], v177 offset:18432
v_mfma_f32_16x16x32_bf16 v[32:35], v[200:203], v[160:163], v[32:35]
ds_read_b64_tr_b16 v[220:221], v239 offset:37120
)"
R"(ds_read_b64_tr_b16 v[222:223], v239 offset:38656
v_mfma_f32_16x16x32_bf16 v[36:39], v[200:203], v[164:167], v[36:39]
s_waitcnt lgkmcnt(12)
v_mfma_f32_16x16x32_bf16 v[44:47], v[204:207], v[164:167], v[44:47]
ds_read_b64_tr_b16 v[224:225], v240 offset:37120
ds_read_b64_tr_b16 v[226:227], v240 offset:38656
v_mfma_f32_16x16x32_bf16 v[40:43], v[204:207], v[160:163], v[40:43]
s_waitcnt lgkmcnt(11)
v_mfma_f32_16x16x32_bf16 v[48:51], v[208:211], v[160:163], v[48:51]
ds_read_b64_tr_b16 v[228:229], v241 offset:37120
ds_read_b64_tr_b16 v[230:231], v241 offset:38656
v_mfma_f32_16x16x32_bf16 v[52:55], v[208:211], v[164:167], v[52:55]
s_waitcnt lgkmcnt(10)
v_mfma_f32_16x16x32_bf16 v[60:63], v[212:215], v[164:167], v[60:63]
ds_read_b64_tr_b16 v[184:185], v238 offset:49152
ds_read_b64_tr_b16 v[186:187], v238 offset:50688
)"
R"(v_mfma_f32_16x16x32_bf16 v[56:59], v[212:215], v[160:163], v[56:59]
s_waitcnt lgkmcnt(9)
v_mfma_f32_16x16x32_bf16 v[64:67], v[216:219], v[160:163], v[64:67]
ds_read_b64_tr_b16 v[188:189], v239 offset:49152
ds_read_b64_tr_b16 v[190:191], v239 offset:50688
v_mfma_f32_16x16x32_bf16 v[68:71], v[216:219], v[164:167], v[68:71]
s_waitcnt lgkmcnt(8)
v_mfma_f32_16x16x32_bf16 v[76:79], v[220:223], v[164:167], v[76:79]
ds_read_b64_tr_b16 v[192:193], v240 offset:49152
ds_read_b64_tr_b16 v[194:195], v240 offset:50688
v_mfma_f32_16x16x32_bf16 v[72:75], v[220:223], v[160:163], v[72:75]
s_waitcnt lgkmcnt(8)
v_mfma_f32_16x16x32_bf16 v[80:83], v[224:227], v[160:163], v[80:83]
ds_read_b64_tr_b16 v[196:197], v241 offset:49152
ds_read_b64_tr_b16 v[198:199], v241 offset:50688
v_mfma_f32_16x16x32_bf16 v[84:87], v[224:227], v[164:167], v[84:87]
)"
R"(s_waitcnt lgkmcnt(8)
v_mfma_f32_16x16x32_bf16 v[92:95], v[228:231], v[164:167], v[92:95]
ds_read_b64_tr_b16 v[200:201], v238 offset:49280
ds_read_b64_tr_b16 v[202:203], v238 offset:50816
v_mfma_f32_16x16x32_bf16 v[88:91], v[228:231], v[160:163], v[88:91]
s_barrier
s_waitcnt lgkmcnt(8)
v_mfma_f32_16x16x32_bf16 v[0:3], v[184:187], v[168:171], v[0:3]
ds_read_b64_tr_b16 v[204:205], v239 offset:49280
ds_read_b64_tr_b16 v[206:207], v239 offset:50816
v_mfma_f32_16x16x32_bf16 v[4:7], v[184:187], v[172:175], v[4:7]
s_waitcnt lgkmcnt(8)
v_mfma_f32_16x16x32_bf16 v[12:15], v[188:191], v[172:175], v[12:15]
ds_read_b64_tr_b16 v[208:209], v240 offset:49280
ds_read_b64_tr_b16 v[210:211], v240 offset:50816
v_mfma_f32_16x16x32_bf16 v[8:11], v[188:191], v[168:171], v[8:11]
)"
R"(s_waitcnt lgkmcnt(8)
v_mfma_f32_16x16x32_bf16 v[16:19], v[192:195], v[168:171], v[16:19]
ds_read_b64_tr_b16 v[212:213], v241 offset:49280
ds_read_b64_tr_b16 v[214:215], v241 offset:50816
v_mfma_f32_16x16x32_bf16 v[20:23], v[192:195], v[172:175], v[20:23]
s_waitcnt lgkmcnt(8)
v_mfma_f32_16x16x32_bf16 v[28:31], v[196:199], v[172:175], v[28:31]
ds_read_b64_tr_b16 v[216:217], v238 offset:49408
ds_read_b64_tr_b16 v[218:219], v238 offset:50944
v_mfma_f32_16x16x32_bf16 v[24:27], v[196:199], v[168:171], v[24:27]
s_waitcnt lgkmcnt(8)
v_mfma_f32_16x16x32_bf16 v[32:35], v[200:203], v[168:171], v[32:35]
ds_read_b64_tr_b16 v[220:221], v239 offset:49408
ds_read_b64_tr_b16 v[222:223], v239 offset:50944
v_mfma_f32_16x16x32_bf16 v[36:39], v[200:203], v[172:175], v[36:39]
s_waitcnt lgkmcnt(8)
)"
R"(v_mfma_f32_16x16x32_bf16 v[44:47], v[204:207], v[172:175], v[44:47]
ds_read_b64_tr_b16 v[224:225], v240 offset:49408
ds_read_b64_tr_b16 v[226:227], v240 offset:50944
v_mfma_f32_16x16x32_bf16 v[40:43], v[204:207], v[168:171], v[40:43]
s_waitcnt lgkmcnt(8)
v_mfma_f32_16x16x32_bf16 v[48:51], v[208:211], v[168:171], v[48:51]
ds_read_b64_tr_b16 v[228:229], v241 offset:49408
ds_read_b64_tr_b16 v[230:231], v241 offset:50944
v_mfma_f32_16x16x32_bf16 v[52:55], v[208:211], v[172:175], v[52:55]
s_waitcnt lgkmcnt(8)
v_mfma_f32_16x16x32_bf16 v[60:63], v[212:215], v[172:175], v[60:63]
ds_read_b64_tr_b16 v[184:185], v238 offset:0
ds_read_b64_tr_b16 v[186:187], v238 offset:1536
v_mfma_f32_16x16x32_bf16 v[56:59], v[212:215], v[168:171], v[56:59]
s_waitcnt lgkmcnt(8)
v_mfma_f32_16x16x32_bf16 v[64:67], v[216:219], v[168:171], v[64:67]
)"
R"(ds_read_b64_tr_b16 v[188:189], v239 offset:0
ds_read_b64_tr_b16 v[190:191], v239 offset:1536
v_mfma_f32_16x16x32_bf16 v[68:71], v[216:219], v[172:175], v[68:71]
s_waitcnt lgkmcnt(8)
v_mfma_f32_16x16x32_bf16 v[76:79], v[220:223], v[172:175], v[76:79]
ds_read_b64_tr_b16 v[192:193], v240 offset:0
ds_read_b64_tr_b16 v[194:195], v240 offset:1536
v_mfma_f32_16x16x32_bf16 v[72:75], v[220:223], v[168:171], v[72:75]
s_waitcnt lgkmcnt(8)
v_mfma_f32_16x16x32_bf16 v[80:83], v[224:227], v[168:171], v[80:83]
ds_read_b64_tr_b16 v[196:197], v241 offset:0
ds_read_b64_tr_b16 v[198:199], v241 offset:1536
v_mfma_f32_16x16x32_bf16 v[84:87], v[224:227], v[172:175], v[84:87]
s_waitcnt lgkmcnt(8)
v_mfma_f32_16x16x32_bf16 v[92:95], v[228:231], v[172:175], v[92:95]
ds_read_b64_tr_b16 v[200:201], v238 offset:128
)"
R"(ds_read_b64_tr_b16 v[202:203], v238 offset:1664
v_mfma_f32_16x16x32_bf16 v[88:91], v[228:231], v[168:171], v[88:91]
s_barrier
s_waitcnt lgkmcnt(8)
v_mfma_f32_16x16x32_bf16 v[0:3], v[184:187], v[96:99], v[0:3]
ds_read_b64_tr_b16 v[204:205], v239 offset:128
ds_read_b64_tr_b16 v[206:207], v239 offset:1664
v_mfma_f32_16x16x32_bf16 v[4:7], v[184:187], v[100:103], v[4:7]
s_waitcnt lgkmcnt(8)
ds_read_b128 v[112:115], v176 offset:0
v_mfma_f32_16x16x32_bf16 v[12:15], v[188:191], v[100:103], v[12:15]
ds_read_b64_tr_b16 v[208:209], v240 offset:128
ds_read_b64_tr_b16 v[210:211], v240 offset:1664
v_mfma_f32_16x16x32_bf16 v[8:11], v[188:191], v[96:99], v[8:11]
s_waitcnt lgkmcnt(9)
ds_read_b128 v[116:119], v176 offset:2048
)"
R"(v_mfma_f32_16x16x32_bf16 v[16:19], v[192:195], v[96:99], v[16:19]
ds_read_b64_tr_b16 v[212:213], v241 offset:128
ds_read_b64_tr_b16 v[214:215], v241 offset:1664
v_mfma_f32_16x16x32_bf16 v[20:23], v[192:195], v[100:103], v[20:23]
s_waitcnt lgkmcnt(10)
ds_read_b128 v[120:123], v177 offset:0
v_mfma_f32_16x16x32_bf16 v[28:31], v[196:199], v[100:103], v[28:31]
ds_read_b64_tr_b16 v[216:217], v238 offset:256
ds_read_b64_tr_b16 v[218:219], v238 offset:1792
v_mfma_f32_16x16x32_bf16 v[24:27], v[196:199], v[96:99], v[24:27]
s_waitcnt lgkmcnt(11)
ds_read_b128 v[124:127], v177 offset:2048
v_mfma_f32_16x16x32_bf16 v[32:35], v[200:203], v[96:99], v[32:35]
ds_read_b64_tr_b16 v[220:221], v239 offset:256
ds_read_b64_tr_b16 v[222:223], v239 offset:1792
v_mfma_f32_16x16x32_bf16 v[36:39], v[200:203], v[100:103], v[36:39]
)"
R"(s_waitcnt lgkmcnt(12)
v_mfma_f32_16x16x32_bf16 v[44:47], v[204:207], v[100:103], v[44:47]
ds_read_b64_tr_b16 v[224:225], v240 offset:256
ds_read_b64_tr_b16 v[226:227], v240 offset:1792
v_mfma_f32_16x16x32_bf16 v[40:43], v[204:207], v[96:99], v[40:43]
s_waitcnt lgkmcnt(11)
v_mfma_f32_16x16x32_bf16 v[48:51], v[208:211], v[96:99], v[48:51]
ds_read_b64_tr_b16 v[228:229], v241 offset:256
ds_read_b64_tr_b16 v[230:231], v241 offset:1792
v_mfma_f32_16x16x32_bf16 v[52:55], v[208:211], v[100:103], v[52:55]
s_waitcnt lgkmcnt(10)
v_mfma_f32_16x16x32_bf16 v[60:63], v[212:215], v[100:103], v[60:63]
ds_read_b64_tr_b16 v[184:185], v238 offset:12288
ds_read_b64_tr_b16 v[186:187], v238 offset:13824
v_mfma_f32_16x16x32_bf16 v[56:59], v[212:215], v[96:99], v[56:59]
s_waitcnt lgkmcnt(9)
)"
R"(v_mfma_f32_16x16x32_bf16 v[64:67], v[216:219], v[96:99], v[64:67]
ds_read_b64_tr_b16 v[188:189], v239 offset:12288
ds_read_b64_tr_b16 v[190:191], v239 offset:13824
v_mfma_f32_16x16x32_bf16 v[68:71], v[216:219], v[100:103], v[68:71]
s_waitcnt lgkmcnt(8)
v_mfma_f32_16x16x32_bf16 v[76:79], v[220:223], v[100:103], v[76:79]
ds_read_b64_tr_b16 v[192:193], v240 offset:12288
ds_read_b64_tr_b16 v[194:195], v240 offset:13824
v_mfma_f32_16x16x32_bf16 v[72:75], v[220:223], v[96:99], v[72:75]
s_waitcnt lgkmcnt(8)
v_mfma_f32_16x16x32_bf16 v[80:83], v[224:227], v[96:99], v[80:83]
ds_read_b64_tr_b16 v[196:197], v241 offset:12288
ds_read_b64_tr_b16 v[198:199], v241 offset:13824
v_mfma_f32_16x16x32_bf16 v[84:87], v[224:227], v[100:103], v[84:87]
s_waitcnt lgkmcnt(8)
v_mfma_f32_16x16x32_bf16 v[92:95], v[228:231], v[100:103], v[92:95]
)"
R"(ds_read_b64_tr_b16 v[200:201], v238 offset:12416
ds_read_b64_tr_b16 v[202:203], v238 offset:13952
v_mfma_f32_16x16x32_bf16 v[88:91], v[228:231], v[96:99], v[88:91]
s_barrier
s_waitcnt lgkmcnt(8)
v_mfma_f32_16x16x32_bf16 v[0:3], v[184:187], v[104:107], v[0:3]
ds_read_b64_tr_b16 v[204:205], v239 offset:12416
ds_read_b64_tr_b16 v[206:207], v239 offset:13952
v_mfma_f32_16x16x32_bf16 v[4:7], v[184:187], v[108:111], v[4:7]
s_waitcnt lgkmcnt(8)
ds_read_b128 v[128:131], v176 offset:16384
v_mfma_f32_16x16x32_bf16 v[12:15], v[188:191], v[108:111], v[12:15]
ds_read_b64_tr_b16 v[208:209], v240 offset:12416
ds_read_b64_tr_b16 v[210:211], v240 offset:13952
v_mfma_f32_16x16x32_bf16 v[8:11], v[188:191], v[104:107], v[8:11]
s_waitcnt lgkmcnt(9)
)"
R"(ds_read_b128 v[132:135], v176 offset:18432
v_mfma_f32_16x16x32_bf16 v[16:19], v[192:195], v[104:107], v[16:19]
ds_read_b64_tr_b16 v[212:213], v241 offset:12416
ds_read_b64_tr_b16 v[214:215], v241 offset:13952
v_mfma_f32_16x16x32_bf16 v[20:23], v[192:195], v[108:111], v[20:23]
s_waitcnt lgkmcnt(10)
ds_read_b128 v[136:139], v177 offset:16384
v_mfma_f32_16x16x32_bf16 v[28:31], v[196:199], v[108:111], v[28:31]
ds_read_b64_tr_b16 v[216:217], v238 offset:12544
ds_read_b64_tr_b16 v[218:219], v238 offset:14080
v_mfma_f32_16x16x32_bf16 v[24:27], v[196:199], v[104:107], v[24:27]
s_waitcnt lgkmcnt(11)
ds_read_b128 v[140:143], v177 offset:18432
v_mfma_f32_16x16x32_bf16 v[32:35], v[200:203], v[104:107], v[32:35]
ds_read_b64_tr_b16 v[220:221], v239 offset:12544
ds_read_b64_tr_b16 v[222:223], v239 offset:14080
)"
R"(v_mfma_f32_16x16x32_bf16 v[36:39], v[200:203], v[108:111], v[36:39]
s_waitcnt lgkmcnt(12)
v_mfma_f32_16x16x32_bf16 v[44:47], v[204:207], v[108:111], v[44:47]
ds_read_b64_tr_b16 v[224:225], v240 offset:12544
ds_read_b64_tr_b16 v[226:227], v240 offset:14080
v_mfma_f32_16x16x32_bf16 v[40:43], v[204:207], v[104:107], v[40:43]
s_waitcnt lgkmcnt(11)
v_mfma_f32_16x16x32_bf16 v[48:51], v[208:211], v[104:107], v[48:51]
ds_read_b64_tr_b16 v[228:229], v241 offset:12544
ds_read_b64_tr_b16 v[230:231], v241 offset:14080
v_mfma_f32_16x16x32_bf16 v[52:55], v[208:211], v[108:111], v[52:55]
s_waitcnt lgkmcnt(10)
v_mfma_f32_16x16x32_bf16 v[60:63], v[212:215], v[108:111], v[60:63]
ds_read_b64_tr_b16 v[184:185], v238 offset:24576
ds_read_b64_tr_b16 v[186:187], v238 offset:26112
v_mfma_f32_16x16x32_bf16 v[56:59], v[212:215], v[104:107], v[56:59]
)"
R"(s_waitcnt lgkmcnt(9)
v_mfma_f32_16x16x32_bf16 v[64:67], v[216:219], v[104:107], v[64:67]
ds_read_b64_tr_b16 v[188:189], v239 offset:24576
ds_read_b64_tr_b16 v[190:191], v239 offset:26112
v_mfma_f32_16x16x32_bf16 v[68:71], v[216:219], v[108:111], v[68:71]
s_waitcnt lgkmcnt(8)
v_mfma_f32_16x16x32_bf16 v[76:79], v[220:223], v[108:111], v[76:79]
ds_read_b64_tr_b16 v[192:193], v240 offset:24576
ds_read_b64_tr_b16 v[194:195], v240 offset:26112
v_mfma_f32_16x16x32_bf16 v[72:75], v[220:223], v[104:107], v[72:75]
s_waitcnt lgkmcnt(8)
v_mfma_f32_16x16x32_bf16 v[80:83], v[224:227], v[104:107], v[80:83]
ds_read_b64_tr_b16 v[196:197], v241 offset:24576
ds_read_b64_tr_b16 v[198:199], v241 offset:26112
v_mfma_f32_16x16x32_bf16 v[84:87], v[224:227], v[108:111], v[84:87]
s_waitcnt lgkmcnt(8)
)"
R"(v_mfma_f32_16x16x32_bf16 v[92:95], v[228:231], v[108:111], v[92:95]
ds_read_b64_tr_b16 v[200:201], v238 offset:24704
ds_read_b64_tr_b16 v[202:203], v238 offset:26240
v_mfma_f32_16x16x32_bf16 v[88:91], v[228:231], v[104:107], v[88:91]
s_barrier
s_waitcnt lgkmcnt(8)
v_mfma_f32_16x16x32_bf16 v[0:3], v[184:187], v[112:115], v[0:3]
ds_read_b64_tr_b16 v[204:205], v239 offset:24704
ds_read_b64_tr_b16 v[206:207], v239 offset:26240
v_mfma_f32_16x16x32_bf16 v[4:7], v[184:187], v[116:119], v[4:7]
s_waitcnt lgkmcnt(8)
v_mfma_f32_16x16x32_bf16 v[12:15], v[188:191], v[116:119], v[12:15]
ds_read_b64_tr_b16 v[208:209], v240 offset:24704
ds_read_b64_tr_b16 v[210:211], v240 offset:26240
v_mfma_f32_16x16x32_bf16 v[8:11], v[188:191], v[112:115], v[8:11]
s_waitcnt lgkmcnt(8)
)"
R"(v_mfma_f32_16x16x32_bf16 v[16:19], v[192:195], v[112:115], v[16:19]
ds_read_b64_tr_b16 v[212:213], v241 offset:24704
ds_read_b64_tr_b16 v[214:215], v241 offset:26240
v_mfma_f32_16x16x32_bf16 v[20:23], v[192:195], v[116:119], v[20:23]
s_waitcnt lgkmcnt(8)
v_mfma_f32_16x16x32_bf16 v[28:31], v[196:199], v[116:119], v[28:31]
ds_read_b64_tr_b16 v[216:217], v238 offset:24832
ds_read_b64_tr_b16 v[218:219], v238 offset:26368
v_mfma_f32_16x16x32_bf16 v[24:27], v[196:199], v[112:115], v[24:27]
s_waitcnt lgkmcnt(8)
v_mfma_f32_16x16x32_bf16 v[32:35], v[200:203], v[112:115], v[32:35]
ds_read_b64_tr_b16 v[220:221], v239 offset:24832
ds_read_b64_tr_b16 v[222:223], v239 offset:26368
v_mfma_f32_16x16x32_bf16 v[36:39], v[200:203], v[116:119], v[36:39]
s_waitcnt lgkmcnt(8)
v_mfma_f32_16x16x32_bf16 v[44:47], v[204:207], v[116:119], v[44:47]
)"
R"(ds_read_b64_tr_b16 v[224:225], v240 offset:24832
ds_read_b64_tr_b16 v[226:227], v240 offset:26368
v_mfma_f32_16x16x32_bf16 v[40:43], v[204:207], v[112:115], v[40:43]
s_waitcnt lgkmcnt(8)
v_mfma_f32_16x16x32_bf16 v[48:51], v[208:211], v[112:115], v[48:51]
ds_read_b64_tr_b16 v[228:229], v241 offset:24832
ds_read_b64_tr_b16 v[230:231], v241 offset:26368
v_mfma_f32_16x16x32_bf16 v[52:55], v[208:211], v[116:119], v[52:55]
s_waitcnt lgkmcnt(8)
v_mfma_f32_16x16x32_bf16 v[60:63], v[212:215], v[116:119], v[60:63]
ds_read_b64_tr_b16 v[184:185], v238 offset:36864
ds_read_b64_tr_b16 v[186:187], v238 offset:38400
v_mfma_f32_16x16x32_bf16 v[56:59], v[212:215], v[112:115], v[56:59]
s_waitcnt lgkmcnt(8)
v_mfma_f32_16x16x32_bf16 v[64:67], v[216:219], v[112:115], v[64:67]
ds_read_b64_tr_b16 v[188:189], v239 offset:36864
)"
R"(ds_read_b64_tr_b16 v[190:191], v239 offset:38400
v_mfma_f32_16x16x32_bf16 v[68:71], v[216:219], v[116:119], v[68:71]
s_waitcnt lgkmcnt(8)
v_mfma_f32_16x16x32_bf16 v[76:79], v[220:223], v[116:119], v[76:79]
ds_read_b64_tr_b16 v[192:193], v240 offset:36864
ds_read_b64_tr_b16 v[194:195], v240 offset:38400
v_mfma_f32_16x16x32_bf16 v[72:75], v[220:223], v[112:115], v[72:75]
s_waitcnt lgkmcnt(8)
v_mfma_f32_16x16x32_bf16 v[80:83], v[224:227], v[112:115], v[80:83]
ds_read_b64_tr_b16 v[196:197], v241 offset:36864
ds_read_b64_tr_b16 v[198:199], v241 offset:38400
v_mfma_f32_16x16x32_bf16 v[84:87], v[224:227], v[116:119], v[84:87]
s_waitcnt lgkmcnt(8)
v_mfma_f32_16x16x32_bf16 v[92:95], v[228:231], v[116:119], v[92:95]
ds_read_b64_tr_b16 v[200:201], v238 offset:36992
ds_read_b64_tr_b16 v[202:203], v238 offset:38528
)"
R"(v_mfma_f32_16x16x32_bf16 v[88:91], v[228:231], v[112:115], v[88:91]
s_barrier
s_waitcnt lgkmcnt(8)
v_mfma_f32_16x16x32_bf16 v[0:3], v[184:187], v[120:123], v[0:3]
ds_read_b64_tr_b16 v[204:205], v239 offset:36992
ds_read_b64_tr_b16 v[206:207], v239 offset:38528
v_mfma_f32_16x16x32_bf16 v[4:7], v[184:187], v[124:127], v[4:7]
s_waitcnt lgkmcnt(8)
ds_read_b128 v[144:147], v176 offset:0
v_mfma_f32_16x16x32_bf16 v[12:15], v[188:191], v[124:127], v[12:15]
ds_read_b64_tr_b16 v[208:209], v240 offset:36992
ds_read_b64_tr_b16 v[210:211], v240 offset:38528
v_mfma_f32_16x16x32_bf16 v[8:11], v[188:191], v[120:123], v[8:11]
s_waitcnt lgkmcnt(9)
ds_read_b128 v[148:151], v176 offset:2048
v_mfma_f32_16x16x32_bf16 v[16:19], v[192:195], v[120:123], v[16:19]
)"
R"(ds_read_b64_tr_b16 v[212:213], v241 offset:36992
ds_read_b64_tr_b16 v[214:215], v241 offset:38528
v_mfma_f32_16x16x32_bf16 v[20:23], v[192:195], v[124:127], v[20:23]
s_waitcnt lgkmcnt(10)
ds_read_b128 v[152:155], v177 offset:0
v_mfma_f32_16x16x32_bf16 v[28:31], v[196:199], v[124:127], v[28:31]
ds_read_b64_tr_b16 v[216:217], v238 offset:37120
ds_read_b64_tr_b16 v[218:219], v238 offset:38656
v_mfma_f32_16x16x32_bf16 v[24:27], v[196:199], v[120:123], v[24:27]
s_waitcnt lgkmcnt(11)
ds_read_b128 v[156:159], v177 offset:2048
v_mfma_f32_16x16x32_bf16 v[32:35], v[200:203], v[120:123], v[32:35]
ds_read_b64_tr_b16 v[220:221], v239 offset:37120
ds_read_b64_tr_b16 v[222:223], v239 offset:38656
v_mfma_f32_16x16x32_bf16 v[36:39], v[200:203], v[124:127], v[36:39]
s_waitcnt lgkmcnt(12)
)"
R"(v_mfma_f32_16x16x32_bf16 v[44:47], v[204:207], v[124:127], v[44:47]
ds_read_b64_tr_b16 v[224:225], v240 offset:37120
ds_read_b64_tr_b16 v[226:227], v240 offset:38656
v_mfma_f32_16x16x32_bf16 v[40:43], v[204:207], v[120:123], v[40:43]
s_waitcnt lgkmcnt(11)
v_mfma_f32_16x16x32_bf16 v[48:51], v[208:211], v[120:123], v[48:51]
ds_read_b64_tr_b16 v[228:229], v241 offset:37120
ds_read_b64_tr_b16 v[230:231], v241 offset:38656
v_mfma_f32_16x16x32_bf16 v[52:55], v[208:211], v[124:127], v[52:55]
s_waitcnt lgkmcnt(10)
v_mfma_f32_16x16x32_bf16 v[60:63], v[212:215], v[124:127], v[60:63]
ds_read_b64_tr_b16 v[184:185], v238 offset:49152
ds_read_b64_tr_b16 v[186:187], v238 offset:50688
v_mfma_f32_16x16x32_bf16 v[56:59], v[212:215], v[120:123], v[56:59]
s_waitcnt lgkmcnt(9)
v_mfma_f32_16x16x32_bf16 v[64:67], v[216:219], v[120:123], v[64:67]
)"
R"(ds_read_b64_tr_b16 v[188:189], v239 offset:49152
ds_read_b64_tr_b16 v[190:191], v239 offset:50688
v_mfma_f32_16x16x32_bf16 v[68:71], v[216:219], v[124:127], v[68:71]
s_waitcnt lgkmcnt(8)
v_mfma_f32_16x16x32_bf16 v[76:79], v[220:223], v[124:127], v[76:79]
ds_read_b64_tr_b16 v[192:193], v240 offset:49152
ds_read_b64_tr_b16 v[194:195], v240 offset:50688
v_mfma_f32_16x16x32_bf16 v[72:75], v[220:223], v[120:123], v[72:75]
s_waitcnt lgkmcnt(8)
v_mfma_f32_16x16x32_bf16 v[80:83], v[224:227], v[120:123], v[80:83]
ds_read_b64_tr_b16 v[196:197], v241 offset:49152
ds_read_b64_tr_b16 v[198:199], v241 offset:50688
v_mfma_f32_16x16x32_bf16 v[84:87], v[224:227], v[124:127], v[84:87]
s_waitcnt lgkmcnt(8)
v_mfma_f32_16x16x32_bf16 v[92:95], v[228:231], v[124:127], v[92:95]
ds_read_b64_tr_b16 v[200:201], v238 offset:49280
)"
R"(ds_read_b64_tr_b16 v[202:203], v238 offset:50816
v_mfma_f32_16x16x32_bf16 v[88:91], v[228:231], v[120:123], v[88:91]
s_barrier
s_waitcnt lgkmcnt(8)
v_mfma_f32_16x16x32_bf16 v[0:3], v[184:187], v[128:131], v[0:3]
ds_read_b64_tr_b16 v[204:205], v239 offset:49280
ds_read_b64_tr_b16 v[206:207], v239 offset:50816
v_mfma_f32_16x16x32_bf16 v[4:7], v[184:187], v[132:135], v[4:7]
s_waitcnt lgkmcnt(8)
ds_read_b128 v[160:163], v176 offset:16384
v_mfma_f32_16x16x32_bf16 v[12:15], v[188:191], v[132:135], v[12:15]
ds_read_b64_tr_b16 v[208:209], v240 offset:49280
ds_read_b64_tr_b16 v[210:211], v240 offset:50816
v_mfma_f32_16x16x32_bf16 v[8:11], v[188:191], v[128:131], v[8:11]
s_waitcnt lgkmcnt(9)
ds_read_b128 v[164:167], v176 offset:18432
)"
R"(v_mfma_f32_16x16x32_bf16 v[16:19], v[192:195], v[128:131], v[16:19]
ds_read_b64_tr_b16 v[212:213], v241 offset:49280
ds_read_b64_tr_b16 v[214:215], v241 offset:50816
v_mfma_f32_16x16x32_bf16 v[20:23], v[192:195], v[132:135], v[20:23]
s_waitcnt lgkmcnt(10)
ds_read_b128 v[168:171], v177 offset:16384
v_mfma_f32_16x16x32_bf16 v[28:31], v[196:199], v[132:135], v[28:31]
ds_read_b64_tr_b16 v[216:217], v238 offset:49408
ds_read_b64_tr_b16 v[218:219], v238 offset:50944
v_mfma_f32_16x16x32_bf16 v[24:27], v[196:199], v[128:131], v[24:27]
s_waitcnt lgkmcnt(11)
ds_read_b128 v[172:175], v177 offset:18432
v_mfma_f32_16x16x32_bf16 v[32:35], v[200:203], v[128:131], v[32:35]
ds_read_b64_tr_b16 v[220:221], v239 offset:49408
ds_read_b64_tr_b16 v[222:223], v239 offset:50944
v_mfma_f32_16x16x32_bf16 v[36:39], v[200:203], v[132:135], v[36:39]
)"
R"(s_waitcnt lgkmcnt(12)
v_mfma_f32_16x16x32_bf16 v[44:47], v[204:207], v[132:135], v[44:47]
ds_read_b64_tr_b16 v[224:225], v240 offset:49408
ds_read_b64_tr_b16 v[226:227], v240 offset:50944
v_mfma_f32_16x16x32_bf16 v[40:43], v[204:207], v[128:131], v[40:43]
s_waitcnt lgkmcnt(11)
v_mfma_f32_16x16x32_bf16 v[48:51], v[208:211], v[128:131], v[48:51]
ds_read_b64_tr_b16 v[228:229], v241 offset:49408
ds_read_b64_tr_b16 v[230:231], v241 offset:50944
v_mfma_f32_16x16x32_bf16 v[52:55], v[208:211], v[132:135], v[52:55]
s_waitcnt lgkmcnt(10)
v_mfma_f32_16x16x32_bf16 v[60:63], v[212:215], v[132:135], v[60:63]
ds_read_b64_tr_b16 v[184:185], v238 offset:0
ds_read_b64_tr_b16 v[186:187], v238 offset:1536
v_mfma_f32_16x16x32_bf16 v[56:59], v[212:215], v[128:131], v[56:59]
s_waitcnt lgkmcnt(9)
)"
R"(v_mfma_f32_16x16x32_bf16 v[64:67], v[216:219], v[128:131], v[64:67]
ds_read_b64_tr_b16 v[188:189], v239 offset:0
ds_read_b64_tr_b16 v[190:191], v239 offset:1536
v_mfma_f32_16x16x32_bf16 v[68:71], v[216:219], v[132:135], v[68:71]
s_waitcnt lgkmcnt(8)
v_mfma_f32_16x16x32_bf16 v[76:79], v[220:223], v[132:135], v[76:79]
ds_read_b64_tr_b16 v[192:193], v240 offset:0
ds_read_b64_tr_b16 v[194:195], v240 offset:1536
v_mfma_f32_16x16x32_bf16 v[72:75], v[220:223], v[128:131], v[72:75]
s_waitcnt lgkmcnt(8)
v_mfma_f32_16x16x32_bf16 v[80:83], v[224:227], v[128:131], v[80:83]
ds_read_b64_tr_b16 v[196:197], v241 offset:0
ds_read_b64_tr_b16 v[198:199], v241 offset:1536
v_mfma_f32_16x16x32_bf16 v[84:87], v[224:227], v[132:135], v[84:87]
s_waitcnt lgkmcnt(8)
v_mfma_f32_16x16x32_bf16 v[92:95], v[228:231], v[132:135], v[92:95]
)"
R"(ds_read_b64_tr_b16 v[200:201], v238 offset:128
ds_read_b64_tr_b16 v[202:203], v238 offset:1664
v_mfma_f32_16x16x32_bf16 v[88:91], v[228:231], v[128:131], v[88:91]
s_barrier
s_waitcnt lgkmcnt(8)
v_mfma_f32_16x16x32_bf16 v[0:3], v[184:187], v[136:139], v[0:3]
ds_read_b64_tr_b16 v[204:205], v239 offset:128
ds_read_b64_tr_b16 v[206:207], v239 offset:1664
v_mfma_f32_16x16x32_bf16 v[4:7], v[184:187], v[140:143], v[4:7]
s_waitcnt lgkmcnt(8)
v_mfma_f32_16x16x32_bf16 v[12:15], v[188:191], v[140:143], v[12:15]
ds_read_b64_tr_b16 v[208:209], v240 offset:128
ds_read_b64_tr_b16 v[210:211], v240 offset:1664
v_mfma_f32_16x16x32_bf16 v[8:11], v[188:191], v[136:139], v[8:11]
s_waitcnt lgkmcnt(8)
v_mfma_f32_16x16x32_bf16 v[16:19], v[192:195], v[136:139], v[16:19]
)"
R"(ds_read_b64_tr_b16 v[212:213], v241 offset:128
ds_read_b64_tr_b16 v[214:215], v241 offset:1664
v_mfma_f32_16x16x32_bf16 v[20:23], v[192:195], v[140:143], v[20:23]
s_waitcnt lgkmcnt(8)
v_mfma_f32_16x16x32_bf16 v[28:31], v[196:199], v[140:143], v[28:31]
ds_read_b64_tr_b16 v[216:217], v238 offset:256
ds_read_b64_tr_b16 v[218:219], v238 offset:1792
v_mfma_f32_16x16x32_bf16 v[24:27], v[196:199], v[136:139], v[24:27]
s_waitcnt lgkmcnt(8)
v_mfma_f32_16x16x32_bf16 v[32:35], v[200:203], v[136:139], v[32:35]
ds_read_b64_tr_b16 v[220:221], v239 offset:256
ds_read_b64_tr_b16 v[222:223], v239 offset:1792
v_mfma_f32_16x16x32_bf16 v[36:39], v[200:203], v[140:143], v[36:39]
s_waitcnt lgkmcnt(8)
v_mfma_f32_16x16x32_bf16 v[44:47], v[204:207], v[140:143], v[44:47]
ds_read_b64_tr_b16 v[224:225], v240 offset:256
)"
R"(ds_read_b64_tr_b16 v[226:227], v240 offset:1792
v_mfma_f32_16x16x32_bf16 v[40:43], v[204:207], v[136:139], v[40:43]
s_waitcnt lgkmcnt(8)
v_mfma_f32_16x16x32_bf16 v[48:51], v[208:211], v[136:139], v[48:51]
ds_read_b64_tr_b16 v[228:229], v241 offset:256
ds_read_b64_tr_b16 v[230:231], v241 offset:1792
v_mfma_f32_16x16x32_bf16 v[52:55], v[208:211], v[140:143], v[52:55]
s_waitcnt lgkmcnt(8)
v_mfma_f32_16x16x32_bf16 v[60:63], v[212:215], v[140:143], v[60:63]
ds_read_b64_tr_b16 v[184:185], v238 offset:12288
ds_read_b64_tr_b16 v[186:187], v238 offset:13824
v_mfma_f32_16x16x32_bf16 v[56:59], v[212:215], v[136:139], v[56:59]
s_waitcnt lgkmcnt(8)
v_mfma_f32_16x16x32_bf16 v[64:67], v[216:219], v[136:139], v[64:67]
ds_read_b64_tr_b16 v[188:189], v239 offset:12288
ds_read_b64_tr_b16 v[190:191], v239 offset:13824
)"
R"(v_mfma_f32_16x16x32_bf16 v[68:71], v[216:219], v[140:143], v[68:71]
s_waitcnt lgkmcnt(8)
v_mfma_f32_16x16x32_bf16 v[76:79], v[220:223], v[140:143], v[76:79]
ds_read_b64_tr_b16 v[192:193], v240 offset:12288
ds_read_b64_tr_b16 v[194:195], v240 offset:13824
v_mfma_f32_16x16x32_bf16 v[72:75], v[220:223], v[136:139], v[72:75]
s_waitcnt lgkmcnt(8)
v_mfma_f32_16x16x32_bf16 v[80:83], v[224:227], v[136:139], v[80:83]
ds_read_b64_tr_b16 v[196:197], v241 offset:12288
ds_read_b64_tr_b16 v[198:199], v241 offset:13824
v_mfma_f32_16x16x32_bf16 v[84:87], v[224:227], v[140:143], v[84:87]
s_waitcnt lgkmcnt(8)
v_mfma_f32_16x16x32_bf16 v[92:95], v[228:231], v[140:143], v[92:95]
ds_read_b64_tr_b16 v[200:201], v238 offset:12416
ds_read_b64_tr_b16 v[202:203], v238 offset:13952
v_mfma_f32_16x16x32_bf16 v[88:91], v[228:231], v[136:139], v[88:91]
)"
R"(s_barrier
s_waitcnt lgkmcnt(8)
v_mfma_f32_16x16x32_bf16 v[0:3], v[184:187], v[144:147], v[0:3]
ds_read_b64_tr_b16 v[204:205], v239 offset:12416
ds_read_b64_tr_b16 v[206:207], v239 offset:13952
v_mfma_f32_16x16x32_bf16 v[4:7], v[184:187], v[148:151], v[4:7]
s_waitcnt lgkmcnt(8)
v_mfma_f32_16x16x32_bf16 v[12:15], v[188:191], v[148:151], v[12:15]
ds_read_b64_tr_b16 v[208:209], v240 offset:12416
ds_read_b64_tr_b16 v[210:211], v240 offset:13952
v_mfma_f32_16x16x32_bf16 v[8:11], v[188:191], v[144:147], v[8:11]
s_waitcnt lgkmcnt(8)
v_mfma_f32_16x16x32_bf16 v[16:19], v[192:195], v[144:147], v[16:19]
ds_read_b64_tr_b16 v[212:213], v241 offset:12416
ds_read_b64_tr_b16 v[214:215], v241 offset:13952
v_mfma_f32_16x16x32_bf16 v[20:23], v[192:195], v[148:151], v[20:23]
)"
R"(s_waitcnt lgkmcnt(8)
v_mfma_f32_16x16x32_bf16 v[28:31], v[196:199], v[148:151], v[28:31]
ds_read_b64_tr_b16 v[216:217], v238 offset:12544
ds_read_b64_tr_b16 v[218:219], v238 offset:14080
v_mfma_f32_16x16x32_bf16 v[24:27], v[196:199], v[144:147], v[24:27]
s_waitcnt lgkmcnt(8)
v_mfma_f32_16x16x32_bf16 v[32:35], v[200:203], v[144:147], v[32:35]
ds_read_b64_tr_b16 v[220:221], v239 offset:12544
ds_read_b64_tr_b16 v[222:223], v239 offset:14080
v_mfma_f32_16x16x32_bf16 v[36:39], v[200:203], v[148:151], v[36:39]
s_waitcnt lgkmcnt(8)
v_mfma_f32_16x16x32_bf16 v[44:47], v[204:207], v[148:151], v[44:47]
ds_read_b64_tr_b16 v[224:225], v240 offset:12544
ds_read_b64_tr_b16 v[226:227], v240 offset:14080
v_mfma_f32_16x16x32_bf16 v[40:43], v[204:207], v[144:147], v[40:43]
s_waitcnt lgkmcnt(8)
)"
R"(v_mfma_f32_16x16x32_bf16 v[48:51], v[208:211], v[144:147], v[48:51]
ds_read_b64_tr_b16 v[228:229], v241 offset:12544
ds_read_b64_tr_b16 v[230:231], v241 offset:14080
v_mfma_f32_16x16x32_bf16 v[52:55], v[208:211], v[148:151], v[52:55]
s_waitcnt lgkmcnt(8)
v_mfma_f32_16x16x32_bf16 v[60:63], v[212:215], v[148:151], v[60:63]
ds_read_b64_tr_b16 v[184:185], v238 offset:24576
ds_read_b64_tr_b16 v[186:187], v238 offset:26112
v_mfma_f32_16x16x32_bf16 v[56:59], v[212:215], v[144:147], v[56:59]
s_waitcnt lgkmcnt(8)
v_mfma_f32_16x16x32_bf16 v[64:67], v[216:219], v[144:147], v[64:67]
ds_read_b64_tr_b16 v[188:189], v239 offset:24576
ds_read_b64_tr_b16 v[190:191], v239 offset:26112
v_mfma_f32_16x16x32_bf16 v[68:71], v[216:219], v[148:151], v[68:71]
s_waitcnt lgkmcnt(8)
v_mfma_f32_16x16x32_bf16 v[76:79], v[220:223], v[148:151], v[76:79]
)"
R"(ds_read_b64_tr_b16 v[192:193], v240 offset:24576
ds_read_b64_tr_b16 v[194:195], v240 offset:26112
v_mfma_f32_16x16x32_bf16 v[72:75], v[220:223], v[144:147], v[72:75]
s_waitcnt lgkmcnt(8)
v_mfma_f32_16x16x32_bf16 v[80:83], v[224:227], v[144:147], v[80:83]
ds_read_b64_tr_b16 v[196:197], v241 offset:24576
ds_read_b64_tr_b16 v[198:199], v241 offset:26112
v_mfma_f32_16x16x32_bf16 v[84:87], v[224:227], v[148:151], v[84:87]
s_waitcnt lgkmcnt(8)
v_mfma_f32_16x16x32_bf16 v[92:95], v[228:231], v[148:151], v[92:95]
ds_read_b64_tr_b16 v[200:201], v238 offset:24704
ds_read_b64_tr_b16 v[202:203], v238 offset:26240
v_mfma_f32_16x16x32_bf16 v[88:91], v[228:231], v[144:147], v[88:91]
s_barrier
s_waitcnt lgkmcnt(8)
v_mfma_f32_16x16x32_bf16 v[0:3], v[184:187], v[152:155], v[0:3]
)"
R"(ds_read_b64_tr_b16 v[204:205], v239 offset:24704
ds_read_b64_tr_b16 v[206:207], v239 offset:26240
v_mfma_f32_16x16x32_bf16 v[4:7], v[184:187], v[156:159], v[4:7]
s_waitcnt lgkmcnt(8)
v_mfma_f32_16x16x32_bf16 v[12:15], v[188:191], v[156:159], v[12:15]
ds_read_b64_tr_b16 v[208:209], v240 offset:24704
ds_read_b64_tr_b16 v[210:211], v240 offset:26240
v_mfma_f32_16x16x32_bf16 v[8:11], v[188:191], v[152:155], v[8:11]
s_waitcnt lgkmcnt(8)
v_mfma_f32_16x16x32_bf16 v[16:19], v[192:195], v[152:155], v[16:19]
ds_read_b64_tr_b16 v[212:213], v241 offset:24704
ds_read_b64_tr_b16 v[214:215], v241 offset:26240
v_mfma_f32_16x16x32_bf16 v[20:23], v[192:195], v[156:159], v[20:23]
s_waitcnt lgkmcnt(8)
v_mfma_f32_16x16x32_bf16 v[28:31], v[196:199], v[156:159], v[28:31]
ds_read_b64_tr_b16 v[216:217], v238 offset:24832
)"
R"(ds_read_b64_tr_b16 v[218:219], v238 offset:26368
v_mfma_f32_16x16x32_bf16 v[24:27], v[196:199], v[152:155], v[24:27]
s_waitcnt lgkmcnt(8)
v_mfma_f32_16x16x32_bf16 v[32:35], v[200:203], v[152:155], v[32:35]
ds_read_b64_tr_b16 v[220:221], v239 offset:24832
ds_read_b64_tr_b16 v[222:223], v239 offset:26368
v_mfma_f32_16x16x32_bf16 v[36:39], v[200:203], v[156:159], v[36:39]
s_waitcnt lgkmcnt(8)
v_mfma_f32_16x16x32_bf16 v[44:47], v[204:207], v[156:159], v[44:47]
ds_read_b64_tr_b16 v[224:225], v240 offset:24832
ds_read_b64_tr_b16 v[226:227], v240 offset:26368
v_mfma_f32_16x16x32_bf16 v[40:43], v[204:207], v[152:155], v[40:43]
s_waitcnt lgkmcnt(8)
v_mfma_f32_16x16x32_bf16 v[48:51], v[208:211], v[152:155], v[48:51]
ds_read_b64_tr_b16 v[228:229], v241 offset:24832
ds_read_b64_tr_b16 v[230:231], v241 offset:26368
)"
R"(v_mfma_f32_16x16x32_bf16 v[52:55], v[208:211], v[156:159], v[52:55]
s_waitcnt lgkmcnt(8)
v_mfma_f32_16x16x32_bf16 v[60:63], v[212:215], v[156:159], v[60:63]
ds_read_b64_tr_b16 v[184:185], v238 offset:36864
ds_read_b64_tr_b16 v[186:187], v238 offset:38400
v_mfma_f32_16x16x32_bf16 v[56:59], v[212:215], v[152:155], v[56:59]
s_waitcnt lgkmcnt(8)
v_mfma_f32_16x16x32_bf16 v[64:67], v[216:219], v[152:155], v[64:67]
ds_read_b64_tr_b16 v[188:189], v239 offset:36864
ds_read_b64_tr_b16 v[190:191], v239 offset:38400
v_mfma_f32_16x16x32_bf16 v[68:71], v[216:219], v[156:159], v[68:71]
s_waitcnt lgkmcnt(8)
v_mfma_f32_16x16x32_bf16 v[76:79], v[220:223], v[156:159], v[76:79]
ds_read_b64_tr_b16 v[192:193], v240 offset:36864
ds_read_b64_tr_b16 v[194:195], v240 offset:38400
v_mfma_f32_16x16x32_bf16 v[72:75], v[220:223], v[152:155], v[72:75]
)"
R"(s_waitcnt lgkmcnt(8)
v_mfma_f32_16x16x32_bf16 v[80:83], v[224:227], v[152:155], v[80:83]
ds_read_b64_tr_b16 v[196:197], v241 offset:36864
ds_read_b64_tr_b16 v[198:199], v241 offset:38400
v_mfma_f32_16x16x32_bf16 v[84:87], v[224:227], v[156:159], v[84:87]
s_waitcnt lgkmcnt(8)
v_mfma_f32_16x16x32_bf16 v[92:95], v[228:231], v[156:159], v[92:95]
ds_read_b64_tr_b16 v[200:201], v238 offset:36992
ds_read_b64_tr_b16 v[202:203], v238 offset:38528
v_mfma_f32_16x16x32_bf16 v[88:91], v[228:231], v[152:155], v[88:91]
s_barrier
s_waitcnt lgkmcnt(8)
v_mfma_f32_16x16x32_bf16 v[0:3], v[184:187], v[160:163], v[0:3]
ds_read_b64_tr_b16 v[204:205], v239 offset:36992
ds_read_b64_tr_b16 v[206:207], v239 offset:38528
v_mfma_f32_16x16x32_bf16 v[4:7], v[184:187], v[164:167], v[4:7]
)"
R"(s_waitcnt lgkmcnt(8)
v_mfma_f32_16x16x32_bf16 v[12:15], v[188:191], v[164:167], v[12:15]
ds_read_b64_tr_b16 v[208:209], v240 offset:36992
ds_read_b64_tr_b16 v[210:211], v240 offset:38528
v_mfma_f32_16x16x32_bf16 v[8:11], v[188:191], v[160:163], v[8:11]
s_waitcnt lgkmcnt(8)
v_mfma_f32_16x16x32_bf16 v[16:19], v[192:195], v[160:163], v[16:19]
ds_read_b64_tr_b16 v[212:213], v241 offset:36992
ds_read_b64_tr_b16 v[214:215], v241 offset:38528
v_mfma_f32_16x16x32_bf16 v[20:23], v[192:195], v[164:167], v[20:23]
s_waitcnt lgkmcnt(8)
v_mfma_f32_16x16x32_bf16 v[28:31], v[196:199], v[164:167], v[28:31]
ds_read_b64_tr_b16 v[216:217], v238 offset:37120
ds_read_b64_tr_b16 v[218:219], v238 offset:38656
v_mfma_f32_16x16x32_bf16 v[24:27], v[196:199], v[160:163], v[24:27]
s_waitcnt lgkmcnt(8)
)"
R"(v_mfma_f32_16x16x32_bf16 v[32:35], v[200:203], v[160:163], v[32:35]
ds_read_b64_tr_b16 v[220:221], v239 offset:37120
ds_read_b64_tr_b16 v[222:223], v239 offset:38656
v_mfma_f32_16x16x32_bf16 v[36:39], v[200:203], v[164:167], v[36:39]
s_waitcnt lgkmcnt(8)
v_mfma_f32_16x16x32_bf16 v[44:47], v[204:207], v[164:167], v[44:47]
ds_read_b64_tr_b16 v[224:225], v240 offset:37120
ds_read_b64_tr_b16 v[226:227], v240 offset:38656
v_mfma_f32_16x16x32_bf16 v[40:43], v[204:207], v[160:163], v[40:43]
s_waitcnt lgkmcnt(8)
v_mfma_f32_16x16x32_bf16 v[48:51], v[208:211], v[160:163], v[48:51]
ds_read_b64_tr_b16 v[228:229], v241 offset:37120
ds_read_b64_tr_b16 v[230:231], v241 offset:38656
v_mfma_f32_16x16x32_bf16 v[52:55], v[208:211], v[164:167], v[52:55]
s_waitcnt lgkmcnt(8)
v_mfma_f32_16x16x32_bf16 v[60:63], v[212:215], v[164:167], v[60:63]
)"
R"(ds_read_b64_tr_b16 v[184:185], v238 offset:0
ds_read_b64_tr_b16 v[186:187], v238 offset:1536
v_mfma_f32_16x16x32_bf16 v[56:59], v[212:215], v[160:163], v[56:59]
s_waitcnt lgkmcnt(8)
v_mfma_f32_16x16x32_bf16 v[64:67], v[216:219], v[160:163], v[64:67]
ds_read_b64_tr_b16 v[188:189], v239 offset:0
ds_read_b64_tr_b16 v[190:191], v239 offset:1536
v_mfma_f32_16x16x32_bf16 v[68:71], v[216:219], v[164:167], v[68:71]
s_waitcnt lgkmcnt(8)
v_mfma_f32_16x16x32_bf16 v[76:79], v[220:223], v[164:167], v[76:79]
ds_read_b64_tr_b16 v[192:193], v240 offset:0
ds_read_b64_tr_b16 v[194:195], v240 offset:1536
v_mfma_f32_16x16x32_bf16 v[72:75], v[220:223], v[160:163], v[72:75]
s_waitcnt lgkmcnt(8)
v_mfma_f32_16x16x32_bf16 v[80:83], v[224:227], v[160:163], v[80:83]
ds_read_b64_tr_b16 v[196:197], v241 offset:0
)"
R"(ds_read_b64_tr_b16 v[198:199], v241 offset:1536
v_mfma_f32_16x16x32_bf16 v[84:87], v[224:227], v[164:167], v[84:87]
s_waitcnt lgkmcnt(8)
v_mfma_f32_16x16x32_bf16 v[92:95], v[228:231], v[164:167], v[92:95]
ds_read_b64_tr_b16 v[200:201], v238 offset:128
ds_read_b64_tr_b16 v[202:203], v238 offset:1664
v_mfma_f32_16x16x32_bf16 v[88:91], v[228:231], v[160:163], v[88:91]
s_barrier
s_waitcnt lgkmcnt(8)
v_mfma_f32_16x16x32_bf16 v[0:3], v[184:187], v[168:171], v[0:3]
ds_read_b64_tr_b16 v[204:205], v239 offset:128
ds_read_b64_tr_b16 v[206:207], v239 offset:1664
v_mfma_f32_16x16x32_bf16 v[4:7], v[184:187], v[172:175], v[4:7]
s_waitcnt lgkmcnt(8)
v_mfma_f32_16x16x32_bf16 v[12:15], v[188:191], v[172:175], v[12:15]
ds_read_b64_tr_b16 v[208:209], v240 offset:128
)"
R"(ds_read_b64_tr_b16 v[210:211], v240 offset:1664
v_mfma_f32_16x16x32_bf16 v[8:11], v[188:191], v[168:171], v[8:11]
s_waitcnt lgkmcnt(8)
v_mfma_f32_16x16x32_bf16 v[16:19], v[192:195], v[168:171], v[16:19]
ds_read_b64_tr_b16 v[212:213], v241 offset:128
ds_read_b64_tr_b16 v[214:215], v241 offset:1664
v_mfma_f32_16x16x32_bf16 v[20:23], v[192:195], v[172:175], v[20:23]
s_waitcnt lgkmcnt(8)
v_mfma_f32_16x16x32_bf16 v[28:31], v[196:199], v[172:175], v[28:31]
ds_read_b64_tr_b16 v[216:217], v238 offset:256
ds_read_b64_tr_b16 v[218:219], v238 offset:1792
v_mfma_f32_16x16x32_bf16 v[24:27], v[196:199], v[168:171], v[24:27]
s_waitcnt lgkmcnt(8)
v_mfma_f32_16x16x32_bf16 v[32:35], v[200:203], v[168:171], v[32:35]
ds_read_b64_tr_b16 v[220:221], v239 offset:256
ds_read_b64_tr_b16 v[222:223], v239 offset:1792
)"
R"(v_mfma_f32_16x16x32_bf16 v[36:39], v[200:203], v[172:175], v[36:39]
s_waitcnt lgkmcnt(8)
v_mfma_f32_16x16x32_bf16 v[44:47], v[204:207], v[172:175], v[44:47]
ds_read_b64_tr_b16 v[224:225], v240 offset:256
ds_read_b64_tr_b16 v[226:227], v240 offset:1792
v_mfma_f32_16x16x32_bf16 v[40:43], v[204:207], v[168:171], v[40:43]
s_waitcnt lgkmcnt(8)
v_mfma_f32_16x16x32_bf16 v[48:51], v[208:211], v[168:171], v[48:51]
ds_read_b64_tr_b16 v[228:229], v241 offset:256
ds_read_b64_tr_b16 v[230:231], v241 offset:1792
v_mfma_f32_16x16x32_bf16 v[52:55], v[208:211], v[172:175], v[52:55]
s_waitcnt lgkmcnt(8)
v_mfma_f32_16x16x32_bf16 v[60:63], v[212:215], v[172:175], v[60:63]
v_mfma_f32_16x16x32_bf16 v[56:59], v[212:215], v[168:171], v[56:59]
s_waitcnt lgkmcnt(6)
v_mfma_f32_16x16x32_bf16 v[64:67], v[216:219], v[168:171], v[64:67]
)"
R"(v_mfma_f32_16x16x32_bf16 v[68:71], v[216:219], v[172:175], v[68:71]
s_waitcnt lgkmcnt(4)
v_mfma_f32_16x16x32_bf16 v[76:79], v[220:223], v[172:175], v[76:79]
v_mfma_f32_16x16x32_bf16 v[72:75], v[220:223], v[168:171], v[72:75]
s_waitcnt lgkmcnt(2)
v_mfma_f32_16x16x32_bf16 v[80:83], v[224:227], v[168:171], v[80:83]
v_mfma_f32_16x16x32_bf16 v[84:87], v[224:227], v[172:175], v[84:87]
s_waitcnt lgkmcnt(0)
v_mfma_f32_16x16x32_bf16 v[92:95], v[228:231], v[172:175], v[92:95]
v_mfma_f32_16x16x32_bf16 v[88:91], v[228:231], v[168:171], v[88:91]
s_waitcnt vmcnt(0)
ds_read_b128 v[184:187], v232 offset:0
ds_read_b128 v[188:191], v232 offset:6144
ds_read_b128 v[192:195], v233 offset:0
ds_read_b128 v[196:199], v233 offset:6144
ds_read_b128 v[200:203], v232 offset:128
)"
R"(v_mov_b32 v242, s57
v_pk_mul_f32 v[0:1], v[0:1], v[242:243] op_sel_hi:[1,0]
v_pk_mul_f32 v[2:3], v[2:3], v[242:243] op_sel_hi:[1,0]
v_pk_mul_f32 v[8:9], v[8:9], v[242:243] op_sel_hi:[1,0]
v_pk_mul_f32 v[10:11], v[10:11], v[242:243] op_sel_hi:[1,0]
v_pk_mul_f32 v[4:5], v[4:5], v[242:243] op_sel_hi:[1,0]
v_pk_mul_f32 v[6:7], v[6:7], v[242:243] op_sel_hi:[1,0]
v_pk_mul_f32 v[12:13], v[12:13], v[242:243] op_sel_hi:[1,0]
v_pk_mul_f32 v[14:15], v[14:15], v[242:243] op_sel_hi:[1,0]
v_cvt_pk_bf16_f32 v96, v0, v1
v_cvt_pk_bf16_f32 v97, v2, v3
v_cvt_pk_bf16_f32 v98, v8, v9
v_cvt_pk_bf16_f32 v99, v10, v11
v_cvt_pk_bf16_f32 v100, v4, v5
v_cvt_pk_bf16_f32 v101, v6, v7
v_cvt_pk_bf16_f32 v102, v12, v13
)"
R"(v_cvt_pk_bf16_f32 v103, v14, v15
v_permlane32_swap_b32 v96, v98
v_permlane32_swap_b32 v97, v99
v_permlane32_swap_b32 v100, v102
v_permlane32_swap_b32 v101, v103
v_permlane16_swap_b32 v96, v98
v_permlane16_swap_b32 v97, v99
v_permlane16_swap_b32 v100, v102
v_permlane16_swap_b32 v101, v103
s_waitcnt lgkmcnt(4)
v_mfma_f32_16x16x32_bf16 v[144:147], v[184:187], v[96:99], 0
v_mfma_f32_16x16x32_bf16 v[152:155], v[184:187], v[100:103], 0
ds_read_b128 v[184:187], v232 offset:6272
v_pk_mul_f32 v[16:17], v[16:17], v[242:243] op_sel_hi:[1,0]
v_pk_mul_f32 v[18:19], v[18:19], v[242:243] op_sel_hi:[1,0]
v_pk_mul_f32 v[24:25], v[24:25], v[242:243] op_sel_hi:[1,0]
)"
R"(v_pk_mul_f32 v[26:27], v[26:27], v[242:243] op_sel_hi:[1,0]
v_pk_mul_f32 v[20:21], v[20:21], v[242:243] op_sel_hi:[1,0]
v_pk_mul_f32 v[22:23], v[22:23], v[242:243] op_sel_hi:[1,0]
v_pk_mul_f32 v[28:29], v[28:29], v[242:243] op_sel_hi:[1,0]
v_pk_mul_f32 v[30:31], v[30:31], v[242:243] op_sel_hi:[1,0]
v_cvt_pk_bf16_f32 v104, v16, v17
v_cvt_pk_bf16_f32 v105, v18, v19
v_cvt_pk_bf16_f32 v106, v24, v25
v_cvt_pk_bf16_f32 v107, v26, v27
v_cvt_pk_bf16_f32 v108, v20, v21
v_cvt_pk_bf16_f32 v109, v22, v23
v_cvt_pk_bf16_f32 v110, v28, v29
v_cvt_pk_bf16_f32 v111, v30, v31
v_permlane32_swap_b32 v104, v106
v_permlane32_swap_b32 v105, v107
v_permlane32_swap_b32 v108, v110
)"
R"(v_permlane32_swap_b32 v109, v111
v_permlane16_swap_b32 v104, v106
v_permlane16_swap_b32 v105, v107
v_permlane16_swap_b32 v108, v110
v_permlane16_swap_b32 v109, v111
s_waitcnt lgkmcnt(4)
v_mfma_f32_16x16x32_bf16 v[156:159], v[188:191], v[100:103], 0
v_mfma_f32_16x16x32_bf16 v[148:151], v[188:191], v[96:99], 0
ds_read_b128 v[188:191], v233 offset:128
s_waitcnt lgkmcnt(4)
v_mfma_f32_16x16x32_bf16 v[144:147], v[192:195], v[104:107], v[144:147]
v_mfma_f32_16x16x32_bf16 v[152:155], v[192:195], v[108:111], v[152:155]
ds_read_b128 v[192:195], v233 offset:6272
v_pk_mul_f32 v[32:33], v[32:33], v[242:243] op_sel_hi:[1,0]
v_pk_mul_f32 v[34:35], v[34:35], v[242:243] op_sel_hi:[1,0]
v_pk_mul_f32 v[40:41], v[40:41], v[242:243] op_sel_hi:[1,0]
)"
R"(v_pk_mul_f32 v[42:43], v[42:43], v[242:243] op_sel_hi:[1,0]
v_pk_mul_f32 v[36:37], v[36:37], v[242:243] op_sel_hi:[1,0]
v_pk_mul_f32 v[38:39], v[38:39], v[242:243] op_sel_hi:[1,0]
v_pk_mul_f32 v[44:45], v[44:45], v[242:243] op_sel_hi:[1,0]
v_pk_mul_f32 v[46:47], v[46:47], v[242:243] op_sel_hi:[1,0]
v_cvt_pk_bf16_f32 v112, v32, v33
v_cvt_pk_bf16_f32 v113, v34, v35
v_cvt_pk_bf16_f32 v114, v40, v41
v_cvt_pk_bf16_f32 v115, v42, v43
v_cvt_pk_bf16_f32 v116, v36, v37
v_cvt_pk_bf16_f32 v117, v38, v39
v_cvt_pk_bf16_f32 v118, v44, v45
v_cvt_pk_bf16_f32 v119, v46, v47
v_permlane32_swap_b32 v112, v114
v_permlane32_swap_b32 v113, v115
v_permlane32_swap_b32 v116, v118
)"
R"(v_permlane32_swap_b32 v117, v119
v_permlane16_swap_b32 v112, v114
v_permlane16_swap_b32 v113, v115
v_permlane16_swap_b32 v116, v118
v_permlane16_swap_b32 v117, v119
s_waitcnt lgkmcnt(4)
v_mfma_f32_16x16x32_bf16 v[156:159], v[196:199], v[108:111], v[156:159]
v_mfma_f32_16x16x32_bf16 v[148:151], v[196:199], v[104:107], v[148:151]
ds_read_b128 v[196:199], v232 offset:256
s_waitcnt lgkmcnt(4)
v_mfma_f32_16x16x32_bf16 v[144:147], v[200:203], v[112:115], v[144:147]
v_mfma_f32_16x16x32_bf16 v[152:155], v[200:203], v[116:119], v[152:155]
ds_read_b128 v[200:203], v232 offset:6400
v_pk_mul_f32 v[48:49], v[48:49], v[242:243] op_sel_hi:[1,0]
v_pk_mul_f32 v[50:51], v[50:51], v[242:243] op_sel_hi:[1,0]
v_pk_mul_f32 v[56:57], v[56:57], v[242:243] op_sel_hi:[1,0]
)"
R"(v_pk_mul_f32 v[58:59], v[58:59], v[242:243] op_sel_hi:[1,0]
v_pk_mul_f32 v[52:53], v[52:53], v[242:243] op_sel_hi:[1,0]
v_pk_mul_f32 v[54:55], v[54:55], v[242:243] op_sel_hi:[1,0]
v_pk_mul_f32 v[60:61], v[60:61], v[242:243] op_sel_hi:[1,0]
v_pk_mul_f32 v[62:63], v[62:63], v[242:243] op_sel_hi:[1,0]
v_cvt_pk_bf16_f32 v120, v48, v49
v_cvt_pk_bf16_f32 v121, v50, v51
v_cvt_pk_bf16_f32 v122, v56, v57
v_cvt_pk_bf16_f32 v123, v58, v59
v_cvt_pk_bf16_f32 v124, v52, v53
v_cvt_pk_bf16_f32 v125, v54, v55
v_cvt_pk_bf16_f32 v126, v60, v61
v_cvt_pk_bf16_f32 v127, v62, v63
v_permlane32_swap_b32 v120, v122
v_permlane32_swap_b32 v121, v123
v_permlane32_swap_b32 v124, v126
)"
R"(v_permlane32_swap_b32 v125, v127
v_permlane16_swap_b32 v120, v122
v_permlane16_swap_b32 v121, v123
v_permlane16_swap_b32 v124, v126
v_permlane16_swap_b32 v125, v127
s_waitcnt lgkmcnt(4)
v_mfma_f32_16x16x32_bf16 v[156:159], v[184:187], v[116:119], v[156:159]
v_mfma_f32_16x16x32_bf16 v[148:151], v[184:187], v[112:115], v[148:151]
ds_read_b128 v[184:187], v233 offset:256
s_waitcnt lgkmcnt(4)
v_mfma_f32_16x16x32_bf16 v[144:147], v[188:191], v[120:123], v[144:147]
v_mfma_f32_16x16x32_bf16 v[152:155], v[188:191], v[124:127], v[152:155]
ds_read_b128 v[188:191], v233 offset:6400
v_pk_mul_f32 v[64:65], v[64:65], v[242:243] op_sel_hi:[1,0]
v_pk_mul_f32 v[66:67], v[66:67], v[242:243] op_sel_hi:[1,0]
v_pk_mul_f32 v[72:73], v[72:73], v[242:243] op_sel_hi:[1,0]
)"
R"(v_pk_mul_f32 v[74:75], v[74:75], v[242:243] op_sel_hi:[1,0]
v_pk_mul_f32 v[68:69], v[68:69], v[242:243] op_sel_hi:[1,0]
v_pk_mul_f32 v[70:71], v[70:71], v[242:243] op_sel_hi:[1,0]
v_pk_mul_f32 v[76:77], v[76:77], v[242:243] op_sel_hi:[1,0]
v_pk_mul_f32 v[78:79], v[78:79], v[242:243] op_sel_hi:[1,0]
v_cvt_pk_bf16_f32 v128, v64, v65
v_cvt_pk_bf16_f32 v129, v66, v67
v_cvt_pk_bf16_f32 v130, v72, v73
v_cvt_pk_bf16_f32 v131, v74, v75
v_cvt_pk_bf16_f32 v132, v68, v69
v_cvt_pk_bf16_f32 v133, v70, v71
v_cvt_pk_bf16_f32 v134, v76, v77
v_cvt_pk_bf16_f32 v135, v78, v79
v_permlane32_swap_b32 v128, v130
v_permlane32_swap_b32 v129, v131
v_permlane32_swap_b32 v132, v134
)"
R"(v_permlane32_swap_b32 v133, v135
v_permlane16_swap_b32 v128, v130
v_permlane16_swap_b32 v129, v131
v_permlane16_swap_b32 v132, v134
v_permlane16_swap_b32 v133, v135
s_waitcnt lgkmcnt(4)
v_mfma_f32_16x16x32_bf16 v[156:159], v[192:195], v[124:127], v[156:159]
v_mfma_f32_16x16x32_bf16 v[148:151], v[192:195], v[120:123], v[148:151]
s_waitcnt lgkmcnt(3)
v_mfma_f32_16x16x32_bf16 v[144:147], v[196:199], v[128:131], v[144:147]
v_mfma_f32_16x16x32_bf16 v[152:155], v[196:199], v[132:135], v[152:155]
v_pk_mul_f32 v[80:81], v[80:81], v[242:243] op_sel_hi:[1,0]
v_pk_mul_f32 v[82:83], v[82:83], v[242:243] op_sel_hi:[1,0]
v_pk_mul_f32 v[88:89], v[88:89], v[242:243] op_sel_hi:[1,0]
v_pk_mul_f32 v[90:91], v[90:91], v[242:243] op_sel_hi:[1,0]
v_pk_mul_f32 v[84:85], v[84:85], v[242:243] op_sel_hi:[1,0]
)"
R"(v_pk_mul_f32 v[86:87], v[86:87], v[242:243] op_sel_hi:[1,0]
v_pk_mul_f32 v[92:93], v[92:93], v[242:243] op_sel_hi:[1,0]
v_pk_mul_f32 v[94:95], v[94:95], v[242:243] op_sel_hi:[1,0]
v_cvt_pk_bf16_f32 v136, v80, v81
v_cvt_pk_bf16_f32 v137, v82, v83
v_cvt_pk_bf16_f32 v138, v88, v89
v_cvt_pk_bf16_f32 v139, v90, v91
v_cvt_pk_bf16_f32 v140, v84, v85
v_cvt_pk_bf16_f32 v141, v86, v87
v_cvt_pk_bf16_f32 v142, v92, v93
v_cvt_pk_bf16_f32 v143, v94, v95
v_permlane32_swap_b32 v136, v138
v_permlane32_swap_b32 v137, v139
v_permlane32_swap_b32 v140, v142
v_permlane32_swap_b32 v141, v143
v_permlane16_swap_b32 v136, v138
)"
R"(v_permlane16_swap_b32 v137, v139
v_permlane16_swap_b32 v140, v142
v_permlane16_swap_b32 v141, v143
s_waitcnt lgkmcnt(2)
v_mfma_f32_16x16x32_bf16 v[156:159], v[200:203], v[132:135], v[156:159]
v_mfma_f32_16x16x32_bf16 v[148:151], v[200:203], v[128:131], v[148:151]
s_waitcnt lgkmcnt(1)
v_mfma_f32_16x16x32_bf16 v[144:147], v[184:187], v[136:139], v[144:147]
v_mfma_f32_16x16x32_bf16 v[152:155], v[184:187], v[140:143], v[152:155]
s_waitcnt lgkmcnt(0)
v_mfma_f32_16x16x32_bf16 v[156:159], v[188:191], v[140:143], v[156:159]
v_mfma_f32_16x16x32_bf16 v[148:151], v[188:191], v[136:139], v[148:151]
v_mov_b32 v238, 0
v_mov_b32 v239, 0
v_mov_b32 v240, 0
v_mov_b32 v241, 0
)"
R"(v_max3_f32 v242, v144, v145, v146
s_nop 2
v_max3_f32 v242, v242, v147, v148
v_max3_f32 v242, v242, v149, v150
v_max_f32 v242, v242, v151
v_max3_f32 v243, v152, v153, v154
v_max3_f32 v243, v243, v155, v156
v_max3_f32 v243, v243, v157, v158
v_max_f32 v243, v243, v159
v_mov_b32 v212, v242
v_mov_b32 v213, v243
s_nop 0
v_permlane32_swap_b32 v242, v212
v_permlane32_swap_b32 v243, v213
v_max_f32 v242, v242, v212
v_max_f32 v243, v243, v213
)"
R"(v_mov_b32 v212, v242
v_mov_b32 v213, v243
s_nop 0
v_permlane16_swap_b32 v242, v212
v_permlane16_swap_b32 v243, v213
v_max_f32 v242, v242, v212
v_max_f32 v243, v243, v213
v_sub_f32 v224, 0, v242
v_sub_f32 v225, 0, v242
v_sub_f32 v226, 0, v242
v_sub_f32 v227, 0, v242
v_sub_f32 v228, 0, v243
v_sub_f32 v229, 0, v243
v_sub_f32 v230, 0, v243
v_sub_f32 v231, 0, v243
v_sub_f32 v144, v144, v242
)"
R"(v_sub_f32 v145, v145, v242
v_sub_f32 v146, v146, v242
v_sub_f32 v147, v147, v242
v_sub_f32 v148, v148, v242
v_sub_f32 v149, v149, v242
v_sub_f32 v150, v150, v242
v_sub_f32 v151, v151, v242
v_sub_f32 v152, v152, v243
v_sub_f32 v153, v153, v243
v_sub_f32 v154, v154, v243
v_sub_f32 v155, v155, v243
v_sub_f32 v156, v156, v243
v_sub_f32 v157, v157, v243
v_sub_f32 v158, v158, v243
v_sub_f32 v159, v159, v243
s_setprio 0
)"
R"(s_barrier
ds_read_b128 v[184:187], v232 offset:12288
ds_read_b128 v[188:191], v232 offset:18432
ds_read_b128 v[192:195], v233 offset:12288
ds_read_b128 v[196:199], v233 offset:18432
ds_read_b128 v[200:203], v232 offset:12416
s_waitcnt lgkmcnt(4)
v_mfma_f32_16x16x32_bf16 v[160:163], v[184:187], v[96:99], v[224:227]
v_exp_f32 v144, v144
v_mfma_f32_16x16x32_bf16 v[168:171], v[184:187], v[100:103], v[228:231]
s_waitcnt lgkmcnt(3)
v_exp_f32 v145, v145
v_mfma_f32_16x16x32_bf16 v[172:175], v[188:191], v[100:103], v[228:231]
v_exp_f32 v146, v146
v_mfma_f32_16x16x32_bf16 v[164:167], v[188:191], v[96:99], v[224:227]
s_waitcnt lgkmcnt(2)
)"
R"(ds_read_b128 v[184:187], v232 offset:18560
ds_read_b128 v[188:191], v233 offset:12416
v_mfma_f32_16x16x32_bf16 v[160:163], v[192:195], v[104:107], v[160:163]
v_exp_f32 v147, v147
v_mfma_f32_16x16x32_bf16 v[168:171], v[192:195], v[108:111], v[168:171]
s_waitcnt lgkmcnt(3)
ds_read_b64_tr_b16 v[204:205], v234 offset:0
v_cvt_pk_bf16_f32 v176, v144, v145
v_cvt_pk_bf16_f32 v177, v146, v147
v_mfma_f32_16x16x32_bf16 v[172:175], v[196:199], v[108:111], v[172:175]
v_exp_f32 v148, v148
v_mfma_f32_16x16x32_bf16 v[164:167], v[196:199], v[104:107], v[164:167]
s_waitcnt lgkmcnt(3)
ds_read_b128 v[192:195], v233 offset:18560
ds_read_b128 v[196:199], v232 offset:12544
v_mfma_f32_16x16x32_bf16 v[160:163], v[200:203], v[112:115], v[160:163]
)"
R"(v_exp_f32 v149, v149
v_mfma_f32_16x16x32_bf16 v[168:171], v[200:203], v[116:119], v[168:171]
s_waitcnt lgkmcnt(4)
ds_read_b64_tr_b16 v[206:207], v234 offset:6144
v_exp_f32 v150, v150
v_mfma_f32_16x16x32_bf16 v[172:175], v[184:187], v[116:119], v[172:175]
v_exp_f32 v151, v151
v_mfma_f32_16x16x32_bf16 v[164:167], v[184:187], v[112:115], v[164:167]
s_waitcnt lgkmcnt(4)
ds_read_b128 v[200:203], v232 offset:18688
ds_read_b128 v[184:187], v233 offset:12544
v_mfma_f32_16x16x32_bf16 v[160:163], v[188:191], v[120:123], v[160:163]
v_exp_f32 v152, v152
v_mfma_f32_16x16x32_bf16 v[168:171], v[188:191], v[124:127], v[168:171]
s_waitcnt lgkmcnt(4)
ds_read_b64_tr_b16 v[208:209], v235 offset:0
)"
R"(v_cvt_pk_bf16_f32 v178, v148, v149
v_cvt_pk_bf16_f32 v179, v150, v151
v_mfma_f32_16x16x32_bf16 v[172:175], v[192:195], v[124:127], v[172:175]
v_exp_f32 v153, v153
v_mfma_f32_16x16x32_bf16 v[164:167], v[192:195], v[120:123], v[164:167]
s_waitcnt lgkmcnt(4)
ds_read_b128 v[188:191], v233 offset:18688
ds_read_b64_tr_b16 v[210:211], v235 offset:6144
v_mfma_f32_16x16x32_bf16 v[160:163], v[196:199], v[128:131], v[160:163]
v_exp_f32 v154, v154
v_mfma_f32_16x16x32_bf16 v[168:171], v[196:199], v[132:135], v[168:171]
s_waitcnt lgkmcnt(4)
ds_read_b64_tr_b16 v[212:213], v236 offset:0
v_exp_f32 v155, v155
v_mfma_f32_16x16x32_bf16 v[172:175], v[200:203], v[132:135], v[172:175]
v_exp_f32 v156, v156
)"
R"(v_mfma_f32_16x16x32_bf16 v[164:167], v[200:203], v[128:131], v[164:167]
s_waitcnt lgkmcnt(4)
ds_read_b64_tr_b16 v[214:215], v236 offset:6144
v_cvt_pk_bf16_f32 v180, v152, v153
v_cvt_pk_bf16_f32 v181, v154, v155
v_mfma_f32_16x16x32_bf16 v[160:163], v[184:187], v[136:139], v[160:163]
v_exp_f32 v157, v157
v_mfma_f32_16x16x32_bf16 v[168:171], v[184:187], v[140:143], v[168:171]
s_waitcnt lgkmcnt(3)
ds_read_b64_tr_b16 v[216:217], v237 offset:0
v_exp_f32 v158, v158
v_mfma_f32_16x16x32_bf16 v[172:175], v[188:191], v[140:143], v[172:175]
v_exp_f32 v159, v159
v_mfma_f32_16x16x32_bf16 v[164:167], v[188:191], v[136:139], v[164:167]
ds_read_b64_tr_b16 v[218:219], v237 offset:6144
v_cvt_pk_bf16_f32 v182, v156, v157
)"
R"(v_cvt_pk_bf16_f32 v183, v158, v159
v_mfma_f32_16x16x32_bf16 v[0:3], v[204:207], v[176:179], 0
ds_read_b64_tr_b16 v[220:221], v234 offset:128
ds_read_b64_tr_b16 v[222:223], v234 offset:6272
v_mfma_f32_16x16x32_bf16 v[4:7], v[204:207], v[180:183], 0
s_waitcnt lgkmcnt(6)
ds_read_b64_tr_b16 v[184:185], v235 offset:128
ds_read_b64_tr_b16 v[186:187], v235 offset:6272
v_mfma_f32_16x16x32_bf16 v[12:15], v[208:211], v[180:183], 0
ds_read_b64_tr_b16 v[188:189], v236 offset:128
v_add_f32 v238, v238, v144
v_mfma_f32_16x16x32_bf16 v[8:11], v[208:211], v[176:179], 0
s_waitcnt lgkmcnt(7)
ds_read_b64_tr_b16 v[190:191], v236 offset:6272
v_add_f32 v240, v240, v152
v_mfma_f32_16x16x32_bf16 v[16:19], v[212:215], v[176:179], 0
)"
R"(ds_read_b64_tr_b16 v[192:193], v237 offset:128
v_max3_f32 v242, v160, v161, v162
v_mfma_f32_16x16x32_bf16 v[20:23], v[212:215], v[180:183], 0
s_waitcnt lgkmcnt(7)
ds_read_b64_tr_b16 v[194:195], v237 offset:6272
v_max3_f32 v243, v167, v168, v169
v_mfma_f32_16x16x32_bf16 v[28:31], v[216:219], v[180:183], 0
ds_read_b64_tr_b16 v[196:197], v234 offset:256
v_max3_f32 v242, v242, v163, v164
v_mfma_f32_16x16x32_bf16 v[24:27], v[216:219], v[176:179], 0
s_waitcnt lgkmcnt(7)
ds_read_b64_tr_b16 v[198:199], v234 offset:6400
ds_read_b64_tr_b16 v[200:201], v235 offset:256
v_mfma_f32_16x16x32_bf16 v[32:35], v[220:223], v[176:179], 0
ds_read_b64_tr_b16 v[202:203], v235 offset:6400
ds_read_b64_tr_b16 v[204:205], v236 offset:256
)"
R"(v_mfma_f32_16x16x32_bf16 v[36:39], v[220:223], v[180:183], 0
s_waitcnt lgkmcnt(9)
ds_read_b64_tr_b16 v[206:207], v236 offset:6400
ds_read_b64_tr_b16 v[208:209], v237 offset:256
v_mfma_f32_16x16x32_bf16 v[44:47], v[184:187], v[180:183], 0
ds_read_b64_tr_b16 v[210:211], v237 offset:6400
v_max3_f32 v243, v243, v170, v171
v_mfma_f32_16x16x32_bf16 v[40:43], v[184:187], v[176:179], 0
s_waitcnt lgkmcnt(10)
v_max3_f32 v242, v242, v165, v166
v_max3_f32 v243, v243, v172, v173
v_mfma_f32_16x16x32_bf16 v[48:51], v[188:191], v[176:179], 0
v_max3_f32 v242, v242, v174, v175
v_add_f32 v239, v239, v145
v_mfma_f32_16x16x32_bf16 v[52:55], v[188:191], v[180:183], 0
s_waitcnt lgkmcnt(8)
)"
R"(v_max_f32 v242, v242, v243
v_add_f32 v241, v241, v153
v_mfma_f32_16x16x32_bf16 v[60:63], v[192:195], v[180:183], 0
v_cmp_lt_f32 vcc, 0x41000000, v242
v_add_f32 v238, v238, v146
v_mfma_f32_16x16x32_bf16 v[56:59], v[192:195], v[176:179], 0
s_waitcnt lgkmcnt(0)
v_add_f32 v240, v240, v154
v_add_f32 v239, v239, v147
s_barrier
v_mfma_f32_16x16x32_bf16 v[64:67], v[196:199], v[176:179], 0
ds_read_b128 v[184:187], v232 offset:24576
ds_read_b128 v[188:191], v232 offset:30720
v_mfma_f32_16x16x32_bf16 v[68:71], v[196:199], v[180:183], 0
ds_read_b128 v[192:195], v233 offset:24576
v_add_f32 v241, v241, v155
)"
R"(v_mfma_f32_16x16x32_bf16 v[76:79], v[200:203], v[180:183], 0
ds_read_b128 v[196:199], v233 offset:30720
v_add_f32 v238, v238, v148
v_mfma_f32_16x16x32_bf16 v[72:75], v[200:203], v[176:179], 0
ds_read_b128 v[200:203], v232 offset:24704
v_add_f32 v240, v240, v156
v_mfma_f32_16x16x32_bf16 v[80:83], v[204:207], v[176:179], 0
v_add_f32 v239, v239, v149
v_add_f32 v241, v241, v157
v_mfma_f32_16x16x32_bf16 v[84:87], v[204:207], v[180:183], 0
v_add_f32 v238, v238, v150
v_add_f32 v240, v240, v158
v_mfma_f32_16x16x32_bf16 v[92:95], v[208:211], v[180:183], 0
v_add_f32 v239, v239, v151
v_add_f32 v241, v241, v159
v_mfma_f32_16x16x32_bf16 v[88:91], v[208:211], v[176:179], 0
)"
R"(s_waitcnt lgkmcnt(4)
s_cbranch_vccz LkA2_0
s_getpc_b64 s[80:81]
s_branch Lrare_1
LkA2_0:
v_mfma_f32_16x16x32_bf16 v[144:147], v[184:187], v[96:99], v[224:227]
v_exp_f32 v160, v160
v_mfma_f32_16x16x32_bf16 v[152:155], v[184:187], v[100:103], v[228:231]
s_waitcnt lgkmcnt(3)
v_exp_f32 v161, v161
v_mfma_f32_16x16x32_bf16 v[156:159], v[188:191], v[100:103], v[228:231]
v_exp_f32 v162, v162
v_mfma_f32_16x16x32_bf16 v[148:151], v[188:191], v[96:99], v[224:227]
s_waitcnt lgkmcnt(2)
ds_read_b128 v[184:187], v232 offset:30848
ds_read_b128 v[188:191], v233 offset:24704
)"
R"(v_mfma_f32_16x16x32_bf16 v[144:147], v[192:195], v[104:107], v[144:147]
v_exp_f32 v163, v163
v_mfma_f32_16x16x32_bf16 v[152:155], v[192:195], v[108:111], v[152:155]
s_waitcnt lgkmcnt(3)
ds_read_b64_tr_b16 v[204:205], v234 offset:12288
v_cvt_pk_bf16_f32 v176, v160, v161
v_cvt_pk_bf16_f32 v177, v162, v163
v_mfma_f32_16x16x32_bf16 v[156:159], v[196:199], v[108:111], v[156:159]
v_exp_f32 v164, v164
v_mfma_f32_16x16x32_bf16 v[148:151], v[196:199], v[104:107], v[148:151]
s_waitcnt lgkmcnt(3)
ds_read_b128 v[192:195], v233 offset:30848
ds_read_b128 v[196:199], v232 offset:24832
v_mfma_f32_16x16x32_bf16 v[144:147], v[200:203], v[112:115], v[144:147]
v_exp_f32 v165, v165
v_mfma_f32_16x16x32_bf16 v[152:155], v[200:203], v[116:119], v[152:155]
)"
R"(s_waitcnt lgkmcnt(4)
ds_read_b64_tr_b16 v[206:207], v234 offset:18432
v_exp_f32 v166, v166
v_mfma_f32_16x16x32_bf16 v[156:159], v[184:187], v[116:119], v[156:159]
v_exp_f32 v167, v167
v_mfma_f32_16x16x32_bf16 v[148:151], v[184:187], v[112:115], v[148:151]
s_waitcnt lgkmcnt(4)
ds_read_b128 v[200:203], v232 offset:30976
ds_read_b128 v[184:187], v233 offset:24832
v_mfma_f32_16x16x32_bf16 v[144:147], v[188:191], v[120:123], v[144:147]
v_exp_f32 v168, v168
v_mfma_f32_16x16x32_bf16 v[152:155], v[188:191], v[124:127], v[152:155]
s_waitcnt lgkmcnt(4)
ds_read_b64_tr_b16 v[208:209], v235 offset:12288
v_cvt_pk_bf16_f32 v178, v164, v165
v_cvt_pk_bf16_f32 v179, v166, v167
)"
R"(v_mfma_f32_16x16x32_bf16 v[156:159], v[192:195], v[124:127], v[156:159]
v_exp_f32 v169, v169
v_mfma_f32_16x16x32_bf16 v[148:151], v[192:195], v[120:123], v[148:151]
s_waitcnt lgkmcnt(4)
ds_read_b128 v[188:191], v233 offset:30976
ds_read_b64_tr_b16 v[210:211], v235 offset:18432
v_mfma_f32_16x16x32_bf16 v[144:147], v[196:199], v[128:131], v[144:147]
v_exp_f32 v170, v170
v_mfma_f32_16x16x32_bf16 v[152:155], v[196:199], v[132:135], v[152:155]
s_waitcnt lgkmcnt(4)
ds_read_b64_tr_b16 v[212:213], v236 offset:12288
v_exp_f32 v171, v171
v_mfma_f32_16x16x32_bf16 v[156:159], v[200:203], v[132:135], v[156:159]
v_exp_f32 v172, v172
v_mfma_f32_16x16x32_bf16 v[148:151], v[200:203], v[128:131], v[148:151]
s_waitcnt lgkmcnt(4)
)"
R"(ds_read_b64_tr_b16 v[214:215], v236 offset:18432
v_cvt_pk_bf16_f32 v180, v168, v169
v_cvt_pk_bf16_f32 v181, v170, v171
v_mfma_f32_16x16x32_bf16 v[144:147], v[184:187], v[136:139], v[144:147]
v_exp_f32 v173, v173
v_mfma_f32_16x16x32_bf16 v[152:155], v[184:187], v[140:143], v[152:155]
s_waitcnt lgkmcnt(3)
ds_read_b64_tr_b16 v[216:217], v237 offset:12288
v_exp_f32 v174, v174
v_mfma_f32_16x16x32_bf16 v[156:159], v[188:191], v[140:143], v[156:159]
v_exp_f32 v175, v175
v_mfma_f32_16x16x32_bf16 v[148:151], v[188:191], v[136:139], v[148:151]
ds_read_b64_tr_b16 v[218:219], v237 offset:18432
v_cvt_pk_bf16_f32 v182, v172, v173
v_cvt_pk_bf16_f32 v183, v174, v175
v_mfma_f32_16x16x32_bf16 v[0:3], v[204:207], v[176:179], v[0:3]
)"
R"(ds_read_b64_tr_b16 v[220:221], v234 offset:12416
ds_read_b64_tr_b16 v[222:223], v234 offset:18560
v_mfma_f32_16x16x32_bf16 v[4:7], v[204:207], v[180:183], v[4:7]
s_waitcnt lgkmcnt(6)
ds_read_b64_tr_b16 v[184:185], v235 offset:12416
ds_read_b64_tr_b16 v[186:187], v235 offset:18560
v_mfma_f32_16x16x32_bf16 v[12:15], v[208:211], v[180:183], v[12:15]
ds_read_b64_tr_b16 v[188:189], v236 offset:12416
v_add_f32 v238, v238, v160
v_mfma_f32_16x16x32_bf16 v[8:11], v[208:211], v[176:179], v[8:11]
s_waitcnt lgkmcnt(7)
ds_read_b64_tr_b16 v[190:191], v236 offset:18560
v_add_f32 v240, v240, v168
v_mfma_f32_16x16x32_bf16 v[16:19], v[212:215], v[176:179], v[16:19]
ds_read_b64_tr_b16 v[192:193], v237 offset:12416
v_max3_f32 v242, v144, v145, v146
)"
R"(v_mfma_f32_16x16x32_bf16 v[20:23], v[212:215], v[180:183], v[20:23]
s_waitcnt lgkmcnt(7)
ds_read_b64_tr_b16 v[194:195], v237 offset:18560
v_max3_f32 v243, v151, v152, v153
v_mfma_f32_16x16x32_bf16 v[28:31], v[216:219], v[180:183], v[28:31]
ds_read_b64_tr_b16 v[196:197], v234 offset:12544
v_max3_f32 v242, v242, v147, v148
v_mfma_f32_16x16x32_bf16 v[24:27], v[216:219], v[176:179], v[24:27]
s_waitcnt lgkmcnt(7)
ds_read_b64_tr_b16 v[198:199], v234 offset:18688
ds_read_b64_tr_b16 v[200:201], v235 offset:12544
v_mfma_f32_16x16x32_bf16 v[32:35], v[220:223], v[176:179], v[32:35]
ds_read_b64_tr_b16 v[202:203], v235 offset:18688
ds_read_b64_tr_b16 v[204:205], v236 offset:12544
v_mfma_f32_16x16x32_bf16 v[36:39], v[220:223], v[180:183], v[36:39]
s_waitcnt lgkmcnt(9)
)"
R"(ds_read_b64_tr_b16 v[206:207], v236 offset:18688
ds_read_b64_tr_b16 v[208:209], v237 offset:12544
v_mfma_f32_16x16x32_bf16 v[44:47], v[184:187], v[180:183], v[44:47]
ds_read_b64_tr_b16 v[210:211], v237 offset:18688
v_max3_f32 v243, v243, v154, v155
v_mfma_f32_16x16x32_bf16 v[40:43], v[184:187], v[176:179], v[40:43]
s_waitcnt lgkmcnt(10)
v_max3_f32 v242, v242, v149, v150
v_max3_f32 v243, v243, v156, v157
v_mfma_f32_16x16x32_bf16 v[48:51], v[188:191], v[176:179], v[48:51]
v_max3_f32 v242, v242, v158, v159
v_add_f32 v239, v239, v161
v_mfma_f32_16x16x32_bf16 v[52:55], v[188:191], v[180:183], v[52:55]
s_waitcnt lgkmcnt(8)
v_max_f32 v242, v242, v243
v_add_f32 v241, v241, v169
)"
R"(v_mfma_f32_16x16x32_bf16 v[60:63], v[192:195], v[180:183], v[60:63]
v_cmp_lt_f32 vcc, 0x41000000, v242
v_add_f32 v238, v238, v162
v_mfma_f32_16x16x32_bf16 v[56:59], v[192:195], v[176:179], v[56:59]
s_waitcnt lgkmcnt(0)
v_add_f32 v240, v240, v170
v_add_f32 v239, v239, v163
s_barrier
v_mfma_f32_16x16x32_bf16 v[64:67], v[196:199], v[176:179], v[64:67]
ds_read_b128 v[184:187], v232 offset:36864
ds_read_b128 v[188:191], v232 offset:43008
v_mfma_f32_16x16x32_bf16 v[68:71], v[196:199], v[180:183], v[68:71]
ds_read_b128 v[192:195], v233 offset:36864
v_add_f32 v241, v241, v171
v_mfma_f32_16x16x32_bf16 v[76:79], v[200:203], v[180:183], v[76:79]
ds_read_b128 v[196:199], v233 offset:43008
)"
R"(v_add_f32 v238, v238, v164
v_mfma_f32_16x16x32_bf16 v[72:75], v[200:203], v[176:179], v[72:75]
ds_read_b128 v[200:203], v232 offset:36992
v_add_f32 v240, v240, v172
v_mfma_f32_16x16x32_bf16 v[80:83], v[204:207], v[176:179], v[80:83]
v_add_f32 v239, v239, v165
v_add_f32 v241, v241, v173
v_mfma_f32_16x16x32_bf16 v[84:87], v[204:207], v[180:183], v[84:87]
v_add_f32 v238, v238, v166
v_add_f32 v240, v240, v174
v_mfma_f32_16x16x32_bf16 v[92:95], v[208:211], v[180:183], v[92:95]
v_add_f32 v239, v239, v167
v_add_f32 v241, v241, v175
v_mfma_f32_16x16x32_bf16 v[88:91], v[208:211], v[176:179], v[88:91]
s_waitcnt lgkmcnt(4)
s_cbranch_vccz LkA2_1
)"
R"(s_getpc_b64 s[80:81]
s_branch Lrare_0
LkA2_1:
v_mfma_f32_16x16x32_bf16 v[160:163], v[184:187], v[96:99], v[224:227]
v_exp_f32 v144, v144
v_mfma_f32_16x16x32_bf16 v[168:171], v[184:187], v[100:103], v[228:231]
s_waitcnt lgkmcnt(3)
v_exp_f32 v145, v145
v_mfma_f32_16x16x32_bf16 v[172:175], v[188:191], v[100:103], v[228:231]
v_exp_f32 v146, v146
v_mfma_f32_16x16x32_bf16 v[164:167], v[188:191], v[96:99], v[224:227]
s_waitcnt lgkmcnt(2)
ds_read_b128 v[184:187], v232 offset:43136
ds_read_b128 v[188:191], v233 offset:36992
v_mfma_f32_16x16x32_bf16 v[160:163], v[192:195], v[104:107], v[160:163]
v_exp_f32 v147, v147
)"
R"(v_mfma_f32_16x16x32_bf16 v[168:171], v[192:195], v[108:111], v[168:171]
s_waitcnt lgkmcnt(3)
ds_read_b64_tr_b16 v[204:205], v234 offset:24576
v_cvt_pk_bf16_f32 v176, v144, v145
v_cvt_pk_bf16_f32 v177, v146, v147
v_mfma_f32_16x16x32_bf16 v[172:175], v[196:199], v[108:111], v[172:175]
v_exp_f32 v148, v148
v_mfma_f32_16x16x32_bf16 v[164:167], v[196:199], v[104:107], v[164:167]
s_waitcnt lgkmcnt(3)
ds_read_b128 v[192:195], v233 offset:43136
ds_read_b128 v[196:199], v232 offset:37120
v_mfma_f32_16x16x32_bf16 v[160:163], v[200:203], v[112:115], v[160:163]
v_exp_f32 v149, v149
v_mfma_f32_16x16x32_bf16 v[168:171], v[200:203], v[116:119], v[168:171]
s_waitcnt lgkmcnt(4)
ds_read_b64_tr_b16 v[206:207], v234 offset:30720
)"
R"(v_exp_f32 v150, v150
v_mfma_f32_16x16x32_bf16 v[172:175], v[184:187], v[116:119], v[172:175]
v_exp_f32 v151, v151
v_mfma_f32_16x16x32_bf16 v[164:167], v[184:187], v[112:115], v[164:167]
s_waitcnt lgkmcnt(4)
ds_read_b128 v[200:203], v232 offset:43264
ds_read_b128 v[184:187], v233 offset:37120
v_mfma_f32_16x16x32_bf16 v[160:163], v[188:191], v[120:123], v[160:163]
v_exp_f32 v152, v152
v_mfma_f32_16x16x32_bf16 v[168:171], v[188:191], v[124:127], v[168:171]
s_waitcnt lgkmcnt(4)
ds_read_b64_tr_b16 v[208:209], v235 offset:24576
v_cvt_pk_bf16_f32 v178, v148, v149
v_cvt_pk_bf16_f32 v179, v150, v151
v_mfma_f32_16x16x32_bf16 v[172:175], v[192:195], v[124:127], v[172:175]
v_exp_f32 v153, v153
)"
R"(v_mfma_f32_16x16x32_bf16 v[164:167], v[192:195], v[120:123], v[164:167]
s_waitcnt lgkmcnt(4)
ds_read_b128 v[188:191], v233 offset:43264
ds_read_b64_tr_b16 v[210:211], v235 offset:30720
v_mfma_f32_16x16x32_bf16 v[160:163], v[196:199], v[128:131], v[160:163]
v_exp_f32 v154, v154
v_mfma_f32_16x16x32_bf16 v[168:171], v[196:199], v[132:135], v[168:171]
s_waitcnt lgkmcnt(4)
ds_read_b64_tr_b16 v[212:213], v236 offset:24576
v_exp_f32 v155, v155
v_mfma_f32_16x16x32_bf16 v[172:175], v[200:203], v[132:135], v[172:175]
v_exp_f32 v156, v156
v_mfma_f32_16x16x32_bf16 v[164:167], v[200:203], v[128:131], v[164:167]
s_waitcnt lgkmcnt(4)
ds_read_b64_tr_b16 v[214:215], v236 offset:30720
v_cvt_pk_bf16_f32 v180, v152, v153
)"
R"(v_cvt_pk_bf16_f32 v181, v154, v155
v_mfma_f32_16x16x32_bf16 v[160:163], v[184:187], v[136:139], v[160:163]
v_exp_f32 v157, v157
v_mfma_f32_16x16x32_bf16 v[168:171], v[184:187], v[140:143], v[168:171]
s_waitcnt lgkmcnt(3)
ds_read_b64_tr_b16 v[216:217], v237 offset:24576
v_exp_f32 v158, v158
v_mfma_f32_16x16x32_bf16 v[172:175], v[188:191], v[140:143], v[172:175]
v_exp_f32 v159, v159
v_mfma_f32_16x16x32_bf16 v[164:167], v[188:191], v[136:139], v[164:167]
ds_read_b64_tr_b16 v[218:219], v237 offset:30720
v_cvt_pk_bf16_f32 v182, v156, v157
v_cvt_pk_bf16_f32 v183, v158, v159
v_mfma_f32_16x16x32_bf16 v[0:3], v[204:207], v[176:179], v[0:3]
ds_read_b64_tr_b16 v[220:221], v234 offset:24704
ds_read_b64_tr_b16 v[222:223], v234 offset:30848
)"
R"(v_mfma_f32_16x16x32_bf16 v[4:7], v[204:207], v[180:183], v[4:7]
s_waitcnt lgkmcnt(6)
ds_read_b64_tr_b16 v[184:185], v235 offset:24704
ds_read_b64_tr_b16 v[186:187], v235 offset:30848
v_mfma_f32_16x16x32_bf16 v[12:15], v[208:211], v[180:183], v[12:15]
ds_read_b64_tr_b16 v[188:189], v236 offset:24704
v_add_f32 v238, v238, v144
v_mfma_f32_16x16x32_bf16 v[8:11], v[208:211], v[176:179], v[8:11]
s_waitcnt lgkmcnt(7)
ds_read_b64_tr_b16 v[190:191], v236 offset:30848
v_add_f32 v240, v240, v152
v_mfma_f32_16x16x32_bf16 v[16:19], v[212:215], v[176:179], v[16:19]
ds_read_b64_tr_b16 v[192:193], v237 offset:24704
v_max3_f32 v242, v160, v161, v162
v_mfma_f32_16x16x32_bf16 v[20:23], v[212:215], v[180:183], v[20:23]
s_waitcnt lgkmcnt(7)
)"
R"(ds_read_b64_tr_b16 v[194:195], v237 offset:30848
v_max3_f32 v243, v167, v168, v169
v_mfma_f32_16x16x32_bf16 v[28:31], v[216:219], v[180:183], v[28:31]
ds_read_b64_tr_b16 v[196:197], v234 offset:24832
v_max3_f32 v242, v242, v163, v164
v_mfma_f32_16x16x32_bf16 v[24:27], v[216:219], v[176:179], v[24:27]
s_waitcnt lgkmcnt(7)
ds_read_b64_tr_b16 v[198:199], v234 offset:30976
ds_read_b64_tr_b16 v[200:201], v235 offset:24832
v_mfma_f32_16x16x32_bf16 v[32:35], v[220:223], v[176:179], v[32:35]
ds_read_b64_tr_b16 v[202:203], v235 offset:30976
ds_read_b64_tr_b16 v[204:205], v236 offset:24832
v_mfma_f32_16x16x32_bf16 v[36:39], v[220:223], v[180:183], v[36:39]
s_waitcnt lgkmcnt(9)
ds_read_b64_tr_b16 v[206:207], v236 offset:30976
ds_read_b64_tr_b16 v[208:209], v237 offset:24832
)"
R"(v_mfma_f32_16x16x32_bf16 v[44:47], v[184:187], v[180:183], v[44:47]
ds_read_b64_tr_b16 v[210:211], v237 offset:30976
v_max3_f32 v243, v243, v170, v171
v_mfma_f32_16x16x32_bf16 v[40:43], v[184:187], v[176:179], v[40:43]
s_waitcnt lgkmcnt(10)
v_max3_f32 v242, v242, v165, v166
v_max3_f32 v243, v243, v172, v173
v_mfma_f32_16x16x32_bf16 v[48:51], v[188:191], v[176:179], v[48:51]
v_max3_f32 v242, v242, v174, v175
v_add_f32 v239, v239, v145
v_mfma_f32_16x16x32_bf16 v[52:55], v[188:191], v[180:183], v[52:55]
s_waitcnt lgkmcnt(8)
v_max_f32 v242, v242, v243
v_add_f32 v241, v241, v153
v_mfma_f32_16x16x32_bf16 v[60:63], v[192:195], v[180:183], v[60:63]
v_cmp_lt_f32 vcc, 0x41000000, v242
)"
R"(v_add_f32 v238, v238, v146
v_mfma_f32_16x16x32_bf16 v[56:59], v[192:195], v[176:179], v[56:59]
s_waitcnt lgkmcnt(0)
v_add_f32 v240, v240, v154
v_add_f32 v239, v239, v147
s_barrier
v_mfma_f32_16x16x32_bf16 v[64:67], v[196:199], v[176:179], v[64:67]
ds_read_b128 v[184:187], v232 offset:0
ds_read_b128 v[188:191], v232 offset:6144
v_mfma_f32_16x16x32_bf16 v[68:71], v[196:199], v[180:183], v[68:71]
ds_read_b128 v[192:195], v233 offset:0
v_add_f32 v241, v241, v155
v_mfma_f32_16x16x32_bf16 v[76:79], v[200:203], v[180:183], v[76:79]
ds_read_b128 v[196:199], v233 offset:6144
v_add_f32 v238, v238, v148
v_mfma_f32_16x16x32_bf16 v[72:75], v[200:203], v[176:179], v[72:75]
)"
R"(ds_read_b128 v[200:203], v232 offset:128
v_add_f32 v240, v240, v156
v_mfma_f32_16x16x32_bf16 v[80:83], v[204:207], v[176:179], v[80:83]
v_add_f32 v239, v239, v149
v_add_f32 v241, v241, v157
v_mfma_f32_16x16x32_bf16 v[84:87], v[204:207], v[180:183], v[84:87]
v_add_f32 v238, v238, v150
v_add_f32 v240, v240, v158
v_mfma_f32_16x16x32_bf16 v[92:95], v[208:211], v[180:183], v[92:95]
v_add_f32 v239, v239, v151
v_add_f32 v241, v241, v159
v_mfma_f32_16x16x32_bf16 v[88:91], v[208:211], v[176:179], v[88:91]
s_waitcnt lgkmcnt(4)
s_cbranch_vccz LkA2_2
s_getpc_b64 s[80:81]
s_branch Lrare_1
)"
R"(LkA2_2:
v_mfma_f32_16x16x32_bf16 v[144:147], v[184:187], v[96:99], v[224:227]
v_exp_f32 v160, v160
v_mfma_f32_16x16x32_bf16 v[152:155], v[184:187], v[100:103], v[228:231]
s_waitcnt lgkmcnt(3)
v_exp_f32 v161, v161
v_mfma_f32_16x16x32_bf16 v[156:159], v[188:191], v[100:103], v[228:231]
v_exp_f32 v162, v162
v_mfma_f32_16x16x32_bf16 v[148:151], v[188:191], v[96:99], v[224:227]
s_waitcnt lgkmcnt(2)
ds_read_b128 v[184:187], v232 offset:6272
ds_read_b128 v[188:191], v233 offset:128
v_mfma_f32_16x16x32_bf16 v[144:147], v[192:195], v[104:107], v[144:147]
v_exp_f32 v163, v163
v_mfma_f32_16x16x32_bf16 v[152:155], v[192:195], v[108:111], v[152:155]
s_waitcnt lgkmcnt(3)
)"
R"(ds_read_b64_tr_b16 v[204:205], v234 offset:36864
v_cvt_pk_bf16_f32 v176, v160, v161
v_cvt_pk_bf16_f32 v177, v162, v163
v_mfma_f32_16x16x32_bf16 v[156:159], v[196:199], v[108:111], v[156:159]
v_exp_f32 v164, v164
v_mfma_f32_16x16x32_bf16 v[148:151], v[196:199], v[104:107], v[148:151]
s_waitcnt lgkmcnt(3)
ds_read_b128 v[192:195], v233 offset:6272
ds_read_b128 v[196:199], v232 offset:256
v_mfma_f32_16x16x32_bf16 v[144:147], v[200:203], v[112:115], v[144:147]
v_exp_f32 v165, v165
v_mfma_f32_16x16x32_bf16 v[152:155], v[200:203], v[116:119], v[152:155]
s_waitcnt lgkmcnt(4)
ds_read_b64_tr_b16 v[206:207], v234 offset:43008
v_exp_f32 v166, v166
v_mfma_f32_16x16x32_bf16 v[156:159], v[184:187], v[116:119], v[156:159]
)"
R"(v_exp_f32 v167, v167
v_mfma_f32_16x16x32_bf16 v[148:151], v[184:187], v[112:115], v[148:151]
s_waitcnt lgkmcnt(4)
ds_read_b128 v[200:203], v232 offset:6400
ds_read_b128 v[184:187], v233 offset:256
v_mfma_f32_16x16x32_bf16 v[144:147], v[188:191], v[120:123], v[144:147]
v_exp_f32 v168, v168
v_mfma_f32_16x16x32_bf16 v[152:155], v[188:191], v[124:127], v[152:155]
s_waitcnt lgkmcnt(4)
ds_read_b64_tr_b16 v[208:209], v235 offset:36864
v_cvt_pk_bf16_f32 v178, v164, v165
v_cvt_pk_bf16_f32 v179, v166, v167
v_mfma_f32_16x16x32_bf16 v[156:159], v[192:195], v[124:127], v[156:159]
v_exp_f32 v169, v169
v_mfma_f32_16x16x32_bf16 v[148:151], v[192:195], v[120:123], v[148:151]
s_waitcnt lgkmcnt(4)
)"
R"(ds_read_b128 v[188:191], v233 offset:6400
ds_read_b64_tr_b16 v[210:211], v235 offset:43008
v_mfma_f32_16x16x32_bf16 v[144:147], v[196:199], v[128:131], v[144:147]
v_exp_f32 v170, v170
v_mfma_f32_16x16x32_bf16 v[152:155], v[196:199], v[132:135], v[152:155]
s_waitcnt lgkmcnt(4)
ds_read_b64_tr_b16 v[212:213], v236 offset:36864
v_exp_f32 v171, v171
v_mfma_f32_16x16x32_bf16 v[156:159], v[200:203], v[132:135], v[156:159]
v_exp_f32 v172, v172
v_mfma_f32_16x16x32_bf16 v[148:151], v[200:203], v[128:131], v[148:151]
s_waitcnt lgkmcnt(4)
ds_read_b64_tr_b16 v[214:215], v236 offset:43008
v_cvt_pk_bf16_f32 v180, v168, v169
v_cvt_pk_bf16_f32 v181, v170, v171
v_mfma_f32_16x16x32_bf16 v[144:147], v[184:187], v[136:139], v[144:147]
)"
R"(v_exp_f32 v173, v173
v_mfma_f32_16x16x32_bf16 v[152:155], v[184:187], v[140:143], v[152:155]
s_waitcnt lgkmcnt(3)
ds_read_b64_tr_b16 v[216:217], v237 offset:36864
v_exp_f32 v174, v174
v_mfma_f32_16x16x32_bf16 v[156:159], v[188:191], v[140:143], v[156:159]
v_exp_f32 v175, v175
v_mfma_f32_16x16x32_bf16 v[148:151], v[188:191], v[136:139], v[148:151]
ds_read_b64_tr_b16 v[218:219], v237 offset:43008
v_cvt_pk_bf16_f32 v182, v172, v173
v_cvt_pk_bf16_f32 v183, v174, v175
v_mfma_f32_16x16x32_bf16 v[0:3], v[204:207], v[176:179], v[0:3]
ds_read_b64_tr_b16 v[220:221], v234 offset:36992
ds_read_b64_tr_b16 v[222:223], v234 offset:43136
v_mfma_f32_16x16x32_bf16 v[4:7], v[204:207], v[180:183], v[4:7]
s_waitcnt lgkmcnt(6)
)"
R"(ds_read_b64_tr_b16 v[184:185], v235 offset:36992
ds_read_b64_tr_b16 v[186:187], v235 offset:43136
v_mfma_f32_16x16x32_bf16 v[12:15], v[208:211], v[180:183], v[12:15]
ds_read_b64_tr_b16 v[188:189], v236 offset:36992
v_add_f32 v238, v238, v160
v_mfma_f32_16x16x32_bf16 v[8:11], v[208:211], v[176:179], v[8:11]
s_waitcnt lgkmcnt(7)
ds_read_b64_tr_b16 v[190:191], v236 offset:43136
v_add_f32 v240, v240, v168
v_mfma_f32_16x16x32_bf16 v[16:19], v[212:215], v[176:179], v[16:19]
ds_read_b64_tr_b16 v[192:193], v237 offset:36992
v_max3_f32 v242, v144, v145, v146
v_mfma_f32_16x16x32_bf16 v[20:23], v[212:215], v[180:183], v[20:23]
s_waitcnt lgkmcnt(7)
ds_read_b64_tr_b16 v[194:195], v237 offset:43136
v_max3_f32 v243, v151, v152, v153
)"
R"(v_mfma_f32_16x16x32_bf16 v[28:31], v[216:219], v[180:183], v[28:31]
ds_read_b64_tr_b16 v[196:197], v234 offset:37120
v_max3_f32 v242, v242, v147, v148
v_mfma_f32_16x16x32_bf16 v[24:27], v[216:219], v[176:179], v[24:27]
s_waitcnt lgkmcnt(7)
ds_read_b64_tr_b16 v[198:199], v234 offset:43264
ds_read_b64_tr_b16 v[200:201], v235 offset:37120
v_mfma_f32_16x16x32_bf16 v[32:35], v[220:223], v[176:179], v[32:35]
ds_read_b64_tr_b16 v[202:203], v235 offset:43264
ds_read_b64_tr_b16 v[204:205], v236 offset:37120
v_mfma_f32_16x16x32_bf16 v[36:39], v[220:223], v[180:183], v[36:39]
s_waitcnt lgkmcnt(9)
ds_read_b64_tr_b16 v[206:207], v236 offset:43264
ds_read_b64_tr_b16 v[208:209], v237 offset:37120
v_mfma_f32_16x16x32_bf16 v[44:47], v[184:187], v[180:183], v[44:47]
ds_read_b64_tr_b16 v[210:211], v237 offset:43264
)"
R"(v_max3_f32 v243, v243, v154, v155
v_mfma_f32_16x16x32_bf16 v[40:43], v[184:187], v[176:179], v[40:43]
s_waitcnt lgkmcnt(10)
v_max3_f32 v242, v242, v149, v150
v_max3_f32 v243, v243, v156, v157
v_mfma_f32_16x16x32_bf16 v[48:51], v[188:191], v[176:179], v[48:51]
v_max3_f32 v242, v242, v158, v159
v_add_f32 v239, v239, v161
v_mfma_f32_16x16x32_bf16 v[52:55], v[188:191], v[180:183], v[52:55]
s_waitcnt lgkmcnt(8)
v_max_f32 v242, v242, v243
v_add_f32 v241, v241, v169
v_mfma_f32_16x16x32_bf16 v[60:63], v[192:195], v[180:183], v[60:63]
v_cmp_lt_f32 vcc, 0x41000000, v242
v_add_f32 v238, v238, v162
v_mfma_f32_16x16x32_bf16 v[56:59], v[192:195], v[176:179], v[56:59]
)"
R"(s_waitcnt lgkmcnt(0)
v_add_f32 v240, v240, v170
v_add_f32 v239, v239, v163
s_mov_b32 s58, 6
Lloop_A3:
s_barrier
v_mfma_f32_16x16x32_bf16 v[64:67], v[196:199], v[176:179], v[64:67]
ds_read_b128 v[184:187], v232 offset:12288
ds_read_b128 v[188:191], v232 offset:18432
v_mfma_f32_16x16x32_bf16 v[68:71], v[196:199], v[180:183], v[68:71]
ds_read_b128 v[192:195], v233 offset:12288
v_add_f32 v241, v241, v171
v_mfma_f32_16x16x32_bf16 v[76:79], v[200:203], v[180:183], v[76:79]
ds_read_b128 v[196:199], v233 offset:18432
v_add_f32 v238, v238, v164
v_mfma_f32_16x16x32_bf16 v[72:75], v[200:203], v[176:179], v[72:75]
)"
R"(ds_read_b128 v[200:203], v232 offset:12416
v_add_f32 v240, v240, v172
v_mfma_f32_16x16x32_bf16 v[80:83], v[204:207], v[176:179], v[80:83]
v_add_f32 v239, v239, v165
v_add_f32 v241, v241, v173
v_mfma_f32_16x16x32_bf16 v[84:87], v[204:207], v[180:183], v[84:87]
v_add_f32 v238, v238, v166
v_add_f32 v240, v240, v174
v_mfma_f32_16x16x32_bf16 v[92:95], v[208:211], v[180:183], v[92:95]
v_add_f32 v239, v239, v167
v_add_f32 v241, v241, v175
v_mfma_f32_16x16x32_bf16 v[88:91], v[208:211], v[176:179], v[88:91]
s_waitcnt lgkmcnt(4)
s_cbranch_vccz LkA3_3
s_getpc_b64 s[80:81]
s_branch Lrare_0
)"
R"(LkA3_3:
v_mfma_f32_16x16x32_bf16 v[160:163], v[184:187], v[96:99], v[224:227]
v_exp_f32 v144, v144
v_mfma_f32_16x16x32_bf16 v[168:171], v[184:187], v[100:103], v[228:231]
s_waitcnt lgkmcnt(3)
v_exp_f32 v145, v145
v_mfma_f32_16x16x32_bf16 v[172:175], v[188:191], v[100:103], v[228:231]
v_exp_f32 v146, v146
v_mfma_f32_16x16x32_bf16 v[164:167], v[188:191], v[96:99], v[224:227]
s_waitcnt lgkmcnt(2)
ds_read_b128 v[184:187], v232 offset:18560
ds_read_b128 v[188:191], v233 offset:12416
v_mfma_f32_16x16x32_bf16 v[160:163], v[192:195], v[104:107], v[160:163]
v_exp_f32 v147, v147
v_mfma_f32_16x16x32_bf16 v[168:171], v[192:195], v[108:111], v[168:171]
s_waitcnt lgkmcnt(3)
)"
R"(ds_read_b64_tr_b16 v[204:205], v234 offset:0
v_cvt_pk_bf16_f32 v176, v144, v145
v_cvt_pk_bf16_f32 v177, v146, v147
v_mfma_f32_16x16x32_bf16 v[172:175], v[196:199], v[108:111], v[172:175]
v_exp_f32 v148, v148
v_mfma_f32_16x16x32_bf16 v[164:167], v[196:199], v[104:107], v[164:167]
s_waitcnt lgkmcnt(3)
ds_read_b128 v[192:195], v233 offset:18560
ds_read_b128 v[196:199], v232 offset:12544
v_mfma_f32_16x16x32_bf16 v[160:163], v[200:203], v[112:115], v[160:163]
v_exp_f32 v149, v149
v_mfma_f32_16x16x32_bf16 v[168:171], v[200:203], v[116:119], v[168:171]
s_waitcnt lgkmcnt(4)
ds_read_b64_tr_b16 v[206:207], v234 offset:6144
v_exp_f32 v150, v150
v_mfma_f32_16x16x32_bf16 v[172:175], v[184:187], v[116:119], v[172:175]
)"
R"(v_exp_f32 v151, v151
v_mfma_f32_16x16x32_bf16 v[164:167], v[184:187], v[112:115], v[164:167]
s_waitcnt lgkmcnt(4)
ds_read_b128 v[200:203], v232 offset:18688
ds_read_b128 v[184:187], v233 offset:12544
v_mfma_f32_16x16x32_bf16 v[160:163], v[188:191], v[120:123], v[160:163]
v_exp_f32 v152, v152
v_mfma_f32_16x16x32_bf16 v[168:171], v[188:191], v[124:127], v[168:171]
s_waitcnt lgkmcnt(4)
ds_read_b64_tr_b16 v[208:209], v235 offset:0
v_cvt_pk_bf16_f32 v178, v148, v149
v_cvt_pk_bf16_f32 v179, v150, v151
v_mfma_f32_16x16x32_bf16 v[172:175], v[192:195], v[124:127], v[172:175]
v_exp_f32 v153, v153
v_mfma_f32_16x16x32_bf16 v[164:167], v[192:195], v[120:123], v[164:167]
s_waitcnt lgkmcnt(4)
)"
R"(ds_read_b128 v[188:191], v233 offset:18688
ds_read_b64_tr_b16 v[210:211], v235 offset:6144
v_mfma_f32_16x16x32_bf16 v[160:163], v[196:199], v[128:131], v[160:163]
v_exp_f32 v154, v154
v_mfma_f32_16x16x32_bf16 v[168:171], v[196:199], v[132:135], v[168:171]
s_waitcnt lgkmcnt(4)
ds_read_b64_tr_b16 v[212:213], v236 offset:0
v_exp_f32 v155, v155
v_mfma_f32_16x16x32_bf16 v[172:175], v[200:203], v[132:135], v[172:175]
v_exp_f32 v156, v156
v_mfma_f32_16x16x32_bf16 v[164:167], v[200:203], v[128:131], v[164:167]
s_waitcnt lgkmcnt(4)
ds_read_b64_tr_b16 v[214:215], v236 offset:6144
v_cvt_pk_bf16_f32 v180, v152, v153
v_cvt_pk_bf16_f32 v181, v154, v155
v_mfma_f32_16x16x32_bf16 v[160:163], v[184:187], v[136:139], v[160:163]
)"
R"(v_exp_f32 v157, v157
v_mfma_f32_16x16x32_bf16 v[168:171], v[184:187], v[140:143], v[168:171]
s_waitcnt lgkmcnt(3)
ds_read_b64_tr_b16 v[216:217], v237 offset:0
v_exp_f32 v158, v158
v_mfma_f32_16x16x32_bf16 v[172:175], v[188:191], v[140:143], v[172:175]
v_exp_f32 v159, v159
v_mfma_f32_16x16x32_bf16 v[164:167], v[188:191], v[136:139], v[164:167]
ds_read_b64_tr_b16 v[218:219], v237 offset:6144
v_cvt_pk_bf16_f32 v182, v156, v157
v_cvt_pk_bf16_f32 v183, v158, v159
v_mfma_f32_16x16x32_bf16 v[0:3], v[204:207], v[176:179], v[0:3]
ds_read_b64_tr_b16 v[220:221], v234 offset:128
ds_read_b64_tr_b16 v[222:223], v234 offset:6272
v_mfma_f32_16x16x32_bf16 v[4:7], v[204:207], v[180:183], v[4:7]
s_waitcnt lgkmcnt(6)
)"
R"(ds_read_b64_tr_b16 v[184:185], v235 offset:128
ds_read_b64_tr_b16 v[186:187], v235 offset:6272
v_mfma_f32_16x16x32_bf16 v[12:15], v[208:211], v[180:183], v[12:15]
ds_read_b64_tr_b16 v[188:189], v236 offset:128
v_add_f32 v238, v238, v144
v_mfma_f32_16x16x32_bf16 v[8:11], v[208:211], v[176:179], v[8:11]
s_waitcnt lgkmcnt(7)
ds_read_b64_tr_b16 v[190:191], v236 offset:6272
v_add_f32 v240, v240, v152
v_mfma_f32_16x16x32_bf16 v[16:19], v[212:215], v[176:179], v[16:19]
ds_read_b64_tr_b16 v[192:193], v237 offset:128
v_max3_f32 v242, v160, v161, v162
v_mfma_f32_16x16x32_bf16 v[20:23], v[212:215], v[180:183], v[20:23]
s_waitcnt lgkmcnt(7)
ds_read_b64_tr_b16 v[194:195], v237 offset:6272
v_max3_f32 v243, v167, v168, v169
)"
R"(v_mfma_f32_16x16x32_bf16 v[28:31], v[216:219], v[180:183], v[28:31]
ds_read_b64_tr_b16 v[196:197], v234 offset:256
v_max3_f32 v242, v242, v163, v164
v_mfma_f32_16x16x32_bf16 v[24:27], v[216:219], v[176:179], v[24:27]
s_waitcnt lgkmcnt(7)
ds_read_b64_tr_b16 v[198:199], v234 offset:6400
ds_read_b64_tr_b16 v[200:201], v235 offset:256
v_mfma_f32_16x16x32_bf16 v[32:35], v[220:223], v[176:179], v[32:35]
ds_read_b64_tr_b16 v[202:203], v235 offset:6400
ds_read_b64_tr_b16 v[204:205], v236 offset:256
v_mfma_f32_16x16x32_bf16 v[36:39], v[220:223], v[180:183], v[36:39]
s_waitcnt lgkmcnt(9)
ds_read_b64_tr_b16 v[206:207], v236 offset:6400
ds_read_b64_tr_b16 v[208:209], v237 offset:256
v_mfma_f32_16x16x32_bf16 v[44:47], v[184:187], v[180:183], v[44:47]
ds_read_b64_tr_b16 v[210:211], v237 offset:6400
)"
R"(v_max3_f32 v243, v243, v170, v171
v_mfma_f32_16x16x32_bf16 v[40:43], v[184:187], v[176:179], v[40:43]
s_waitcnt lgkmcnt(10)
v_max3_f32 v242, v242, v165, v166
v_max3_f32 v243, v243, v172, v173
v_mfma_f32_16x16x32_bf16 v[48:51], v[188:191], v[176:179], v[48:51]
v_max3_f32 v242, v242, v174, v175
v_add_f32 v239, v239, v145
v_mfma_f32_16x16x32_bf16 v[52:55], v[188:191], v[180:183], v[52:55]
s_waitcnt lgkmcnt(8)
v_max_f32 v242, v242, v243
v_add_f32 v241, v241, v153
v_mfma_f32_16x16x32_bf16 v[60:63], v[192:195], v[180:183], v[60:63]
v_cmp_lt_f32 vcc, 0x41000000, v242
v_add_f32 v238, v238, v146
v_mfma_f32_16x16x32_bf16 v[56:59], v[192:195], v[176:179], v[56:59]
)"
R"(s_waitcnt lgkmcnt(0)
v_add_f32 v240, v240, v154
v_add_f32 v239, v239, v147
s_barrier
v_mfma_f32_16x16x32_bf16 v[64:67], v[196:199], v[176:179], v[64:67]
ds_read_b128 v[184:187], v232 offset:24576
ds_read_b128 v[188:191], v232 offset:30720
v_mfma_f32_16x16x32_bf16 v[68:71], v[196:199], v[180:183], v[68:71]
ds_read_b128 v[192:195], v233 offset:24576
v_add_f32 v241, v241, v155
v_mfma_f32_16x16x32_bf16 v[76:79], v[200:203], v[180:183], v[76:79]
ds_read_b128 v[196:199], v233 offset:30720
v_add_f32 v238, v238, v148
v_mfma_f32_16x16x32_bf16 v[72:75], v[200:203], v[176:179], v[72:75]
ds_read_b128 v[200:203], v232 offset:24704
v_add_f32 v240, v240, v156
)"
R"(v_mfma_f32_16x16x32_bf16 v[80:83], v[204:207], v[176:179], v[80:83]
v_add_f32 v239, v239, v149
v_add_f32 v241, v241, v157
v_mfma_f32_16x16x32_bf16 v[84:87], v[204:207], v[180:183], v[84:87]
v_add_f32 v238, v238, v150
v_add_f32 v240, v240, v158
v_mfma_f32_16x16x32_bf16 v[92:95], v[208:211], v[180:183], v[92:95]
v_add_f32 v239, v239, v151
v_add_f32 v241, v241, v159
v_mfma_f32_16x16x32_bf16 v[88:91], v[208:211], v[176:179], v[88:91]
s_waitcnt lgkmcnt(4)
s_cbranch_vccz LkA3_0
s_getpc_b64 s[80:81]
s_branch Lrare_1
LkA3_0:
v_mfma_f32_16x16x32_bf16 v[144:147], v[184:187], v[96:99], v[224:227]
)"
R"(v_exp_f32 v160, v160
v_mfma_f32_16x16x32_bf16 v[152:155], v[184:187], v[100:103], v[228:231]
s_waitcnt lgkmcnt(3)
v_exp_f32 v161, v161
v_mfma_f32_16x16x32_bf16 v[156:159], v[188:191], v[100:103], v[228:231]
v_exp_f32 v162, v162
v_mfma_f32_16x16x32_bf16 v[148:151], v[188:191], v[96:99], v[224:227]
s_waitcnt lgkmcnt(2)
ds_read_b128 v[184:187], v232 offset:30848
ds_read_b128 v[188:191], v233 offset:24704
v_mfma_f32_16x16x32_bf16 v[144:147], v[192:195], v[104:107], v[144:147]
v_exp_f32 v163, v163
v_mfma_f32_16x16x32_bf16 v[152:155], v[192:195], v[108:111], v[152:155]
s_waitcnt lgkmcnt(3)
ds_read_b64_tr_b16 v[204:205], v234 offset:12288
v_cvt_pk_bf16_f32 v176, v160, v161
)"
R"(v_cvt_pk_bf16_f32 v177, v162, v163
v_mfma_f32_16x16x32_bf16 v[156:159], v[196:199], v[108:111], v[156:159]
v_exp_f32 v164, v164
v_mfma_f32_16x16x32_bf16 v[148:151], v[196:199], v[104:107], v[148:151]
s_waitcnt lgkmcnt(3)
ds_read_b128 v[192:195], v233 offset:30848
ds_read_b128 v[196:199], v232 offset:24832
v_mfma_f32_16x16x32_bf16 v[144:147], v[200:203], v[112:115], v[144:147]
v_exp_f32 v165, v165
v_mfma_f32_16x16x32_bf16 v[152:155], v[200:203], v[116:119], v[152:155]
s_waitcnt lgkmcnt(4)
ds_read_b64_tr_b16 v[206:207], v234 offset:18432
v_exp_f32 v166, v166
v_mfma_f32_16x16x32_bf16 v[156:159], v[184:187], v[116:119], v[156:159]
v_exp_f32 v167, v167
v_mfma_f32_16x16x32_bf16 v[148:151], v[184:187], v[112:115], v[148:151]
)"
R"(s_waitcnt lgkmcnt(4)
ds_read_b128 v[200:203], v232 offset:30976
ds_read_b128 v[184:187], v233 offset:24832
v_mfma_f32_16x16x32_bf16 v[144:147], v[188:191], v[120:123], v[144:147]
v_exp_f32 v168, v168
v_mfma_f32_16x16x32_bf16 v[152:155], v[188:191], v[124:127], v[152:155]
s_waitcnt lgkmcnt(4)
ds_read_b64_tr_b16 v[208:209], v235 offset:12288
v_cvt_pk_bf16_f32 v178, v164, v165
v_cvt_pk_bf16_f32 v179, v166, v167
v_mfma_f32_16x16x32_bf16 v[156:159], v[192:195], v[124:127], v[156:159]
v_exp_f32 v169, v169
v_mfma_f32_16x16x32_bf16 v[148:151], v[192:195], v[120:123], v[148:151]
s_waitcnt lgkmcnt(4)
ds_read_b128 v[188:191], v233 offset:30976
ds_read_b64_tr_b16 v[210:211], v235 offset:18432
)"
R"(v_mfma_f32_16x16x32_bf16 v[144:147], v[196:199], v[128:131], v[144:147]
v_exp_f32 v170, v170
v_mfma_f32_16x16x32_bf16 v[152:155], v[196:199], v[132:135], v[152:155]
s_waitcnt lgkmcnt(4)
ds_read_b64_tr_b16 v[212:213], v236 offset:12288
v_exp_f32 v171, v171
v_mfma_f32_16x16x32_bf16 v[156:159], v[200:203], v[132:135], v[156:159]
v_exp_f32 v172, v172
v_mfma_f32_16x16x32_bf16 v[148:151], v[200:203], v[128:131], v[148:151]
s_waitcnt lgkmcnt(4)
ds_read_b64_tr_b16 v[214:215], v236 offset:18432
v_cvt_pk_bf16_f32 v180, v168, v169
v_cvt_pk_bf16_f32 v181, v170, v171
v_mfma_f32_16x16x32_bf16 v[144:147], v[184:187], v[136:139], v[144:147]
v_exp_f32 v173, v173
v_mfma_f32_16x16x32_bf16 v[152:155], v[184:187], v[140:143], v[152:155]
)"
R"(s_waitcnt lgkmcnt(3)
ds_read_b64_tr_b16 v[216:217], v237 offset:12288
v_exp_f32 v174, v174
v_mfma_f32_16x16x32_bf16 v[156:159], v[188:191], v[140:143], v[156:159]
v_exp_f32 v175, v175
v_mfma_f32_16x16x32_bf16 v[148:151], v[188:191], v[136:139], v[148:151]
ds_read_b64_tr_b16 v[218:219], v237 offset:18432
v_cvt_pk_bf16_f32 v182, v172, v173
v_cvt_pk_bf16_f32 v183, v174, v175
v_mfma_f32_16x16x32_bf16 v[0:3], v[204:207], v[176:179], v[0:3]
ds_read_b64_tr_b16 v[220:221], v234 offset:12416
ds_read_b64_tr_b16 v[222:223], v234 offset:18560
v_mfma_f32_16x16x32_bf16 v[4:7], v[204:207], v[180:183], v[4:7]
s_waitcnt lgkmcnt(6)
ds_read_b64_tr_b16 v[184:185], v235 offset:12416
ds_read_b64_tr_b16 v[186:187], v235 offset:18560
)"
R"(v_mfma_f32_16x16x32_bf16 v[12:15], v[208:211], v[180:183], v[12:15]
ds_read_b64_tr_b16 v[188:189], v236 offset:12416
v_add_f32 v238, v238, v160
v_mfma_f32_16x16x32_bf16 v[8:11], v[208:211], v[176:179], v[8:11]
s_waitcnt lgkmcnt(7)
ds_read_b64_tr_b16 v[190:191], v236 offset:18560
v_add_f32 v240, v240, v168
v_mfma_f32_16x16x32_bf16 v[16:19], v[212:215], v[176:179], v[16:19]
ds_read_b64_tr_b16 v[192:193], v237 offset:12416
v_max3_f32 v242, v144, v145, v146
v_mfma_f32_16x16x32_bf16 v[20:23], v[212:215], v[180:183], v[20:23]
s_waitcnt lgkmcnt(7)
ds_read_b64_tr_b16 v[194:195], v237 offset:18560
v_max3_f32 v243, v151, v152, v153
v_mfma_f32_16x16x32_bf16 v[28:31], v[216:219], v[180:183], v[28:31]
ds_read_b64_tr_b16 v[196:197], v234 offset:12544
)"
R"(v_max3_f32 v242, v242, v147, v148
v_mfma_f32_16x16x32_bf16 v[24:27], v[216:219], v[176:179], v[24:27]
s_waitcnt lgkmcnt(7)
ds_read_b64_tr_b16 v[198:199], v234 offset:18688
ds_read_b64_tr_b16 v[200:201], v235 offset:12544
v_mfma_f32_16x16x32_bf16 v[32:35], v[220:223], v[176:179], v[32:35]
ds_read_b64_tr_b16 v[202:203], v235 offset:18688
ds_read_b64_tr_b16 v[204:205], v236 offset:12544
v_mfma_f32_16x16x32_bf16 v[36:39], v[220:223], v[180:183], v[36:39]
s_waitcnt lgkmcnt(9)
ds_read_b64_tr_b16 v[206:207], v236 offset:18688
ds_read_b64_tr_b16 v[208:209], v237 offset:12544
v_mfma_f32_16x16x32_bf16 v[44:47], v[184:187], v[180:183], v[44:47]
ds_read_b64_tr_b16 v[210:211], v237 offset:18688
v_max3_f32 v243, v243, v154, v155
v_mfma_f32_16x16x32_bf16 v[40:43], v[184:187], v[176:179], v[40:43]
)"
R"(s_waitcnt lgkmcnt(10)
v_max3_f32 v242, v242, v149, v150
v_max3_f32 v243, v243, v156, v157
v_mfma_f32_16x16x32_bf16 v[48:51], v[188:191], v[176:179], v[48:51]
v_max3_f32 v242, v242, v158, v159
v_add_f32 v239, v239, v161
v_mfma_f32_16x16x32_bf16 v[52:55], v[188:191], v[180:183], v[52:55]
s_waitcnt lgkmcnt(8)
v_max_f32 v242, v242, v243
v_add_f32 v241, v241, v169
v_mfma_f32_16x16x32_bf16 v[60:63], v[192:195], v[180:183], v[60:63]
v_cmp_lt_f32 vcc, 0x41000000, v242
v_add_f32 v238, v238, v162
v_mfma_f32_16x16x32_bf16 v[56:59], v[192:195], v[176:179], v[56:59]
s_waitcnt lgkmcnt(0)
v_add_f32 v240, v240, v170
)"
R"(v_add_f32 v239, v239, v163
s_barrier
v_mfma_f32_16x16x32_bf16 v[64:67], v[196:199], v[176:179], v[64:67]
ds_read_b128 v[184:187], v232 offset:36864
ds_read_b128 v[188:191], v232 offset:43008
v_mfma_f32_16x16x32_bf16 v[68:71], v[196:199], v[180:183], v[68:71]
ds_read_b128 v[192:195], v233 offset:36864
v_add_f32 v241, v241, v171
v_mfma_f32_16x16x32_bf16 v[76:79], v[200:203], v[180:183], v[76:79]
ds_read_b128 v[196:199], v233 offset:43008
v_add_f32 v238, v238, v164
v_mfma_f32_16x16x32_bf16 v[72:75], v[200:203], v[176:179], v[72:75]
ds_read_b128 v[200:203], v232 offset:36992
v_add_f32 v240, v240, v172
v_mfma_f32_16x16x32_bf16 v[80:83], v[204:207], v[176:179], v[80:83]
v_add_f32 v239, v239, v165
)"
R"(v_add_f32 v241, v241, v173
v_mfma_f32_16x16x32_bf16 v[84:87], v[204:207], v[180:183], v[84:87]
v_add_f32 v238, v238, v166
v_add_f32 v240, v240, v174
v_mfma_f32_16x16x32_bf16 v[92:95], v[208:211], v[180:183], v[92:95]
v_add_f32 v239, v239, v167
v_add_f32 v241, v241, v175
v_mfma_f32_16x16x32_bf16 v[88:91], v[208:211], v[176:179], v[88:91]
s_waitcnt lgkmcnt(4)
s_cbranch_vccz LkA3_1
s_getpc_b64 s[80:81]
s_branch Lrare_0
LkA3_1:
v_mfma_f32_16x16x32_bf16 v[160:163], v[184:187], v[96:99], v[224:227]
v_exp_f32 v144, v144
v_mfma_f32_16x16x32_bf16 v[168:171], v[184:187], v[100:103], v[228:231]
)"
R"(s_waitcnt lgkmcnt(3)
v_exp_f32 v145, v145
v_mfma_f32_16x16x32_bf16 v[172:175], v[188:191], v[100:103], v[228:231]
v_exp_f32 v146, v146
v_mfma_f32_16x16x32_bf16 v[164:167], v[188:191], v[96:99], v[224:227]
s_waitcnt lgkmcnt(2)
ds_read_b128 v[184:187], v232 offset:43136
ds_read_b128 v[188:191], v233 offset:36992
v_mfma_f32_16x16x32_bf16 v[160:163], v[192:195], v[104:107], v[160:163]
v_exp_f32 v147, v147
v_mfma_f32_16x16x32_bf16 v[168:171], v[192:195], v[108:111], v[168:171]
s_waitcnt lgkmcnt(3)
ds_read_b64_tr_b16 v[204:205], v234 offset:24576
v_cvt_pk_bf16_f32 v176, v144, v145
v_cvt_pk_bf16_f32 v177, v146, v147
v_mfma_f32_16x16x32_bf16 v[172:175], v[196:199], v[108:111], v[172:175]
)"
R"(v_exp_f32 v148, v148
v_mfma_f32_16x16x32_bf16 v[164:167], v[196:199], v[104:107], v[164:167]
s_waitcnt lgkmcnt(3)
ds_read_b128 v[192:195], v233 offset:43136
ds_read_b128 v[196:199], v232 offset:37120
v_mfma_f32_16x16x32_bf16 v[160:163], v[200:203], v[112:115], v[160:163]
v_exp_f32 v149, v149
v_mfma_f32_16x16x32_bf16 v[168:171], v[200:203], v[116:119], v[168:171]
s_waitcnt lgkmcnt(4)
ds_read_b64_tr_b16 v[206:207], v234 offset:30720
v_exp_f32 v150, v150
v_mfma_f32_16x16x32_bf16 v[172:175], v[184:187], v[116:119], v[172:175]
v_exp_f32 v151, v151
v_mfma_f32_16x16x32_bf16 v[164:167], v[184:187], v[112:115], v[164:167]
s_waitcnt lgkmcnt(4)
ds_read_b128 v[200:203], v232 offset:43264
)"
R"(ds_read_b128 v[184:187], v233 offset:37120
v_mfma_f32_16x16x32_bf16 v[160:163], v[188:191], v[120:123], v[160:163]
v_exp_f32 v152, v152
v_mfma_f32_16x16x32_bf16 v[168:171], v[188:191], v[124:127], v[168:171]
s_waitcnt lgkmcnt(4)
ds_read_b64_tr_b16 v[208:209], v235 offset:24576
v_cvt_pk_bf16_f32 v178, v148, v149
v_cvt_pk_bf16_f32 v179, v150, v151
v_mfma_f32_16x16x32_bf16 v[172:175], v[192:195], v[124:127], v[172:175]
v_exp_f32 v153, v153
v_mfma_f32_16x16x32_bf16 v[164:167], v[192:195], v[120:123], v[164:167]
s_waitcnt lgkmcnt(4)
ds_read_b128 v[188:191], v233 offset:43264
ds_read_b64_tr_b16 v[210:211], v235 offset:30720
v_mfma_f32_16x16x32_bf16 v[160:163], v[196:199], v[128:131], v[160:163]
v_exp_f32 v154, v154
)"
R"(v_mfma_f32_16x16x32_bf16 v[168:171], v[196:199], v[132:135], v[168:171]
s_waitcnt lgkmcnt(4)
ds_read_b64_tr_b16 v[212:213], v236 offset:24576
v_exp_f32 v155, v155
v_mfma_f32_16x16x32_bf16 v[172:175], v[200:203], v[132:135], v[172:175]
v_exp_f32 v156, v156
v_mfma_f32_16x16x32_bf16 v[164:167], v[200:203], v[128:131], v[164:167]
s_waitcnt lgkmcnt(4)
ds_read_b64_tr_b16 v[214:215], v236 offset:30720
v_cvt_pk_bf16_f32 v180, v152, v153
v_cvt_pk_bf16_f32 v181, v154, v155
v_mfma_f32_16x16x32_bf16 v[160:163], v[184:187], v[136:139], v[160:163]
v_exp_f32 v157, v157
v_mfma_f32_16x16x32_bf16 v[168:171], v[184:187], v[140:143], v[168:171]
s_waitcnt lgkmcnt(3)
ds_read_b64_tr_b16 v[216:217], v237 offset:24576
)"
R"(v_exp_f32 v158, v158
v_mfma_f32_16x16x32_bf16 v[172:175], v[188:191], v[140:143], v[172:175]
v_exp_f32 v159, v159
v_mfma_f32_16x16x32_bf16 v[164:167], v[188:191], v[136:139], v[164:167]
ds_read_b64_tr_b16 v[218:219], v237 offset:30720
v_cvt_pk_bf16_f32 v182, v156, v157
v_cvt_pk_bf16_f32 v183, v158, v159
v_mfma_f32_16x16x32_bf16 v[0:3], v[204:207], v[176:179], v[0:3]
ds_read_b64_tr_b16 v[220:221], v234 offset:24704
ds_read_b64_tr_b16 v[222:223], v234 offset:30848
v_mfma_f32_16x16x32_bf16 v[4:7], v[204:207], v[180:183], v[4:7]
s_waitcnt lgkmcnt(6)
ds_read_b64_tr_b16 v[184:185], v235 offset:24704
ds_read_b64_tr_b16 v[186:187], v235 offset:30848
v_mfma_f32_16x16x32_bf16 v[12:15], v[208:211], v[180:183], v[12:15]
ds_read_b64_tr_b16 v[188:189], v236 offset:24704
)"
R"(v_add_f32 v238, v238, v144
v_mfma_f32_16x16x32_bf16 v[8:11], v[208:211], v[176:179], v[8:11]
s_waitcnt lgkmcnt(7)
ds_read_b64_tr_b16 v[190:191], v236 offset:30848
v_add_f32 v240, v240, v152
v_mfma_f32_16x16x32_bf16 v[16:19], v[212:215], v[176:179], v[16:19]
ds_read_b64_tr_b16 v[192:193], v237 offset:24704
v_max3_f32 v242, v160, v161, v162
v_mfma_f32_16x16x32_bf16 v[20:23], v[212:215], v[180:183], v[20:23]
s_waitcnt lgkmcnt(7)
ds_read_b64_tr_b16 v[194:195], v237 offset:30848
v_max3_f32 v243, v167, v168, v169
v_mfma_f32_16x16x32_bf16 v[28:31], v[216:219], v[180:183], v[28:31]
ds_read_b64_tr_b16 v[196:197], v234 offset:24832
v_max3_f32 v242, v242, v163, v164
v_mfma_f32_16x16x32_bf16 v[24:27], v[216:219], v[176:179], v[24:27]
)"
R"(s_waitcnt lgkmcnt(7)
ds_read_b64_tr_b16 v[198:199], v234 offset:30976
ds_read_b64_tr_b16 v[200:201], v235 offset:24832
v_mfma_f32_16x16x32_bf16 v[32:35], v[220:223], v[176:179], v[32:35]
ds_read_b64_tr_b16 v[202:203], v235 offset:30976
ds_read_b64_tr_b16 v[204:205], v236 offset:24832
v_mfma_f32_16x16x32_bf16 v[36:39], v[220:223], v[180:183], v[36:39]
s_waitcnt lgkmcnt(9)
ds_read_b64_tr_b16 v[206:207], v236 offset:30976
ds_read_b64_tr_b16 v[208:209], v237 offset:24832
v_mfma_f32_16x16x32_bf16 v[44:47], v[184:187], v[180:183], v[44:47]
ds_read_b64_tr_b16 v[210:211], v237 offset:30976
v_max3_f32 v243, v243, v170, v171
v_mfma_f32_16x16x32_bf16 v[40:43], v[184:187], v[176:179], v[40:43]
s_waitcnt lgkmcnt(10)
v_max3_f32 v242, v242, v165, v166
)"
R"(v_max3_f32 v243, v243, v172, v173
v_mfma_f32_16x16x32_bf16 v[48:51], v[188:191], v[176:179], v[48:51]
v_max3_f32 v242, v242, v174, v175
v_add_f32 v239, v239, v145
v_mfma_f32_16x16x32_bf16 v[52:55], v[188:191], v[180:183], v[52:55]
s_waitcnt lgkmcnt(8)
v_max_f32 v242, v242, v243
v_add_f32 v241, v241, v153
v_mfma_f32_16x16x32_bf16 v[60:63], v[192:195], v[180:183], v[60:63]
v_cmp_lt_f32 vcc, 0x41000000, v242
v_add_f32 v238, v238, v146
v_mfma_f32_16x16x32_bf16 v[56:59], v[192:195], v[176:179], v[56:59]
s_waitcnt lgkmcnt(0)
v_add_f32 v240, v240, v154
v_add_f32 v239, v239, v147
s_barrier
)"
R"(v_mfma_f32_16x16x32_bf16 v[64:67], v[196:199], v[176:179], v[64:67]
ds_read_b128 v[184:187], v232 offset:0
ds_read_b128 v[188:191], v232 offset:6144
v_mfma_f32_16x16x32_bf16 v[68:71], v[196:199], v[180:183], v[68:71]
ds_read_b128 v[192:195], v233 offset:0
v_add_f32 v241, v241, v155
v_mfma_f32_16x16x32_bf16 v[76:79], v[200:203], v[180:183], v[76:79]
ds_read_b128 v[196:199], v233 offset:6144
v_add_f32 v238, v238, v148
v_mfma_f32_16x16x32_bf16 v[72:75], v[200:203], v[176:179], v[72:75]
ds_read_b128 v[200:203], v232 offset:128
v_add_f32 v240, v240, v156
v_mfma_f32_16x16x32_bf16 v[80:83], v[204:207], v[176:179], v[80:83]
v_add_f32 v239, v239, v149
v_add_f32 v241, v241, v157
v_mfma_f32_16x16x32_bf16 v[84:87], v[204:207], v[180:183], v[84:87]
)"
R"(v_add_f32 v238, v238, v150
v_add_f32 v240, v240, v158
v_mfma_f32_16x16x32_bf16 v[92:95], v[208:211], v[180:183], v[92:95]
v_add_f32 v239, v239, v151
v_add_f32 v241, v241, v159
v_mfma_f32_16x16x32_bf16 v[88:91], v[208:211], v[176:179], v[88:91]
s_waitcnt lgkmcnt(4)
s_cbranch_vccz LkA3_2
s_getpc_b64 s[80:81]
s_branch Lrare_1
LkA3_2:
v_mfma_f32_16x16x32_bf16 v[144:147], v[184:187], v[96:99], v[224:227]
v_exp_f32 v160, v160
v_mfma_f32_16x16x32_bf16 v[152:155], v[184:187], v[100:103], v[228:231]
s_waitcnt lgkmcnt(3)
v_exp_f32 v161, v161
)"
R"(v_mfma_f32_16x16x32_bf16 v[156:159], v[188:191], v[100:103], v[228:231]
v_exp_f32 v162, v162
v_mfma_f32_16x16x32_bf16 v[148:151], v[188:191], v[96:99], v[224:227]
s_waitcnt lgkmcnt(2)
ds_read_b128 v[184:187], v232 offset:6272
ds_read_b128 v[188:191], v233 offset:128
v_mfma_f32_16x16x32_bf16 v[144:147], v[192:195], v[104:107], v[144:147]
v_exp_f32 v163, v163
v_mfma_f32_16x16x32_bf16 v[152:155], v[192:195], v[108:111], v[152:155]
s_waitcnt lgkmcnt(3)
ds_read_b64_tr_b16 v[204:205], v234 offset:36864
v_cvt_pk_bf16_f32 v176, v160, v161
v_cvt_pk_bf16_f32 v177, v162, v163
v_mfma_f32_16x16x32_bf16 v[156:159], v[196:199], v[108:111], v[156:159]
v_exp_f32 v164, v164
v_mfma_f32_16x16x32_bf16 v[148:151], v[196:199], v[104:107], v[148:151]
)"
R"(s_waitcnt lgkmcnt(3)
ds_read_b128 v[192:195], v233 offset:6272
ds_read_b128 v[196:199], v232 offset:256
v_mfma_f32_16x16x32_bf16 v[144:147], v[200:203], v[112:115], v[144:147]
v_exp_f32 v165, v165
v_mfma_f32_16x16x32_bf16 v[152:155], v[200:203], v[116:119], v[152:155]
s_waitcnt lgkmcnt(4)
ds_read_b64_tr_b16 v[206:207], v234 offset:43008
v_exp_f32 v166, v166
v_mfma_f32_16x16x32_bf16 v[156:159], v[184:187], v[116:119], v[156:159]
v_exp_f32 v167, v167
v_mfma_f32_16x16x32_bf16 v[148:151], v[184:187], v[112:115], v[148:151]
s_waitcnt lgkmcnt(4)
ds_read_b128 v[200:203], v232 offset:6400
ds_read_b128 v[184:187], v233 offset:256
v_mfma_f32_16x16x32_bf16 v[144:147], v[188:191], v[120:123], v[144:147]
)"
R"(v_exp_f32 v168, v168
v_mfma_f32_16x16x32_bf16 v[152:155], v[188:191], v[124:127], v[152:155]
s_waitcnt lgkmcnt(4)
ds_read_b64_tr_b16 v[208:209], v235 offset:36864
v_cvt_pk_bf16_f32 v178, v164, v165
v_cvt_pk_bf16_f32 v179, v166, v167
v_mfma_f32_16x16x32_bf16 v[156:159], v[192:195], v[124:127], v[156:159]
v_exp_f32 v169, v169
v_mfma_f32_16x16x32_bf16 v[148:151], v[192:195], v[120:123], v[148:151]
s_waitcnt lgkmcnt(4)
ds_read_b128 v[188:191], v233 offset:6400
ds_read_b64_tr_b16 v[210:211], v235 offset:43008
v_mfma_f32_16x16x32_bf16 v[144:147], v[196:199], v[128:131], v[144:147]
v_exp_f32 v170, v170
v_mfma_f32_16x16x32_bf16 v[152:155], v[196:199], v[132:135], v[152:155]
s_waitcnt lgkmcnt(4)
)"
R"(ds_read_b64_tr_b16 v[212:213], v236 offset:36864
v_exp_f32 v171, v171
v_mfma_f32_16x16x32_bf16 v[156:159], v[200:203], v[132:135], v[156:159]
v_exp_f32 v172, v172
v_mfma_f32_16x16x32_bf16 v[148:151], v[200:203], v[128:131], v[148:151]
s_waitcnt lgkmcnt(4)
ds_read_b64_tr_b16 v[214:215], v236 offset:43008
v_cvt_pk_bf16_f32 v180, v168, v169
v_cvt_pk_bf16_f32 v181, v170, v171
v_mfma_f32_16x16x32_bf16 v[144:147], v[184:187], v[136:139], v[144:147]
v_exp_f32 v173, v173
v_mfma_f32_16x16x32_bf16 v[152:155], v[184:187], v[140:143], v[152:155]
s_waitcnt lgkmcnt(3)
ds_read_b64_tr_b16 v[216:217], v237 offset:36864
v_exp_f32 v174, v174
v_mfma_f32_16x16x32_bf16 v[156:159], v[188:191], v[140:143], v[156:159]
)"
R"(v_exp_f32 v175, v175
v_mfma_f32_16x16x32_bf16 v[148:151], v[188:191], v[136:139], v[148:151]
ds_read_b64_tr_b16 v[218:219], v237 offset:43008
v_cvt_pk_bf16_f32 v182, v172, v173
v_cvt_pk_bf16_f32 v183, v174, v175
v_mfma_f32_16x16x32_bf16 v[0:3], v[204:207], v[176:179], v[0:3]
ds_read_b64_tr_b16 v[220:221], v234 offset:36992
ds_read_b64_tr_b16 v[222:223], v234 offset:43136
v_mfma_f32_16x16x32_bf16 v[4:7], v[204:207], v[180:183], v[4:7]
s_waitcnt lgkmcnt(6)
ds_read_b64_tr_b16 v[184:185], v235 offset:36992
ds_read_b64_tr_b16 v[186:187], v235 offset:43136
v_mfma_f32_16x16x32_bf16 v[12:15], v[208:211], v[180:183], v[12:15]
ds_read_b64_tr_b16 v[188:189], v236 offset:36992
v_add_f32 v238, v238, v160
v_mfma_f32_16x16x32_bf16 v[8:11], v[208:211], v[176:179], v[8:11]
)"
R"(s_waitcnt lgkmcnt(7)
ds_read_b64_tr_b16 v[190:191], v236 offset:43136
v_add_f32 v240, v240, v168
v_mfma_f32_16x16x32_bf16 v[16:19], v[212:215], v[176:179], v[16:19]
ds_read_b64_tr_b16 v[192:193], v237 offset:36992
v_max3_f32 v242, v144, v145, v146
v_mfma_f32_16x16x32_bf16 v[20:23], v[212:215], v[180:183], v[20:23]
s_waitcnt lgkmcnt(7)
ds_read_b64_tr_b16 v[194:195], v237 offset:43136
v_max3_f32 v243, v151, v152, v153
v_mfma_f32_16x16x32_bf16 v[28:31], v[216:219], v[180:183], v[28:31]
ds_read_b64_tr_b16 v[196:197], v234 offset:37120
v_max3_f32 v242, v242, v147, v148
v_mfma_f32_16x16x32_bf16 v[24:27], v[216:219], v[176:179], v[24:27]
s_waitcnt lgkmcnt(7)
ds_read_b64_tr_b16 v[198:199], v234 offset:43264
)"
R"(ds_read_b64_tr_b16 v[200:201], v235 offset:37120
v_mfma_f32_16x16x32_bf16 v[32:35], v[220:223], v[176:179], v[32:35]
ds_read_b64_tr_b16 v[202:203], v235 offset:43264
ds_read_b64_tr_b16 v[204:205], v236 offset:37120
v_mfma_f32_16x16x32_bf16 v[36:39], v[220:223], v[180:183], v[36:39]
s_waitcnt lgkmcnt(9)
ds_read_b64_tr_b16 v[206:207], v236 offset:43264
ds_read_b64_tr_b16 v[208:209], v237 offset:37120
v_mfma_f32_16x16x32_bf16 v[44:47], v[184:187], v[180:183], v[44:47]
ds_read_b64_tr_b16 v[210:211], v237 offset:43264
v_max3_f32 v243, v243, v154, v155
v_mfma_f32_16x16x32_bf16 v[40:43], v[184:187], v[176:179], v[40:43]
s_waitcnt lgkmcnt(10)
v_max3_f32 v242, v242, v149, v150
v_max3_f32 v243, v243, v156, v157
v_mfma_f32_16x16x32_bf16 v[48:51], v[188:191], v[176:179], v[48:51]
)"
R"(v_max3_f32 v242, v242, v158, v159
v_add_f32 v239, v239, v161
v_mfma_f32_16x16x32_bf16 v[52:55], v[188:191], v[180:183], v[52:55]
s_waitcnt lgkmcnt(8)
v_max_f32 v242, v242, v243
v_add_f32 v241, v241, v169
v_mfma_f32_16x16x32_bf16 v[60:63], v[192:195], v[180:183], v[60:63]
v_cmp_lt_f32 vcc, 0x41000000, v242
v_add_f32 v238, v238, v162
v_mfma_f32_16x16x32_bf16 v[56:59], v[192:195], v[176:179], v[56:59]
s_waitcnt lgkmcnt(0)
v_add_f32 v240, v240, v170
v_add_f32 v239, v239, v163
s_sub_u32 s58, s58, 1
s_cmp_lg_u32 s58, 0
s_cbranch_scc1 Lloop_A3
)"
R"(s_barrier
v_mfma_f32_16x16x32_bf16 v[64:67], v[196:199], v[176:179], v[64:67]
ds_read_b128 v[184:187], v232 offset:12288
ds_read_b128 v[188:191], v232 offset:18432
v_mfma_f32_16x16x32_bf16 v[68:71], v[196:199], v[180:183], v[68:71]
ds_read_b128 v[192:195], v233 offset:12288
v_add_f32 v241, v241, v171
v_mfma_f32_16x16x32_bf16 v[76:79], v[200:203], v[180:183], v[76:79]
ds_read_b128 v[196:199], v233 offset:18432
v_add_f32 v238, v238, v164
v_mfma_f32_16x16x32_bf16 v[72:75], v[200:203], v[176:179], v[72:75]
ds_read_b128 v[200:203], v232 offset:12416
v_add_f32 v240, v240, v172
v_mfma_f32_16x16x32_bf16 v[80:83], v[204:207], v[176:179], v[80:83]
v_add_f32 v239, v239, v165
v_add_f32 v241, v241, v173
)"
R"(v_mfma_f32_16x16x32_bf16 v[84:87], v[204:207], v[180:183], v[84:87]
v_add_f32 v238, v238, v166
v_add_f32 v240, v240, v174
v_mfma_f32_16x16x32_bf16 v[92:95], v[208:211], v[180:183], v[92:95]
v_add_f32 v239, v239, v167
v_add_f32 v241, v241, v175
v_mfma_f32_16x16x32_bf16 v[88:91], v[208:211], v[176:179], v[88:91]
s_waitcnt lgkmcnt(4)
s_cbranch_vccz LkA4_3
s_getpc_b64 s[80:81]
s_branch Lrare_0
LkA4_3:
v_mfma_f32_16x16x32_bf16 v[160:163], v[184:187], v[96:99], v[224:227]
v_exp_f32 v144, v144
v_mfma_f32_16x16x32_bf16 v[168:171], v[184:187], v[100:103], v[228:231]
s_waitcnt lgkmcnt(3)
)"
R"(v_exp_f32 v145, v145
v_mfma_f32_16x16x32_bf16 v[172:175], v[188:191], v[100:103], v[228:231]
v_exp_f32 v146, v146
v_mfma_f32_16x16x32_bf16 v[164:167], v[188:191], v[96:99], v[224:227]
s_waitcnt lgkmcnt(2)
ds_read_b128 v[184:187], v232 offset:18560
ds_read_b128 v[188:191], v233 offset:12416
v_mfma_f32_16x16x32_bf16 v[160:163], v[192:195], v[104:107], v[160:163]
v_exp_f32 v147, v147
v_mfma_f32_16x16x32_bf16 v[168:171], v[192:195], v[108:111], v[168:171]
s_waitcnt lgkmcnt(3)
ds_read_b64_tr_b16 v[204:205], v234 offset:0
v_cvt_pk_bf16_f32 v176, v144, v145
v_cvt_pk_bf16_f32 v177, v146, v147
v_mfma_f32_16x16x32_bf16 v[172:175], v[196:199], v[108:111], v[172:175]
v_exp_f32 v148, v148
)"
R"(v_mfma_f32_16x16x32_bf16 v[164:167], v[196:199], v[104:107], v[164:167]
s_waitcnt lgkmcnt(3)
ds_read_b128 v[192:195], v233 offset:18560
ds_read_b128 v[196:199], v232 offset:12544
v_mfma_f32_16x16x32_bf16 v[160:163], v[200:203], v[112:115], v[160:163]
v_exp_f32 v149, v149
v_mfma_f32_16x16x32_bf16 v[168:171], v[200:203], v[116:119], v[168:171]
s_waitcnt lgkmcnt(4)
ds_read_b64_tr_b16 v[206:207], v234 offset:6144
v_exp_f32 v150, v150
v_mfma_f32_16x16x32_bf16 v[172:175], v[184:187], v[116:119], v[172:175]
v_exp_f32 v151, v151
v_mfma_f32_16x16x32_bf16 v[164:167], v[184:187], v[112:115], v[164:167]
s_waitcnt lgkmcnt(4)
ds_read_b128 v[200:203], v232 offset:18688
ds_read_b128 v[184:187], v233 offset:12544
)"
R"(v_mfma_f32_16x16x32_bf16 v[160:163], v[188:191], v[120:123], v[160:163]
v_exp_f32 v152, v152
v_mfma_f32_16x16x32_bf16 v[168:171], v[188:191], v[124:127], v[168:171]
s_waitcnt lgkmcnt(4)
ds_read_b64_tr_b16 v[208:209], v235 offset:0
v_cvt_pk_bf16_f32 v178, v148, v149
v_cvt_pk_bf16_f32 v179, v150, v151
v_mfma_f32_16x16x32_bf16 v[172:175], v[192:195], v[124:127], v[172:175]
v_exp_f32 v153, v153
v_mfma_f32_16x16x32_bf16 v[164:167], v[192:195], v[120:123], v[164:167]
s_waitcnt lgkmcnt(4)
ds_read_b128 v[188:191], v233 offset:18688
ds_read_b64_tr_b16 v[210:211], v235 offset:6144
v_mfma_f32_16x16x32_bf16 v[160:163], v[196:199], v[128:131], v[160:163]
v_exp_f32 v154, v154
v_mfma_f32_16x16x32_bf16 v[168:171], v[196:199], v[132:135], v[168:171]
)"
R"(s_waitcnt lgkmcnt(4)
ds_read_b64_tr_b16 v[212:213], v236 offset:0
v_exp_f32 v155, v155
v_mfma_f32_16x16x32_bf16 v[172:175], v[200:203], v[132:135], v[172:175]
v_exp_f32 v156, v156
v_mfma_f32_16x16x32_bf16 v[164:167], v[200:203], v[128:131], v[164:167]
s_waitcnt lgkmcnt(4)
ds_read_b64_tr_b16 v[214:215], v236 offset:6144
v_cvt_pk_bf16_f32 v180, v152, v153
v_cvt_pk_bf16_f32 v181, v154, v155
v_mfma_f32_16x16x32_bf16 v[160:163], v[184:187], v[136:139], v[160:163]
v_exp_f32 v157, v157
v_mfma_f32_16x16x32_bf16 v[168:171], v[184:187], v[140:143], v[168:171]
s_waitcnt lgkmcnt(3)
ds_read_b64_tr_b16 v[216:217], v237 offset:0
v_exp_f32 v158, v158
)"
R"(v_mfma_f32_16x16x32_bf16 v[172:175], v[188:191], v[140:143], v[172:175]
v_exp_f32 v159, v159
v_mfma_f32_16x16x32_bf16 v[164:167], v[188:191], v[136:139], v[164:167]
ds_read_b64_tr_b16 v[218:219], v237 offset:6144
v_cvt_pk_bf16_f32 v182, v156, v157
v_cvt_pk_bf16_f32 v183, v158, v159
v_mfma_f32_16x16x32_bf16 v[0:3], v[204:207], v[176:179], v[0:3]
ds_read_b64_tr_b16 v[220:221], v234 offset:128
ds_read_b64_tr_b16 v[222:223], v234 offset:6272
v_mfma_f32_16x16x32_bf16 v[4:7], v[204:207], v[180:183], v[4:7]
s_waitcnt lgkmcnt(6)
ds_read_b64_tr_b16 v[184:185], v235 offset:128
ds_read_b64_tr_b16 v[186:187], v235 offset:6272
v_mfma_f32_16x16x32_bf16 v[12:15], v[208:211], v[180:183], v[12:15]
ds_read_b64_tr_b16 v[188:189], v236 offset:128
v_add_f32 v238, v238, v144
)"
R"(v_mfma_f32_16x16x32_bf16 v[8:11], v[208:211], v[176:179], v[8:11]
s_waitcnt lgkmcnt(7)
ds_read_b64_tr_b16 v[190:191], v236 offset:6272
v_add_f32 v240, v240, v152
v_mfma_f32_16x16x32_bf16 v[16:19], v[212:215], v[176:179], v[16:19]
ds_read_b64_tr_b16 v[192:193], v237 offset:128
v_max3_f32 v242, v160, v161, v162
v_mfma_f32_16x16x32_bf16 v[20:23], v[212:215], v[180:183], v[20:23]
s_waitcnt lgkmcnt(7)
ds_read_b64_tr_b16 v[194:195], v237 offset:6272
v_max3_f32 v243, v167, v168, v169
v_mfma_f32_16x16x32_bf16 v[28:31], v[216:219], v[180:183], v[28:31]
ds_read_b64_tr_b16 v[196:197], v234 offset:256
v_max3_f32 v242, v242, v163, v164
v_mfma_f32_16x16x32_bf16 v[24:27], v[216:219], v[176:179], v[24:27]
s_waitcnt lgkmcnt(7)
)"
R"(ds_read_b64_tr_b16 v[198:199], v234 offset:6400
ds_read_b64_tr_b16 v[200:201], v235 offset:256
v_mfma_f32_16x16x32_bf16 v[32:35], v[220:223], v[176:179], v[32:35]
ds_read_b64_tr_b16 v[202:203], v235 offset:6400
ds_read_b64_tr_b16 v[204:205], v236 offset:256
v_mfma_f32_16x16x32_bf16 v[36:39], v[220:223], v[180:183], v[36:39]
s_waitcnt lgkmcnt(9)
ds_read_b64_tr_b16 v[206:207], v236 offset:6400
ds_read_b64_tr_b16 v[208:209], v237 offset:256
v_mfma_f32_16x16x32_bf16 v[44:47], v[184:187], v[180:183], v[44:47]
ds_read_b64_tr_b16 v[210:211], v237 offset:6400
v_max3_f32 v243, v243, v170, v171
v_mfma_f32_16x16x32_bf16 v[40:43], v[184:187], v[176:179], v[40:43]
s_waitcnt lgkmcnt(10)
v_max3_f32 v242, v242, v165, v166
v_max3_f32 v243, v243, v172, v173
)"
R"(v_mfma_f32_16x16x32_bf16 v[48:51], v[188:191], v[176:179], v[48:51]
v_max3_f32 v242, v242, v174, v175
v_add_f32 v239, v239, v145
v_mfma_f32_16x16x32_bf16 v[52:55], v[188:191], v[180:183], v[52:55]
s_waitcnt lgkmcnt(8)
v_max_f32 v242, v242, v243
v_add_f32 v241, v241, v153
v_mfma_f32_16x16x32_bf16 v[60:63], v[192:195], v[180:183], v[60:63]
v_cmp_lt_f32 vcc, 0x41000000, v242
v_add_f32 v238, v238, v146
v_mfma_f32_16x16x32_bf16 v[56:59], v[192:195], v[176:179], v[56:59]
s_waitcnt lgkmcnt(0)
v_add_f32 v240, v240, v154
v_add_f32 v239, v239, v147
s_barrier
v_mfma_f32_16x16x32_bf16 v[64:67], v[196:199], v[176:179], v[64:67]
)"
R"(ds_read_b128 v[184:187], v232 offset:24576
ds_read_b128 v[188:191], v232 offset:30720
v_mfma_f32_16x16x32_bf16 v[68:71], v[196:199], v[180:183], v[68:71]
ds_read_b128 v[192:195], v233 offset:24576
v_add_f32 v241, v241, v155
v_mfma_f32_16x16x32_bf16 v[76:79], v[200:203], v[180:183], v[76:79]
ds_read_b128 v[196:199], v233 offset:30720
v_add_f32 v238, v238, v148
v_mfma_f32_16x16x32_bf16 v[72:75], v[200:203], v[176:179], v[72:75]
ds_read_b128 v[200:203], v232 offset:24704
v_add_f32 v240, v240, v156
v_mfma_f32_16x16x32_bf16 v[80:83], v[204:207], v[176:179], v[80:83]
v_add_f32 v239, v239, v149
v_add_f32 v241, v241, v157
v_mfma_f32_16x16x32_bf16 v[84:87], v[204:207], v[180:183], v[84:87]
v_add_f32 v238, v238, v150
)"
R"(v_add_f32 v240, v240, v158
v_mfma_f32_16x16x32_bf16 v[92:95], v[208:211], v[180:183], v[92:95]
v_add_f32 v239, v239, v151
v_add_f32 v241, v241, v159
v_mfma_f32_16x16x32_bf16 v[88:91], v[208:211], v[176:179], v[88:91]
s_waitcnt lgkmcnt(4)
s_cbranch_vccz LkA4_0
s_getpc_b64 s[80:81]
s_branch Lrare_1
LkA4_0:
v_mfma_f32_16x16x32_bf16 v[144:147], v[184:187], v[96:99], v[224:227]
v_exp_f32 v160, v160
v_mfma_f32_16x16x32_bf16 v[152:155], v[184:187], v[100:103], v[228:231]
s_waitcnt lgkmcnt(3)
v_exp_f32 v161, v161
v_mfma_f32_16x16x32_bf16 v[156:159], v[188:191], v[100:103], v[228:231]
)"
R"(v_exp_f32 v162, v162
v_mfma_f32_16x16x32_bf16 v[148:151], v[188:191], v[96:99], v[224:227]
s_waitcnt lgkmcnt(2)
ds_read_b128 v[184:187], v232 offset:30848
ds_read_b128 v[188:191], v233 offset:24704
v_mfma_f32_16x16x32_bf16 v[144:147], v[192:195], v[104:107], v[144:147]
v_exp_f32 v163, v163
v_mfma_f32_16x16x32_bf16 v[152:155], v[192:195], v[108:111], v[152:155]
s_waitcnt lgkmcnt(3)
ds_read_b64_tr_b16 v[204:205], v234 offset:12288
v_cvt_pk_bf16_f32 v176, v160, v161
v_cvt_pk_bf16_f32 v177, v162, v163
v_mfma_f32_16x16x32_bf16 v[156:159], v[196:199], v[108:111], v[156:159]
v_exp_f32 v164, v164
v_mfma_f32_16x16x32_bf16 v[148:151], v[196:199], v[104:107], v[148:151]
s_waitcnt lgkmcnt(3)
)"
R"(ds_read_b128 v[192:195], v233 offset:30848
ds_read_b128 v[196:199], v232 offset:24832
v_mfma_f32_16x16x32_bf16 v[144:147], v[200:203], v[112:115], v[144:147]
v_exp_f32 v165, v165
v_mfma_f32_16x16x32_bf16 v[152:155], v[200:203], v[116:119], v[152:155]
s_waitcnt lgkmcnt(4)
ds_read_b64_tr_b16 v[206:207], v234 offset:18432
v_exp_f32 v166, v166
v_mfma_f32_16x16x32_bf16 v[156:159], v[184:187], v[116:119], v[156:159]
v_exp_f32 v167, v167
v_mfma_f32_16x16x32_bf16 v[148:151], v[184:187], v[112:115], v[148:151]
s_waitcnt lgkmcnt(4)
ds_read_b128 v[200:203], v232 offset:30976
ds_read_b128 v[184:187], v233 offset:24832
v_mfma_f32_16x16x32_bf16 v[144:147], v[188:191], v[120:123], v[144:147]
v_exp_f32 v168, v168
)"
R"(v_mfma_f32_16x16x32_bf16 v[152:155], v[188:191], v[124:127], v[152:155]
s_waitcnt lgkmcnt(4)
ds_read_b64_tr_b16 v[208:209], v235 offset:12288
v_cvt_pk_bf16_f32 v178, v164, v165
v_cvt_pk_bf16_f32 v179, v166, v167
v_mfma_f32_16x16x32_bf16 v[156:159], v[192:195], v[124:127], v[156:159]
v_exp_f32 v169, v169
v_mfma_f32_16x16x32_bf16 v[148:151], v[192:195], v[120:123], v[148:151]
s_waitcnt lgkmcnt(4)
ds_read_b128 v[188:191], v233 offset:30976
ds_read_b64_tr_b16 v[210:211], v235 offset:18432
v_mfma_f32_16x16x32_bf16 v[144:147], v[196:199], v[128:131], v[144:147]
v_exp_f32 v170, v170
v_mfma_f32_16x16x32_bf16 v[152:155], v[196:199], v[132:135], v[152:155]
s_waitcnt lgkmcnt(4)
ds_read_b64_tr_b16 v[212:213], v236 offset:12288
)"
R"(v_exp_f32 v171, v171
v_mfma_f32_16x16x32_bf16 v[156:159], v[200:203], v[132:135], v[156:159]
v_exp_f32 v172, v172
v_mfma_f32_16x16x32_bf16 v[148:151], v[200:203], v[128:131], v[148:151]
s_waitcnt lgkmcnt(4)
ds_read_b64_tr_b16 v[214:215], v236 offset:18432
v_cvt_pk_bf16_f32 v180, v168, v169
v_cvt_pk_bf16_f32 v181, v170, v171
v_mfma_f32_16x16x32_bf16 v[144:147], v[184:187], v[136:139], v[144:147]
v_exp_f32 v173, v173
v_mfma_f32_16x16x32_bf16 v[152:155], v[184:187], v[140:143], v[152:155]
s_waitcnt lgkmcnt(3)
ds_read_b64_tr_b16 v[216:217], v237 offset:12288
v_exp_f32 v174, v174
v_mfma_f32_16x16x32_bf16 v[156:159], v[188:191], v[140:143], v[156:159]
v_exp_f32 v175, v175
)"
R"(v_mfma_f32_16x16x32_bf16 v[148:151], v[188:191], v[136:139], v[148:151]
ds_read_b64_tr_b16 v[218:219], v237 offset:18432
v_cvt_pk_bf16_f32 v182, v172, v173
v_cvt_pk_bf16_f32 v183, v174, v175
v_mfma_f32_16x16x32_bf16 v[0:3], v[204:207], v[176:179], v[0:3]
ds_read_b64_tr_b16 v[220:221], v234 offset:12416
ds_read_b64_tr_b16 v[222:223], v234 offset:18560
v_mfma_f32_16x16x32_bf16 v[4:7], v[204:207], v[180:183], v[4:7]
s_waitcnt lgkmcnt(6)
ds_read_b64_tr_b16 v[184:185], v235 offset:12416
ds_read_b64_tr_b16 v[186:187], v235 offset:18560
v_mfma_f32_16x16x32_bf16 v[12:15], v[208:211], v[180:183], v[12:15]
ds_read_b64_tr_b16 v[188:189], v236 offset:12416
v_add_f32 v238, v238, v160
v_mfma_f32_16x16x32_bf16 v[8:11], v[208:211], v[176:179], v[8:11]
s_waitcnt lgkmcnt(7)
)"
R"(ds_read_b64_tr_b16 v[190:191], v236 offset:18560
v_add_f32 v240, v240, v168
v_mfma_f32_16x16x32_bf16 v[16:19], v[212:215], v[176:179], v[16:19]
ds_read_b64_tr_b16 v[192:193], v237 offset:12416
v_max3_f32 v242, v144, v145, v146
v_mfma_f32_16x16x32_bf16 v[20:23], v[212:215], v[180:183], v[20:23]
s_waitcnt lgkmcnt(7)
ds_read_b64_tr_b16 v[194:195], v237 offset:18560
v_max3_f32 v243, v151, v152, v153
v_mfma_f32_16x16x32_bf16 v[28:31], v[216:219], v[180:183], v[28:31]
ds_read_b64_tr_b16 v[196:197], v234 offset:12544
v_max3_f32 v242, v242, v147, v148
v_mfma_f32_16x16x32_bf16 v[24:27], v[216:219], v[176:179], v[24:27]
s_waitcnt lgkmcnt(7)
ds_read_b64_tr_b16 v[198:199], v234 offset:18688
ds_read_b64_tr_b16 v[200:201], v235 offset:12544
)"
R"(v_mfma_f32_16x16x32_bf16 v[32:35], v[220:223], v[176:179], v[32:35]
ds_read_b64_tr_b16 v[202:203], v235 offset:18688
ds_read_b64_tr_b16 v[204:205], v236 offset:12544
v_mfma_f32_16x16x32_bf16 v[36:39], v[220:223], v[180:183], v[36:39]
s_waitcnt lgkmcnt(9)
ds_read_b64_tr_b16 v[206:207], v236 offset:18688
ds_read_b64_tr_b16 v[208:209], v237 offset:12544
v_mfma_f32_16x16x32_bf16 v[44:47], v[184:187], v[180:183], v[44:47]
ds_read_b64_tr_b16 v[210:211], v237 offset:18688
v_max3_f32 v243, v243, v154, v155
v_mfma_f32_16x16x32_bf16 v[40:43], v[184:187], v[176:179], v[40:43]
s_waitcnt lgkmcnt(10)
v_max3_f32 v242, v242, v149, v150
v_max3_f32 v243, v243, v156, v157
v_mfma_f32_16x16x32_bf16 v[48:51], v[188:191], v[176:179], v[48:51]
v_max3_f32 v242, v242, v158, v159
)"
R"(v_add_f32 v239, v239, v161
v_mfma_f32_16x16x32_bf16 v[52:55], v[188:191], v[180:183], v[52:55]
s_waitcnt lgkmcnt(8)
v_max_f32 v242, v242, v243
v_add_f32 v241, v241, v169
v_mfma_f32_16x16x32_bf16 v[60:63], v[192:195], v[180:183], v[60:63]
v_cmp_lt_f32 vcc, 0x41000000, v242
v_add_f32 v238, v238, v162
v_mfma_f32_16x16x32_bf16 v[56:59], v[192:195], v[176:179], v[56:59]
s_waitcnt lgkmcnt(0)
v_add_f32 v240, v240, v170
v_add_f32 v239, v239, v163
s_barrier
v_mfma_f32_16x16x32_bf16 v[64:67], v[196:199], v[176:179], v[64:67]
ds_read_b128 v[184:187], v232 offset:36864
ds_read_b128 v[188:191], v232 offset:43008
)"
R"(v_mfma_f32_16x16x32_bf16 v[68:71], v[196:199], v[180:183], v[68:71]
ds_read_b128 v[192:195], v233 offset:36864
v_add_f32 v241, v241, v171
v_mfma_f32_16x16x32_bf16 v[76:79], v[200:203], v[180:183], v[76:79]
ds_read_b128 v[196:199], v233 offset:43008
v_add_f32 v238, v238, v164
v_mfma_f32_16x16x32_bf16 v[72:75], v[200:203], v[176:179], v[72:75]
ds_read_b128 v[200:203], v232 offset:36992
v_add_f32 v240, v240, v172
v_mfma_f32_16x16x32_bf16 v[80:83], v[204:207], v[176:179], v[80:83]
v_add_f32 v239, v239, v165
v_add_f32 v241, v241, v173
v_mfma_f32_16x16x32_bf16 v[84:87], v[204:207], v[180:183], v[84:87]
v_add_f32 v238, v238, v166
v_add_f32 v240, v240, v174
v_mfma_f32_16x16x32_bf16 v[92:95], v[208:211], v[180:183], v[92:95]
)"
R"(v_add_f32 v239, v239, v167
v_add_f32 v241, v241, v175
v_mfma_f32_16x16x32_bf16 v[88:91], v[208:211], v[176:179], v[88:91]
s_waitcnt lgkmcnt(4)
s_cbranch_vccz LkA4_1
s_getpc_b64 s[80:81]
s_branch Lrare_0
LkA4_1:
v_mfma_f32_16x16x32_bf16 v[160:163], v[184:187], v[96:99], v[224:227]
v_exp_f32 v144, v144
v_mfma_f32_16x16x32_bf16 v[168:171], v[184:187], v[100:103], v[228:231]
s_waitcnt lgkmcnt(3)
v_exp_f32 v145, v145
v_mfma_f32_16x16x32_bf16 v[172:175], v[188:191], v[100:103], v[228:231]
v_exp_f32 v146, v146
v_mfma_f32_16x16x32_bf16 v[164:167], v[188:191], v[96:99], v[224:227]
)"
R"(s_waitcnt lgkmcnt(2)
ds_read_b128 v[184:187], v232 offset:43136
ds_read_b128 v[188:191], v233 offset:36992
v_mfma_f32_16x16x32_bf16 v[160:163], v[192:195], v[104:107], v[160:163]
v_exp_f32 v147, v147
v_mfma_f32_16x16x32_bf16 v[168:171], v[192:195], v[108:111], v[168:171]
s_waitcnt lgkmcnt(3)
ds_read_b64_tr_b16 v[204:205], v234 offset:24576
v_cvt_pk_bf16_f32 v176, v144, v145
v_cvt_pk_bf16_f32 v177, v146, v147
v_mfma_f32_16x16x32_bf16 v[172:175], v[196:199], v[108:111], v[172:175]
v_exp_f32 v148, v148
v_mfma_f32_16x16x32_bf16 v[164:167], v[196:199], v[104:107], v[164:167]
s_waitcnt lgkmcnt(3)
ds_read_b128 v[192:195], v233 offset:43136
ds_read_b128 v[196:199], v232 offset:37120
)"
R"(v_mfma_f32_16x16x32_bf16 v[160:163], v[200:203], v[112:115], v[160:163]
v_exp_f32 v149, v149
v_mfma_f32_16x16x32_bf16 v[168:171], v[200:203], v[116:119], v[168:171]
s_waitcnt lgkmcnt(4)
ds_read_b64_tr_b16 v[206:207], v234 offset:30720
v_exp_f32 v150, v150
v_mfma_f32_16x16x32_bf16 v[172:175], v[184:187], v[116:119], v[172:175]
v_exp_f32 v151, v151
v_mfma_f32_16x16x32_bf16 v[164:167], v[184:187], v[112:115], v[164:167]
s_waitcnt lgkmcnt(4)
ds_read_b128 v[200:203], v232 offset:43264
ds_read_b128 v[184:187], v233 offset:37120
v_mfma_f32_16x16x32_bf16 v[160:163], v[188:191], v[120:123], v[160:163]
v_exp_f32 v152, v152
v_mfma_f32_16x16x32_bf16 v[168:171], v[188:191], v[124:127], v[168:171]
s_waitcnt lgkmcnt(4)
)"
R"(ds_read_b64_tr_b16 v[208:209], v235 offset:24576
v_cvt_pk_bf16_f32 v178, v148, v149
v_cvt_pk_bf16_f32 v179, v150, v151
v_mfma_f32_16x16x32_bf16 v[172:175], v[192:195], v[124:127], v[172:175]
v_exp_f32 v153, v153
v_mfma_f32_16x16x32_bf16 v[164:167], v[192:195], v[120:123], v[164:167]
s_waitcnt lgkmcnt(4)
ds_read_b128 v[188:191], v233 offset:43264
ds_read_b64_tr_b16 v[210:211], v235 offset:30720
v_mfma_f32_16x16x32_bf16 v[160:163], v[196:199], v[128:131], v[160:163]
v_exp_f32 v154, v154
v_mfma_f32_16x16x32_bf16 v[168:171], v[196:199], v[132:135], v[168:171]
s_waitcnt lgkmcnt(4)
ds_read_b64_tr_b16 v[212:213], v236 offset:24576
v_exp_f32 v155, v155
v_mfma_f32_16x16x32_bf16 v[172:175], v[200:203], v[132:135], v[172:175]
)"
R"(v_exp_f32 v156, v156
v_mfma_f32_16x16x32_bf16 v[164:167], v[200:203], v[128:131], v[164:167]
s_waitcnt lgkmcnt(4)
ds_read_b64_tr_b16 v[214:215], v236 offset:30720
v_cvt_pk_bf16_f32 v180, v152, v153
v_cvt_pk_bf16_f32 v181, v154, v155
v_mfma_f32_16x16x32_bf16 v[160:163], v[184:187], v[136:139], v[160:163]
v_exp_f32 v157, v157
v_mfma_f32_16x16x32_bf16 v[168:171], v[184:187], v[140:143], v[168:171]
s_waitcnt lgkmcnt(3)
ds_read_b64_tr_b16 v[216:217], v237 offset:24576
v_exp_f32 v158, v158
v_mfma_f32_16x16x32_bf16 v[172:175], v[188:191], v[140:143], v[172:175]
v_exp_f32 v159, v159
v_mfma_f32_16x16x32_bf16 v[164:167], v[188:191], v[136:139], v[164:167]
ds_read_b64_tr_b16 v[218:219], v237 offset:30720
)"
R"(v_cvt_pk_bf16_f32 v182, v156, v157
v_cvt_pk_bf16_f32 v183, v158, v159
v_mfma_f32_16x16x32_bf16 v[0:3], v[204:207], v[176:179], v[0:3]
ds_read_b64_tr_b16 v[220:221], v234 offset:24704
ds_read_b64_tr_b16 v[222:223], v234 offset:30848
v_mfma_f32_16x16x32_bf16 v[4:7], v[204:207], v[180:183], v[4:7]
s_waitcnt lgkmcnt(6)
ds_read_b64_tr_b16 v[184:185], v235 offset:24704
ds_read_b64_tr_b16 v[186:187], v235 offset:30848
v_mfma_f32_16x16x32_bf16 v[12:15], v[208:211], v[180:183], v[12:15]
ds_read_b64_tr_b16 v[188:189], v236 offset:24704
v_add_f32 v238, v238, v144
v_mfma_f32_16x16x32_bf16 v[8:11], v[208:211], v[176:179], v[8:11]
s_waitcnt lgkmcnt(7)
ds_read_b64_tr_b16 v[190:191], v236 offset:30848
v_add_f32 v240, v240, v152
)"
R"(v_mfma_f32_16x16x32_bf16 v[16:19], v[212:215], v[176:179], v[16:19]
ds_read_b64_tr_b16 v[192:193], v237 offset:24704
v_max3_f32 v242, v160, v161, v162
v_mfma_f32_16x16x32_bf16 v[20:23], v[212:215], v[180:183], v[20:23]
s_waitcnt lgkmcnt(7)
ds_read_b64_tr_b16 v[194:195], v237 offset:30848
v_max3_f32 v243, v167, v168, v169
v_mfma_f32_16x16x32_bf16 v[28:31], v[216:219], v[180:183], v[28:31]
ds_read_b64_tr_b16 v[196:197], v234 offset:24832
v_max3_f32 v242, v242, v163, v164
v_mfma_f32_16x16x32_bf16 v[24:27], v[216:219], v[176:179], v[24:27]
s_waitcnt lgkmcnt(7)
ds_read_b64_tr_b16 v[198:199], v234 offset:30976
ds_read_b64_tr_b16 v[200:201], v235 offset:24832
v_mfma_f32_16x16x32_bf16 v[32:35], v[220:223], v[176:179], v[32:35]
ds_read_b64_tr_b16 v[202:203], v235 offset:30976
)"
R"(ds_read_b64_tr_b16 v[204:205], v236 offset:24832
v_mfma_f32_16x16x32_bf16 v[36:39], v[220:223], v[180:183], v[36:39]
s_waitcnt lgkmcnt(9)
ds_read_b64_tr_b16 v[206:207], v236 offset:30976
ds_read_b64_tr_b16 v[208:209], v237 offset:24832
v_mfma_f32_16x16x32_bf16 v[44:47], v[184:187], v[180:183], v[44:47]
ds_read_b64_tr_b16 v[210:211], v237 offset:30976
v_max3_f32 v243, v243, v170, v171
v_mfma_f32_16x16x32_bf16 v[40:43], v[184:187], v[176:179], v[40:43]
s_waitcnt lgkmcnt(10)
v_max3_f32 v242, v242, v165, v166
v_max3_f32 v243, v243, v172, v173
v_mfma_f32_16x16x32_bf16 v[48:51], v[188:191], v[176:179], v[48:51]
v_max3_f32 v242, v242, v174, v175
v_add_f32 v239, v239, v145
v_mfma_f32_16x16x32_bf16 v[52:55], v[188:191], v[180:183], v[52:55]
)"
R"(s_waitcnt lgkmcnt(8)
v_max_f32 v242, v242, v243
v_add_f32 v241, v241, v153
v_mfma_f32_16x16x32_bf16 v[60:63], v[192:195], v[180:183], v[60:63]
v_cmp_lt_f32 vcc, 0x41000000, v242
v_add_f32 v238, v238, v146
v_mfma_f32_16x16x32_bf16 v[56:59], v[192:195], v[176:179], v[56:59]
s_waitcnt lgkmcnt(0)
v_add_f32 v240, v240, v154
v_add_f32 v239, v239, v147
s_barrier
v_mfma_f32_16x16x32_bf16 v[64:67], v[196:199], v[176:179], v[64:67]
v_mfma_f32_16x16x32_bf16 v[68:71], v[196:199], v[180:183], v[68:71]
v_add_f32 v241, v241, v155
v_mfma_f32_16x16x32_bf16 v[76:79], v[200:203], v[180:183], v[76:79]
v_add_f32 v238, v238, v148
)"
R"(v_mfma_f32_16x16x32_bf16 v[72:75], v[200:203], v[176:179], v[72:75]
v_add_f32 v240, v240, v156
v_mfma_f32_16x16x32_bf16 v[80:83], v[204:207], v[176:179], v[80:83]
v_add_f32 v239, v239, v149
v_add_f32 v241, v241, v157
v_mfma_f32_16x16x32_bf16 v[84:87], v[204:207], v[180:183], v[84:87]
v_add_f32 v238, v238, v150
v_add_f32 v240, v240, v158
v_mfma_f32_16x16x32_bf16 v[92:95], v[208:211], v[180:183], v[92:95]
v_add_f32 v239, v239, v151
v_add_f32 v241, v241, v159
v_mfma_f32_16x16x32_bf16 v[88:91], v[208:211], v[176:179], v[88:91]
s_cbranch_vccz LkA4_2
s_getpc_b64 s[80:81]
s_branch Lrare_1
LkA4_2:
)"
R"(ds_read_b64_tr_b16 v[204:205], v234 offset:36864
ds_read_b64_tr_b16 v[206:207], v234 offset:43008
ds_read_b64_tr_b16 v[208:209], v235 offset:36864
ds_read_b64_tr_b16 v[210:211], v235 offset:43008
ds_read_b64_tr_b16 v[212:213], v236 offset:36864
ds_read_b64_tr_b16 v[214:215], v236 offset:43008
ds_read_b64_tr_b16 v[216:217], v237 offset:36864
ds_read_b64_tr_b16 v[218:219], v237 offset:43008
ds_read_b64_tr_b16 v[220:221], v234 offset:36992
ds_read_b64_tr_b16 v[222:223], v234 offset:43136
ds_read_b64_tr_b16 v[184:185], v235 offset:36992
ds_read_b64_tr_b16 v[186:187], v235 offset:43136
v_exp_f32 v160, v160
v_exp_f32 v161, v161
v_exp_f32 v162, v162
v_exp_f32 v163, v163
)"
R"(v_exp_f32 v164, v164
v_exp_f32 v165, v165
v_exp_f32 v166, v166
v_exp_f32 v167, v167
v_exp_f32 v168, v168
v_exp_f32 v169, v169
v_exp_f32 v170, v170
v_exp_f32 v171, v171
v_exp_f32 v172, v172
v_exp_f32 v173, v173
v_exp_f32 v174, v174
v_exp_f32 v175, v175
v_cvt_pk_bf16_f32 v176, v160, v161
v_cvt_pk_bf16_f32 v177, v162, v163
v_cvt_pk_bf16_f32 v178, v164, v165
v_cvt_pk_bf16_f32 v179, v166, v167
)"
R"(v_cvt_pk_bf16_f32 v180, v168, v169
v_cvt_pk_bf16_f32 v181, v170, v171
v_cvt_pk_bf16_f32 v182, v172, v173
v_cvt_pk_bf16_f32 v183, v174, v175
s_waitcnt lgkmcnt(10)
v_mfma_f32_16x16x32_bf16 v[0:3], v[204:207], v[176:179], v[0:3]
ds_read_b64_tr_b16 v[188:189], v236 offset:36992
ds_read_b64_tr_b16 v[190:191], v236 offset:43136
v_mfma_f32_16x16x32_bf16 v[4:7], v[204:207], v[180:183], v[4:7]
v_add_f32 v238, v238, v160
v_add_f32 v240, v240, v168
s_waitcnt lgkmcnt(10)
v_mfma_f32_16x16x32_bf16 v[12:15], v[208:211], v[180:183], v[12:15]
ds_read_b64_tr_b16 v[192:193], v237 offset:36992
ds_read_b64_tr_b16 v[194:195], v237 offset:43136
v_mfma_f32_16x16x32_bf16 v[8:11], v[208:211], v[176:179], v[8:11]
)"
R"(v_add_f32 v239, v239, v161
v_add_f32 v241, v241, v169
s_waitcnt lgkmcnt(10)
v_mfma_f32_16x16x32_bf16 v[16:19], v[212:215], v[176:179], v[16:19]
ds_read_b64_tr_b16 v[196:197], v234 offset:37120
ds_read_b64_tr_b16 v[198:199], v234 offset:43264
v_mfma_f32_16x16x32_bf16 v[20:23], v[212:215], v[180:183], v[20:23]
v_add_f32 v238, v238, v162
v_add_f32 v240, v240, v170
s_waitcnt lgkmcnt(10)
v_mfma_f32_16x16x32_bf16 v[28:31], v[216:219], v[180:183], v[28:31]
ds_read_b64_tr_b16 v[200:201], v235 offset:37120
ds_read_b64_tr_b16 v[202:203], v235 offset:43264
v_mfma_f32_16x16x32_bf16 v[24:27], v[216:219], v[176:179], v[24:27]
v_add_f32 v239, v239, v163
v_add_f32 v241, v241, v171
)"
R"(s_waitcnt lgkmcnt(10)
v_mfma_f32_16x16x32_bf16 v[32:35], v[220:223], v[176:179], v[32:35]
ds_read_b64_tr_b16 v[204:205], v236 offset:37120
ds_read_b64_tr_b16 v[206:207], v236 offset:43264
v_mfma_f32_16x16x32_bf16 v[36:39], v[220:223], v[180:183], v[36:39]
v_add_f32 v238, v238, v164
v_add_f32 v240, v240, v172
s_waitcnt lgkmcnt(10)
v_mfma_f32_16x16x32_bf16 v[44:47], v[184:187], v[180:183], v[44:47]
ds_read_b64_tr_b16 v[208:209], v237 offset:37120
ds_read_b64_tr_b16 v[210:211], v237 offset:43264
v_mfma_f32_16x16x32_bf16 v[40:43], v[184:187], v[176:179], v[40:43]
v_add_f32 v239, v239, v165
v_add_f32 v241, v241, v173
s_waitcnt lgkmcnt(10)
v_mfma_f32_16x16x32_bf16 v[48:51], v[188:191], v[176:179], v[48:51]
)"
R"(v_add_f32 v238, v238, v166
v_add_f32 v240, v240, v174
v_mfma_f32_16x16x32_bf16 v[52:55], v[188:191], v[180:183], v[52:55]
v_add_f32 v239, v239, v167
v_add_f32 v241, v241, v175
s_waitcnt lgkmcnt(8)
v_mfma_f32_16x16x32_bf16 v[60:63], v[192:195], v[180:183], v[60:63]
v_mfma_f32_16x16x32_bf16 v[56:59], v[192:195], v[176:179], v[56:59]
s_waitcnt lgkmcnt(0)
s_cmp_eq_u32 s73, -1
s_cbranch_scc0 LpeA5_2_0
LplA5_2_0:
s_memrealtime s[88:89]
s_waitcnt lgkmcnt(0)
s_sub_u32 s89, s88, s94
s_cmp_lt_i32 s89, 0
)"
R"(s_cbranch_scc1 LplA5_2_0
s_add_u32 s95, s95, 274
s_sub_u32 s89, s88, s95
s_add_u32 s88, s88, 92
s_cmp_gt_i32 s89, 746
s_cselect_b32 s68, 20, 0
s_add_u32 s88, s88, s68
s_sub_u32 s89, s88, s95
s_cmp_gt_i32 s89, 0
s_cselect_b32 s94, s88, s95
LpeA5_2_0:
s_barrier
v_mfma_f32_16x16x32_bf16 v[64:67], v[196:199], v[176:179], v[64:67]
v_mfma_f32_16x16x32_bf16 v[68:71], v[196:199], v[180:183], v[68:71]
v_mfma_f32_16x16x32_bf16 v[76:79], v[200:203], v[180:183], v[76:79]
v_mfma_f32_16x16x32_bf16 v[72:75], v[200:203], v[176:179], v[72:75]
)"
R"(v_mfma_f32_16x16x32_bf16 v[80:83], v[204:207], v[176:179], v[80:83]
v_mfma_f32_16x16x32_bf16 v[84:87], v[204:207], v[180:183], v[84:87]
v_mfma_f32_16x16x32_bf16 v[92:95], v[208:211], v[180:183], v[92:95]
v_mfma_f32_16x16x32_bf16 v[88:91], v[208:211], v[176:179], v[88:91]
s_cmp_eq_u32 s65, 1
s_cselect_b64 vcc, -1, 0
s_setprio 2
s_mul_i32 s50, s90, 245760
s_add_u32 s50, s50, s52
s_add_u32 m0, s56, 98304
s_nop 0
buffer_load_dwordx4 v244, s[32:35], s50 offen lds
buffer_load_dwordx4 v245, s[32:35], s50 offen offset:1024 lds
buffer_load_dwordx4 v246, s[32:35], s50 offen offset:2048 lds
s_add_u32 s50, s50, 40960
s_add_u32 m0, s56, 110592
)"
R"(s_nop 0
buffer_load_dwordx4 v244, s[32:35], s50 offen lds
buffer_load_dwordx4 v245, s[32:35], s50 offen offset:1024 lds
buffer_load_dwordx4 v246, s[32:35], s50 offen offset:2048 lds
s_add_u32 s50, s50, 40960
s_add_u32 m0, s56, 0
s_nop 0
buffer_load_dwordx4 v248, s[20:23], s48 offen nt lds
buffer_load_dwordx4 v249, s[20:23], s48 offen offset:1024 nt lds
buffer_load_dwordx4 v250, s[20:23], s48 offen offset:2048 nt lds
s_add_u32 s48, s48, 12288
s_add_u32 m0, s56, 86016
s_nop 0
buffer_load_dwordx4 v251, s[24:27], s49 offen nt lds
buffer_load_dwordx4 v252, s[24:27], s49 offen offset:1024 nt lds
buffer_load_dwordx4 v253, s[24:27], s49 offen offset:2048 nt lds
)"
R"(s_add_u32 s49, s49, 12288
s_add_u32 s86, s83, 4096
s_add_u32 s87, s83, 8192
v_add_f32 v238, v238, v239
v_add_f32 v240, v240, v241
v_mov_b32 v242, v238
v_mov_b32 v243, v240
s_nop 0
v_permlane32_swap_b32 v238, v242
v_permlane32_swap_b32 v240, v243
v_add_f32 v238, v238, v242
v_add_f32 v240, v240, v243
v_mov_b32 v242, v238
v_mov_b32 v243, v240
s_nop 0
v_permlane16_swap_b32 v238, v242
)"
R"(v_permlane16_swap_b32 v240, v243
v_add_f32 v238, v238, v242
v_add_f32 v240, v240, v243
v_rcp_f32 v180, v238
v_rcp_f32 v182, v240
v_add_u32 v234, 49152, v234
v_add_u32 v235, 49152, v235
v_add_u32 v236, 49152, v236
v_add_u32 v237, 49152, v237
v_pk_mul_f32 v[0:1], v[0:1], v[180:181] op_sel_hi:[1,0]
v_pk_mul_f32 v[2:3], v[2:3], v[180:181] op_sel_hi:[1,0]
v_pk_mul_f32 v[8:9], v[8:9], v[180:181] op_sel_hi:[1,0]
v_pk_mul_f32 v[10:11], v[10:11], v[180:181] op_sel_hi:[1,0]
v_cvt_pk_bf16_f32 v96, v0, v1
v_cvt_pk_bf16_f32 v97, v2, v3
v_cvt_pk_bf16_f32 v98, v8, v9
)"
R"(v_cvt_pk_bf16_f32 v99, v10, v11
buffer_store_dwordx4 v[96:99], v247, s[40:43], s83 offen sc1 nt
v_pk_mul_f32 v[4:5], v[4:5], v[182:183] op_sel_hi:[1,0]
v_pk_mul_f32 v[6:7], v[6:7], v[182:183] op_sel_hi:[1,0]
v_pk_mul_f32 v[12:13], v[12:13], v[182:183] op_sel_hi:[1,0]
v_pk_mul_f32 v[14:15], v[14:15], v[182:183] op_sel_hi:[1,0]
v_cvt_pk_bf16_f32 v100, v4, v5
v_cvt_pk_bf16_f32 v101, v6, v7
v_cvt_pk_bf16_f32 v102, v12, v13
v_cvt_pk_bf16_f32 v103, v14, v15
buffer_store_dwordx4 v[100:103], v247, s[40:43], s83 offen offset:1024 sc1 nt
v_pk_mul_f32 v[16:17], v[16:17], v[180:181] op_sel_hi:[1,0]
v_pk_mul_f32 v[18:19], v[18:19], v[180:181] op_sel_hi:[1,0]
v_pk_mul_f32 v[24:25], v[24:25], v[180:181] op_sel_hi:[1,0]
v_pk_mul_f32 v[26:27], v[26:27], v[180:181] op_sel_hi:[1,0]
v_cvt_pk_bf16_f32 v104, v16, v17
)"
R"(v_cvt_pk_bf16_f32 v105, v18, v19
v_cvt_pk_bf16_f32 v106, v24, v25
v_cvt_pk_bf16_f32 v107, v26, v27
buffer_store_dwordx4 v[104:107], v247, s[40:43], s83 offen offset:2048 sc1 nt
v_pk_mul_f32 v[20:21], v[20:21], v[182:183] op_sel_hi:[1,0]
v_pk_mul_f32 v[22:23], v[22:23], v[182:183] op_sel_hi:[1,0]
v_pk_mul_f32 v[28:29], v[28:29], v[182:183] op_sel_hi:[1,0]
v_pk_mul_f32 v[30:31], v[30:31], v[182:183] op_sel_hi:[1,0]
v_cvt_pk_bf16_f32 v108, v20, v21
v_cvt_pk_bf16_f32 v109, v22, v23
v_cvt_pk_bf16_f32 v110, v28, v29
v_cvt_pk_bf16_f32 v111, v30, v31
buffer_store_dwordx4 v[108:111], v247, s[40:43], s83 offen offset:3072 sc1 nt
v_pk_mul_f32 v[32:33], v[32:33], v[180:181] op_sel_hi:[1,0]
v_pk_mul_f32 v[34:35], v[34:35], v[180:181] op_sel_hi:[1,0]
v_pk_mul_f32 v[40:41], v[40:41], v[180:181] op_sel_hi:[1,0]
)"
R"(v_pk_mul_f32 v[42:43], v[42:43], v[180:181] op_sel_hi:[1,0]
v_cvt_pk_bf16_f32 v112, v32, v33
v_cvt_pk_bf16_f32 v113, v34, v35
v_cvt_pk_bf16_f32 v114, v40, v41
v_cvt_pk_bf16_f32 v115, v42, v43
buffer_store_dwordx4 v[112:115], v247, s[40:43], s86 offen sc1 nt
v_pk_mul_f32 v[36:37], v[36:37], v[182:183] op_sel_hi:[1,0]
v_pk_mul_f32 v[38:39], v[38:39], v[182:183] op_sel_hi:[1,0]
v_pk_mul_f32 v[44:45], v[44:45], v[182:183] op_sel_hi:[1,0]
v_pk_mul_f32 v[46:47], v[46:47], v[182:183] op_sel_hi:[1,0]
v_cvt_pk_bf16_f32 v116, v36, v37
v_cvt_pk_bf16_f32 v117, v38, v39
v_cvt_pk_bf16_f32 v118, v44, v45
v_cvt_pk_bf16_f32 v119, v46, v47
buffer_store_dwordx4 v[116:119], v247, s[40:43], s86 offen offset:1024 sc1 nt
v_pk_mul_f32 v[48:49], v[48:49], v[180:181] op_sel_hi:[1,0]
)"
R"(v_pk_mul_f32 v[50:51], v[50:51], v[180:181] op_sel_hi:[1,0]
v_pk_mul_f32 v[56:57], v[56:57], v[180:181] op_sel_hi:[1,0]
v_pk_mul_f32 v[58:59], v[58:59], v[180:181] op_sel_hi:[1,0]
v_cvt_pk_bf16_f32 v120, v48, v49
v_cvt_pk_bf16_f32 v121, v50, v51
v_cvt_pk_bf16_f32 v122, v56, v57
v_cvt_pk_bf16_f32 v123, v58, v59
buffer_store_dwordx4 v[120:123], v247, s[40:43], s86 offen offset:2048 sc1 nt
v_pk_mul_f32 v[52:53], v[52:53], v[182:183] op_sel_hi:[1,0]
v_pk_mul_f32 v[54:55], v[54:55], v[182:183] op_sel_hi:[1,0]
v_pk_mul_f32 v[60:61], v[60:61], v[182:183] op_sel_hi:[1,0]
v_pk_mul_f32 v[62:63], v[62:63], v[182:183] op_sel_hi:[1,0]
v_cvt_pk_bf16_f32 v124, v52, v53
v_cvt_pk_bf16_f32 v125, v54, v55
v_cvt_pk_bf16_f32 v126, v60, v61
v_cvt_pk_bf16_f32 v127, v62, v63
)"
R"(buffer_store_dwordx4 v[124:127], v247, s[40:43], s86 offen offset:3072 sc1 nt
v_pk_mul_f32 v[64:65], v[64:65], v[180:181] op_sel_hi:[1,0]
v_pk_mul_f32 v[66:67], v[66:67], v[180:181] op_sel_hi:[1,0]
v_pk_mul_f32 v[72:73], v[72:73], v[180:181] op_sel_hi:[1,0]
v_pk_mul_f32 v[74:75], v[74:75], v[180:181] op_sel_hi:[1,0]
v_cvt_pk_bf16_f32 v128, v64, v65
v_cvt_pk_bf16_f32 v129, v66, v67
v_cvt_pk_bf16_f32 v130, v72, v73
v_cvt_pk_bf16_f32 v131, v74, v75
buffer_store_dwordx4 v[128:131], v247, s[40:43], s87 offen sc1 nt
v_pk_mul_f32 v[68:69], v[68:69], v[182:183] op_sel_hi:[1,0]
v_pk_mul_f32 v[70:71], v[70:71], v[182:183] op_sel_hi:[1,0]
v_pk_mul_f32 v[76:77], v[76:77], v[182:183] op_sel_hi:[1,0]
v_pk_mul_f32 v[78:79], v[78:79], v[182:183] op_sel_hi:[1,0]
v_cvt_pk_bf16_f32 v132, v68, v69
v_cvt_pk_bf16_f32 v133, v70, v71
)"
R"(v_cvt_pk_bf16_f32 v134, v76, v77
v_cvt_pk_bf16_f32 v135, v78, v79
buffer_store_dwordx4 v[132:135], v247, s[40:43], s87 offen offset:1024 sc1 nt
v_pk_mul_f32 v[80:81], v[80:81], v[180:181] op_sel_hi:[1,0]
v_pk_mul_f32 v[82:83], v[82:83], v[180:181] op_sel_hi:[1,0]
v_pk_mul_f32 v[88:89], v[88:89], v[180:181] op_sel_hi:[1,0]
v_pk_mul_f32 v[90:91], v[90:91], v[180:181] op_sel_hi:[1,0]
v_cvt_pk_bf16_f32 v136, v80, v81
v_cvt_pk_bf16_f32 v137, v82, v83
v_cvt_pk_bf16_f32 v138, v88, v89
v_cvt_pk_bf16_f32 v139, v90, v91
buffer_store_dwordx4 v[136:139], v247, s[40:43], s87 offen offset:2048 sc1 nt
v_pk_mul_f32 v[84:85], v[84:85], v[182:183] op_sel_hi:[1,0]
v_pk_mul_f32 v[86:87], v[86:87], v[182:183] op_sel_hi:[1,0]
v_pk_mul_f32 v[92:93], v[92:93], v[182:183] op_sel_hi:[1,0]
v_pk_mul_f32 v[94:95], v[94:95], v[182:183] op_sel_hi:[1,0]
)"
R"(v_cvt_pk_bf16_f32 v140, v84, v85
v_cvt_pk_bf16_f32 v141, v86, v87
v_cvt_pk_bf16_f32 v142, v92, v93
v_cvt_pk_bf16_f32 v143, v94, v95
buffer_store_dwordx4 v[140:143], v247, s[40:43], s87 offen offset:3072 sc1 nt
s_cmp_eq_u32 s73, -1
s_cbranch_scc0 LpeA5_2_1
LplA5_2_1:
s_memrealtime s[88:89]
s_waitcnt lgkmcnt(0)
s_sub_u32 s89, s88, s94
s_cmp_lt_i32 s89, 0
s_cbranch_scc1 LplA5_2_1
s_add_u32 s95, s95, 104
s_sub_u32 s89, s88, s95
s_add_u32 s88, s88, 92
)"
R"(s_cmp_gt_i32 s89, 746
s_cselect_b32 s68, 20, 0
s_add_u32 s88, s88, s68
s_sub_u32 s89, s88, s95
s_cmp_gt_i32 s89, 0
s_cselect_b32 s94, s88, s95
LpeA5_2_1:
s_barrier
s_add_u32 m0, s56, 122880
s_nop 0
buffer_load_dwordx4 v244, s[32:35], s50 offen lds
buffer_load_dwordx4 v245, s[32:35], s50 offen offset:1024 lds
buffer_load_dwordx4 v246, s[32:35], s50 offen offset:2048 lds
s_add_u32 s50, s50, 40960
s_add_u32 m0, s56, 12288
s_nop 0
)"
R"(buffer_load_dwordx4 v248, s[20:23], s48 offen nt lds
buffer_load_dwordx4 v249, s[20:23], s48 offen offset:1024 nt lds
buffer_load_dwordx4 v250, s[20:23], s48 offen offset:2048 nt lds
s_add_u32 s48, s48, 12288
s_add_u32 m0, s56, 49152
s_nop 0
buffer_load_dwordx4 v251, s[24:27], s49 offen nt lds
buffer_load_dwordx4 v252, s[24:27], s49 offen offset:1024 nt lds
buffer_load_dwordx4 v253, s[24:27], s49 offen offset:2048 nt lds
s_add_u32 s49, s49, 12288
s_cmp_eq_u32 s73, -1
s_cbranch_scc0 LpeA5_2_2
LplA5_2_2:
s_memrealtime s[88:89]
s_waitcnt lgkmcnt(0)
s_sub_u32 s89, s88, s94
)"
R"(s_cmp_lt_i32 s89, 0
s_cbranch_scc1 LplA5_2_2
s_add_u32 s95, s95, 104
s_sub_u32 s89, s88, s95
s_add_u32 s88, s88, 92
s_cmp_gt_i32 s89, 746
s_cselect_b32 s68, 20, 0
s_add_u32 s88, s88, s68
s_sub_u32 s89, s88, s95
s_cmp_gt_i32 s89, 0
s_cselect_b32 s94, s88, s95
LpeA5_2_2:
s_barrier
s_add_u32 m0, s56, 135168
s_nop 0
buffer_load_dwordx4 v244, s[32:35], s50 offen lds
)"
R"(buffer_load_dwordx4 v245, s[32:35], s50 offen offset:1024 lds
buffer_load_dwordx4 v246, s[32:35], s50 offen offset:2048 lds
s_add_u32 s50, s50, 40960
s_add_u32 m0, s56, 24576
s_nop 0
buffer_load_dwordx4 v248, s[20:23], s48 offen nt lds
buffer_load_dwordx4 v249, s[20:23], s48 offen offset:1024 nt lds
buffer_load_dwordx4 v250, s[20:23], s48 offen offset:2048 nt lds
s_add_u32 s48, s48, 12288
s_add_u32 m0, s56, 61440
s_nop 0
buffer_load_dwordx4 v251, s[24:27], s49 offen nt lds
buffer_load_dwordx4 v252, s[24:27], s49 offen offset:1024 nt lds
buffer_load_dwordx4 v253, s[24:27], s49 offen offset:2048 nt lds
s_add_u32 s49, s49, 12288
s_cmp_eq_u32 s73, -1
)"
R"(s_cbranch_scc0 LpeA5_2_3
LplA5_2_3:
s_memrealtime s[88:89]
s_waitcnt lgkmcnt(0)
s_sub_u32 s89, s88, s94
s_cmp_lt_i32 s89, 0
s_cbranch_scc1 LplA5_2_3
s_add_u32 s95, s95, 104
s_sub_u32 s89, s88, s95
s_add_u32 s88, s88, 92
s_cmp_gt_i32 s89, 746
s_cselect_b32 s68, 20, 0
s_add_u32 s88, s88, s68
s_sub_u32 s89, s88, s95
s_cmp_gt_i32 s89, 0
s_cselect_b32 s94, s88, s95
)"
R"(LpeA5_2_3:
s_waitcnt vmcnt(18)
s_barrier
ds_read_b64_tr_b16 v[184:185], v234 offset:0
ds_read_b64_tr_b16 v[186:187], v234 offset:6144
ds_read_b64_tr_b16 v[188:189], v235 offset:0
ds_read_b64_tr_b16 v[190:191], v235 offset:6144
ds_read_b64_tr_b16 v[192:193], v236 offset:0
ds_read_b64_tr_b16 v[194:195], v236 offset:6144
ds_read_b64_tr_b16 v[196:197], v237 offset:0
ds_read_b64_tr_b16 v[198:199], v237 offset:6144
ds_read_b64_tr_b16 v[200:201], v234 offset:128
ds_read_b64_tr_b16 v[202:203], v234 offset:6272
s_waitcnt lgkmcnt(8)
v_mfma_f32_16x16x32_bf16 v[0:3], v[184:187], v[96:99], 0
ds_read_b64_tr_b16 v[204:205], v235 offset:128
)"
R"(ds_read_b64_tr_b16 v[206:207], v235 offset:6272
v_mfma_f32_16x16x32_bf16 v[40:43], v[184:187], v[100:103], 0
s_waitcnt lgkmcnt(8)
v_mfma_f32_16x16x32_bf16 v[44:47], v[188:191], v[100:103], 0
ds_read_b64_tr_b16 v[208:209], v236 offset:128
ds_read_b64_tr_b16 v[210:211], v236 offset:6272
v_mfma_f32_16x16x32_bf16 v[4:7], v[188:191], v[96:99], 0
s_waitcnt lgkmcnt(8)
v_mfma_f32_16x16x32_bf16 v[8:11], v[192:195], v[96:99], 0
ds_read_b64_tr_b16 v[212:213], v237 offset:128
ds_read_b64_tr_b16 v[214:215], v237 offset:6272
v_mfma_f32_16x16x32_bf16 v[48:51], v[192:195], v[100:103], 0
s_waitcnt lgkmcnt(8)
v_mfma_f32_16x16x32_bf16 v[52:55], v[196:199], v[100:103], 0
v_mfma_f32_16x16x32_bf16 v[12:15], v[196:199], v[96:99], 0
s_waitcnt lgkmcnt(6)
)"
R"(v_mfma_f32_16x16x32_bf16 v[16:19], v[200:203], v[96:99], 0
v_mfma_f32_16x16x32_bf16 v[56:59], v[200:203], v[100:103], 0
s_waitcnt lgkmcnt(4)
v_mfma_f32_16x16x32_bf16 v[60:63], v[204:207], v[100:103], 0
v_mfma_f32_16x16x32_bf16 v[20:23], v[204:207], v[96:99], 0
s_waitcnt lgkmcnt(2)
v_mfma_f32_16x16x32_bf16 v[24:27], v[208:211], v[96:99], 0
v_mfma_f32_16x16x32_bf16 v[64:67], v[208:211], v[100:103], 0
s_waitcnt lgkmcnt(0)
v_mfma_f32_16x16x32_bf16 v[68:71], v[212:215], v[100:103], 0
v_mfma_f32_16x16x32_bf16 v[28:31], v[212:215], v[96:99], 0
s_add_u32 m0, s56, 36864
s_nop 0
buffer_load_dwordx4 v248, s[20:23], s48 offen nt lds
buffer_load_dwordx4 v249, s[20:23], s48 offen offset:1024 nt lds
buffer_load_dwordx4 v250, s[20:23], s48 offen offset:2048 nt lds
)"
R"(s_add_u32 s48, s48, 12288
ds_read_b64_tr_b16 v[184:185], v234 offset:256
ds_read_b64_tr_b16 v[186:187], v234 offset:6400
ds_read_b64_tr_b16 v[188:189], v235 offset:256
ds_read_b64_tr_b16 v[190:191], v235 offset:6400
ds_read_b64_tr_b16 v[192:193], v234 offset:12288
ds_read_b64_tr_b16 v[194:195], v234 offset:18432
ds_read_b64_tr_b16 v[196:197], v235 offset:12288
ds_read_b64_tr_b16 v[198:199], v235 offset:18432
ds_read_b64_tr_b16 v[200:201], v236 offset:12288
ds_read_b64_tr_b16 v[202:203], v236 offset:18432
s_add_u32 m0, s56, 73728
s_nop 0
buffer_load_dwordx4 v251, s[24:27], s49 offen nt lds
buffer_load_dwordx4 v252, s[24:27], s49 offen offset:1024 nt lds
buffer_load_dwordx4 v253, s[24:27], s49 offen offset:2048 nt lds
)"
R"(s_add_u32 s49, s49, 12288
ds_read_b64_tr_b16 v[204:205], v237 offset:12288
ds_read_b64_tr_b16 v[206:207], v237 offset:18432
ds_read_b64_tr_b16 v[208:209], v234 offset:12416
ds_read_b64_tr_b16 v[210:211], v234 offset:18560
ds_read_b64_tr_b16 v[212:213], v235 offset:12416
s_waitcnt lgkmcnt(11)
ds_read_b64_tr_b16 v[214:215], v235 offset:18560
s_waitcnt lgkmcnt(0)
s_cmp_eq_u32 s73, -1
s_cbranch_scc0 LpeA6_2_0
LplA6_2_0:
s_memrealtime s[88:89]
s_waitcnt lgkmcnt(0)
s_sub_u32 s89, s88, s94
s_cmp_lt_i32 s89, 0
)"
R"(s_cbranch_scc1 LplA6_2_0
s_add_u32 s95, s95, 104
s_sub_u32 s89, s88, s95
s_add_u32 s88, s88, 92
s_cmp_gt_i32 s89, 746
s_cselect_b32 s68, 20, 0
s_add_u32 s88, s88, s68
s_sub_u32 s89, s88, s95
s_cmp_gt_i32 s89, 0
s_cselect_b32 s94, s88, s95
LpeA6_2_0:
s_waitcnt vmcnt(15)
s_barrier
s_add_u32 m0, s56, 98304
s_nop 0
buffer_load_dwordx4 v244, s[32:35], s50 offen lds
)"
R"(buffer_load_dwordx4 v245, s[32:35], s50 offen offset:1024 lds
buffer_load_dwordx4 v246, s[32:35], s50 offen offset:2048 lds
s_add_u32 s50, s50, 40960
v_mfma_f32_16x16x32_bf16 v[32:35], v[184:187], v[96:99], 0
v_mfma_f32_16x16x32_bf16 v[72:75], v[184:187], v[100:103], 0
v_mfma_f32_16x16x32_bf16 v[76:79], v[188:191], v[100:103], 0
ds_read_b64_tr_b16 v[184:185], v236 offset:12416
ds_read_b64_tr_b16 v[186:187], v236 offset:18560
v_mfma_f32_16x16x32_bf16 v[36:39], v[188:191], v[96:99], 0
v_mfma_f32_16x16x32_bf16 v[0:3], v[192:195], v[104:107], v[0:3]
ds_read_b64_tr_b16 v[188:189], v237 offset:12416
ds_read_b64_tr_b16 v[190:191], v237 offset:18560
v_mfma_f32_16x16x32_bf16 v[40:43], v[192:195], v[108:111], v[40:43]
v_mfma_f32_16x16x32_bf16 v[44:47], v[196:199], v[108:111], v[44:47]
ds_read_b64_tr_b16 v[192:193], v234 offset:12544
ds_read_b64_tr_b16 v[194:195], v234 offset:18688
)"
R"(v_mfma_f32_16x16x32_bf16 v[4:7], v[196:199], v[104:107], v[4:7]
v_mfma_f32_16x16x32_bf16 v[8:11], v[200:203], v[104:107], v[8:11]
ds_read_b64_tr_b16 v[196:197], v235 offset:12544
ds_read_b64_tr_b16 v[198:199], v235 offset:18688
v_mfma_f32_16x16x32_bf16 v[48:51], v[200:203], v[108:111], v[48:51]
v_mfma_f32_16x16x32_bf16 v[52:55], v[204:207], v[108:111], v[52:55]
ds_read_b64_tr_b16 v[200:201], v234 offset:24576
ds_read_b64_tr_b16 v[202:203], v234 offset:30720
v_mfma_f32_16x16x32_bf16 v[12:15], v[204:207], v[104:107], v[12:15]
v_mfma_f32_16x16x32_bf16 v[16:19], v[208:211], v[104:107], v[16:19]
ds_read_b64_tr_b16 v[204:205], v235 offset:24576
ds_read_b64_tr_b16 v[206:207], v235 offset:30720
v_mfma_f32_16x16x32_bf16 v[56:59], v[208:211], v[108:111], v[56:59]
v_mfma_f32_16x16x32_bf16 v[60:63], v[212:215], v[108:111], v[60:63]
ds_read_b64_tr_b16 v[208:209], v236 offset:24576
ds_read_b64_tr_b16 v[210:211], v236 offset:30720
)"
R"(v_mfma_f32_16x16x32_bf16 v[20:23], v[212:215], v[104:107], v[20:23]
s_add_u32 m0, s56, 0
s_nop 0
buffer_load_dwordx4 v248, s[20:23], s48 offen nt lds
buffer_load_dwordx4 v249, s[20:23], s48 offen offset:1024 nt lds
buffer_load_dwordx4 v250, s[20:23], s48 offen offset:2048 nt lds
s_add_u32 s48, s48, 12288
ds_read_b64_tr_b16 v[212:213], v237 offset:24576
s_waitcnt lgkmcnt(11)
ds_read_b64_tr_b16 v[214:215], v237 offset:30720
s_add_u32 m0, s56, 86016
s_nop 0
buffer_load_dwordx4 v251, s[24:27], s49 offen nt lds
buffer_load_dwordx4 v252, s[24:27], s49 offen offset:1024 nt lds
buffer_load_dwordx4 v253, s[24:27], s49 offen offset:2048 nt lds
s_add_u32 s49, s49, 12288
)"
R"(s_waitcnt lgkmcnt(0)
s_cmp_eq_u32 s73, -1
s_cbranch_scc0 LpeA6_2_1
LplA6_2_1:
s_memrealtime s[88:89]
s_waitcnt lgkmcnt(0)
s_sub_u32 s89, s88, s94
s_cmp_lt_i32 s89, 0
s_cbranch_scc1 LplA6_2_1
s_add_u32 s95, s95, 104
s_sub_u32 s89, s88, s95
s_add_u32 s88, s88, 92
s_cmp_gt_i32 s89, 746
s_cselect_b32 s68, 20, 0
s_add_u32 s88, s88, s68
s_sub_u32 s89, s88, s95
)"
R"(s_cmp_gt_i32 s89, 0
s_cselect_b32 s94, s88, s95
LpeA6_2_1:
s_waitcnt vmcnt(15)
s_barrier
s_add_u32 m0, s56, 110592
s_nop 0
buffer_load_dwordx4 v244, s[32:35], s50 offen lds
buffer_load_dwordx4 v245, s[32:35], s50 offen offset:1024 lds
buffer_load_dwordx4 v246, s[32:35], s50 offen offset:2048 lds
s_add_u32 s50, s50, 40960
v_mfma_f32_16x16x32_bf16 v[24:27], v[184:187], v[104:107], v[24:27]
v_mfma_f32_16x16x32_bf16 v[64:67], v[184:187], v[108:111], v[64:67]
v_mfma_f32_16x16x32_bf16 v[68:71], v[188:191], v[108:111], v[68:71]
ds_read_b64_tr_b16 v[184:185], v234 offset:24704
ds_read_b64_tr_b16 v[186:187], v234 offset:30848
)"
R"(v_mfma_f32_16x16x32_bf16 v[28:31], v[188:191], v[104:107], v[28:31]
v_mfma_f32_16x16x32_bf16 v[32:35], v[192:195], v[104:107], v[32:35]
ds_read_b64_tr_b16 v[188:189], v235 offset:24704
ds_read_b64_tr_b16 v[190:191], v235 offset:30848
v_mfma_f32_16x16x32_bf16 v[72:75], v[192:195], v[108:111], v[72:75]
v_mfma_f32_16x16x32_bf16 v[76:79], v[196:199], v[108:111], v[76:79]
ds_read_b64_tr_b16 v[192:193], v236 offset:24704
ds_read_b64_tr_b16 v[194:195], v236 offset:30848
v_mfma_f32_16x16x32_bf16 v[36:39], v[196:199], v[104:107], v[36:39]
v_mfma_f32_16x16x32_bf16 v[0:3], v[200:203], v[112:115], v[0:3]
ds_read_b64_tr_b16 v[196:197], v237 offset:24704
ds_read_b64_tr_b16 v[198:199], v237 offset:30848
v_mfma_f32_16x16x32_bf16 v[40:43], v[200:203], v[116:119], v[40:43]
v_mfma_f32_16x16x32_bf16 v[44:47], v[204:207], v[116:119], v[44:47]
ds_read_b64_tr_b16 v[200:201], v234 offset:24832
ds_read_b64_tr_b16 v[202:203], v234 offset:30976
)"
R"(v_mfma_f32_16x16x32_bf16 v[4:7], v[204:207], v[112:115], v[4:7]
v_mfma_f32_16x16x32_bf16 v[8:11], v[208:211], v[112:115], v[8:11]
ds_read_b64_tr_b16 v[204:205], v235 offset:24832
ds_read_b64_tr_b16 v[206:207], v235 offset:30976
v_mfma_f32_16x16x32_bf16 v[48:51], v[208:211], v[116:119], v[48:51]
v_mfma_f32_16x16x32_bf16 v[52:55], v[212:215], v[116:119], v[52:55]
ds_read_b64_tr_b16 v[208:209], v234 offset:36864
ds_read_b64_tr_b16 v[210:211], v234 offset:43008
v_mfma_f32_16x16x32_bf16 v[12:15], v[212:215], v[112:115], v[12:15]
s_add_u32 m0, s56, 12288
s_nop 0
buffer_load_dwordx4 v248, s[20:23], s48 offen nt lds
buffer_load_dwordx4 v249, s[20:23], s48 offen offset:1024 nt lds
buffer_load_dwordx4 v250, s[20:23], s48 offen offset:2048 nt lds
s_add_u32 s48, s48, 12288
ds_read_b64_tr_b16 v[212:213], v235 offset:36864
)"
R"(s_waitcnt lgkmcnt(11)
ds_read_b64_tr_b16 v[214:215], v235 offset:43008
ds_read_b64_tr_b16 v[216:217], v236 offset:36864
ds_read_b64_tr_b16 v[218:219], v236 offset:43008
s_add_u32 m0, s56, 49152
s_nop 0
buffer_load_dwordx4 v251, s[24:27], s49 offen nt lds
buffer_load_dwordx4 v252, s[24:27], s49 offen offset:1024 nt lds
buffer_load_dwordx4 v253, s[24:27], s49 offen offset:2048 nt lds
s_add_u32 s49, s49, 12288
s_waitcnt lgkmcnt(0)
s_cmp_eq_u32 s73, -1
s_cbranch_scc0 LpeA6_2_2
LplA6_2_2:
s_memrealtime s[88:89]
s_waitcnt lgkmcnt(0)
)"
R"(s_sub_u32 s89, s88, s94
s_cmp_lt_i32 s89, 0
s_cbranch_scc1 LplA6_2_2
s_add_u32 s95, s95, 104
s_sub_u32 s89, s88, s95
s_add_u32 s88, s88, 92
s_cmp_gt_i32 s89, 746
s_cselect_b32 s68, 20, 0
s_add_u32 s88, s88, s68
s_sub_u32 s89, s88, s95
s_cmp_gt_i32 s89, 0
s_cselect_b32 s94, s88, s95
LpeA6_2_2:
s_waitcnt vmcnt(15)
s_barrier
s_mul_i32 s50, s91, 245760
)"
R"(s_add_u32 s50, s50, s52
s_add_u32 m0, s56, 122880
s_nop 0
buffer_load_dwordx4 v244, s[32:35], s50 offen lds
buffer_load_dwordx4 v245, s[32:35], s50 offen offset:1024 lds
buffer_load_dwordx4 v246, s[32:35], s50 offen offset:2048 lds
s_add_u32 s50, s50, 40960
v_mfma_f32_16x16x32_bf16 v[16:19], v[184:187], v[112:115], v[16:19]
v_mfma_f32_16x16x32_bf16 v[56:59], v[184:187], v[116:119], v[56:59]
v_mfma_f32_16x16x32_bf16 v[60:63], v[188:191], v[116:119], v[60:63]
ds_read_b64_tr_b16 v[184:185], v237 offset:36864
ds_read_b64_tr_b16 v[186:187], v237 offset:43008
v_mfma_f32_16x16x32_bf16 v[20:23], v[188:191], v[112:115], v[20:23]
v_mfma_f32_16x16x32_bf16 v[24:27], v[192:195], v[112:115], v[24:27]
ds_read_b64_tr_b16 v[188:189], v234 offset:36992
ds_read_b64_tr_b16 v[190:191], v234 offset:43136
)"
R"(v_mfma_f32_16x16x32_bf16 v[64:67], v[192:195], v[116:119], v[64:67]
v_mfma_f32_16x16x32_bf16 v[68:71], v[196:199], v[116:119], v[68:71]
ds_read_b64_tr_b16 v[192:193], v235 offset:36992
ds_read_b64_tr_b16 v[194:195], v235 offset:43136
v_mfma_f32_16x16x32_bf16 v[28:31], v[196:199], v[112:115], v[28:31]
v_mfma_f32_16x16x32_bf16 v[32:35], v[200:203], v[112:115], v[32:35]
ds_read_b64_tr_b16 v[196:197], v236 offset:36992
ds_read_b64_tr_b16 v[198:199], v236 offset:43136
v_mfma_f32_16x16x32_bf16 v[72:75], v[200:203], v[116:119], v[72:75]
v_mfma_f32_16x16x32_bf16 v[76:79], v[204:207], v[116:119], v[76:79]
ds_read_b64_tr_b16 v[200:201], v237 offset:36992
ds_read_b64_tr_b16 v[202:203], v237 offset:43136
v_mfma_f32_16x16x32_bf16 v[36:39], v[204:207], v[112:115], v[36:39]
v_mfma_f32_16x16x32_bf16 v[0:3], v[208:211], v[120:123], v[0:3]
ds_read_b64_tr_b16 v[204:205], v234 offset:37120
ds_read_b64_tr_b16 v[206:207], v234 offset:43264
)"
R"(v_mfma_f32_16x16x32_bf16 v[40:43], v[208:211], v[124:127], v[40:43]
v_mfma_f32_16x16x32_bf16 v[44:47], v[212:215], v[124:127], v[44:47]
ds_read_b64_tr_b16 v[208:209], v235 offset:37120
ds_read_b64_tr_b16 v[210:211], v235 offset:43264
v_mfma_f32_16x16x32_bf16 v[4:7], v[212:215], v[120:123], v[4:7]
v_mfma_f32_16x16x32_bf16 v[8:11], v[216:219], v[120:123], v[8:11]
ds_read_b64_tr_b16 v[212:213], v234 offset:0
s_waitcnt lgkmcnt(11)
ds_read_b64_tr_b16 v[214:215], v234 offset:6144
v_mfma_f32_16x16x32_bf16 v[48:51], v[216:219], v[124:127], v[48:51]
s_add_u32 m0, s56, 24576
s_nop 0
buffer_load_dwordx4 v248, s[20:23], s48 offen nt lds
buffer_load_dwordx4 v249, s[20:23], s48 offen offset:1024 nt lds
buffer_load_dwordx4 v250, s[20:23], s48 offen offset:2048 nt lds
s_add_u32 s48, s48, 12288
)"
R"(s_add_u32 m0, s56, 61440
s_nop 0
buffer_load_dwordx4 v251, s[24:27], s49 offen nt lds
buffer_load_dwordx4 v252, s[24:27], s49 offen offset:1024 nt lds
buffer_load_dwordx4 v253, s[24:27], s49 offen offset:2048 nt lds
s_add_u32 s49, s49, 12288
s_waitcnt lgkmcnt(0)
s_cmp_eq_u32 s73, -1
s_cbranch_scc0 LpeA6_2_3
LplA6_2_3:
s_memrealtime s[88:89]
s_waitcnt lgkmcnt(0)
s_sub_u32 s89, s88, s94
s_cmp_lt_i32 s89, 0
s_cbranch_scc1 LplA6_2_3
s_add_u32 s95, s95, 104
)"
R"(s_sub_u32 s89, s88, s95
s_add_u32 s88, s88, 92
s_cmp_gt_i32 s89, 746
s_cselect_b32 s68, 20, 0
s_add_u32 s88, s88, s68
s_sub_u32 s89, s88, s95
s_cmp_gt_i32 s89, 0
s_cselect_b32 s94, s88, s95
LpeA6_2_3:
s_waitcnt vmcnt(18)
s_barrier
s_add_u32 m0, s56, 135168
s_nop 0
buffer_load_dwordx4 v244, s[32:35], s50 offen lds
buffer_load_dwordx4 v245, s[32:35], s50 offen offset:1024 lds
buffer_load_dwordx4 v246, s[32:35], s50 offen offset:2048 lds
)"
R"(s_add_u32 s50, s50, 40960
v_mfma_f32_16x16x32_bf16 v[52:55], v[184:187], v[124:127], v[52:55]
v_mfma_f32_16x16x32_bf16 v[12:15], v[184:187], v[120:123], v[12:15]
v_mfma_f32_16x16x32_bf16 v[16:19], v[188:191], v[120:123], v[16:19]
ds_read_b64_tr_b16 v[184:185], v235 offset:0
ds_read_b64_tr_b16 v[186:187], v235 offset:6144
v_mfma_f32_16x16x32_bf16 v[56:59], v[188:191], v[124:127], v[56:59]
v_mfma_f32_16x16x32_bf16 v[60:63], v[192:195], v[124:127], v[60:63]
ds_read_b64_tr_b16 v[188:189], v236 offset:0
ds_read_b64_tr_b16 v[190:191], v236 offset:6144
v_mfma_f32_16x16x32_bf16 v[20:23], v[192:195], v[120:123], v[20:23]
v_mfma_f32_16x16x32_bf16 v[24:27], v[196:199], v[120:123], v[24:27]
ds_read_b64_tr_b16 v[192:193], v237 offset:0
ds_read_b64_tr_b16 v[194:195], v237 offset:6144
v_mfma_f32_16x16x32_bf16 v[64:67], v[196:199], v[124:127], v[64:67]
v_mfma_f32_16x16x32_bf16 v[68:71], v[200:203], v[124:127], v[68:71]
)"
R"(ds_read_b64_tr_b16 v[196:197], v234 offset:128
ds_read_b64_tr_b16 v[198:199], v234 offset:6272
v_mfma_f32_16x16x32_bf16 v[28:31], v[200:203], v[120:123], v[28:31]
v_mfma_f32_16x16x32_bf16 v[32:35], v[204:207], v[120:123], v[32:35]
ds_read_b64_tr_b16 v[200:201], v235 offset:128
ds_read_b64_tr_b16 v[202:203], v235 offset:6272
v_mfma_f32_16x16x32_bf16 v[72:75], v[204:207], v[124:127], v[72:75]
v_mfma_f32_16x16x32_bf16 v[76:79], v[208:211], v[124:127], v[76:79]
ds_read_b64_tr_b16 v[204:205], v236 offset:128
ds_read_b64_tr_b16 v[206:207], v236 offset:6272
v_mfma_f32_16x16x32_bf16 v[36:39], v[208:211], v[120:123], v[36:39]
v_mfma_f32_16x16x32_bf16 v[0:3], v[212:215], v[128:131], v[0:3]
ds_read_b64_tr_b16 v[208:209], v237 offset:128
ds_read_b64_tr_b16 v[210:211], v237 offset:6272
v_mfma_f32_16x16x32_bf16 v[40:43], v[212:215], v[132:135], v[40:43]
s_add_u32 m0, s56, 36864
)"
R"(s_nop 0
buffer_load_dwordx4 v248, s[20:23], s48 offen nt lds
buffer_load_dwordx4 v249, s[20:23], s48 offen offset:1024 nt lds
buffer_load_dwordx4 v250, s[20:23], s48 offen offset:2048 nt lds
s_add_u32 s48, s48, 12288
ds_read_b64_tr_b16 v[212:213], v234 offset:256
s_waitcnt lgkmcnt(11)
ds_read_b64_tr_b16 v[214:215], v234 offset:6400
s_add_u32 m0, s56, 73728
s_nop 0
buffer_load_dwordx4 v251, s[24:27], s49 offen nt lds
buffer_load_dwordx4 v252, s[24:27], s49 offen offset:1024 nt lds
buffer_load_dwordx4 v253, s[24:27], s49 offen offset:2048 nt lds
s_add_u32 s49, s49, 12288
s_waitcnt lgkmcnt(0)
s_mul_i32 s51, s91, 12288
)"
R"(s_add_u32 s51, s51, s54
buffer_load_dwordx4 v[160:163], v247, s[40:43], s51 offen sc1
buffer_load_dwordx4 v[164:167], v247, s[40:43], s51 offen offset:1024 sc1
s_add_u32 s51, s51, 2048
s_cmp_eq_u32 s73, -1
s_cbranch_scc0 LpeA7_2_0
LplA7_2_0:
s_memrealtime s[88:89]
s_waitcnt lgkmcnt(0)
s_sub_u32 s89, s88, s94
s_cmp_lt_i32 s89, 0
s_cbranch_scc1 LplA7_2_0
s_add_u32 s95, s95, 104
s_sub_u32 s89, s88, s95
s_add_u32 s88, s88, 92
s_cmp_gt_i32 s89, 746
)"
R"(s_cselect_b32 s68, 20, 0
s_add_u32 s88, s88, s68
s_sub_u32 s89, s88, s95
s_cmp_gt_i32 s89, 0
s_cselect_b32 s94, s88, s95
LpeA7_2_0:
s_waitcnt vmcnt(20)
s_barrier
s_add_u32 m0, s56, 0
s_nop 0
buffer_load_dwordx4 v248, s[20:23], s48 offen nt lds
buffer_load_dwordx4 v249, s[20:23], s48 offen offset:1024 nt lds
buffer_load_dwordx4 v250, s[20:23], s48 offen offset:2048 nt lds
s_add_u32 s48, s48, 12288
v_mfma_f32_16x16x32_bf16 v[44:47], v[184:187], v[132:135], v[44:47]
v_mfma_f32_16x16x32_bf16 v[4:7], v[184:187], v[128:131], v[4:7]
)"
R"(v_mfma_f32_16x16x32_bf16 v[8:11], v[188:191], v[128:131], v[8:11]
ds_read_b64_tr_b16 v[184:185], v235 offset:256
ds_read_b64_tr_b16 v[186:187], v235 offset:6400
v_mfma_f32_16x16x32_bf16 v[48:51], v[188:191], v[132:135], v[48:51]
v_mfma_f32_16x16x32_bf16 v[52:55], v[192:195], v[132:135], v[52:55]
ds_read_b64_tr_b16 v[188:189], v234 offset:12288
ds_read_b64_tr_b16 v[190:191], v234 offset:18432
v_mfma_f32_16x16x32_bf16 v[12:15], v[192:195], v[128:131], v[12:15]
v_mfma_f32_16x16x32_bf16 v[16:19], v[196:199], v[128:131], v[16:19]
ds_read_b64_tr_b16 v[192:193], v235 offset:12288
ds_read_b64_tr_b16 v[194:195], v235 offset:18432
v_mfma_f32_16x16x32_bf16 v[56:59], v[196:199], v[132:135], v[56:59]
v_mfma_f32_16x16x32_bf16 v[60:63], v[200:203], v[132:135], v[60:63]
ds_read_b64_tr_b16 v[196:197], v236 offset:12288
ds_read_b64_tr_b16 v[198:199], v236 offset:18432
v_mfma_f32_16x16x32_bf16 v[20:23], v[200:203], v[128:131], v[20:23]
)"
R"(v_mfma_f32_16x16x32_bf16 v[24:27], v[204:207], v[128:131], v[24:27]
ds_read_b64_tr_b16 v[200:201], v237 offset:12288
ds_read_b64_tr_b16 v[202:203], v237 offset:18432
v_mfma_f32_16x16x32_bf16 v[64:67], v[204:207], v[132:135], v[64:67]
v_mfma_f32_16x16x32_bf16 v[68:71], v[208:211], v[132:135], v[68:71]
ds_read_b64_tr_b16 v[204:205], v234 offset:12416
ds_read_b64_tr_b16 v[206:207], v234 offset:18560
v_mfma_f32_16x16x32_bf16 v[28:31], v[208:211], v[128:131], v[28:31]
v_mfma_f32_16x16x32_bf16 v[32:35], v[212:215], v[128:131], v[32:35]
ds_read_b64_tr_b16 v[208:209], v235 offset:12416
ds_read_b64_tr_b16 v[210:211], v235 offset:18560
v_mfma_f32_16x16x32_bf16 v[72:75], v[212:215], v[132:135], v[72:75]
s_add_u32 m0, s56, 86016
s_nop 0
buffer_load_dwordx4 v251, s[24:27], s49 offen nt lds
ds_read_b64_tr_b16 v[212:213], v236 offset:12416
)"
R"(s_waitcnt lgkmcnt(11)
ds_read_b64_tr_b16 v[214:215], v236 offset:18560
buffer_load_dwordx4 v252, s[24:27], s49 offen offset:1024 nt lds
buffer_load_dwordx4 v253, s[24:27], s49 offen offset:2048 nt lds
s_add_u32 s49, s49, 12288
s_waitcnt lgkmcnt(0)
buffer_load_dwordx4 v[168:171], v247, s[40:43], s51 offen sc1
buffer_load_dwordx4 v[172:175], v247, s[40:43], s51 offen offset:1024 sc1
s_add_u32 s51, s51, 2048
s_cmp_eq_u32 s73, -1
s_cbranch_scc0 LpeA7_2_1
LplA7_2_1:
s_memrealtime s[88:89]
s_waitcnt lgkmcnt(0)
s_sub_u32 s89, s88, s94
s_cmp_lt_i32 s89, 0
)"
R"(s_cbranch_scc1 LplA7_2_1
s_add_u32 s95, s95, 104
s_sub_u32 s89, s88, s95
s_add_u32 s88, s88, 92
s_cmp_gt_i32 s89, 746
s_cselect_b32 s68, 20, 0
s_add_u32 s88, s88, s68
s_sub_u32 s89, s88, s95
s_cmp_gt_i32 s89, 0
s_cselect_b32 s94, s88, s95
LpeA7_2_1:
s_waitcnt vmcnt(19)
s_barrier
s_add_u32 m0, s56, 98304
s_nop 0
buffer_load_dwordx4 v244, s[32:35], s50 offen lds
)"
R"(buffer_load_dwordx4 v245, s[32:35], s50 offen offset:1024 lds
buffer_load_dwordx4 v246, s[32:35], s50 offen offset:2048 lds
s_add_u32 s50, s50, 40960
v_mfma_f32_16x16x32_bf16 v[76:79], v[184:187], v[132:135], v[76:79]
v_mfma_f32_16x16x32_bf16 v[36:39], v[184:187], v[128:131], v[36:39]
v_mfma_f32_16x16x32_bf16 v[0:3], v[188:191], v[136:139], v[0:3]
ds_read_b64_tr_b16 v[184:185], v237 offset:12416
ds_read_b64_tr_b16 v[186:187], v237 offset:18560
v_mfma_f32_16x16x32_bf16 v[40:43], v[188:191], v[140:143], v[40:43]
v_mfma_f32_16x16x32_bf16 v[44:47], v[192:195], v[140:143], v[44:47]
ds_read_b64_tr_b16 v[188:189], v234 offset:12544
ds_read_b64_tr_b16 v[190:191], v234 offset:18688
v_mfma_f32_16x16x32_bf16 v[4:7], v[192:195], v[136:139], v[4:7]
v_mfma_f32_16x16x32_bf16 v[8:11], v[196:199], v[136:139], v[8:11]
ds_read_b64_tr_b16 v[192:193], v235 offset:12544
ds_read_b64_tr_b16 v[194:195], v235 offset:18688
)"
R"(v_mfma_f32_16x16x32_bf16 v[48:51], v[196:199], v[140:143], v[48:51]
v_mfma_f32_16x16x32_bf16 v[52:55], v[200:203], v[140:143], v[52:55]
ds_read_b64_tr_b16 v[196:197], v234 offset:24576
ds_read_b64_tr_b16 v[198:199], v234 offset:30720
v_mfma_f32_16x16x32_bf16 v[12:15], v[200:203], v[136:139], v[12:15]
v_mfma_f32_16x16x32_bf16 v[16:19], v[204:207], v[136:139], v[16:19]
ds_read_b64_tr_b16 v[200:201], v235 offset:24576
ds_read_b64_tr_b16 v[202:203], v235 offset:30720
v_mfma_f32_16x16x32_bf16 v[56:59], v[204:207], v[140:143], v[56:59]
v_mfma_f32_16x16x32_bf16 v[60:63], v[208:211], v[140:143], v[60:63]
ds_read_b64_tr_b16 v[204:205], v236 offset:24576
ds_read_b64_tr_b16 v[206:207], v236 offset:30720
v_mfma_f32_16x16x32_bf16 v[20:23], v[208:211], v[136:139], v[20:23]
v_mfma_f32_16x16x32_bf16 v[24:27], v[212:215], v[136:139], v[24:27]
ds_read_b64_tr_b16 v[208:209], v237 offset:24576
ds_read_b64_tr_b16 v[210:211], v237 offset:30720
)"
R"(v_mfma_f32_16x16x32_bf16 v[64:67], v[212:215], v[140:143], v[64:67]
s_add_u32 m0, s56, 12288
s_nop 0
buffer_load_dwordx4 v248, s[20:23], s48 offen nt lds
buffer_load_dwordx4 v249, s[20:23], s48 offen offset:1024 nt lds
buffer_load_dwordx4 v250, s[20:23], s48 offen offset:2048 nt lds
s_add_u32 s48, s48, 12288
ds_read_b64_tr_b16 v[212:213], v234 offset:24704
s_waitcnt lgkmcnt(11)
ds_read_b64_tr_b16 v[214:215], v234 offset:30848
ds_read_b64_tr_b16 v[216:217], v235 offset:24704
ds_read_b64_tr_b16 v[218:219], v235 offset:30848
s_add_u32 m0, s56, 49152
s_nop 0
buffer_load_dwordx4 v251, s[24:27], s49 offen nt lds
buffer_load_dwordx4 v252, s[24:27], s49 offen offset:1024 nt lds
)"
R"(buffer_load_dwordx4 v253, s[24:27], s49 offen offset:2048 nt lds
s_add_u32 s49, s49, 12288
s_waitcnt lgkmcnt(0)
buffer_load_dwordx4 v[144:147], v247, s[40:43], s51 offen sc1
buffer_load_dwordx4 v[148:151], v247, s[40:43], s51 offen offset:1024 sc1
s_add_u32 s51, s51, 2048
s_cmp_eq_u32 s73, -1
s_cbranch_scc0 LpeA7_2_2
LplA7_2_2:
s_memrealtime s[88:89]
s_waitcnt lgkmcnt(0)
s_sub_u32 s89, s88, s94
s_cmp_lt_i32 s89, 0
s_cbranch_scc1 LplA7_2_2
s_add_u32 s95, s95, 104
s_sub_u32 s89, s88, s95
)"
R"(s_add_u32 s88, s88, 92
s_cmp_gt_i32 s89, 746
s_cselect_b32 s68, 20, 0
s_add_u32 s88, s88, s68
s_sub_u32 s89, s88, s95
s_cmp_gt_i32 s89, 0
s_cselect_b32 s94, s88, s95
LpeA7_2_2:
s_waitcnt vmcnt(21)
s_barrier
s_add_u32 m0, s56, 110592
s_nop 0
buffer_load_dwordx4 v244, s[32:35], s50 offen lds
buffer_load_dwordx4 v245, s[32:35], s50 offen offset:1024 lds
buffer_load_dwordx4 v246, s[32:35], s50 offen offset:2048 lds
s_add_u32 s50, s50, 40960
)"
R"(v_mfma_f32_16x16x32_bf16 v[68:71], v[184:187], v[140:143], v[68:71]
v_mfma_f32_16x16x32_bf16 v[28:31], v[184:187], v[136:139], v[28:31]
v_mfma_f32_16x16x32_bf16 v[32:35], v[188:191], v[136:139], v[32:35]
ds_read_b64_tr_b16 v[184:185], v236 offset:24704
ds_read_b64_tr_b16 v[186:187], v236 offset:30848
v_mfma_f32_16x16x32_bf16 v[72:75], v[188:191], v[140:143], v[72:75]
v_mfma_f32_16x16x32_bf16 v[76:79], v[192:195], v[140:143], v[76:79]
ds_read_b64_tr_b16 v[188:189], v237 offset:24704
ds_read_b64_tr_b16 v[190:191], v237 offset:30848
v_mfma_f32_16x16x32_bf16 v[36:39], v[192:195], v[136:139], v[36:39]
s_waitcnt vmcnt(22)
v_mfma_f32_16x16x32_bf16 v[0:3], v[196:199], v[160:163], v[0:3]
ds_read_b64_tr_b16 v[192:193], v234 offset:24832
ds_read_b64_tr_b16 v[194:195], v234 offset:30976
v_mfma_f32_16x16x32_bf16 v[40:43], v[196:199], v[164:167], v[40:43]
v_mfma_f32_16x16x32_bf16 v[44:47], v[200:203], v[164:167], v[44:47]
)"
R"(ds_read_b64_tr_b16 v[196:197], v235 offset:24832
ds_read_b64_tr_b16 v[198:199], v235 offset:30976
v_mfma_f32_16x16x32_bf16 v[4:7], v[200:203], v[160:163], v[4:7]
v_mfma_f32_16x16x32_bf16 v[8:11], v[204:207], v[160:163], v[8:11]
ds_read_b64_tr_b16 v[200:201], v234 offset:36864
ds_read_b64_tr_b16 v[202:203], v234 offset:43008
v_mfma_f32_16x16x32_bf16 v[48:51], v[204:207], v[164:167], v[48:51]
v_mfma_f32_16x16x32_bf16 v[52:55], v[208:211], v[164:167], v[52:55]
ds_read_b64_tr_b16 v[204:205], v235 offset:36864
ds_read_b64_tr_b16 v[206:207], v235 offset:43008
v_mfma_f32_16x16x32_bf16 v[12:15], v[208:211], v[160:163], v[12:15]
v_mfma_f32_16x16x32_bf16 v[16:19], v[212:215], v[160:163], v[16:19]
ds_read_b64_tr_b16 v[208:209], v236 offset:36864
ds_read_b64_tr_b16 v[210:211], v236 offset:43008
v_mfma_f32_16x16x32_bf16 v[56:59], v[212:215], v[164:167], v[56:59]
v_mfma_f32_16x16x32_bf16 v[60:63], v[216:219], v[164:167], v[60:63]
)"
R"(ds_read_b64_tr_b16 v[212:213], v237 offset:36864
s_waitcnt lgkmcnt(11)
ds_read_b64_tr_b16 v[214:215], v237 offset:43008
v_mfma_f32_16x16x32_bf16 v[20:23], v[216:219], v[160:163], v[20:23]
s_add_u32 m0, s56, 24576
s_nop 0
buffer_load_dwordx4 v248, s[20:23], s48 offen nt lds
buffer_load_dwordx4 v249, s[20:23], s48 offen offset:1024 nt lds
buffer_load_dwordx4 v250, s[20:23], s48 offen offset:2048 nt lds
s_add_u32 s48, s48, 12288
s_add_u32 m0, s56, 61440
s_nop 0
buffer_load_dwordx4 v251, s[24:27], s49 offen nt lds
buffer_load_dwordx4 v252, s[24:27], s49 offen offset:1024 nt lds
buffer_load_dwordx4 v253, s[24:27], s49 offen offset:2048 nt lds
s_add_u32 s49, s49, 12288
)"
R"(s_waitcnt lgkmcnt(0)
buffer_load_dwordx4 v[152:155], v247, s[40:43], s51 offen sc1
buffer_load_dwordx4 v[156:159], v247, s[40:43], s51 offen offset:1024 sc1
s_add_u32 s51, s51, 2048
s_cmp_eq_u32 s73, -1
s_cbranch_scc0 LpeA7_2_3
LplA7_2_3:
s_memrealtime s[88:89]
s_waitcnt lgkmcnt(0)
s_sub_u32 s89, s88, s94
s_cmp_lt_i32 s89, 0
s_cbranch_scc1 LplA7_2_3
s_add_u32 s95, s95, 104
s_sub_u32 s89, s88, s95
s_add_u32 s88, s88, 92
s_cmp_gt_i32 s89, 746
)"
R"(s_cselect_b32 s68, 20, 0
s_add_u32 s88, s88, s68
s_sub_u32 s89, s88, s95
s_cmp_gt_i32 s89, 0
s_cselect_b32 s94, s88, s95
LpeA7_2_3:
s_waitcnt vmcnt(19)
s_barrier
s_add_u32 m0, s56, 122880
s_nop 0
buffer_load_dwordx4 v244, s[32:35], s50 offen lds
buffer_load_dwordx4 v245, s[32:35], s50 offen offset:1024 lds
buffer_load_dwordx4 v246, s[32:35], s50 offen offset:2048 lds
s_add_u32 s50, s50, 40960
v_mfma_f32_16x16x32_bf16 v[24:27], v[184:187], v[160:163], v[24:27]
v_mfma_f32_16x16x32_bf16 v[64:67], v[184:187], v[164:167], v[64:67]
)"
R"(v_mfma_f32_16x16x32_bf16 v[68:71], v[188:191], v[164:167], v[68:71]
ds_read_b64_tr_b16 v[184:185], v234 offset:36992
ds_read_b64_tr_b16 v[186:187], v234 offset:43136
v_mfma_f32_16x16x32_bf16 v[28:31], v[188:191], v[160:163], v[28:31]
v_mfma_f32_16x16x32_bf16 v[32:35], v[192:195], v[160:163], v[32:35]
ds_read_b64_tr_b16 v[188:189], v235 offset:36992
ds_read_b64_tr_b16 v[190:191], v235 offset:43136
v_mfma_f32_16x16x32_bf16 v[72:75], v[192:195], v[164:167], v[72:75]
v_mfma_f32_16x16x32_bf16 v[76:79], v[196:199], v[164:167], v[76:79]
ds_read_b64_tr_b16 v[192:193], v236 offset:36992
ds_read_b64_tr_b16 v[194:195], v236 offset:43136
v_mfma_f32_16x16x32_bf16 v[36:39], v[196:199], v[160:163], v[36:39]
v_mfma_f32_16x16x32_bf16 v[0:3], v[200:203], v[168:171], v[0:3]
ds_read_b64_tr_b16 v[196:197], v237 offset:36992
ds_read_b64_tr_b16 v[198:199], v237 offset:43136
v_mfma_f32_16x16x32_bf16 v[40:43], v[200:203], v[172:175], v[40:43]
)"
R"(v_mfma_f32_16x16x32_bf16 v[44:47], v[204:207], v[172:175], v[44:47]
ds_read_b64_tr_b16 v[200:201], v234 offset:37120
ds_read_b64_tr_b16 v[202:203], v234 offset:43264
v_mfma_f32_16x16x32_bf16 v[4:7], v[204:207], v[168:171], v[4:7]
v_mfma_f32_16x16x32_bf16 v[8:11], v[208:211], v[168:171], v[8:11]
ds_read_b64_tr_b16 v[204:205], v235 offset:37120
ds_read_b64_tr_b16 v[206:207], v235 offset:43264
v_mfma_f32_16x16x32_bf16 v[48:51], v[208:211], v[172:175], v[48:51]
v_mfma_f32_16x16x32_bf16 v[52:55], v[212:215], v[172:175], v[52:55]
ds_read_b64_tr_b16 v[208:209], v234 offset:0
ds_read_b64_tr_b16 v[210:211], v234 offset:6144
v_mfma_f32_16x16x32_bf16 v[12:15], v[212:215], v[168:171], v[12:15]
s_add_u32 m0, s56, 36864
s_nop 0
buffer_load_dwordx4 v248, s[20:23], s48 offen nt lds
buffer_load_dwordx4 v249, s[20:23], s48 offen offset:1024 nt lds
)"
R"(buffer_load_dwordx4 v250, s[20:23], s48 offen offset:2048 nt lds
s_add_u32 s48, s48, 12288
ds_read_b64_tr_b16 v[212:213], v235 offset:0
s_waitcnt lgkmcnt(11)
ds_read_b64_tr_b16 v[214:215], v235 offset:6144
s_add_u32 m0, s56, 73728
s_nop 0
buffer_load_dwordx4 v251, s[24:27], s49 offen nt lds
buffer_load_dwordx4 v252, s[24:27], s49 offen offset:1024 nt lds
buffer_load_dwordx4 v253, s[24:27], s49 offen offset:2048 nt lds
s_add_u32 s49, s49, 12288
s_waitcnt lgkmcnt(0)
s_cmp_eq_u32 s73, -1
s_cbranch_scc0 LpeA8_2_0
LplA8_2_0:
s_memrealtime s[88:89]
)"
R"(s_waitcnt lgkmcnt(0)
s_sub_u32 s89, s88, s94
s_cmp_lt_i32 s89, 0
s_cbranch_scc1 LplA8_2_0
s_add_u32 s95, s95, 104
s_sub_u32 s89, s88, s95
s_add_u32 s88, s88, 92
s_cmp_gt_i32 s89, 746
s_cselect_b32 s68, 20, 0
s_add_u32 s88, s88, s68
s_sub_u32 s89, s88, s95
s_cmp_gt_i32 s89, 0
s_cselect_b32 s94, s88, s95
LpeA8_2_0:
s_waitcnt vmcnt(17)
s_barrier
)"
R"(s_add_u32 m0, s56, 135168
s_nop 0
buffer_load_dwordx4 v244, s[32:35], s50 offen lds
buffer_load_dwordx4 v245, s[32:35], s50 offen offset:1024 lds
buffer_load_dwordx4 v246, s[32:35], s50 offen offset:2048 lds
s_add_u32 s50, s50, 40960
v_mfma_f32_16x16x32_bf16 v[16:19], v[184:187], v[168:171], v[16:19]
v_mfma_f32_16x16x32_bf16 v[56:59], v[184:187], v[172:175], v[56:59]
v_mfma_f32_16x16x32_bf16 v[60:63], v[188:191], v[172:175], v[60:63]
ds_read_b64_tr_b16 v[184:185], v236 offset:0
ds_read_b64_tr_b16 v[186:187], v236 offset:6144
v_mfma_f32_16x16x32_bf16 v[20:23], v[188:191], v[168:171], v[20:23]
v_mfma_f32_16x16x32_bf16 v[24:27], v[192:195], v[168:171], v[24:27]
ds_read_b64_tr_b16 v[188:189], v237 offset:0
ds_read_b64_tr_b16 v[190:191], v237 offset:6144
v_mfma_f32_16x16x32_bf16 v[64:67], v[192:195], v[172:175], v[64:67]
)"
R"(v_mfma_f32_16x16x32_bf16 v[68:71], v[196:199], v[172:175], v[68:71]
ds_read_b64_tr_b16 v[192:193], v234 offset:128
ds_read_b64_tr_b16 v[194:195], v234 offset:6272
v_mfma_f32_16x16x32_bf16 v[28:31], v[196:199], v[168:171], v[28:31]
v_mfma_f32_16x16x32_bf16 v[32:35], v[200:203], v[168:171], v[32:35]
ds_read_b64_tr_b16 v[196:197], v235 offset:128
ds_read_b64_tr_b16 v[198:199], v235 offset:6272
v_mfma_f32_16x16x32_bf16 v[72:75], v[200:203], v[172:175], v[72:75]
v_mfma_f32_16x16x32_bf16 v[76:79], v[204:207], v[172:175], v[76:79]
ds_read_b64_tr_b16 v[200:201], v236 offset:128
ds_read_b64_tr_b16 v[202:203], v236 offset:6272
v_mfma_f32_16x16x32_bf16 v[36:39], v[204:207], v[168:171], v[36:39]
v_mfma_f32_16x16x32_bf16 v[0:3], v[208:211], v[144:147], v[0:3]
ds_read_b64_tr_b16 v[204:205], v237 offset:128
ds_read_b64_tr_b16 v[206:207], v237 offset:6272
v_mfma_f32_16x16x32_bf16 v[40:43], v[208:211], v[148:151], v[40:43]
)"
R"(v_mfma_f32_16x16x32_bf16 v[44:47], v[212:215], v[148:151], v[44:47]
ds_read_b64_tr_b16 v[208:209], v234 offset:256
ds_read_b64_tr_b16 v[210:211], v234 offset:6400
v_mfma_f32_16x16x32_bf16 v[4:7], v[212:215], v[144:147], v[4:7]
s_add_u32 m0, s56, 0
s_nop 0
buffer_load_dwordx4 v248, s[20:23], s48 offen nt lds
buffer_load_dwordx4 v249, s[20:23], s48 offen offset:1024 nt lds
buffer_load_dwordx4 v250, s[20:23], s48 offen offset:2048 nt lds
s_add_u32 s48, s48, 12288
ds_read_b64_tr_b16 v[212:213], v235 offset:256
s_waitcnt lgkmcnt(11)
ds_read_b64_tr_b16 v[214:215], v235 offset:6400
ds_read_b64_tr_b16 v[216:217], v234 offset:12288
ds_read_b64_tr_b16 v[218:219], v234 offset:18432
s_add_u32 m0, s56, 86016
)"
R"(s_nop 0
buffer_load_dwordx4 v251, s[24:27], s49 offen nt lds
buffer_load_dwordx4 v252, s[24:27], s49 offen offset:1024 nt lds
buffer_load_dwordx4 v253, s[24:27], s49 offen offset:2048 nt lds
s_add_u32 s49, s49, 12288
s_waitcnt lgkmcnt(0)
buffer_load_dwordx4 v[160:163], v247, s[40:43], s51 offen sc1
buffer_load_dwordx4 v[164:167], v247, s[40:43], s51 offen offset:1024 sc1
s_add_u32 s51, s51, 2048
s_cmp_eq_u32 s73, -1
s_cbranch_scc0 LpeA8_2_1
LplA8_2_1:
s_memrealtime s[88:89]
s_waitcnt lgkmcnt(0)
s_sub_u32 s89, s88, s94
s_cmp_lt_i32 s89, 0
)"
R"(s_cbranch_scc1 LplA8_2_1
s_add_u32 s95, s95, 104
s_sub_u32 s89, s88, s95
s_add_u32 s88, s88, 92
s_cmp_gt_i32 s89, 746
s_cselect_b32 s68, 20, 0
s_add_u32 s88, s88, s68
s_sub_u32 s89, s88, s95
s_cmp_gt_i32 s89, 0
s_cselect_b32 s94, s88, s95
LpeA8_2_1:
s_waitcnt vmcnt(22)
s_barrier
s_mul_i32 s50, s92, 245760
s_add_u32 s50, s50, s52
s_add_u32 m0, s56, 98304
)"
R"(s_nop 0
buffer_load_dwordx4 v244, s[32:35], s50 offen lds
buffer_load_dwordx4 v245, s[32:35], s50 offen offset:1024 lds
buffer_load_dwordx4 v246, s[32:35], s50 offen offset:2048 lds
s_add_u32 s50, s50, 40960
v_mfma_f32_16x16x32_bf16 v[8:11], v[184:187], v[144:147], v[8:11]
v_mfma_f32_16x16x32_bf16 v[48:51], v[184:187], v[148:151], v[48:51]
v_mfma_f32_16x16x32_bf16 v[52:55], v[188:191], v[148:151], v[52:55]
ds_read_b64_tr_b16 v[184:185], v235 offset:12288
ds_read_b64_tr_b16 v[186:187], v235 offset:18432
v_mfma_f32_16x16x32_bf16 v[12:15], v[188:191], v[144:147], v[12:15]
v_mfma_f32_16x16x32_bf16 v[16:19], v[192:195], v[144:147], v[16:19]
ds_read_b64_tr_b16 v[188:189], v236 offset:12288
ds_read_b64_tr_b16 v[190:191], v236 offset:18432
v_mfma_f32_16x16x32_bf16 v[56:59], v[192:195], v[148:151], v[56:59]
v_mfma_f32_16x16x32_bf16 v[60:63], v[196:199], v[148:151], v[60:63]
)"
R"(ds_read_b64_tr_b16 v[192:193], v237 offset:12288
ds_read_b64_tr_b16 v[194:195], v237 offset:18432
v_mfma_f32_16x16x32_bf16 v[20:23], v[196:199], v[144:147], v[20:23]
v_mfma_f32_16x16x32_bf16 v[24:27], v[200:203], v[144:147], v[24:27]
ds_read_b64_tr_b16 v[196:197], v234 offset:12416
ds_read_b64_tr_b16 v[198:199], v234 offset:18560
v_mfma_f32_16x16x32_bf16 v[64:67], v[200:203], v[148:151], v[64:67]
v_mfma_f32_16x16x32_bf16 v[68:71], v[204:207], v[148:151], v[68:71]
ds_read_b64_tr_b16 v[200:201], v235 offset:12416
ds_read_b64_tr_b16 v[202:203], v235 offset:18560
v_mfma_f32_16x16x32_bf16 v[28:31], v[204:207], v[144:147], v[28:31]
v_mfma_f32_16x16x32_bf16 v[32:35], v[208:211], v[144:147], v[32:35]
ds_read_b64_tr_b16 v[204:205], v236 offset:12416
ds_read_b64_tr_b16 v[206:207], v236 offset:18560
v_mfma_f32_16x16x32_bf16 v[72:75], v[208:211], v[148:151], v[72:75]
v_mfma_f32_16x16x32_bf16 v[76:79], v[212:215], v[148:151], v[76:79]
)"
R"(ds_read_b64_tr_b16 v[208:209], v237 offset:12416
ds_read_b64_tr_b16 v[210:211], v237 offset:18560
v_mfma_f32_16x16x32_bf16 v[36:39], v[212:215], v[144:147], v[36:39]
s_waitcnt vmcnt(23)
v_mfma_f32_16x16x32_bf16 v[0:3], v[216:219], v[152:155], v[0:3]
ds_read_b64_tr_b16 v[212:213], v234 offset:12544
s_waitcnt lgkmcnt(11)
ds_read_b64_tr_b16 v[214:215], v234 offset:18688
v_mfma_f32_16x16x32_bf16 v[40:43], v[216:219], v[156:159], v[40:43]
s_add_u32 m0, s56, 12288
s_nop 0
buffer_load_dwordx4 v248, s[20:23], s48 offen nt lds
buffer_load_dwordx4 v249, s[20:23], s48 offen offset:1024 nt lds
buffer_load_dwordx4 v250, s[20:23], s48 offen offset:2048 nt lds
s_add_u32 s48, s48, 12288
s_add_u32 m0, s56, 49152
)"
R"(s_nop 0
buffer_load_dwordx4 v251, s[24:27], s49 offen nt lds
buffer_load_dwordx4 v252, s[24:27], s49 offen offset:1024 nt lds
buffer_load_dwordx4 v253, s[24:27], s49 offen offset:2048 nt lds
s_add_u32 s49, s49, 12288
s_waitcnt lgkmcnt(0)
buffer_load_dwordx4 v[168:171], v247, s[40:43], s51 offen sc1
buffer_load_dwordx4 v[172:175], v247, s[40:43], s51 offen offset:1024 sc1
s_add_u32 s51, s51, 2048
s_cmp_eq_u32 s73, -1
s_cbranch_scc0 LpeA8_2_2
LplA8_2_2:
s_memrealtime s[88:89]
s_waitcnt lgkmcnt(0)
s_sub_u32 s89, s88, s94
s_cmp_lt_i32 s89, 0
)"
R"(s_cbranch_scc1 LplA8_2_2
s_add_u32 s95, s95, 104
s_sub_u32 s89, s88, s95
s_add_u32 s88, s88, 92
s_cmp_gt_i32 s89, 746
s_cselect_b32 s68, 20, 0
s_add_u32 s88, s88, s68
s_sub_u32 s89, s88, s95
s_cmp_gt_i32 s89, 0
s_cselect_b32 s94, s88, s95
LpeA8_2_2:
s_waitcnt vmcnt(22)
s_barrier
s_add_u32 m0, s56, 24576
s_nop 0
buffer_load_dwordx4 v248, s[20:23], s48 offen nt lds
)"
R"(buffer_load_dwordx4 v249, s[20:23], s48 offen offset:1024 nt lds
buffer_load_dwordx4 v250, s[20:23], s48 offen offset:2048 nt lds
s_add_u32 s48, s48, 12288
v_mfma_f32_16x16x32_bf16 v[44:47], v[184:187], v[156:159], v[44:47]
v_mfma_f32_16x16x32_bf16 v[4:7], v[184:187], v[152:155], v[4:7]
v_mfma_f32_16x16x32_bf16 v[8:11], v[188:191], v[152:155], v[8:11]
ds_read_b64_tr_b16 v[184:185], v235 offset:12544
ds_read_b64_tr_b16 v[186:187], v235 offset:18688
v_mfma_f32_16x16x32_bf16 v[48:51], v[188:191], v[156:159], v[48:51]
v_mfma_f32_16x16x32_bf16 v[52:55], v[192:195], v[156:159], v[52:55]
ds_read_b64_tr_b16 v[188:189], v234 offset:24576
ds_read_b64_tr_b16 v[190:191], v234 offset:30720
v_mfma_f32_16x16x32_bf16 v[12:15], v[192:195], v[152:155], v[12:15]
v_mfma_f32_16x16x32_bf16 v[16:19], v[196:199], v[152:155], v[16:19]
ds_read_b64_tr_b16 v[192:193], v235 offset:24576
ds_read_b64_tr_b16 v[194:195], v235 offset:30720
)"
R"(v_mfma_f32_16x16x32_bf16 v[56:59], v[196:199], v[156:159], v[56:59]
v_mfma_f32_16x16x32_bf16 v[60:63], v[200:203], v[156:159], v[60:63]
ds_read_b64_tr_b16 v[196:197], v236 offset:24576
ds_read_b64_tr_b16 v[198:199], v236 offset:30720
v_mfma_f32_16x16x32_bf16 v[20:23], v[200:203], v[152:155], v[20:23]
v_mfma_f32_16x16x32_bf16 v[24:27], v[204:207], v[152:155], v[24:27]
ds_read_b64_tr_b16 v[200:201], v237 offset:24576
ds_read_b64_tr_b16 v[202:203], v237 offset:30720
v_mfma_f32_16x16x32_bf16 v[64:67], v[204:207], v[156:159], v[64:67]
v_mfma_f32_16x16x32_bf16 v[68:71], v[208:211], v[156:159], v[68:71]
ds_read_b64_tr_b16 v[204:205], v234 offset:24704
ds_read_b64_tr_b16 v[206:207], v234 offset:30848
v_mfma_f32_16x16x32_bf16 v[28:31], v[208:211], v[152:155], v[28:31]
v_mfma_f32_16x16x32_bf16 v[32:35], v[212:215], v[152:155], v[32:35]
ds_read_b64_tr_b16 v[208:209], v235 offset:24704
ds_read_b64_tr_b16 v[210:211], v235 offset:30848
)"
R"(v_mfma_f32_16x16x32_bf16 v[72:75], v[212:215], v[156:159], v[72:75]
s_add_u32 m0, s56, 61440
s_nop 0
buffer_load_dwordx4 v251, s[24:27], s49 offen nt lds
ds_read_b64_tr_b16 v[212:213], v236 offset:24704
s_waitcnt lgkmcnt(11)
ds_read_b64_tr_b16 v[214:215], v236 offset:30848
buffer_load_dwordx4 v252, s[24:27], s49 offen offset:1024 nt lds
buffer_load_dwordx4 v253, s[24:27], s49 offen offset:2048 nt lds
s_add_u32 s49, s49, 12288
s_waitcnt lgkmcnt(0)
s_mul_i32 s51, s92, 12288
s_add_u32 s51, s51, s54
buffer_load_dwordx4 v[144:147], v247, s[40:43], s51 offen sc1
buffer_load_dwordx4 v[148:151], v247, s[40:43], s51 offen offset:1024 sc1
s_add_u32 s51, s51, 2048
)"
R"(s_cmp_eq_u32 s73, -1
s_cbranch_scc0 LpeA8_2_3
LplA8_2_3:
s_memrealtime s[88:89]
s_waitcnt lgkmcnt(0)
s_sub_u32 s89, s88, s94
s_cmp_lt_i32 s89, 0
s_cbranch_scc1 LplA8_2_3
s_add_u32 s95, s95, 104
s_sub_u32 s89, s88, s95
s_add_u32 s88, s88, 92
s_cmp_gt_i32 s89, 746
s_cselect_b32 s68, 20, 0
s_add_u32 s88, s88, s68
s_sub_u32 s89, s88, s95
s_cmp_gt_i32 s89, 0
)"
R"(s_cselect_b32 s94, s88, s95
LpeA8_2_3:
s_waitcnt vmcnt(21)
s_barrier
s_add_u32 m0, s56, 110592
s_nop 0
buffer_load_dwordx4 v244, s[32:35], s50 offen lds
buffer_load_dwordx4 v245, s[32:35], s50 offen offset:1024 lds
buffer_load_dwordx4 v246, s[32:35], s50 offen offset:2048 lds
s_add_u32 s50, s50, 40960
v_mfma_f32_16x16x32_bf16 v[76:79], v[184:187], v[156:159], v[76:79]
v_mfma_f32_16x16x32_bf16 v[36:39], v[184:187], v[152:155], v[36:39]
s_waitcnt vmcnt(22)
v_mfma_f32_16x16x32_bf16 v[0:3], v[188:191], v[160:163], v[0:3]
ds_read_b64_tr_b16 v[184:185], v237 offset:24704
ds_read_b64_tr_b16 v[186:187], v237 offset:30848
)"
R"(v_mfma_f32_16x16x32_bf16 v[40:43], v[188:191], v[164:167], v[40:43]
v_mfma_f32_16x16x32_bf16 v[44:47], v[192:195], v[164:167], v[44:47]
ds_read_b64_tr_b16 v[188:189], v234 offset:24832
ds_read_b64_tr_b16 v[190:191], v234 offset:30976
v_mfma_f32_16x16x32_bf16 v[4:7], v[192:195], v[160:163], v[4:7]
v_mfma_f32_16x16x32_bf16 v[8:11], v[196:199], v[160:163], v[8:11]
ds_read_b64_tr_b16 v[192:193], v235 offset:24832
ds_read_b64_tr_b16 v[194:195], v235 offset:30976
v_mfma_f32_16x16x32_bf16 v[48:51], v[196:199], v[164:167], v[48:51]
v_mfma_f32_16x16x32_bf16 v[52:55], v[200:203], v[164:167], v[52:55]
ds_read_b64_tr_b16 v[196:197], v234 offset:36864
ds_read_b64_tr_b16 v[198:199], v234 offset:43008
v_mfma_f32_16x16x32_bf16 v[12:15], v[200:203], v[160:163], v[12:15]
v_mfma_f32_16x16x32_bf16 v[16:19], v[204:207], v[160:163], v[16:19]
ds_read_b64_tr_b16 v[200:201], v235 offset:36864
ds_read_b64_tr_b16 v[202:203], v235 offset:43008
)"
R"(v_mfma_f32_16x16x32_bf16 v[56:59], v[204:207], v[164:167], v[56:59]
v_mfma_f32_16x16x32_bf16 v[60:63], v[208:211], v[164:167], v[60:63]
ds_read_b64_tr_b16 v[204:205], v236 offset:36864
ds_read_b64_tr_b16 v[206:207], v236 offset:43008
v_mfma_f32_16x16x32_bf16 v[20:23], v[208:211], v[160:163], v[20:23]
v_mfma_f32_16x16x32_bf16 v[24:27], v[212:215], v[160:163], v[24:27]
ds_read_b64_tr_b16 v[208:209], v237 offset:36864
ds_read_b64_tr_b16 v[210:211], v237 offset:43008
v_mfma_f32_16x16x32_bf16 v[64:67], v[212:215], v[164:167], v[64:67]
s_add_u32 m0, s56, 36864
s_nop 0
buffer_load_dwordx4 v248, s[20:23], s48 offen nt lds
buffer_load_dwordx4 v249, s[20:23], s48 offen offset:1024 nt lds
buffer_load_dwordx4 v250, s[20:23], s48 offen offset:2048 nt lds
s_add_u32 s48, s48, 12288
ds_read_b64_tr_b16 v[212:213], v234 offset:36992
)"
R"(s_waitcnt lgkmcnt(11)
ds_read_b64_tr_b16 v[214:215], v234 offset:43136
s_add_u32 m0, s56, 73728
s_nop 0
buffer_load_dwordx4 v251, s[24:27], s49 offen nt lds
buffer_load_dwordx4 v252, s[24:27], s49 offen offset:1024 nt lds
buffer_load_dwordx4 v253, s[24:27], s49 offen offset:2048 nt lds
s_add_u32 s49, s49, 12288
s_waitcnt lgkmcnt(0)
buffer_load_dwordx4 v[152:155], v247, s[40:43], s51 offen sc1
buffer_load_dwordx4 v[156:159], v247, s[40:43], s51 offen offset:1024 sc1
s_add_u32 s51, s51, 2048
s_cmp_eq_u32 s73, -1
s_cbranch_scc0 LpeA9_2_0
LplA9_2_0:
s_memrealtime s[88:89]
)"
R"(s_waitcnt lgkmcnt(0)
s_sub_u32 s89, s88, s94
s_cmp_lt_i32 s89, 0
s_cbranch_scc1 LplA9_2_0
s_add_u32 s95, s95, 104
s_sub_u32 s89, s88, s95
s_add_u32 s88, s88, 92
s_cmp_gt_i32 s89, 746
s_cselect_b32 s68, 20, 0
s_add_u32 s88, s88, s68
s_sub_u32 s89, s88, s95
s_cmp_gt_i32 s89, 0
s_cselect_b32 s94, s88, s95
LpeA9_2_0:
s_waitcnt vmcnt(21)
s_barrier
)"
R"(s_add_u32 m0, s56, 122880
s_nop 0
buffer_load_dwordx4 v244, s[32:35], s50 offen lds
buffer_load_dwordx4 v245, s[32:35], s50 offen offset:1024 lds
buffer_load_dwordx4 v246, s[32:35], s50 offen offset:2048 lds
s_add_u32 s50, s50, 40960
v_mfma_f32_16x16x32_bf16 v[68:71], v[184:187], v[164:167], v[68:71]
v_mfma_f32_16x16x32_bf16 v[28:31], v[184:187], v[160:163], v[28:31]
v_mfma_f32_16x16x32_bf16 v[32:35], v[188:191], v[160:163], v[32:35]
ds_read_b64_tr_b16 v[184:185], v235 offset:36992
ds_read_b64_tr_b16 v[186:187], v235 offset:43136
v_mfma_f32_16x16x32_bf16 v[72:75], v[188:191], v[164:167], v[72:75]
v_mfma_f32_16x16x32_bf16 v[76:79], v[192:195], v[164:167], v[76:79]
ds_read_b64_tr_b16 v[188:189], v236 offset:36992
ds_read_b64_tr_b16 v[190:191], v236 offset:43136
v_mfma_f32_16x16x32_bf16 v[36:39], v[192:195], v[160:163], v[36:39]
)"
R"(s_waitcnt vmcnt(22)
v_mfma_f32_16x16x32_bf16 v[0:3], v[196:199], v[168:171], v[0:3]
ds_read_b64_tr_b16 v[192:193], v237 offset:36992
ds_read_b64_tr_b16 v[194:195], v237 offset:43136
v_mfma_f32_16x16x32_bf16 v[40:43], v[196:199], v[172:175], v[40:43]
v_mfma_f32_16x16x32_bf16 v[44:47], v[200:203], v[172:175], v[44:47]
ds_read_b64_tr_b16 v[196:197], v234 offset:37120
ds_read_b64_tr_b16 v[198:199], v234 offset:43264
v_mfma_f32_16x16x32_bf16 v[4:7], v[200:203], v[168:171], v[4:7]
v_mfma_f32_16x16x32_bf16 v[8:11], v[204:207], v[168:171], v[8:11]
ds_read_b64_tr_b16 v[200:201], v235 offset:37120
ds_read_b64_tr_b16 v[202:203], v235 offset:43264
v_mfma_f32_16x16x32_bf16 v[48:51], v[204:207], v[172:175], v[48:51]
v_mfma_f32_16x16x32_bf16 v[52:55], v[208:211], v[172:175], v[52:55]
ds_read_b64_tr_b16 v[204:205], v234 offset:0
ds_read_b64_tr_b16 v[206:207], v234 offset:6144
)"
R"(v_mfma_f32_16x16x32_bf16 v[12:15], v[208:211], v[168:171], v[12:15]
v_mfma_f32_16x16x32_bf16 v[16:19], v[212:215], v[168:171], v[16:19]
ds_read_b64_tr_b16 v[208:209], v235 offset:0
ds_read_b64_tr_b16 v[210:211], v235 offset:6144
v_mfma_f32_16x16x32_bf16 v[56:59], v[212:215], v[172:175], v[56:59]
s_add_u32 m0, s56, 0
s_nop 0
buffer_load_dwordx4 v248, s[20:23], s48 offen nt lds
buffer_load_dwordx4 v249, s[20:23], s48 offen offset:1024 nt lds
buffer_load_dwordx4 v250, s[20:23], s48 offen offset:2048 nt lds
s_add_u32 s48, s48, 12288
ds_read_b64_tr_b16 v[212:213], v236 offset:0
s_waitcnt lgkmcnt(11)
ds_read_b64_tr_b16 v[214:215], v236 offset:6144
ds_read_b64_tr_b16 v[216:217], v237 offset:0
ds_read_b64_tr_b16 v[218:219], v237 offset:6144
)"
R"(s_add_u32 m0, s56, 86016
s_nop 0
buffer_load_dwordx4 v251, s[24:27], s49 offen nt lds
buffer_load_dwordx4 v252, s[24:27], s49 offen offset:1024 nt lds
buffer_load_dwordx4 v253, s[24:27], s49 offen offset:2048 nt lds
s_add_u32 s49, s49, 12288
s_waitcnt lgkmcnt(0)
s_cmp_eq_u32 s73, -1
s_cbranch_scc0 LpeA9_2_1
LplA9_2_1:
s_memrealtime s[88:89]
s_waitcnt lgkmcnt(0)
s_sub_u32 s89, s88, s94
s_cmp_lt_i32 s89, 0
s_cbranch_scc1 LplA9_2_1
s_add_u32 s95, s95, 104
)"
R"(s_sub_u32 s89, s88, s95
s_add_u32 s88, s88, 92
s_cmp_gt_i32 s89, 746
s_cselect_b32 s68, 20, 0
s_add_u32 s88, s88, s68
s_sub_u32 s89, s88, s95
s_cmp_gt_i32 s89, 0
s_cselect_b32 s94, s88, s95
LpeA9_2_1:
s_waitcnt vmcnt(17)
s_barrier
s_add_u32 m0, s56, 135168
s_nop 0
buffer_load_dwordx4 v244, s[32:35], s50 offen lds
buffer_load_dwordx4 v245, s[32:35], s50 offen offset:1024 lds
buffer_load_dwordx4 v246, s[32:35], s50 offen offset:2048 lds
)"
R"(s_add_u32 s50, s50, 40960
v_mfma_f32_16x16x32_bf16 v[60:63], v[184:187], v[172:175], v[60:63]
v_mfma_f32_16x16x32_bf16 v[20:23], v[184:187], v[168:171], v[20:23]
v_mfma_f32_16x16x32_bf16 v[24:27], v[188:191], v[168:171], v[24:27]
ds_read_b64_tr_b16 v[184:185], v234 offset:128
ds_read_b64_tr_b16 v[186:187], v234 offset:6272
v_mfma_f32_16x16x32_bf16 v[64:67], v[188:191], v[172:175], v[64:67]
v_mfma_f32_16x16x32_bf16 v[68:71], v[192:195], v[172:175], v[68:71]
ds_read_b64_tr_b16 v[188:189], v235 offset:128
ds_read_b64_tr_b16 v[190:191], v235 offset:6272
v_mfma_f32_16x16x32_bf16 v[28:31], v[192:195], v[168:171], v[28:31]
v_mfma_f32_16x16x32_bf16 v[32:35], v[196:199], v[168:171], v[32:35]
ds_read_b64_tr_b16 v[192:193], v236 offset:128
ds_read_b64_tr_b16 v[194:195], v236 offset:6272
v_mfma_f32_16x16x32_bf16 v[72:75], v[196:199], v[172:175], v[72:75]
v_mfma_f32_16x16x32_bf16 v[76:79], v[200:203], v[172:175], v[76:79]
)"
R"(ds_read_b64_tr_b16 v[196:197], v237 offset:128
ds_read_b64_tr_b16 v[198:199], v237 offset:6272
v_mfma_f32_16x16x32_bf16 v[36:39], v[200:203], v[168:171], v[36:39]
v_mfma_f32_16x16x32_bf16 v[0:3], v[204:207], v[144:147], v[0:3]
ds_read_b64_tr_b16 v[200:201], v234 offset:256
ds_read_b64_tr_b16 v[202:203], v234 offset:6400
v_mfma_f32_16x16x32_bf16 v[40:43], v[204:207], v[148:151], v[40:43]
v_mfma_f32_16x16x32_bf16 v[44:47], v[208:211], v[148:151], v[44:47]
ds_read_b64_tr_b16 v[204:205], v235 offset:256
ds_read_b64_tr_b16 v[206:207], v235 offset:6400
v_mfma_f32_16x16x32_bf16 v[4:7], v[208:211], v[144:147], v[4:7]
v_mfma_f32_16x16x32_bf16 v[8:11], v[212:215], v[144:147], v[8:11]
ds_read_b64_tr_b16 v[208:209], v234 offset:12288
ds_read_b64_tr_b16 v[210:211], v234 offset:18432
v_mfma_f32_16x16x32_bf16 v[48:51], v[212:215], v[148:151], v[48:51]
v_mfma_f32_16x16x32_bf16 v[52:55], v[216:219], v[148:151], v[52:55]
)"
R"(ds_read_b64_tr_b16 v[212:213], v235 offset:12288
s_waitcnt lgkmcnt(11)
ds_read_b64_tr_b16 v[214:215], v235 offset:18432
v_mfma_f32_16x16x32_bf16 v[12:15], v[216:219], v[144:147], v[12:15]
s_add_u32 m0, s56, 12288
s_nop 0
buffer_load_dwordx4 v248, s[20:23], s48 offen nt lds
buffer_load_dwordx4 v249, s[20:23], s48 offen offset:1024 nt lds
buffer_load_dwordx4 v250, s[20:23], s48 offen offset:2048 nt lds
s_add_u32 s48, s48, 12288
s_add_u32 m0, s56, 49152
s_nop 0
buffer_load_dwordx4 v251, s[24:27], s49 offen nt lds
buffer_load_dwordx4 v252, s[24:27], s49 offen offset:1024 nt lds
buffer_load_dwordx4 v253, s[24:27], s49 offen offset:2048 nt lds
s_add_u32 s49, s49, 12288
)"
R"(s_waitcnt lgkmcnt(0)
buffer_load_dwordx4 v[160:163], v247, s[40:43], s51 offen sc1
buffer_load_dwordx4 v[164:167], v247, s[40:43], s51 offen offset:1024 sc1
s_add_u32 s51, s51, 2048
s_cmp_eq_u32 s73, -1
s_cbranch_scc0 LpeA9_2_2
LplA9_2_2:
s_memrealtime s[88:89]
s_waitcnt lgkmcnt(0)
s_sub_u32 s89, s88, s94
s_cmp_lt_i32 s89, 0
s_cbranch_scc1 LplA9_2_2
s_add_u32 s95, s95, 104
s_sub_u32 s89, s88, s95
s_add_u32 s88, s88, 92
s_cmp_gt_i32 s89, 746
)"
R"(s_cselect_b32 s68, 20, 0
s_add_u32 s88, s88, s68
s_sub_u32 s89, s88, s95
s_cmp_gt_i32 s89, 0
s_cselect_b32 s94, s88, s95
LpeA9_2_2:
s_waitcnt vmcnt(22)
s_barrier
s_add_u32 m0, s56, 98304
s_nop 0
buffer_load_dwordx4 v244, s[32:35], s50 offen lds
buffer_load_dwordx4 v245, s[32:35], s50 offen offset:1024 lds
buffer_load_dwordx4 v246, s[32:35], s50 offen offset:2048 lds
s_add_u32 s50, s50, 40960
v_mfma_f32_16x16x32_bf16 v[16:19], v[184:187], v[144:147], v[16:19]
v_mfma_f32_16x16x32_bf16 v[56:59], v[184:187], v[148:151], v[56:59]
)"
R"(v_mfma_f32_16x16x32_bf16 v[60:63], v[188:191], v[148:151], v[60:63]
ds_read_b64_tr_b16 v[184:185], v236 offset:12288
ds_read_b64_tr_b16 v[186:187], v236 offset:18432
v_mfma_f32_16x16x32_bf16 v[20:23], v[188:191], v[144:147], v[20:23]
v_mfma_f32_16x16x32_bf16 v[24:27], v[192:195], v[144:147], v[24:27]
ds_read_b64_tr_b16 v[188:189], v237 offset:12288
ds_read_b64_tr_b16 v[190:191], v237 offset:18432
v_mfma_f32_16x16x32_bf16 v[64:67], v[192:195], v[148:151], v[64:67]
v_mfma_f32_16x16x32_bf16 v[68:71], v[196:199], v[148:151], v[68:71]
ds_read_b64_tr_b16 v[192:193], v234 offset:12416
ds_read_b64_tr_b16 v[194:195], v234 offset:18560
v_mfma_f32_16x16x32_bf16 v[28:31], v[196:199], v[144:147], v[28:31]
v_mfma_f32_16x16x32_bf16 v[32:35], v[200:203], v[144:147], v[32:35]
ds_read_b64_tr_b16 v[196:197], v235 offset:12416
ds_read_b64_tr_b16 v[198:199], v235 offset:18560
v_mfma_f32_16x16x32_bf16 v[72:75], v[200:203], v[148:151], v[72:75]
)"
R"(v_mfma_f32_16x16x32_bf16 v[76:79], v[204:207], v[148:151], v[76:79]
ds_read_b64_tr_b16 v[200:201], v236 offset:12416
ds_read_b64_tr_b16 v[202:203], v236 offset:18560
v_mfma_f32_16x16x32_bf16 v[36:39], v[204:207], v[144:147], v[36:39]
s_waitcnt vmcnt(23)
v_mfma_f32_16x16x32_bf16 v[0:3], v[208:211], v[152:155], v[0:3]
ds_read_b64_tr_b16 v[204:205], v237 offset:12416
ds_read_b64_tr_b16 v[206:207], v237 offset:18560
v_mfma_f32_16x16x32_bf16 v[40:43], v[208:211], v[156:159], v[40:43]
v_mfma_f32_16x16x32_bf16 v[44:47], v[212:215], v[156:159], v[44:47]
ds_read_b64_tr_b16 v[208:209], v234 offset:12544
ds_read_b64_tr_b16 v[210:211], v234 offset:18688
v_mfma_f32_16x16x32_bf16 v[4:7], v[212:215], v[152:155], v[4:7]
s_add_u32 m0, s56, 24576
s_nop 0
buffer_load_dwordx4 v248, s[20:23], s48 offen nt lds
)"
R"(buffer_load_dwordx4 v249, s[20:23], s48 offen offset:1024 nt lds
buffer_load_dwordx4 v250, s[20:23], s48 offen offset:2048 nt lds
s_add_u32 s48, s48, 12288
ds_read_b64_tr_b16 v[212:213], v235 offset:12544
s_waitcnt lgkmcnt(11)
ds_read_b64_tr_b16 v[214:215], v235 offset:18688
s_add_u32 m0, s56, 61440
s_nop 0
buffer_load_dwordx4 v251, s[24:27], s49 offen nt lds
buffer_load_dwordx4 v252, s[24:27], s49 offen offset:1024 nt lds
buffer_load_dwordx4 v253, s[24:27], s49 offen offset:2048 nt lds
s_add_u32 s49, s49, 12288
s_waitcnt lgkmcnt(0)
buffer_load_dwordx4 v[168:171], v247, s[40:43], s51 offen sc1
buffer_load_dwordx4 v[172:175], v247, s[40:43], s51 offen offset:1024 sc1
s_add_u32 s51, s51, 2048
)"
R"(s_cmp_eq_u32 s73, -1
s_cbranch_scc0 LpeA9_2_3
LplA9_2_3:
s_memrealtime s[88:89]
s_waitcnt lgkmcnt(0)
s_sub_u32 s89, s88, s94
s_cmp_lt_i32 s89, 0
s_cbranch_scc1 LplA9_2_3
s_add_u32 s95, s95, 104
s_sub_u32 s89, s88, s95
s_add_u32 s88, s88, 92
s_cmp_gt_i32 s89, 746
s_cselect_b32 s68, 20, 0
s_add_u32 s88, s88, s68
s_sub_u32 s89, s88, s95
s_cmp_gt_i32 s89, 0
)"
R"(s_cselect_b32 s94, s88, s95
LpeA9_2_3:
s_waitcnt vmcnt(22)
s_barrier
s_add_u32 m0, s56, 110592
s_nop 0
buffer_load_dwordx4 v244, s[32:35], s50 offen lds
buffer_load_dwordx4 v245, s[32:35], s50 offen offset:1024 lds
buffer_load_dwordx4 v246, s[32:35], s50 offen offset:2048 lds
s_add_u32 s50, s50, 40960
v_mfma_f32_16x16x32_bf16 v[8:11], v[184:187], v[152:155], v[8:11]
v_mfma_f32_16x16x32_bf16 v[48:51], v[184:187], v[156:159], v[48:51]
v_mfma_f32_16x16x32_bf16 v[52:55], v[188:191], v[156:159], v[52:55]
ds_read_b64_tr_b16 v[184:185], v234 offset:24576
ds_read_b64_tr_b16 v[186:187], v234 offset:30720
v_mfma_f32_16x16x32_bf16 v[12:15], v[188:191], v[152:155], v[12:15]
)"
R"(v_mfma_f32_16x16x32_bf16 v[16:19], v[192:195], v[152:155], v[16:19]
ds_read_b64_tr_b16 v[188:189], v235 offset:24576
ds_read_b64_tr_b16 v[190:191], v235 offset:30720
v_mfma_f32_16x16x32_bf16 v[56:59], v[192:195], v[156:159], v[56:59]
v_mfma_f32_16x16x32_bf16 v[60:63], v[196:199], v[156:159], v[60:63]
ds_read_b64_tr_b16 v[192:193], v236 offset:24576
ds_read_b64_tr_b16 v[194:195], v236 offset:30720
v_mfma_f32_16x16x32_bf16 v[20:23], v[196:199], v[152:155], v[20:23]
v_mfma_f32_16x16x32_bf16 v[24:27], v[200:203], v[152:155], v[24:27]
ds_read_b64_tr_b16 v[196:197], v237 offset:24576
ds_read_b64_tr_b16 v[198:199], v237 offset:30720
v_mfma_f32_16x16x32_bf16 v[64:67], v[200:203], v[156:159], v[64:67]
v_mfma_f32_16x16x32_bf16 v[68:71], v[204:207], v[156:159], v[68:71]
ds_read_b64_tr_b16 v[200:201], v234 offset:24704
ds_read_b64_tr_b16 v[202:203], v234 offset:30848
v_mfma_f32_16x16x32_bf16 v[28:31], v[204:207], v[152:155], v[28:31]
)"
R"(v_mfma_f32_16x16x32_bf16 v[32:35], v[208:211], v[152:155], v[32:35]
ds_read_b64_tr_b16 v[204:205], v235 offset:24704
ds_read_b64_tr_b16 v[206:207], v235 offset:30848
v_mfma_f32_16x16x32_bf16 v[72:75], v[208:211], v[156:159], v[72:75]
v_mfma_f32_16x16x32_bf16 v[76:79], v[212:215], v[156:159], v[76:79]
ds_read_b64_tr_b16 v[208:209], v236 offset:24704
ds_read_b64_tr_b16 v[210:211], v236 offset:30848
v_mfma_f32_16x16x32_bf16 v[36:39], v[212:215], v[152:155], v[36:39]
s_add_u32 m0, s56, 36864
s_nop 0
buffer_load_dwordx4 v248, s[20:23], s48 offen nt lds
buffer_load_dwordx4 v249, s[20:23], s48 offen offset:1024 nt lds
buffer_load_dwordx4 v250, s[20:23], s48 offen offset:2048 nt lds
s_add_u32 s48, s48, 12288
ds_read_b64_tr_b16 v[212:213], v237 offset:24704
s_waitcnt lgkmcnt(11)
)"
R"(ds_read_b64_tr_b16 v[214:215], v237 offset:30848
s_add_u32 m0, s56, 73728
s_nop 0
buffer_load_dwordx4 v251, s[24:27], s49 offen nt lds
buffer_load_dwordx4 v252, s[24:27], s49 offen offset:1024 nt lds
buffer_load_dwordx4 v253, s[24:27], s49 offen offset:2048 nt lds
s_add_u32 s49, s49, 12288
s_waitcnt lgkmcnt(0)
buffer_load_dwordx4 v[144:147], v247, s[40:43], s51 offen sc1
buffer_load_dwordx4 v[148:151], v247, s[40:43], s51 offen offset:1024 sc1
s_add_u32 s51, s51, 2048
s_cmp_eq_u32 s73, -1
s_cbranch_scc0 LpeA10_2_0
LplA10_2_0:
s_memrealtime s[88:89]
s_waitcnt lgkmcnt(0)
)"
R"(s_sub_u32 s89, s88, s94
s_cmp_lt_i32 s89, 0
s_cbranch_scc1 LplA10_2_0
s_add_u32 s95, s95, 104
s_sub_u32 s89, s88, s95
s_add_u32 s88, s88, 92
s_cmp_gt_i32 s89, 746
s_cselect_b32 s68, 20, 0
s_add_u32 s88, s88, s68
s_sub_u32 s89, s88, s95
s_cmp_gt_i32 s89, 0
s_cselect_b32 s94, s88, s95
LpeA10_2_0:
s_waitcnt vmcnt(24)
s_barrier
s_add_u32 m0, s56, 0
)"
R"(s_nop 0
buffer_load_dwordx4 v248, s[20:23], s48 offen nt lds
buffer_load_dwordx4 v249, s[20:23], s48 offen offset:1024 nt lds
buffer_load_dwordx4 v250, s[20:23], s48 offen offset:2048 nt lds
s_add_u32 s48, s48, 12288
s_waitcnt vmcnt(25)
v_mfma_f32_16x16x32_bf16 v[0:3], v[184:187], v[160:163], v[0:3]
v_mfma_f32_16x16x32_bf16 v[40:43], v[184:187], v[164:167], v[40:43]
v_mfma_f32_16x16x32_bf16 v[44:47], v[188:191], v[164:167], v[44:47]
ds_read_b64_tr_b16 v[184:185], v234 offset:24832
ds_read_b64_tr_b16 v[186:187], v234 offset:30976
v_mfma_f32_16x16x32_bf16 v[4:7], v[188:191], v[160:163], v[4:7]
v_mfma_f32_16x16x32_bf16 v[8:11], v[192:195], v[160:163], v[8:11]
ds_read_b64_tr_b16 v[188:189], v235 offset:24832
ds_read_b64_tr_b16 v[190:191], v235 offset:30976
v_mfma_f32_16x16x32_bf16 v[48:51], v[192:195], v[164:167], v[48:51]
)"
R"(v_mfma_f32_16x16x32_bf16 v[52:55], v[196:199], v[164:167], v[52:55]
ds_read_b64_tr_b16 v[192:193], v234 offset:36864
ds_read_b64_tr_b16 v[194:195], v234 offset:43008
v_mfma_f32_16x16x32_bf16 v[12:15], v[196:199], v[160:163], v[12:15]
v_mfma_f32_16x16x32_bf16 v[16:19], v[200:203], v[160:163], v[16:19]
ds_read_b64_tr_b16 v[196:197], v235 offset:36864
ds_read_b64_tr_b16 v[198:199], v235 offset:43008
v_mfma_f32_16x16x32_bf16 v[56:59], v[200:203], v[164:167], v[56:59]
v_mfma_f32_16x16x32_bf16 v[60:63], v[204:207], v[164:167], v[60:63]
ds_read_b64_tr_b16 v[200:201], v236 offset:36864
ds_read_b64_tr_b16 v[202:203], v236 offset:43008
v_mfma_f32_16x16x32_bf16 v[20:23], v[204:207], v[160:163], v[20:23]
v_mfma_f32_16x16x32_bf16 v[24:27], v[208:211], v[160:163], v[24:27]
ds_read_b64_tr_b16 v[204:205], v237 offset:36864
ds_read_b64_tr_b16 v[206:207], v237 offset:43008
v_mfma_f32_16x16x32_bf16 v[64:67], v[208:211], v[164:167], v[64:67]
)"
R"(v_mfma_f32_16x16x32_bf16 v[68:71], v[212:215], v[164:167], v[68:71]
ds_read_b64_tr_b16 v[208:209], v234 offset:36992
ds_read_b64_tr_b16 v[210:211], v234 offset:43136
v_mfma_f32_16x16x32_bf16 v[28:31], v[212:215], v[160:163], v[28:31]
s_add_u32 m0, s56, 86016
s_nop 0
buffer_load_dwordx4 v251, s[24:27], s49 offen nt lds
ds_read_b64_tr_b16 v[212:213], v235 offset:36992
s_waitcnt lgkmcnt(11)
ds_read_b64_tr_b16 v[214:215], v235 offset:43136
ds_read_b64_tr_b16 v[216:217], v236 offset:36992
ds_read_b64_tr_b16 v[218:219], v236 offset:43136
buffer_load_dwordx4 v252, s[24:27], s49 offen offset:1024 nt lds
buffer_load_dwordx4 v253, s[24:27], s49 offen offset:2048 nt lds
s_add_u32 s49, s49, 12288
s_waitcnt lgkmcnt(0)
)"
R"(buffer_load_dwordx4 v[152:155], v247, s[40:43], s51 offen sc1
buffer_load_dwordx4 v[156:159], v247, s[40:43], s51 offen offset:1024 sc1
s_add_u32 s51, s51, 2048
s_cmp_eq_u32 s73, -1
s_cbranch_scc0 LpeA10_2_1
LplA10_2_1:
s_memrealtime s[88:89]
s_waitcnt lgkmcnt(0)
s_sub_u32 s89, s88, s94
s_cmp_lt_i32 s89, 0
s_cbranch_scc1 LplA10_2_1
s_add_u32 s95, s95, 104
s_sub_u32 s89, s88, s95
s_add_u32 s88, s88, 92
s_cmp_gt_i32 s89, 746
s_cselect_b32 s68, 20, 0
)"
R"(s_add_u32 s88, s88, s68
s_sub_u32 s89, s88, s95
s_cmp_gt_i32 s89, 0
s_cselect_b32 s94, s88, s95
LpeA10_2_1:
s_waitcnt vmcnt(21)
s_barrier
s_mul_i32 s50, s93, 245760
s_add_u32 s50, s50, s52
s_add_u32 m0, s56, 122880
s_nop 0
buffer_load_dwordx4 v244, s[32:35], s50 offen lds
buffer_load_dwordx4 v245, s[32:35], s50 offen offset:1024 lds
buffer_load_dwordx4 v246, s[32:35], s50 offen offset:2048 lds
s_add_u32 s50, s50, 40960
v_mfma_f32_16x16x32_bf16 v[32:35], v[184:187], v[160:163], v[32:35]
)"
R"(v_mfma_f32_16x16x32_bf16 v[72:75], v[184:187], v[164:167], v[72:75]
v_mfma_f32_16x16x32_bf16 v[76:79], v[188:191], v[164:167], v[76:79]
ds_read_b64_tr_b16 v[184:185], v237 offset:36992
ds_read_b64_tr_b16 v[186:187], v237 offset:43136
v_mfma_f32_16x16x32_bf16 v[36:39], v[188:191], v[160:163], v[36:39]
s_waitcnt vmcnt(22)
v_mfma_f32_16x16x32_bf16 v[0:3], v[192:195], v[168:171], v[0:3]
ds_read_b64_tr_b16 v[188:189], v234 offset:37120
ds_read_b64_tr_b16 v[190:191], v234 offset:43264
v_mfma_f32_16x16x32_bf16 v[40:43], v[192:195], v[172:175], v[40:43]
v_mfma_f32_16x16x32_bf16 v[44:47], v[196:199], v[172:175], v[44:47]
ds_read_b64_tr_b16 v[192:193], v235 offset:37120
ds_read_b64_tr_b16 v[194:195], v235 offset:43264
v_mfma_f32_16x16x32_bf16 v[4:7], v[196:199], v[168:171], v[4:7]
v_mfma_f32_16x16x32_bf16 v[8:11], v[200:203], v[168:171], v[8:11]
ds_read_b64_tr_b16 v[196:197], v234 offset:0
)"
R"(ds_read_b64_tr_b16 v[198:199], v234 offset:6144
v_mfma_f32_16x16x32_bf16 v[48:51], v[200:203], v[172:175], v[48:51]
v_mfma_f32_16x16x32_bf16 v[52:55], v[204:207], v[172:175], v[52:55]
ds_read_b64_tr_b16 v[200:201], v235 offset:0
ds_read_b64_tr_b16 v[202:203], v235 offset:6144
v_mfma_f32_16x16x32_bf16 v[12:15], v[204:207], v[168:171], v[12:15]
v_mfma_f32_16x16x32_bf16 v[16:19], v[208:211], v[168:171], v[16:19]
ds_read_b64_tr_b16 v[204:205], v236 offset:0
ds_read_b64_tr_b16 v[206:207], v236 offset:6144
v_mfma_f32_16x16x32_bf16 v[56:59], v[208:211], v[172:175], v[56:59]
v_mfma_f32_16x16x32_bf16 v[60:63], v[212:215], v[172:175], v[60:63]
ds_read_b64_tr_b16 v[208:209], v237 offset:0
ds_read_b64_tr_b16 v[210:211], v237 offset:6144
v_mfma_f32_16x16x32_bf16 v[20:23], v[212:215], v[168:171], v[20:23]
v_mfma_f32_16x16x32_bf16 v[24:27], v[216:219], v[168:171], v[24:27]
ds_read_b64_tr_b16 v[212:213], v234 offset:128
)"
R"(s_waitcnt lgkmcnt(11)
ds_read_b64_tr_b16 v[214:215], v234 offset:6272
v_mfma_f32_16x16x32_bf16 v[64:67], v[216:219], v[172:175], v[64:67]
s_add_u32 m0, s56, 12288
s_nop 0
buffer_load_dwordx4 v248, s[20:23], s48 offen nt lds
buffer_load_dwordx4 v249, s[20:23], s48 offen offset:1024 nt lds
buffer_load_dwordx4 v250, s[20:23], s48 offen offset:2048 nt lds
s_add_u32 s48, s48, 12288
s_add_u32 m0, s56, 49152
s_nop 0
buffer_load_dwordx4 v251, s[24:27], s49 offen nt lds
buffer_load_dwordx4 v252, s[24:27], s49 offen offset:1024 nt lds
buffer_load_dwordx4 v253, s[24:27], s49 offen offset:2048 nt lds
s_add_u32 s49, s49, 12288
s_waitcnt lgkmcnt(0)
)"
R"(s_mul_i32 s51, s93, 12288
s_add_u32 s51, s51, s54
buffer_load_dwordx4 v[160:163], v247, s[40:43], s51 offen sc1
buffer_load_dwordx4 v[164:167], v247, s[40:43], s51 offen offset:1024 sc1
s_add_u32 s51, s51, 2048
s_cmp_eq_u32 s73, -1
s_cbranch_scc0 LpeA10_2_2
LplA10_2_2:
s_memrealtime s[88:89]
s_waitcnt lgkmcnt(0)
s_sub_u32 s89, s88, s94
s_cmp_lt_i32 s89, 0
s_cbranch_scc1 LplA10_2_2
s_add_u32 s95, s95, 104
s_sub_u32 s89, s88, s95
s_add_u32 s88, s88, 92
)"
R"(s_cmp_gt_i32 s89, 746
s_cselect_b32 s68, 20, 0
s_add_u32 s88, s88, s68
s_sub_u32 s89, s88, s95
s_cmp_gt_i32 s89, 0
s_cselect_b32 s94, s88, s95
LpeA10_2_2:
s_waitcnt vmcnt(21)
s_barrier
s_cmp_eq_u32 s60, 0
s_cbranch_scc0 Lcpf2
s_getpc_b64 s[68:69]
Lcpc2:
s_sub_u32 s68, s68, Lcpc2-Lcode0
s_subb_u32 s69, s69, 0
s_and_b32 s69, s69, 0xffff
)"
R"(s_mov_b32 s70, Lcode1-Lcode0
s_mov_b32 s71, 0x20000
s_lshl_b32 s72, s66, 13
v_lshlrev_b32 v255, 3, v247
buffer_load_dword v255, v255, s[68:71], s72 offen
Lcpf2:
s_add_u32 m0, s56, 135168
s_nop 0
buffer_load_dwordx4 v244, s[32:35], s50 offen lds
buffer_load_dwordx4 v245, s[32:35], s50 offen offset:1024 lds
buffer_load_dwordx4 v246, s[32:35], s50 offen offset:2048 lds
s_add_u32 s50, s50, 40960
v_mfma_f32_16x16x32_bf16 v[68:71], v[184:187], v[172:175], v[68:71]
v_mfma_f32_16x16x32_bf16 v[28:31], v[184:187], v[168:171], v[28:31]
v_mfma_f32_16x16x32_bf16 v[32:35], v[188:191], v[168:171], v[32:35]
ds_read_b64_tr_b16 v[184:185], v235 offset:128
)"
R"(ds_read_b64_tr_b16 v[186:187], v235 offset:6272
v_mfma_f32_16x16x32_bf16 v[72:75], v[188:191], v[172:175], v[72:75]
v_mfma_f32_16x16x32_bf16 v[76:79], v[192:195], v[172:175], v[76:79]
ds_read_b64_tr_b16 v[188:189], v236 offset:128
ds_read_b64_tr_b16 v[190:191], v236 offset:6272
v_mfma_f32_16x16x32_bf16 v[36:39], v[192:195], v[168:171], v[36:39]
s_waitcnt vmcnt(22)
v_mfma_f32_16x16x32_bf16 v[0:3], v[196:199], v[144:147], v[0:3]
ds_read_b64_tr_b16 v[192:193], v237 offset:128
ds_read_b64_tr_b16 v[194:195], v237 offset:6272
v_mfma_f32_16x16x32_bf16 v[40:43], v[196:199], v[148:151], v[40:43]
v_mfma_f32_16x16x32_bf16 v[44:47], v[200:203], v[148:151], v[44:47]
ds_read_b64_tr_b16 v[196:197], v234 offset:256
ds_read_b64_tr_b16 v[198:199], v234 offset:6400
v_mfma_f32_16x16x32_bf16 v[4:7], v[200:203], v[144:147], v[4:7]
v_mfma_f32_16x16x32_bf16 v[8:11], v[204:207], v[144:147], v[8:11]
)"
R"(ds_read_b64_tr_b16 v[200:201], v235 offset:256
ds_read_b64_tr_b16 v[202:203], v235 offset:6400
v_mfma_f32_16x16x32_bf16 v[48:51], v[204:207], v[148:151], v[48:51]
v_mfma_f32_16x16x32_bf16 v[52:55], v[208:211], v[148:151], v[52:55]
ds_read_b64_tr_b16 v[204:205], v234 offset:12288
ds_read_b64_tr_b16 v[206:207], v234 offset:18432
v_mfma_f32_16x16x32_bf16 v[12:15], v[208:211], v[144:147], v[12:15]
v_mfma_f32_16x16x32_bf16 v[16:19], v[212:215], v[144:147], v[16:19]
ds_read_b64_tr_b16 v[208:209], v235 offset:12288
ds_read_b64_tr_b16 v[210:211], v235 offset:18432
v_mfma_f32_16x16x32_bf16 v[56:59], v[212:215], v[148:151], v[56:59]
s_add_u32 m0, s56, 24576
s_nop 0
buffer_load_dwordx4 v248, s[20:23], s48 offen nt lds
buffer_load_dwordx4 v249, s[20:23], s48 offen offset:1024 nt lds
buffer_load_dwordx4 v250, s[20:23], s48 offen offset:2048 nt lds
)"
R"(s_add_u32 s48, s48, 12288
ds_read_b64_tr_b16 v[212:213], v236 offset:12288
s_waitcnt lgkmcnt(11)
ds_read_b64_tr_b16 v[214:215], v236 offset:18432
s_add_u32 m0, s56, 61440
s_nop 0
buffer_load_dwordx4 v251, s[24:27], s49 offen nt lds
buffer_load_dwordx4 v252, s[24:27], s49 offen offset:1024 nt lds
buffer_load_dwordx4 v253, s[24:27], s49 offen offset:2048 nt lds
s_add_u32 s49, s49, 12288
s_waitcnt lgkmcnt(0)
s_cmp_eq_u32 s73, -1
s_cbranch_scc0 LpeA10_2_3
LplA10_2_3:
s_memrealtime s[88:89]
s_waitcnt lgkmcnt(0)
)"
R"(s_sub_u32 s89, s88, s94
s_cmp_lt_i32 s89, 0
s_cbranch_scc1 LplA10_2_3
s_add_u32 s95, s95, 104
s_sub_u32 s89, s88, s95
s_add_u32 s88, s88, 92
s_cmp_gt_i32 s89, 746
s_cselect_b32 s68, 20, 0
s_add_u32 s88, s88, s68
s_sub_u32 s89, s88, s95
s_cmp_gt_i32 s89, 0
s_cselect_b32 s94, s88, s95
LpeA10_2_3:
s_waitcnt vmcnt(17)
s_barrier
s_add_u32 m0, s56, 98304
)"
R"(s_nop 0
buffer_load_dwordx4 v244, s[32:35], s50 offen lds
buffer_load_dwordx4 v245, s[32:35], s50 offen offset:1024 lds
buffer_load_dwordx4 v246, s[32:35], s50 offen offset:2048 lds
s_add_u32 s50, s50, 40960
v_mfma_f32_16x16x32_bf16 v[60:63], v[184:187], v[148:151], v[60:63]
v_mfma_f32_16x16x32_bf16 v[20:23], v[184:187], v[144:147], v[20:23]
v_mfma_f32_16x16x32_bf16 v[24:27], v[188:191], v[144:147], v[24:27]
ds_read_b64_tr_b16 v[184:185], v237 offset:12288
ds_read_b64_tr_b16 v[186:187], v237 offset:18432
v_mfma_f32_16x16x32_bf16 v[64:67], v[188:191], v[148:151], v[64:67]
v_mfma_f32_16x16x32_bf16 v[68:71], v[192:195], v[148:151], v[68:71]
ds_read_b64_tr_b16 v[188:189], v234 offset:12416
ds_read_b64_tr_b16 v[190:191], v234 offset:18560
v_mfma_f32_16x16x32_bf16 v[28:31], v[192:195], v[144:147], v[28:31]
v_mfma_f32_16x16x32_bf16 v[32:35], v[196:199], v[144:147], v[32:35]
)"
R"(ds_read_b64_tr_b16 v[192:193], v235 offset:12416
ds_read_b64_tr_b16 v[194:195], v235 offset:18560
v_mfma_f32_16x16x32_bf16 v[72:75], v[196:199], v[148:151], v[72:75]
v_mfma_f32_16x16x32_bf16 v[76:79], v[200:203], v[148:151], v[76:79]
ds_read_b64_tr_b16 v[196:197], v236 offset:12416
ds_read_b64_tr_b16 v[198:199], v236 offset:18560
v_mfma_f32_16x16x32_bf16 v[36:39], v[200:203], v[144:147], v[36:39]
v_mfma_f32_16x16x32_bf16 v[0:3], v[204:207], v[152:155], v[0:3]
ds_read_b64_tr_b16 v[200:201], v237 offset:12416
ds_read_b64_tr_b16 v[202:203], v237 offset:18560
v_mfma_f32_16x16x32_bf16 v[40:43], v[204:207], v[156:159], v[40:43]
v_mfma_f32_16x16x32_bf16 v[44:47], v[208:211], v[156:159], v[44:47]
ds_read_b64_tr_b16 v[204:205], v234 offset:12544
ds_read_b64_tr_b16 v[206:207], v234 offset:18688
v_mfma_f32_16x16x32_bf16 v[4:7], v[208:211], v[152:155], v[4:7]
v_mfma_f32_16x16x32_bf16 v[8:11], v[212:215], v[152:155], v[8:11]
)"
R"(ds_read_b64_tr_b16 v[208:209], v235 offset:12544
ds_read_b64_tr_b16 v[210:211], v235 offset:18688
v_mfma_f32_16x16x32_bf16 v[48:51], v[212:215], v[156:159], v[48:51]
s_add_u32 m0, s56, 36864
s_nop 0
buffer_load_dwordx4 v248, s[20:23], s48 offen nt lds
buffer_load_dwordx4 v249, s[20:23], s48 offen offset:1024 nt lds
buffer_load_dwordx4 v250, s[20:23], s48 offen offset:2048 nt lds
s_add_u32 s48, s48, 12288
ds_read_b64_tr_b16 v[212:213], v234 offset:24576
s_waitcnt lgkmcnt(11)
ds_read_b64_tr_b16 v[214:215], v234 offset:30720
ds_read_b64_tr_b16 v[216:217], v235 offset:24576
ds_read_b64_tr_b16 v[218:219], v235 offset:30720
s_add_u32 m0, s56, 73728
s_nop 0
)"
R"(buffer_load_dwordx4 v251, s[24:27], s49 offen nt lds
buffer_load_dwordx4 v252, s[24:27], s49 offen offset:1024 nt lds
buffer_load_dwordx4 v253, s[24:27], s49 offen offset:2048 nt lds
s_add_u32 s49, s49, 12288
s_waitcnt lgkmcnt(0)
buffer_load_dwordx4 v[168:171], v247, s[40:43], s51 offen sc1
buffer_load_dwordx4 v[172:175], v247, s[40:43], s51 offen offset:1024 sc1
s_add_u32 s51, s51, 2048
s_cmp_eq_u32 s73, -1
s_cbranch_scc0 LpeA11_2_0
LplA11_2_0:
s_memrealtime s[88:89]
s_waitcnt lgkmcnt(0)
s_sub_u32 s89, s88, s94
s_cmp_lt_i32 s89, 0
s_cbranch_scc1 LplA11_2_0
)"
R"(s_add_u32 s95, s95, 104
s_sub_u32 s89, s88, s95
s_add_u32 s88, s88, 92
s_cmp_gt_i32 s89, 746
s_cselect_b32 s68, 20, 0
s_add_u32 s88, s88, s68
s_sub_u32 s89, s88, s95
s_cmp_gt_i32 s89, 0
s_cselect_b32 s94, s88, s95
LpeA11_2_0:
s_waitcnt vmcnt(22)
s_barrier
s_add_u32 m0, s56, 110592
s_nop 0
buffer_load_dwordx4 v244, s[32:35], s50 offen lds
buffer_load_dwordx4 v245, s[32:35], s50 offen offset:1024 lds
)"
R"(buffer_load_dwordx4 v246, s[32:35], s50 offen offset:2048 lds
s_add_u32 s50, s50, 40960
v_mfma_f32_16x16x32_bf16 v[52:55], v[184:187], v[156:159], v[52:55]
v_mfma_f32_16x16x32_bf16 v[12:15], v[184:187], v[152:155], v[12:15]
v_mfma_f32_16x16x32_bf16 v[16:19], v[188:191], v[152:155], v[16:19]
ds_read_b64_tr_b16 v[184:185], v236 offset:24576
ds_read_b64_tr_b16 v[186:187], v236 offset:30720
v_mfma_f32_16x16x32_bf16 v[56:59], v[188:191], v[156:159], v[56:59]
v_mfma_f32_16x16x32_bf16 v[60:63], v[192:195], v[156:159], v[60:63]
ds_read_b64_tr_b16 v[188:189], v237 offset:24576
ds_read_b64_tr_b16 v[190:191], v237 offset:30720
v_mfma_f32_16x16x32_bf16 v[20:23], v[192:195], v[152:155], v[20:23]
v_mfma_f32_16x16x32_bf16 v[24:27], v[196:199], v[152:155], v[24:27]
ds_read_b64_tr_b16 v[192:193], v234 offset:24704
ds_read_b64_tr_b16 v[194:195], v234 offset:30848
v_mfma_f32_16x16x32_bf16 v[64:67], v[196:199], v[156:159], v[64:67]
)"
R"(v_mfma_f32_16x16x32_bf16 v[68:71], v[200:203], v[156:159], v[68:71]
ds_read_b64_tr_b16 v[196:197], v235 offset:24704
ds_read_b64_tr_b16 v[198:199], v235 offset:30848
v_mfma_f32_16x16x32_bf16 v[28:31], v[200:203], v[152:155], v[28:31]
v_mfma_f32_16x16x32_bf16 v[32:35], v[204:207], v[152:155], v[32:35]
ds_read_b64_tr_b16 v[200:201], v236 offset:24704
ds_read_b64_tr_b16 v[202:203], v236 offset:30848
v_mfma_f32_16x16x32_bf16 v[72:75], v[204:207], v[156:159], v[72:75]
v_mfma_f32_16x16x32_bf16 v[76:79], v[208:211], v[156:159], v[76:79]
ds_read_b64_tr_b16 v[204:205], v237 offset:24704
ds_read_b64_tr_b16 v[206:207], v237 offset:30848
v_mfma_f32_16x16x32_bf16 v[36:39], v[208:211], v[152:155], v[36:39]
s_waitcnt vmcnt(23)
v_mfma_f32_16x16x32_bf16 v[0:3], v[212:215], v[160:163], v[0:3]
ds_read_b64_tr_b16 v[208:209], v234 offset:24832
ds_read_b64_tr_b16 v[210:211], v234 offset:30976
)"
R"(v_mfma_f32_16x16x32_bf16 v[40:43], v[212:215], v[164:167], v[40:43]
v_mfma_f32_16x16x32_bf16 v[44:47], v[216:219], v[164:167], v[44:47]
ds_read_b64_tr_b16 v[212:213], v235 offset:24832
s_waitcnt lgkmcnt(11)
ds_read_b64_tr_b16 v[214:215], v235 offset:30976
v_mfma_f32_16x16x32_bf16 v[4:7], v[216:219], v[160:163], v[4:7]
s_add_u32 m0, s56, 0
s_nop 0
buffer_load_dwordx4 v248, s[20:23], s48 offen nt lds
buffer_load_dwordx4 v249, s[20:23], s48 offen offset:1024 nt lds
buffer_load_dwordx4 v250, s[20:23], s48 offen offset:2048 nt lds
s_add_u32 s48, s48, 12288
s_add_u32 m0, s56, 86016
s_nop 0
buffer_load_dwordx4 v251, s[24:27], s49 offen nt lds
buffer_load_dwordx4 v252, s[24:27], s49 offen offset:1024 nt lds
)"
R"(buffer_load_dwordx4 v253, s[24:27], s49 offen offset:2048 nt lds
s_add_u32 s49, s49, 12288
s_waitcnt lgkmcnt(0)
buffer_load_dwordx4 v[144:147], v247, s[40:43], s51 offen sc1
buffer_load_dwordx4 v[148:151], v247, s[40:43], s51 offen offset:1024 sc1
s_add_u32 s51, s51, 2048
s_cmp_eq_u32 s73, -1
s_cbranch_scc0 LpeA11_2_1
LplA11_2_1:
s_memrealtime s[88:89]
s_waitcnt lgkmcnt(0)
s_sub_u32 s89, s88, s94
s_cmp_lt_i32 s89, 0
s_cbranch_scc1 LplA11_2_1
s_add_u32 s95, s95, 104
s_sub_u32 s89, s88, s95
)"
R"(s_add_u32 s88, s88, 92
s_cmp_gt_i32 s89, 746
s_cselect_b32 s68, 20, 0
s_add_u32 s88, s88, s68
s_sub_u32 s89, s88, s95
s_cmp_gt_i32 s89, 0
s_cselect_b32 s94, s88, s95
LpeA11_2_1:
s_waitcnt vmcnt(22)
s_barrier
s_add_u32 m0, s56, 122880
s_nop 0
buffer_load_dwordx4 v244, s[32:35], s50 offen lds
buffer_load_dwordx4 v245, s[32:35], s50 offen offset:1024 lds
buffer_load_dwordx4 v246, s[32:35], s50 offen offset:2048 lds
s_add_u32 s50, s50, 40960
)"
R"(v_mfma_f32_16x16x32_bf16 v[8:11], v[184:187], v[160:163], v[8:11]
v_mfma_f32_16x16x32_bf16 v[48:51], v[184:187], v[164:167], v[48:51]
v_mfma_f32_16x16x32_bf16 v[52:55], v[188:191], v[164:167], v[52:55]
ds_read_b64_tr_b16 v[184:185], v234 offset:36864
ds_read_b64_tr_b16 v[186:187], v234 offset:43008
v_mfma_f32_16x16x32_bf16 v[12:15], v[188:191], v[160:163], v[12:15]
v_mfma_f32_16x16x32_bf16 v[16:19], v[192:195], v[160:163], v[16:19]
ds_read_b64_tr_b16 v[188:189], v235 offset:36864
ds_read_b64_tr_b16 v[190:191], v235 offset:43008
v_mfma_f32_16x16x32_bf16 v[56:59], v[192:195], v[164:167], v[56:59]
v_mfma_f32_16x16x32_bf16 v[60:63], v[196:199], v[164:167], v[60:63]
ds_read_b64_tr_b16 v[192:193], v236 offset:36864
ds_read_b64_tr_b16 v[194:195], v236 offset:43008
v_mfma_f32_16x16x32_bf16 v[20:23], v[196:199], v[160:163], v[20:23]
v_mfma_f32_16x16x32_bf16 v[24:27], v[200:203], v[160:163], v[24:27]
ds_read_b64_tr_b16 v[196:197], v237 offset:36864
)"
R"(ds_read_b64_tr_b16 v[198:199], v237 offset:43008
v_mfma_f32_16x16x32_bf16 v[64:67], v[200:203], v[164:167], v[64:67]
v_mfma_f32_16x16x32_bf16 v[68:71], v[204:207], v[164:167], v[68:71]
ds_read_b64_tr_b16 v[200:201], v234 offset:36992
ds_read_b64_tr_b16 v[202:203], v234 offset:43136
v_mfma_f32_16x16x32_bf16 v[28:31], v[204:207], v[160:163], v[28:31]
v_mfma_f32_16x16x32_bf16 v[32:35], v[208:211], v[160:163], v[32:35]
ds_read_b64_tr_b16 v[204:205], v235 offset:36992
ds_read_b64_tr_b16 v[206:207], v235 offset:43136
v_mfma_f32_16x16x32_bf16 v[72:75], v[208:211], v[164:167], v[72:75]
v_mfma_f32_16x16x32_bf16 v[76:79], v[212:215], v[164:167], v[76:79]
ds_read_b64_tr_b16 v[208:209], v236 offset:36992
ds_read_b64_tr_b16 v[210:211], v236 offset:43136
v_mfma_f32_16x16x32_bf16 v[36:39], v[212:215], v[160:163], v[36:39]
s_add_u32 m0, s56, 12288
s_nop 0
)"
R"(buffer_load_dwordx4 v248, s[20:23], s48 offen nt lds
buffer_load_dwordx4 v249, s[20:23], s48 offen offset:1024 nt lds
buffer_load_dwordx4 v250, s[20:23], s48 offen offset:2048 nt lds
s_add_u32 s48, s48, 12288
ds_read_b64_tr_b16 v[212:213], v237 offset:36992
s_waitcnt lgkmcnt(11)
ds_read_b64_tr_b16 v[214:215], v237 offset:43136
s_add_u32 m0, s56, 49152
s_nop 0
buffer_load_dwordx4 v251, s[24:27], s49 offen nt lds
buffer_load_dwordx4 v252, s[24:27], s49 offen offset:1024 nt lds
buffer_load_dwordx4 v253, s[24:27], s49 offen offset:2048 nt lds
s_add_u32 s49, s49, 12288
s_waitcnt lgkmcnt(0)
buffer_load_dwordx4 v[152:155], v247, s[40:43], s51 offen sc1
buffer_load_dwordx4 v[156:159], v247, s[40:43], s51 offen offset:1024 sc1
)"
R"(s_add_u32 s51, s51, 2048
s_cmp_eq_u32 s73, -1
s_cbranch_scc0 LpeA11_2_2
LplA11_2_2:
s_memrealtime s[88:89]
s_waitcnt lgkmcnt(0)
s_sub_u32 s89, s88, s94
s_cmp_lt_i32 s89, 0
s_cbranch_scc1 LplA11_2_2
s_add_u32 s95, s95, 104
s_sub_u32 s89, s88, s95
s_add_u32 s88, s88, 92
s_cmp_gt_i32 s89, 746
s_cselect_b32 s68, 20, 0
s_add_u32 s88, s88, s68
s_sub_u32 s89, s88, s95
)"
R"(s_cmp_gt_i32 s89, 0
s_cselect_b32 s94, s88, s95
LpeA11_2_2:
s_waitcnt vmcnt(24)
s_barrier
s_add_u32 m0, s56, 24576
s_nop 0
buffer_load_dwordx4 v248, s[20:23], s48 offen nt lds
buffer_load_dwordx4 v249, s[20:23], s48 offen offset:1024 nt lds
buffer_load_dwordx4 v250, s[20:23], s48 offen offset:2048 nt lds
s_add_u32 s48, s48, 12288
s_waitcnt vmcnt(25)
v_mfma_f32_16x16x32_bf16 v[0:3], v[184:187], v[168:171], v[0:3]
v_mfma_f32_16x16x32_bf16 v[40:43], v[184:187], v[172:175], v[40:43]
v_mfma_f32_16x16x32_bf16 v[44:47], v[188:191], v[172:175], v[44:47]
ds_read_b64_tr_b16 v[184:185], v234 offset:37120
)"
R"(ds_read_b64_tr_b16 v[186:187], v234 offset:43264
v_mfma_f32_16x16x32_bf16 v[4:7], v[188:191], v[168:171], v[4:7]
v_mfma_f32_16x16x32_bf16 v[8:11], v[192:195], v[168:171], v[8:11]
ds_read_b64_tr_b16 v[188:189], v235 offset:37120
ds_read_b64_tr_b16 v[190:191], v235 offset:43264
v_mfma_f32_16x16x32_bf16 v[48:51], v[192:195], v[172:175], v[48:51]
v_mfma_f32_16x16x32_bf16 v[52:55], v[196:199], v[172:175], v[52:55]
ds_read_b64_tr_b16 v[192:193], v234 offset:0
ds_read_b64_tr_b16 v[194:195], v234 offset:6144
v_mfma_f32_16x16x32_bf16 v[12:15], v[196:199], v[168:171], v[12:15]
v_mfma_f32_16x16x32_bf16 v[16:19], v[200:203], v[168:171], v[16:19]
ds_read_b64_tr_b16 v[196:197], v235 offset:0
ds_read_b64_tr_b16 v[198:199], v235 offset:6144
v_mfma_f32_16x16x32_bf16 v[56:59], v[200:203], v[172:175], v[56:59]
v_mfma_f32_16x16x32_bf16 v[60:63], v[204:207], v[172:175], v[60:63]
ds_read_b64_tr_b16 v[200:201], v236 offset:0
)"
R"(ds_read_b64_tr_b16 v[202:203], v236 offset:6144
v_mfma_f32_16x16x32_bf16 v[20:23], v[204:207], v[168:171], v[20:23]
v_mfma_f32_16x16x32_bf16 v[24:27], v[208:211], v[168:171], v[24:27]
ds_read_b64_tr_b16 v[204:205], v237 offset:0
ds_read_b64_tr_b16 v[206:207], v237 offset:6144
v_mfma_f32_16x16x32_bf16 v[64:67], v[208:211], v[172:175], v[64:67]
v_mfma_f32_16x16x32_bf16 v[68:71], v[212:215], v[172:175], v[68:71]
ds_read_b64_tr_b16 v[208:209], v234 offset:128
ds_read_b64_tr_b16 v[210:211], v234 offset:6272
v_mfma_f32_16x16x32_bf16 v[28:31], v[212:215], v[168:171], v[28:31]
s_add_u32 m0, s56, 61440
s_nop 0
buffer_load_dwordx4 v251, s[24:27], s49 offen nt lds
ds_read_b64_tr_b16 v[212:213], v235 offset:128
s_waitcnt lgkmcnt(11)
ds_read_b64_tr_b16 v[214:215], v235 offset:6272
)"
R"(buffer_load_dwordx4 v252, s[24:27], s49 offen offset:1024 nt lds
buffer_load_dwordx4 v253, s[24:27], s49 offen offset:2048 nt lds
s_add_u32 s49, s49, 12288
s_waitcnt lgkmcnt(0)
buffer_load_dwordx4 v[160:163], v247, s[40:43], s51 offen sc1
buffer_load_dwordx4 v[164:167], v247, s[40:43], s51 offen offset:1024 sc1
s_add_u32 s51, s51, 2048
s_cmp_eq_u32 s73, -1
s_cbranch_scc0 LpeA11_2_3
LplA11_2_3:
s_memrealtime s[88:89]
s_waitcnt lgkmcnt(0)
s_sub_u32 s89, s88, s94
s_cmp_lt_i32 s89, 0
s_cbranch_scc1 LplA11_2_3
s_add_u32 s95, s95, 104
)"
R"(s_sub_u32 s89, s88, s95
s_add_u32 s88, s88, 92
s_cmp_gt_i32 s89, 746
s_cselect_b32 s68, 20, 0
s_add_u32 s88, s88, s68
s_sub_u32 s89, s88, s95
s_cmp_gt_i32 s89, 0
s_cselect_b32 s94, s88, s95
LpeA11_2_3:
s_waitcnt vmcnt(21)
s_barrier
s_add_u32 m0, s56, 135168
s_nop 0
buffer_load_dwordx4 v244, s[32:35], s50 offen lds
buffer_load_dwordx4 v245, s[32:35], s50 offen offset:1024 lds
buffer_load_dwordx4 v246, s[32:35], s50 offen offset:2048 lds
)"
R"(s_add_u32 s50, s50, 40960
v_mfma_f32_16x16x32_bf16 v[32:35], v[184:187], v[168:171], v[32:35]
v_mfma_f32_16x16x32_bf16 v[72:75], v[184:187], v[172:175], v[72:75]
v_mfma_f32_16x16x32_bf16 v[76:79], v[188:191], v[172:175], v[76:79]
ds_read_b64_tr_b16 v[184:185], v236 offset:128
ds_read_b64_tr_b16 v[186:187], v236 offset:6272
v_mfma_f32_16x16x32_bf16 v[36:39], v[188:191], v[168:171], v[36:39]
s_waitcnt vmcnt(22)
v_mfma_f32_16x16x32_bf16 v[0:3], v[192:195], v[144:147], v[0:3]
ds_read_b64_tr_b16 v[188:189], v237 offset:128
ds_read_b64_tr_b16 v[190:191], v237 offset:6272
v_mfma_f32_16x16x32_bf16 v[40:43], v[192:195], v[148:151], v[40:43]
v_mfma_f32_16x16x32_bf16 v[44:47], v[196:199], v[148:151], v[44:47]
ds_read_b64_tr_b16 v[192:193], v234 offset:256
ds_read_b64_tr_b16 v[194:195], v234 offset:6400
v_mfma_f32_16x16x32_bf16 v[4:7], v[196:199], v[144:147], v[4:7]
)"
R"(v_mfma_f32_16x16x32_bf16 v[8:11], v[200:203], v[144:147], v[8:11]
ds_read_b64_tr_b16 v[196:197], v235 offset:256
ds_read_b64_tr_b16 v[198:199], v235 offset:6400
v_mfma_f32_16x16x32_bf16 v[48:51], v[200:203], v[148:151], v[48:51]
v_mfma_f32_16x16x32_bf16 v[52:55], v[204:207], v[148:151], v[52:55]
ds_read_b64_tr_b16 v[200:201], v234 offset:12288
ds_read_b64_tr_b16 v[202:203], v234 offset:18432
v_mfma_f32_16x16x32_bf16 v[12:15], v[204:207], v[144:147], v[12:15]
v_mfma_f32_16x16x32_bf16 v[16:19], v[208:211], v[144:147], v[16:19]
ds_read_b64_tr_b16 v[204:205], v235 offset:12288
ds_read_b64_tr_b16 v[206:207], v235 offset:18432
v_mfma_f32_16x16x32_bf16 v[56:59], v[208:211], v[148:151], v[56:59]
v_mfma_f32_16x16x32_bf16 v[60:63], v[212:215], v[148:151], v[60:63]
ds_read_b64_tr_b16 v[208:209], v236 offset:12288
ds_read_b64_tr_b16 v[210:211], v236 offset:18432
v_mfma_f32_16x16x32_bf16 v[20:23], v[212:215], v[144:147], v[20:23]
)"
R"(s_add_u32 m0, s56, 36864
s_nop 0
buffer_load_dwordx4 v248, s[20:23], s48 offen nt lds
buffer_load_dwordx4 v249, s[20:23], s48 offen offset:1024 nt lds
buffer_load_dwordx4 v250, s[20:23], s48 offen offset:2048 nt lds
s_add_u32 s48, s48, 12288
ds_read_b64_tr_b16 v[212:213], v237 offset:12288
s_waitcnt lgkmcnt(11)
ds_read_b64_tr_b16 v[214:215], v237 offset:18432
ds_read_b64_tr_b16 v[216:217], v234 offset:12416
ds_read_b64_tr_b16 v[218:219], v234 offset:18560
s_add_u32 m0, s56, 73728
s_nop 0
buffer_load_dwordx4 v251, s[24:27], s49 offen nt lds
buffer_load_dwordx4 v252, s[24:27], s49 offen offset:1024 nt lds
buffer_load_dwordx4 v253, s[24:27], s49 offen offset:2048 nt lds
)"
R"(s_add_u32 s49, s49, 12288
s_waitcnt lgkmcnt(0)
v_lshrrev_b32 v176, 4, v247
v_lshlrev_b32 v177, 7, v176
v_cmp_gt_u32 vcc, 60, v176
s_mul_i32 s55, s66, 30720
s_mul_i32 s68, s64, 1966080
s_add_u32 s55, s55, s68
s_add_u32 s55, s55, 983040
v_mov_b32 v178, 0x80000000
v_cndmask_b32 v176, v178, v177, vcc
s_cmp_eq_u32 s65, 1
s_cselect_b64 vcc, -1, 0
s_cmp_eq_u32 s60, 3
s_cbranch_scc0 2
buffer_load_dword v177, v176, s[32:35], s55 offen
)"
R"(s_add_u32 s55, s55, 7680
buffer_load_dwordx4 v[168:171], v247, s[40:43], s51 offen sc1
buffer_load_dwordx4 v[172:175], v247, s[40:43], s51 offen offset:1024 sc1
s_add_u32 s51, s51, 2048
s_cmp_eq_u32 s73, -1
s_cbranch_scc0 LpeA12_2_0
LplA12_2_0:
s_memrealtime s[88:89]
s_waitcnt lgkmcnt(0)
s_sub_u32 s89, s88, s94
s_cmp_lt_i32 s89, 0
s_cbranch_scc1 LplA12_2_0
s_add_u32 s95, s95, 104
s_sub_u32 s89, s88, s95
s_add_u32 s88, s88, 92
s_cmp_gt_i32 s89, 746
)"
R"(s_cselect_b32 s68, 20, 0
s_add_u32 s88, s88, s68
s_sub_u32 s89, s88, s95
s_cmp_gt_i32 s89, 0
s_cselect_b32 s94, s88, s95
LpeA12_2_0:
s_waitcnt vmcnt(21)
s_barrier
s_add_u32 m0, s56, 86016
s_nop 0
buffer_load_dwordx4 v251, s[24:27], s49 offen nt lds
buffer_load_dwordx4 v252, s[24:27], s49 offen offset:1024 nt lds
buffer_load_dwordx4 v253, s[24:27], s49 offen offset:2048 nt lds
s_add_u32 s49, s49, 12288
v_mfma_f32_16x16x32_bf16 v[24:27], v[184:187], v[144:147], v[24:27]
v_mfma_f32_16x16x32_bf16 v[64:67], v[184:187], v[148:151], v[64:67]
)"
R"(v_mfma_f32_16x16x32_bf16 v[68:71], v[188:191], v[148:151], v[68:71]
ds_read_b64_tr_b16 v[184:185], v235 offset:12416
ds_read_b64_tr_b16 v[186:187], v235 offset:18560
v_mfma_f32_16x16x32_bf16 v[28:31], v[188:191], v[144:147], v[28:31]
v_mfma_f32_16x16x32_bf16 v[32:35], v[192:195], v[144:147], v[32:35]
ds_read_b64_tr_b16 v[188:189], v236 offset:12416
ds_read_b64_tr_b16 v[190:191], v236 offset:18560
v_mfma_f32_16x16x32_bf16 v[72:75], v[192:195], v[148:151], v[72:75]
v_mfma_f32_16x16x32_bf16 v[76:79], v[196:199], v[148:151], v[76:79]
ds_read_b64_tr_b16 v[192:193], v237 offset:12416
ds_read_b64_tr_b16 v[194:195], v237 offset:18560
v_mfma_f32_16x16x32_bf16 v[36:39], v[196:199], v[144:147], v[36:39]
s_waitcnt vmcnt(22)
v_mfma_f32_16x16x32_bf16 v[0:3], v[200:203], v[152:155], v[0:3]
ds_read_b64_tr_b16 v[196:197], v234 offset:12544
ds_read_b64_tr_b16 v[198:199], v234 offset:18688
)"
R"(v_mfma_f32_16x16x32_bf16 v[40:43], v[200:203], v[156:159], v[40:43]
v_mfma_f32_16x16x32_bf16 v[44:47], v[204:207], v[156:159], v[44:47]
ds_read_b64_tr_b16 v[200:201], v235 offset:12544
ds_read_b64_tr_b16 v[202:203], v235 offset:18688
v_mfma_f32_16x16x32_bf16 v[4:7], v[204:207], v[152:155], v[4:7]
v_mfma_f32_16x16x32_bf16 v[8:11], v[208:211], v[152:155], v[8:11]
ds_read_b64_tr_b16 v[204:205], v234 offset:24576
ds_read_b64_tr_b16 v[206:207], v234 offset:30720
v_mfma_f32_16x16x32_bf16 v[48:51], v[208:211], v[156:159], v[48:51]
v_mfma_f32_16x16x32_bf16 v[52:55], v[212:215], v[156:159], v[52:55]
ds_read_b64_tr_b16 v[208:209], v235 offset:24576
ds_read_b64_tr_b16 v[210:211], v235 offset:30720
v_mfma_f32_16x16x32_bf16 v[12:15], v[212:215], v[152:155], v[12:15]
v_mfma_f32_16x16x32_bf16 v[16:19], v[216:219], v[152:155], v[16:19]
ds_read_b64_tr_b16 v[212:213], v236 offset:24576
s_waitcnt lgkmcnt(11)
)"
R"(ds_read_b64_tr_b16 v[214:215], v236 offset:30720
v_mfma_f32_16x16x32_bf16 v[56:59], v[216:219], v[156:159], v[56:59]
s_mul_i32 s50, s96, 245760
s_add_u32 s50, s50, s52
s_add_u32 s50, s50, 983040
s_add_u32 m0, s56, 147456
s_nop 0
buffer_load_dwordx4 v244, s[32:35], s50 offen lds
buffer_load_dwordx4 v245, s[32:35], s50 offen offset:1024 lds
buffer_load_dwordx4 v246, s[32:35], s50 offen offset:2048 lds
s_add_u32 s50, s50, 40960
s_add_u32 m0, s56, 0
s_nop 0
buffer_load_dwordx4 v244, s[32:35], s50 offen lds
buffer_load_dwordx4 v245, s[32:35], s50 offen offset:1024 lds
buffer_load_dwordx4 v246, s[32:35], s50 offen offset:2048 lds
)"
R"(s_add_u32 s50, s50, 40960
s_waitcnt lgkmcnt(0)
s_cmp_eq_u32 s60, 0
s_cbranch_scc0 2
buffer_load_dword v177, v176, s[32:35], s55 offen
s_add_u32 s55, s55, 7680
s_cmp_eq_u32 s73, -1
s_cbranch_scc0 LpeA12_2_1
LplA12_2_1:
s_memrealtime s[88:89]
s_waitcnt lgkmcnt(0)
s_sub_u32 s89, s88, s94
s_cmp_lt_i32 s89, 0
s_cbranch_scc1 LplA12_2_1
s_add_u32 s95, s95, 75
s_add_u32 s88, s88, 75
)"
R"(s_sub_u32 s89, s88, s95
s_cmp_gt_i32 s89, 0
s_cselect_b32 s94, s88, s95
LpeA12_2_1:
s_waitcnt vmcnt(17)
s_barrier
s_add_u32 m0, s56, 12288
s_nop 0
buffer_load_dwordx4 v244, s[32:35], s50 offen lds
buffer_load_dwordx4 v245, s[32:35], s50 offen offset:1024 lds
buffer_load_dwordx4 v246, s[32:35], s50 offen offset:2048 lds
s_add_u32 s50, s50, 40960
v_mfma_f32_16x16x32_bf16 v[60:63], v[184:187], v[156:159], v[60:63]
v_mfma_f32_16x16x32_bf16 v[20:23], v[184:187], v[152:155], v[20:23]
v_mfma_f32_16x16x32_bf16 v[24:27], v[188:191], v[152:155], v[24:27]
ds_read_b64_tr_b16 v[184:185], v237 offset:24576
)"
R"(ds_read_b64_tr_b16 v[186:187], v237 offset:30720
v_mfma_f32_16x16x32_bf16 v[64:67], v[188:191], v[156:159], v[64:67]
v_mfma_f32_16x16x32_bf16 v[68:71], v[192:195], v[156:159], v[68:71]
ds_read_b64_tr_b16 v[188:189], v234 offset:24704
ds_read_b64_tr_b16 v[190:191], v234 offset:30848
v_mfma_f32_16x16x32_bf16 v[28:31], v[192:195], v[152:155], v[28:31]
v_mfma_f32_16x16x32_bf16 v[32:35], v[196:199], v[152:155], v[32:35]
ds_read_b64_tr_b16 v[192:193], v235 offset:24704
ds_read_b64_tr_b16 v[194:195], v235 offset:30848
v_mfma_f32_16x16x32_bf16 v[72:75], v[196:199], v[156:159], v[72:75]
v_mfma_f32_16x16x32_bf16 v[76:79], v[200:203], v[156:159], v[76:79]
ds_read_b64_tr_b16 v[196:197], v236 offset:24704
ds_read_b64_tr_b16 v[198:199], v236 offset:30848
v_mfma_f32_16x16x32_bf16 v[36:39], v[200:203], v[152:155], v[36:39]
v_mfma_f32_16x16x32_bf16 v[0:3], v[204:207], v[160:163], v[0:3]
ds_read_b64_tr_b16 v[200:201], v237 offset:24704
)"
R"(ds_read_b64_tr_b16 v[202:203], v237 offset:30848
v_mfma_f32_16x16x32_bf16 v[40:43], v[204:207], v[164:167], v[40:43]
v_mfma_f32_16x16x32_bf16 v[44:47], v[208:211], v[164:167], v[44:47]
ds_read_b64_tr_b16 v[204:205], v234 offset:24832
ds_read_b64_tr_b16 v[206:207], v234 offset:30976
v_mfma_f32_16x16x32_bf16 v[4:7], v[208:211], v[160:163], v[4:7]
v_mfma_f32_16x16x32_bf16 v[8:11], v[212:215], v[160:163], v[8:11]
ds_read_b64_tr_b16 v[208:209], v235 offset:24832
ds_read_b64_tr_b16 v[210:211], v235 offset:30976
v_mfma_f32_16x16x32_bf16 v[48:51], v[212:215], v[164:167], v[48:51]
s_add_u32 m0, s56, 49152
s_nop 0
buffer_load_dwordx4 v244, s[32:35], s50 offen lds
buffer_load_dwordx4 v245, s[32:35], s50 offen offset:1024 lds
buffer_load_dwordx4 v246, s[32:35], s50 offen offset:2048 lds
s_add_u32 s50, s50, 40960
)"
R"(ds_read_b64_tr_b16 v[212:213], v234 offset:36864
s_waitcnt lgkmcnt(11)
ds_read_b64_tr_b16 v[214:215], v234 offset:43008
s_add_u32 m0, s56, 98304
s_nop 0
buffer_load_dwordx4 v244, s[32:35], s50 offen lds
buffer_load_dwordx4 v245, s[32:35], s50 offen offset:1024 lds
buffer_load_dwordx4 v246, s[32:35], s50 offen offset:2048 lds
s_add_u32 s50, s50, 40960
s_waitcnt lgkmcnt(0)
s_cmp_eq_u32 s60, 1
s_cbranch_scc0 2
buffer_load_dword v177, v176, s[32:35], s55 offen
s_add_u32 s55, s55, 7680
s_cmp_eq_u32 s73, -1
s_cbranch_scc0 LpeA12_2_2
)"
R"(LplA12_2_2:
s_memrealtime s[88:89]
s_waitcnt lgkmcnt(0)
s_sub_u32 s89, s88, s94
s_cmp_lt_i32 s89, 0
s_cbranch_scc1 LplA12_2_2
s_add_u32 s95, s95, 75
s_add_u32 s88, s88, 75
s_sub_u32 s89, s88, s95
s_cmp_gt_i32 s89, 0
s_cselect_b32 s94, s88, s95
LpeA12_2_2:
s_waitcnt vmcnt(20)
s_barrier
s_add_u32 m0, s56, 24576
s_nop 0
)"
R"(buffer_load_dwordx4 v244, s[32:35], s50 offen lds
buffer_load_dwordx4 v245, s[32:35], s50 offen offset:1024 lds
buffer_load_dwordx4 v246, s[32:35], s50 offen offset:2048 lds
s_add_u32 s50, s50, 40960
v_mfma_f32_16x16x32_bf16 v[52:55], v[184:187], v[164:167], v[52:55]
v_mfma_f32_16x16x32_bf16 v[12:15], v[184:187], v[160:163], v[12:15]
v_mfma_f32_16x16x32_bf16 v[16:19], v[188:191], v[160:163], v[16:19]
ds_read_b64_tr_b16 v[184:185], v235 offset:36864
ds_read_b64_tr_b16 v[186:187], v235 offset:43008
v_mfma_f32_16x16x32_bf16 v[56:59], v[188:191], v[164:167], v[56:59]
v_mfma_f32_16x16x32_bf16 v[60:63], v[192:195], v[164:167], v[60:63]
ds_read_b64_tr_b16 v[188:189], v236 offset:36864
ds_read_b64_tr_b16 v[190:191], v236 offset:43008
v_mfma_f32_16x16x32_bf16 v[20:23], v[192:195], v[160:163], v[20:23]
v_mfma_f32_16x16x32_bf16 v[24:27], v[196:199], v[160:163], v[24:27]
ds_read_b64_tr_b16 v[192:193], v237 offset:36864
)"
R"(ds_read_b64_tr_b16 v[194:195], v237 offset:43008
v_mfma_f32_16x16x32_bf16 v[64:67], v[196:199], v[164:167], v[64:67]
v_mfma_f32_16x16x32_bf16 v[68:71], v[200:203], v[164:167], v[68:71]
ds_read_b64_tr_b16 v[196:197], v234 offset:36992
ds_read_b64_tr_b16 v[198:199], v234 offset:43136
v_mfma_f32_16x16x32_bf16 v[28:31], v[200:203], v[160:163], v[28:31]
v_mfma_f32_16x16x32_bf16 v[32:35], v[204:207], v[160:163], v[32:35]
ds_read_b64_tr_b16 v[200:201], v235 offset:36992
ds_read_b64_tr_b16 v[202:203], v235 offset:43136
v_mfma_f32_16x16x32_bf16 v[72:75], v[204:207], v[164:167], v[72:75]
v_mfma_f32_16x16x32_bf16 v[76:79], v[208:211], v[164:167], v[76:79]
ds_read_b64_tr_b16 v[204:205], v236 offset:36992
ds_read_b64_tr_b16 v[206:207], v236 offset:43136
v_mfma_f32_16x16x32_bf16 v[36:39], v[208:211], v[160:163], v[36:39]
s_waitcnt vmcnt(21)
v_mfma_f32_16x16x32_bf16 v[0:3], v[212:215], v[168:171], v[0:3]
)"
R"(ds_read_b64_tr_b16 v[208:209], v237 offset:36992
ds_read_b64_tr_b16 v[210:211], v237 offset:43136
v_mfma_f32_16x16x32_bf16 v[40:43], v[212:215], v[172:175], v[40:43]
s_mul_i32 s50, s97, 245760
s_add_u32 s50, s50, s52
s_add_u32 s50, s50, 983040
s_add_u32 m0, s56, 61440
s_nop 0
buffer_load_dwordx4 v244, s[32:35], s50 offen lds
ds_read_b64_tr_b16 v[212:213], v234 offset:37120
s_waitcnt lgkmcnt(11)
ds_read_b64_tr_b16 v[214:215], v234 offset:43264
ds_read_b64_tr_b16 v[216:217], v235 offset:37120
ds_read_b64_tr_b16 v[218:219], v235 offset:43264
buffer_load_dwordx4 v245, s[32:35], s50 offen offset:1024 lds
buffer_load_dwordx4 v246, s[32:35], s50 offen offset:2048 lds
)"
R"(s_add_u32 s50, s50, 40960
s_waitcnt lgkmcnt(0)
s_cmp_eq_u32 s60, 2
s_cbranch_scc0 2
buffer_load_dword v177, v176, s[32:35], s55 offen
s_add_u32 s55, s55, 7680
s_cmp_eq_u32 s73, -1
s_cbranch_scc0 LpeA12_2_3
LplA12_2_3:
s_memrealtime s[88:89]
s_waitcnt lgkmcnt(0)
s_sub_u32 s89, s88, s94
s_cmp_lt_i32 s89, 0
s_cbranch_scc1 LplA12_2_3
s_add_u32 s95, s95, 75
s_add_u32 s88, s88, 75
)"
R"(s_sub_u32 s89, s88, s95
s_cmp_gt_i32 s89, 0
s_cselect_b32 s94, s88, s95
LpeA12_2_3:
s_waitcnt vmcnt(21)
s_barrier
v_mfma_f32_16x16x32_bf16 v[44:47], v[184:187], v[172:175], v[44:47]
v_mfma_f32_16x16x32_bf16 v[4:7], v[184:187], v[168:171], v[4:7]
v_mfma_f32_16x16x32_bf16 v[8:11], v[188:191], v[168:171], v[8:11]
v_mfma_f32_16x16x32_bf16 v[48:51], v[188:191], v[172:175], v[48:51]
v_mfma_f32_16x16x32_bf16 v[52:55], v[192:195], v[172:175], v[52:55]
v_mfma_f32_16x16x32_bf16 v[12:15], v[192:195], v[168:171], v[12:15]
v_mfma_f32_16x16x32_bf16 v[16:19], v[196:199], v[168:171], v[16:19]
v_mfma_f32_16x16x32_bf16 v[56:59], v[196:199], v[172:175], v[56:59]
v_mfma_f32_16x16x32_bf16 v[60:63], v[200:203], v[172:175], v[60:63]
v_mfma_f32_16x16x32_bf16 v[20:23], v[200:203], v[168:171], v[20:23]
)"
R"(v_mfma_f32_16x16x32_bf16 v[24:27], v[204:207], v[168:171], v[24:27]
v_mfma_f32_16x16x32_bf16 v[64:67], v[204:207], v[172:175], v[64:67]
v_mfma_f32_16x16x32_bf16 v[68:71], v[208:211], v[172:175], v[68:71]
v_mfma_f32_16x16x32_bf16 v[28:31], v[208:211], v[168:171], v[28:31]
v_mfma_f32_16x16x32_bf16 v[32:35], v[212:215], v[168:171], v[32:35]
v_mfma_f32_16x16x32_bf16 v[72:75], v[212:215], v[172:175], v[72:75]
v_mfma_f32_16x16x32_bf16 v[76:79], v[216:219], v[172:175], v[76:79]
v_mfma_f32_16x16x32_bf16 v[36:39], v[216:219], v[168:171], v[36:39]
v_subrev_u32 v232, 98304, v234
v_subrev_u32 v233, 98304, v235
v_subrev_u32 v238, 98304, v236
v_subrev_u32 v239, 98304, v237
v_subrev_u32 v240, 49152, v234
v_subrev_u32 v241, 49152, v235
v_subrev_u32 v242, 49152, v236
v_subrev_u32 v243, 49152, v237
)"
R"(s_setprio 2
s_mov_b32 s68, 122880
s_cmp_eq_u32 s60, 1
s_cselect_b32 s68, 73728, s68
s_cmp_eq_u32 s60, 2
s_cselect_b32 s68, 36864, s68
s_cmp_eq_u32 s60, 3
s_cselect_b32 s68, 110592, s68
v_add_u32 v248, s68, v247
s_waitcnt vmcnt(0)
s_barrier
ds_read_b128 v[80:83], v248 offset:0
ds_read_b128 v[84:87], v248 offset:1024
ds_read_b64_tr_b16 v[208:209], v234 offset:49152
ds_read_b64_tr_b16 v[210:211], v234 offset:55296
ds_read_b64_tr_b16 v[212:213], v235 offset:49152
)"
R"(ds_read_b64_tr_b16 v[214:215], v235 offset:55296
ds_read_b64_tr_b16 v[216:217], v236 offset:49152
ds_read_b64_tr_b16 v[218:219], v236 offset:55296
ds_read_b64_tr_b16 v[220:221], v237 offset:49152
ds_read_b64_tr_b16 v[222:223], v237 offset:55296
ds_read_b64_tr_b16 v[224:225], v234 offset:49280
ds_read_b64_tr_b16 v[226:227], v234 offset:55424
s_waitcnt lgkmcnt(8)
v_mfma_f32_16x16x32_bf16 v[0:3], v[208:211], v[80:83], v[0:3]
ds_read_b64_tr_b16 v[228:229], v235 offset:49280
ds_read_b64_tr_b16 v[230:231], v235 offset:55424
v_mfma_f32_16x16x32_bf16 v[40:43], v[208:211], v[84:87], v[40:43]
s_waitcnt lgkmcnt(8)
ds_read_b128 v[88:91], v248 offset:2048
v_mfma_f32_16x16x32_bf16 v[44:47], v[212:215], v[84:87], v[44:47]
ds_read_b64_tr_b16 v[208:209], v236 offset:49280
)"
R"(ds_read_b64_tr_b16 v[210:211], v236 offset:55424
v_mfma_f32_16x16x32_bf16 v[4:7], v[212:215], v[80:83], v[4:7]
s_waitcnt lgkmcnt(9)
v_mfma_f32_16x16x32_bf16 v[8:11], v[216:219], v[80:83], v[8:11]
ds_read_b64_tr_b16 v[212:213], v237 offset:49280
ds_read_b64_tr_b16 v[214:215], v237 offset:55424
v_mfma_f32_16x16x32_bf16 v[48:51], v[216:219], v[84:87], v[48:51]
s_waitcnt lgkmcnt(9)
ds_read_b128 v[92:95], v248 offset:3072
v_mfma_f32_16x16x32_bf16 v[52:55], v[220:223], v[84:87], v[52:55]
ds_read_b64_tr_b16 v[216:217], v234 offset:49408
ds_read_b64_tr_b16 v[218:219], v234 offset:55552
v_mfma_f32_16x16x32_bf16 v[12:15], v[220:223], v[80:83], v[12:15]
s_waitcnt lgkmcnt(10)
v_mfma_f32_16x16x32_bf16 v[16:19], v[224:227], v[80:83], v[16:19]
ds_read_b64_tr_b16 v[220:221], v235 offset:49408
)"
R"(ds_read_b64_tr_b16 v[222:223], v235 offset:55552
v_mfma_f32_16x16x32_bf16 v[56:59], v[224:227], v[84:87], v[56:59]
s_waitcnt lgkmcnt(10)
ds_read_b128 v[96:99], v248 offset:4096
v_mfma_f32_16x16x32_bf16 v[60:63], v[228:231], v[84:87], v[60:63]
ds_read_b64_tr_b16 v[224:225], v232 offset:0
ds_read_b64_tr_b16 v[226:227], v232 offset:6144
v_mfma_f32_16x16x32_bf16 v[20:23], v[228:231], v[80:83], v[20:23]
s_waitcnt lgkmcnt(10)
v_mfma_f32_16x16x32_bf16 v[24:27], v[208:211], v[80:83], v[24:27]
ds_read_b64_tr_b16 v[228:229], v233 offset:0
ds_read_b64_tr_b16 v[230:231], v233 offset:6144
v_mfma_f32_16x16x32_bf16 v[64:67], v[208:211], v[84:87], v[64:67]
s_waitcnt lgkmcnt(10)
ds_read_b128 v[100:103], v248 offset:5120
v_mfma_f32_16x16x32_bf16 v[68:71], v[212:215], v[84:87], v[68:71]
)"
R"(ds_read_b64_tr_b16 v[208:209], v238 offset:0
ds_read_b64_tr_b16 v[210:211], v238 offset:6144
v_mfma_f32_16x16x32_bf16 v[28:31], v[212:215], v[80:83], v[28:31]
s_waitcnt lgkmcnt(10)
v_mfma_f32_16x16x32_bf16 v[32:35], v[216:219], v[80:83], v[32:35]
ds_read_b64_tr_b16 v[212:213], v239 offset:0
ds_read_b64_tr_b16 v[214:215], v239 offset:6144
v_mfma_f32_16x16x32_bf16 v[72:75], v[216:219], v[84:87], v[72:75]
s_waitcnt lgkmcnt(10)
ds_read_b128 v[104:107], v248 offset:6144
v_mfma_f32_16x16x32_bf16 v[76:79], v[220:223], v[84:87], v[76:79]
ds_read_b64_tr_b16 v[216:217], v232 offset:128
ds_read_b64_tr_b16 v[218:219], v232 offset:6272
v_mfma_f32_16x16x32_bf16 v[36:39], v[220:223], v[80:83], v[36:39]
s_waitcnt lgkmcnt(10)
v_mfma_f32_16x16x32_bf16 v[0:3], v[224:227], v[88:91], v[0:3]
)"
R"(ds_read_b64_tr_b16 v[220:221], v233 offset:128
ds_read_b64_tr_b16 v[222:223], v233 offset:6272
v_mfma_f32_16x16x32_bf16 v[40:43], v[224:227], v[92:95], v[40:43]
s_waitcnt lgkmcnt(10)
ds_read_b128 v[108:111], v248 offset:7168
v_mfma_f32_16x16x32_bf16 v[44:47], v[228:231], v[92:95], v[44:47]
ds_read_b64_tr_b16 v[224:225], v238 offset:128
ds_read_b64_tr_b16 v[226:227], v238 offset:6272
v_mfma_f32_16x16x32_bf16 v[4:7], v[228:231], v[88:91], v[4:7]
s_waitcnt lgkmcnt(10)
v_mfma_f32_16x16x32_bf16 v[8:11], v[208:211], v[88:91], v[8:11]
ds_read_b64_tr_b16 v[228:229], v239 offset:128
ds_read_b64_tr_b16 v[230:231], v239 offset:6272
v_mfma_f32_16x16x32_bf16 v[48:51], v[208:211], v[92:95], v[48:51]
s_waitcnt lgkmcnt(10)
ds_read_b128 v[112:115], v248 offset:8192
)"
R"(v_mfma_f32_16x16x32_bf16 v[52:55], v[212:215], v[92:95], v[52:55]
ds_read_b64_tr_b16 v[208:209], v232 offset:256
ds_read_b64_tr_b16 v[210:211], v232 offset:6400
v_mfma_f32_16x16x32_bf16 v[12:15], v[212:215], v[88:91], v[12:15]
s_waitcnt lgkmcnt(10)
v_mfma_f32_16x16x32_bf16 v[16:19], v[216:219], v[88:91], v[16:19]
ds_read_b64_tr_b16 v[212:213], v233 offset:256
ds_read_b64_tr_b16 v[214:215], v233 offset:6400
v_mfma_f32_16x16x32_bf16 v[56:59], v[216:219], v[92:95], v[56:59]
s_waitcnt lgkmcnt(10)
ds_read_b128 v[116:119], v248 offset:9216
v_mfma_f32_16x16x32_bf16 v[60:63], v[220:223], v[92:95], v[60:63]
ds_read_b64_tr_b16 v[216:217], v232 offset:12288
ds_read_b64_tr_b16 v[218:219], v232 offset:18432
v_mfma_f32_16x16x32_bf16 v[20:23], v[220:223], v[88:91], v[20:23]
s_waitcnt lgkmcnt(10)
)"
R"(v_mfma_f32_16x16x32_bf16 v[24:27], v[224:227], v[88:91], v[24:27]
ds_read_b64_tr_b16 v[220:221], v233 offset:12288
ds_read_b64_tr_b16 v[222:223], v233 offset:18432
v_mfma_f32_16x16x32_bf16 v[64:67], v[224:227], v[92:95], v[64:67]
s_waitcnt lgkmcnt(10)
ds_read_b128 v[120:123], v248 offset:10240
v_mfma_f32_16x16x32_bf16 v[68:71], v[228:231], v[92:95], v[68:71]
ds_read_b64_tr_b16 v[224:225], v238 offset:12288
ds_read_b64_tr_b16 v[226:227], v238 offset:18432
v_mfma_f32_16x16x32_bf16 v[28:31], v[228:231], v[88:91], v[28:31]
s_waitcnt lgkmcnt(10)
v_mfma_f32_16x16x32_bf16 v[32:35], v[208:211], v[88:91], v[32:35]
ds_read_b64_tr_b16 v[228:229], v239 offset:12288
ds_read_b64_tr_b16 v[230:231], v239 offset:18432
v_mfma_f32_16x16x32_bf16 v[72:75], v[208:211], v[92:95], v[72:75]
s_waitcnt lgkmcnt(10)
)"
R"(ds_read_b128 v[124:127], v248 offset:11264
v_mfma_f32_16x16x32_bf16 v[76:79], v[212:215], v[92:95], v[76:79]
ds_read_b64_tr_b16 v[208:209], v232 offset:12416
ds_read_b64_tr_b16 v[210:211], v232 offset:18560
v_mfma_f32_16x16x32_bf16 v[36:39], v[212:215], v[88:91], v[36:39]
s_waitcnt lgkmcnt(10)
v_mfma_f32_16x16x32_bf16 v[0:3], v[216:219], v[96:99], v[0:3]
ds_read_b64_tr_b16 v[212:213], v233 offset:12416
ds_read_b64_tr_b16 v[214:215], v233 offset:18560
v_mfma_f32_16x16x32_bf16 v[40:43], v[216:219], v[100:103], v[40:43]
s_waitcnt lgkmcnt(10)
v_mfma_f32_16x16x32_bf16 v[44:47], v[220:223], v[100:103], v[44:47]
ds_read_b64_tr_b16 v[216:217], v238 offset:12416
ds_read_b64_tr_b16 v[218:219], v238 offset:18560
v_mfma_f32_16x16x32_bf16 v[4:7], v[220:223], v[96:99], v[4:7]
s_waitcnt lgkmcnt(9)
)"
R"(v_mfma_f32_16x16x32_bf16 v[8:11], v[224:227], v[96:99], v[8:11]
ds_read_b64_tr_b16 v[220:221], v239 offset:12416
ds_read_b64_tr_b16 v[222:223], v239 offset:18560
v_mfma_f32_16x16x32_bf16 v[48:51], v[224:227], v[100:103], v[48:51]
s_waitcnt lgkmcnt(9)
v_mfma_f32_16x16x32_bf16 v[52:55], v[228:231], v[100:103], v[52:55]
ds_read_b64_tr_b16 v[224:225], v232 offset:12544
ds_read_b64_tr_b16 v[226:227], v232 offset:18688
v_mfma_f32_16x16x32_bf16 v[12:15], v[228:231], v[96:99], v[12:15]
s_waitcnt lgkmcnt(8)
v_mfma_f32_16x16x32_bf16 v[16:19], v[208:211], v[96:99], v[16:19]
ds_read_b64_tr_b16 v[228:229], v233 offset:12544
ds_read_b64_tr_b16 v[230:231], v233 offset:18688
v_mfma_f32_16x16x32_bf16 v[56:59], v[208:211], v[100:103], v[56:59]
s_waitcnt lgkmcnt(8)
v_mfma_f32_16x16x32_bf16 v[60:63], v[212:215], v[100:103], v[60:63]
)"
R"(ds_read_b64_tr_b16 v[208:209], v240 offset:0
ds_read_b64_tr_b16 v[210:211], v240 offset:6144
v_mfma_f32_16x16x32_bf16 v[20:23], v[212:215], v[96:99], v[20:23]
s_waitcnt lgkmcnt(8)
v_mfma_f32_16x16x32_bf16 v[24:27], v[216:219], v[96:99], v[24:27]
ds_read_b64_tr_b16 v[212:213], v241 offset:0
ds_read_b64_tr_b16 v[214:215], v241 offset:6144
v_mfma_f32_16x16x32_bf16 v[64:67], v[216:219], v[100:103], v[64:67]
s_waitcnt lgkmcnt(8)
v_mfma_f32_16x16x32_bf16 v[68:71], v[220:223], v[100:103], v[68:71]
ds_read_b64_tr_b16 v[216:217], v242 offset:0
ds_read_b64_tr_b16 v[218:219], v242 offset:6144
v_mfma_f32_16x16x32_bf16 v[28:31], v[220:223], v[96:99], v[28:31]
s_waitcnt lgkmcnt(8)
v_mfma_f32_16x16x32_bf16 v[32:35], v[224:227], v[96:99], v[32:35]
ds_read_b64_tr_b16 v[220:221], v243 offset:0
)"
R"(ds_read_b64_tr_b16 v[222:223], v243 offset:6144
v_mfma_f32_16x16x32_bf16 v[72:75], v[224:227], v[100:103], v[72:75]
s_waitcnt lgkmcnt(8)
v_mfma_f32_16x16x32_bf16 v[76:79], v[228:231], v[100:103], v[76:79]
ds_read_b64_tr_b16 v[224:225], v240 offset:128
ds_read_b64_tr_b16 v[226:227], v240 offset:6272
v_mfma_f32_16x16x32_bf16 v[36:39], v[228:231], v[96:99], v[36:39]
s_waitcnt lgkmcnt(8)
s_barrier
v_mfma_f32_16x16x32_bf16 v[0:3], v[208:211], v[104:107], v[0:3]
ds_read_b64_tr_b16 v[228:229], v241 offset:128
ds_read_b64_tr_b16 v[230:231], v241 offset:6272
v_mfma_f32_16x16x32_bf16 v[40:43], v[208:211], v[108:111], v[40:43]
s_waitcnt lgkmcnt(8)
v_mfma_f32_16x16x32_bf16 v[44:47], v[212:215], v[108:111], v[44:47]
ds_read_b64_tr_b16 v[208:209], v242 offset:128
)"
R"(ds_read_b64_tr_b16 v[210:211], v242 offset:6272
v_mfma_f32_16x16x32_bf16 v[4:7], v[212:215], v[104:107], v[4:7]
s_waitcnt lgkmcnt(8)
v_mfma_f32_16x16x32_bf16 v[8:11], v[216:219], v[104:107], v[8:11]
ds_read_b64_tr_b16 v[212:213], v243 offset:128
ds_read_b64_tr_b16 v[214:215], v243 offset:6272
v_mfma_f32_16x16x32_bf16 v[48:51], v[216:219], v[108:111], v[48:51]
s_waitcnt lgkmcnt(8)
v_mfma_f32_16x16x32_bf16 v[52:55], v[220:223], v[108:111], v[52:55]
ds_read_b64_tr_b16 v[216:217], v240 offset:256
ds_read_b64_tr_b16 v[218:219], v240 offset:6400
v_mfma_f32_16x16x32_bf16 v[12:15], v[220:223], v[104:107], v[12:15]
s_waitcnt lgkmcnt(8)
v_mfma_f32_16x16x32_bf16 v[16:19], v[224:227], v[104:107], v[16:19]
ds_read_b64_tr_b16 v[220:221], v241 offset:256
ds_read_b64_tr_b16 v[222:223], v241 offset:6400
)"
R"(v_mfma_f32_16x16x32_bf16 v[56:59], v[224:227], v[108:111], v[56:59]
s_waitcnt lgkmcnt(8)
v_mfma_f32_16x16x32_bf16 v[60:63], v[228:231], v[108:111], v[60:63]
ds_read_b64_tr_b16 v[224:225], v234 offset:0
ds_read_b64_tr_b16 v[226:227], v234 offset:6144
v_mfma_f32_16x16x32_bf16 v[20:23], v[228:231], v[104:107], v[20:23]
s_waitcnt lgkmcnt(8)
v_mfma_f32_16x16x32_bf16 v[24:27], v[208:211], v[104:107], v[24:27]
ds_read_b64_tr_b16 v[228:229], v235 offset:0
ds_read_b64_tr_b16 v[230:231], v235 offset:6144
v_mfma_f32_16x16x32_bf16 v[64:67], v[208:211], v[108:111], v[64:67]
s_waitcnt lgkmcnt(8)
v_mfma_f32_16x16x32_bf16 v[68:71], v[212:215], v[108:111], v[68:71]
ds_read_b64_tr_b16 v[208:209], v236 offset:0
ds_read_b64_tr_b16 v[210:211], v236 offset:6144
v_mfma_f32_16x16x32_bf16 v[28:31], v[212:215], v[104:107], v[28:31]
)"
R"(s_waitcnt lgkmcnt(8)
v_mfma_f32_16x16x32_bf16 v[32:35], v[216:219], v[104:107], v[32:35]
ds_read_b64_tr_b16 v[212:213], v237 offset:0
ds_read_b64_tr_b16 v[214:215], v237 offset:6144
v_mfma_f32_16x16x32_bf16 v[72:75], v[216:219], v[108:111], v[72:75]
s_waitcnt lgkmcnt(8)
v_mfma_f32_16x16x32_bf16 v[76:79], v[220:223], v[108:111], v[76:79]
ds_read_b64_tr_b16 v[216:217], v234 offset:128
ds_read_b64_tr_b16 v[218:219], v234 offset:6272
v_mfma_f32_16x16x32_bf16 v[36:39], v[220:223], v[104:107], v[36:39]
s_waitcnt lgkmcnt(8)
v_mfma_f32_16x16x32_bf16 v[0:3], v[224:227], v[112:115], v[0:3]
ds_read_b64_tr_b16 v[220:221], v235 offset:128
ds_read_b64_tr_b16 v[222:223], v235 offset:6272
v_mfma_f32_16x16x32_bf16 v[40:43], v[224:227], v[116:119], v[40:43]
s_waitcnt lgkmcnt(8)
)"
R"(v_mfma_f32_16x16x32_bf16 v[44:47], v[228:231], v[116:119], v[44:47]
ds_read_b64_tr_b16 v[224:225], v236 offset:128
ds_read_b64_tr_b16 v[226:227], v236 offset:6272
v_mfma_f32_16x16x32_bf16 v[4:7], v[228:231], v[112:115], v[4:7]
s_waitcnt lgkmcnt(8)
v_mfma_f32_16x16x32_bf16 v[8:11], v[208:211], v[112:115], v[8:11]
ds_read_b64_tr_b16 v[228:229], v237 offset:128
ds_read_b64_tr_b16 v[230:231], v237 offset:6272
v_mfma_f32_16x16x32_bf16 v[48:51], v[208:211], v[116:119], v[48:51]
s_waitcnt lgkmcnt(8)
v_mfma_f32_16x16x32_bf16 v[52:55], v[212:215], v[116:119], v[52:55]
ds_read_b64_tr_b16 v[208:209], v234 offset:256
ds_read_b64_tr_b16 v[210:211], v234 offset:6400
v_mfma_f32_16x16x32_bf16 v[12:15], v[212:215], v[112:115], v[12:15]
s_waitcnt lgkmcnt(8)
v_mfma_f32_16x16x32_bf16 v[16:19], v[216:219], v[112:115], v[16:19]
)"
R"(ds_read_b64_tr_b16 v[212:213], v235 offset:256
ds_read_b64_tr_b16 v[214:215], v235 offset:6400
v_mfma_f32_16x16x32_bf16 v[56:59], v[216:219], v[116:119], v[56:59]
s_waitcnt lgkmcnt(8)
v_mfma_f32_16x16x32_bf16 v[60:63], v[220:223], v[116:119], v[60:63]
ds_read_b64_tr_b16 v[216:217], v232 offset:24576
ds_read_b64_tr_b16 v[218:219], v232 offset:30720
v_mfma_f32_16x16x32_bf16 v[20:23], v[220:223], v[112:115], v[20:23]
s_waitcnt lgkmcnt(8)
v_mfma_f32_16x16x32_bf16 v[24:27], v[224:227], v[112:115], v[24:27]
ds_read_b64_tr_b16 v[220:221], v233 offset:24576
ds_read_b64_tr_b16 v[222:223], v233 offset:30720
v_mfma_f32_16x16x32_bf16 v[64:67], v[224:227], v[116:119], v[64:67]
s_waitcnt lgkmcnt(8)
v_mfma_f32_16x16x32_bf16 v[68:71], v[228:231], v[116:119], v[68:71]
ds_read_b64_tr_b16 v[224:225], v238 offset:24576
)"
R"(ds_read_b64_tr_b16 v[226:227], v238 offset:30720
v_mfma_f32_16x16x32_bf16 v[28:31], v[228:231], v[112:115], v[28:31]
s_waitcnt lgkmcnt(8)
v_mfma_f32_16x16x32_bf16 v[32:35], v[208:211], v[112:115], v[32:35]
ds_read_b64_tr_b16 v[228:229], v239 offset:24576
ds_read_b64_tr_b16 v[230:231], v239 offset:30720
v_mfma_f32_16x16x32_bf16 v[72:75], v[208:211], v[116:119], v[72:75]
s_waitcnt lgkmcnt(8)
v_mfma_f32_16x16x32_bf16 v[76:79], v[212:215], v[116:119], v[76:79]
ds_read_b64_tr_b16 v[208:209], v232 offset:24704
ds_read_b64_tr_b16 v[210:211], v232 offset:30848
v_mfma_f32_16x16x32_bf16 v[36:39], v[212:215], v[112:115], v[36:39]
s_waitcnt lgkmcnt(8)
v_mfma_f32_16x16x32_bf16 v[0:3], v[216:219], v[120:123], v[0:3]
ds_read_b64_tr_b16 v[212:213], v233 offset:24704
ds_read_b64_tr_b16 v[214:215], v233 offset:30848
)"
R"(v_mfma_f32_16x16x32_bf16 v[40:43], v[216:219], v[124:127], v[40:43]
s_waitcnt lgkmcnt(8)
v_mfma_f32_16x16x32_bf16 v[44:47], v[220:223], v[124:127], v[44:47]
ds_read_b64_tr_b16 v[216:217], v238 offset:24704
ds_read_b64_tr_b16 v[218:219], v238 offset:30848
v_mfma_f32_16x16x32_bf16 v[4:7], v[220:223], v[120:123], v[4:7]
s_waitcnt lgkmcnt(8)
v_mfma_f32_16x16x32_bf16 v[8:11], v[224:227], v[120:123], v[8:11]
ds_read_b64_tr_b16 v[220:221], v239 offset:24704
ds_read_b64_tr_b16 v[222:223], v239 offset:30848
v_mfma_f32_16x16x32_bf16 v[48:51], v[224:227], v[124:127], v[48:51]
s_waitcnt lgkmcnt(8)
v_mfma_f32_16x16x32_bf16 v[52:55], v[228:231], v[124:127], v[52:55]
ds_read_b64_tr_b16 v[224:225], v232 offset:24832
ds_read_b64_tr_b16 v[226:227], v232 offset:30976
v_mfma_f32_16x16x32_bf16 v[12:15], v[228:231], v[120:123], v[12:15]
)"
R"(s_waitcnt lgkmcnt(8)
v_mfma_f32_16x16x32_bf16 v[16:19], v[208:211], v[120:123], v[16:19]
ds_read_b64_tr_b16 v[228:229], v233 offset:24832
ds_read_b64_tr_b16 v[230:231], v233 offset:30976
v_mfma_f32_16x16x32_bf16 v[56:59], v[208:211], v[124:127], v[56:59]
s_waitcnt lgkmcnt(8)
v_mfma_f32_16x16x32_bf16 v[60:63], v[212:215], v[124:127], v[60:63]
ds_read_b64_tr_b16 v[208:209], v240 offset:12288
ds_read_b64_tr_b16 v[210:211], v240 offset:18432
v_mfma_f32_16x16x32_bf16 v[20:23], v[212:215], v[120:123], v[20:23]
s_waitcnt lgkmcnt(8)
v_mfma_f32_16x16x32_bf16 v[24:27], v[216:219], v[120:123], v[24:27]
ds_read_b64_tr_b16 v[212:213], v241 offset:12288
ds_read_b64_tr_b16 v[214:215], v241 offset:18432
v_mfma_f32_16x16x32_bf16 v[64:67], v[216:219], v[124:127], v[64:67]
s_waitcnt lgkmcnt(8)
)"
R"(v_mfma_f32_16x16x32_bf16 v[68:71], v[220:223], v[124:127], v[68:71]
ds_read_b64_tr_b16 v[216:217], v242 offset:12288
ds_read_b64_tr_b16 v[218:219], v242 offset:18432
v_mfma_f32_16x16x32_bf16 v[28:31], v[220:223], v[120:123], v[28:31]
s_waitcnt lgkmcnt(8)
v_mfma_f32_16x16x32_bf16 v[32:35], v[224:227], v[120:123], v[32:35]
ds_read_b64_tr_b16 v[220:221], v243 offset:12288
ds_read_b64_tr_b16 v[222:223], v243 offset:18432
v_mfma_f32_16x16x32_bf16 v[72:75], v[224:227], v[124:127], v[72:75]
s_waitcnt lgkmcnt(8)
v_mfma_f32_16x16x32_bf16 v[76:79], v[228:231], v[124:127], v[76:79]
ds_read_b64_tr_b16 v[224:225], v240 offset:12416
ds_read_b64_tr_b16 v[226:227], v240 offset:18560
v_mfma_f32_16x16x32_bf16 v[36:39], v[228:231], v[120:123], v[36:39]
s_waitcnt lgkmcnt(8)
s_barrier
)"
R"(s_mul_i32 s51, s97, 12288
s_add_u32 s51, s51, s54
s_add_u32 s51, s51, 12582912
buffer_load_dwordx4 v[128:131], v247, s[40:43], s51 offen sc1
buffer_load_dwordx4 v[132:135], v247, s[40:43], s51 offen offset:1024 sc1
buffer_load_dwordx4 v[136:139], v247, s[40:43], s51 offen offset:2048 sc1
buffer_load_dwordx4 v[140:143], v247, s[40:43], s51 offen offset:3072 sc1
s_add_u32 s51, s51, 4096
buffer_load_dwordx4 v[144:147], v247, s[40:43], s51 offen sc1
buffer_load_dwordx4 v[148:151], v247, s[40:43], s51 offen offset:1024 sc1
s_waitcnt vmcnt(4)
s_barrier
v_mfma_f32_16x16x32_bf16 v[0:3], v[208:211], v[128:131], v[0:3]
ds_read_b64_tr_b16 v[228:229], v241 offset:12416
ds_read_b64_tr_b16 v[230:231], v241 offset:18560
v_mfma_f32_16x16x32_bf16 v[40:43], v[208:211], v[132:135], v[40:43]
)"
R"(s_waitcnt lgkmcnt(8)
v_mfma_f32_16x16x32_bf16 v[44:47], v[212:215], v[132:135], v[44:47]
ds_read_b64_tr_b16 v[208:209], v242 offset:12416
ds_read_b64_tr_b16 v[210:211], v242 offset:18560
v_mfma_f32_16x16x32_bf16 v[4:7], v[212:215], v[128:131], v[4:7]
s_waitcnt lgkmcnt(8)
v_mfma_f32_16x16x32_bf16 v[8:11], v[216:219], v[128:131], v[8:11]
ds_read_b64_tr_b16 v[212:213], v243 offset:12416
ds_read_b64_tr_b16 v[214:215], v243 offset:18560
v_mfma_f32_16x16x32_bf16 v[48:51], v[216:219], v[132:135], v[48:51]
s_waitcnt lgkmcnt(8)
buffer_load_dwordx4 v[152:155], v247, s[40:43], s51 offen offset:2048 sc1
v_mfma_f32_16x16x32_bf16 v[52:55], v[220:223], v[132:135], v[52:55]
ds_read_b64_tr_b16 v[216:217], v240 offset:12544
ds_read_b64_tr_b16 v[218:219], v240 offset:18688
v_mfma_f32_16x16x32_bf16 v[12:15], v[220:223], v[128:131], v[12:15]
)"
R"(s_waitcnt lgkmcnt(8)
v_mfma_f32_16x16x32_bf16 v[16:19], v[224:227], v[128:131], v[16:19]
ds_read_b64_tr_b16 v[220:221], v241 offset:12544
ds_read_b64_tr_b16 v[222:223], v241 offset:18688
v_mfma_f32_16x16x32_bf16 v[56:59], v[224:227], v[132:135], v[56:59]
s_waitcnt lgkmcnt(8)
v_mfma_f32_16x16x32_bf16 v[60:63], v[228:231], v[132:135], v[60:63]
ds_read_b64_tr_b16 v[224:225], v240 offset:36864
ds_read_b64_tr_b16 v[226:227], v240 offset:43008
v_mfma_f32_16x16x32_bf16 v[20:23], v[228:231], v[128:131], v[20:23]
s_waitcnt lgkmcnt(8)
v_mfma_f32_16x16x32_bf16 v[24:27], v[208:211], v[128:131], v[24:27]
ds_read_b64_tr_b16 v[228:229], v241 offset:36864
ds_read_b64_tr_b16 v[230:231], v241 offset:43008
v_mfma_f32_16x16x32_bf16 v[64:67], v[208:211], v[132:135], v[64:67]
s_waitcnt lgkmcnt(8)
)"
R"(v_mfma_f32_16x16x32_bf16 v[68:71], v[212:215], v[132:135], v[68:71]
ds_read_b64_tr_b16 v[208:209], v242 offset:36864
ds_read_b64_tr_b16 v[210:211], v242 offset:43008
v_mfma_f32_16x16x32_bf16 v[28:31], v[212:215], v[128:131], v[28:31]
s_waitcnt lgkmcnt(8)
buffer_load_dwordx4 v[156:159], v247, s[40:43], s51 offen offset:3072 sc1
v_mfma_f32_16x16x32_bf16 v[32:35], v[216:219], v[128:131], v[32:35]
ds_read_b64_tr_b16 v[212:213], v243 offset:36864
ds_read_b64_tr_b16 v[214:215], v243 offset:43008
v_mfma_f32_16x16x32_bf16 v[72:75], v[216:219], v[132:135], v[72:75]
s_waitcnt lgkmcnt(8)
v_mfma_f32_16x16x32_bf16 v[76:79], v[220:223], v[132:135], v[76:79]
ds_read_b64_tr_b16 v[216:217], v240 offset:36992
ds_read_b64_tr_b16 v[218:219], v240 offset:43136
v_mfma_f32_16x16x32_bf16 v[36:39], v[220:223], v[128:131], v[36:39]
s_waitcnt lgkmcnt(8)
)"
R"(s_waitcnt vmcnt(4)
v_mfma_f32_16x16x32_bf16 v[0:3], v[224:227], v[136:139], v[0:3]
ds_read_b64_tr_b16 v[220:221], v241 offset:36992
ds_read_b64_tr_b16 v[222:223], v241 offset:43136
v_mfma_f32_16x16x32_bf16 v[40:43], v[224:227], v[140:143], v[40:43]
s_waitcnt lgkmcnt(8)
v_mfma_f32_16x16x32_bf16 v[44:47], v[228:231], v[140:143], v[44:47]
ds_read_b64_tr_b16 v[224:225], v242 offset:36992
ds_read_b64_tr_b16 v[226:227], v242 offset:43136
v_mfma_f32_16x16x32_bf16 v[4:7], v[228:231], v[136:139], v[4:7]
s_waitcnt lgkmcnt(8)
v_mfma_f32_16x16x32_bf16 v[8:11], v[208:211], v[136:139], v[8:11]
ds_read_b64_tr_b16 v[228:229], v243 offset:36992
ds_read_b64_tr_b16 v[230:231], v243 offset:43136
v_mfma_f32_16x16x32_bf16 v[48:51], v[208:211], v[140:143], v[48:51]
s_waitcnt lgkmcnt(8)
)"
R"(s_add_u32 s51, s51, 4096
buffer_load_dwordx4 v[160:163], v247, s[40:43], s51 offen sc1
v_mfma_f32_16x16x32_bf16 v[52:55], v[212:215], v[140:143], v[52:55]
ds_read_b64_tr_b16 v[208:209], v240 offset:37120
ds_read_b64_tr_b16 v[210:211], v240 offset:43264
v_mfma_f32_16x16x32_bf16 v[12:15], v[212:215], v[136:139], v[12:15]
s_waitcnt lgkmcnt(8)
v_mfma_f32_16x16x32_bf16 v[16:19], v[216:219], v[136:139], v[16:19]
ds_read_b64_tr_b16 v[212:213], v241 offset:37120
ds_read_b64_tr_b16 v[214:215], v241 offset:43264
v_mfma_f32_16x16x32_bf16 v[56:59], v[216:219], v[140:143], v[56:59]
s_waitcnt lgkmcnt(8)
v_mfma_f32_16x16x32_bf16 v[60:63], v[220:223], v[140:143], v[60:63]
ds_read_b64_tr_b16 v[216:217], v234 offset:36864
ds_read_b64_tr_b16 v[218:219], v234 offset:43008
v_mfma_f32_16x16x32_bf16 v[20:23], v[220:223], v[136:139], v[20:23]
)"
R"(s_waitcnt lgkmcnt(8)
v_mfma_f32_16x16x32_bf16 v[24:27], v[224:227], v[136:139], v[24:27]
ds_read_b64_tr_b16 v[220:221], v235 offset:36864
ds_read_b64_tr_b16 v[222:223], v235 offset:43008
v_mfma_f32_16x16x32_bf16 v[64:67], v[224:227], v[140:143], v[64:67]
s_waitcnt lgkmcnt(8)
v_mfma_f32_16x16x32_bf16 v[68:71], v[228:231], v[140:143], v[68:71]
ds_read_b64_tr_b16 v[224:225], v236 offset:36864
ds_read_b64_tr_b16 v[226:227], v236 offset:43008
v_mfma_f32_16x16x32_bf16 v[28:31], v[228:231], v[136:139], v[28:31]
s_waitcnt lgkmcnt(8)
buffer_load_dwordx4 v[164:167], v247, s[40:43], s51 offen offset:1024 sc1
v_mfma_f32_16x16x32_bf16 v[32:35], v[208:211], v[136:139], v[32:35]
ds_read_b64_tr_b16 v[228:229], v237 offset:36864
ds_read_b64_tr_b16 v[230:231], v237 offset:43008
v_mfma_f32_16x16x32_bf16 v[72:75], v[208:211], v[140:143], v[72:75]
)"
R"(s_waitcnt lgkmcnt(8)
v_mfma_f32_16x16x32_bf16 v[76:79], v[212:215], v[140:143], v[76:79]
ds_read_b64_tr_b16 v[208:209], v234 offset:36992
ds_read_b64_tr_b16 v[210:211], v234 offset:43136
v_mfma_f32_16x16x32_bf16 v[36:39], v[212:215], v[136:139], v[36:39]
s_waitcnt lgkmcnt(8)
s_waitcnt vmcnt(4)
v_mfma_f32_16x16x32_bf16 v[0:3], v[216:219], v[144:147], v[0:3]
ds_read_b64_tr_b16 v[212:213], v235 offset:36992
ds_read_b64_tr_b16 v[214:215], v235 offset:43136
v_mfma_f32_16x16x32_bf16 v[40:43], v[216:219], v[148:151], v[40:43]
s_waitcnt lgkmcnt(8)
v_mfma_f32_16x16x32_bf16 v[44:47], v[220:223], v[148:151], v[44:47]
ds_read_b64_tr_b16 v[216:217], v236 offset:36992
ds_read_b64_tr_b16 v[218:219], v236 offset:43136
v_mfma_f32_16x16x32_bf16 v[4:7], v[220:223], v[144:147], v[4:7]
)"
R"(s_waitcnt lgkmcnt(8)
v_mfma_f32_16x16x32_bf16 v[8:11], v[224:227], v[144:147], v[8:11]
ds_read_b64_tr_b16 v[220:221], v237 offset:36992
ds_read_b64_tr_b16 v[222:223], v237 offset:43136
v_mfma_f32_16x16x32_bf16 v[48:51], v[224:227], v[148:151], v[48:51]
s_waitcnt lgkmcnt(8)
buffer_load_dwordx4 v[168:171], v247, s[40:43], s51 offen offset:2048 sc1
v_mfma_f32_16x16x32_bf16 v[52:55], v[228:231], v[148:151], v[52:55]
ds_read_b64_tr_b16 v[224:225], v234 offset:37120
ds_read_b64_tr_b16 v[226:227], v234 offset:43264
v_mfma_f32_16x16x32_bf16 v[12:15], v[228:231], v[144:147], v[12:15]
s_waitcnt lgkmcnt(8)
v_mfma_f32_16x16x32_bf16 v[16:19], v[208:211], v[144:147], v[16:19]
ds_read_b64_tr_b16 v[228:229], v235 offset:37120
ds_read_b64_tr_b16 v[230:231], v235 offset:43264
v_mfma_f32_16x16x32_bf16 v[56:59], v[208:211], v[148:151], v[56:59]
)"
R"(s_waitcnt lgkmcnt(8)
v_mfma_f32_16x16x32_bf16 v[60:63], v[212:215], v[148:151], v[60:63]
ds_read_b64_tr_b16 v[208:209], v234 offset:24576
ds_read_b64_tr_b16 v[210:211], v234 offset:30720
v_mfma_f32_16x16x32_bf16 v[20:23], v[212:215], v[144:147], v[20:23]
s_waitcnt lgkmcnt(8)
v_mfma_f32_16x16x32_bf16 v[24:27], v[216:219], v[144:147], v[24:27]
ds_read_b64_tr_b16 v[212:213], v235 offset:24576
ds_read_b64_tr_b16 v[214:215], v235 offset:30720
v_mfma_f32_16x16x32_bf16 v[64:67], v[216:219], v[148:151], v[64:67]
s_waitcnt lgkmcnt(8)
v_mfma_f32_16x16x32_bf16 v[68:71], v[220:223], v[148:151], v[68:71]
ds_read_b64_tr_b16 v[216:217], v236 offset:24576
ds_read_b64_tr_b16 v[218:219], v236 offset:30720
v_mfma_f32_16x16x32_bf16 v[28:31], v[220:223], v[144:147], v[28:31]
s_waitcnt lgkmcnt(8)
)"
R"(buffer_load_dwordx4 v[172:175], v247, s[40:43], s51 offen offset:3072 sc1
v_mfma_f32_16x16x32_bf16 v[32:35], v[224:227], v[144:147], v[32:35]
ds_read_b64_tr_b16 v[220:221], v237 offset:24576
ds_read_b64_tr_b16 v[222:223], v237 offset:30720
v_mfma_f32_16x16x32_bf16 v[72:75], v[224:227], v[148:151], v[72:75]
s_waitcnt lgkmcnt(8)
v_mfma_f32_16x16x32_bf16 v[76:79], v[228:231], v[148:151], v[76:79]
ds_read_b64_tr_b16 v[224:225], v234 offset:24704
ds_read_b64_tr_b16 v[226:227], v234 offset:30848
v_mfma_f32_16x16x32_bf16 v[36:39], v[228:231], v[144:147], v[36:39]
s_waitcnt lgkmcnt(8)
s_barrier
s_waitcnt vmcnt(4)
v_mfma_f32_16x16x32_bf16 v[0:3], v[208:211], v[152:155], v[0:3]
ds_read_b64_tr_b16 v[228:229], v235 offset:24704
ds_read_b64_tr_b16 v[230:231], v235 offset:30848
)"
R"(v_mfma_f32_16x16x32_bf16 v[40:43], v[208:211], v[156:159], v[40:43]
s_waitcnt lgkmcnt(8)
v_mfma_f32_16x16x32_bf16 v[44:47], v[212:215], v[156:159], v[44:47]
ds_read_b64_tr_b16 v[208:209], v236 offset:24704
ds_read_b64_tr_b16 v[210:211], v236 offset:30848
v_mfma_f32_16x16x32_bf16 v[4:7], v[212:215], v[152:155], v[4:7]
s_waitcnt lgkmcnt(8)
v_mfma_f32_16x16x32_bf16 v[8:11], v[216:219], v[152:155], v[8:11]
ds_read_b64_tr_b16 v[212:213], v237 offset:24704
ds_read_b64_tr_b16 v[214:215], v237 offset:30848
v_mfma_f32_16x16x32_bf16 v[48:51], v[216:219], v[156:159], v[48:51]
s_waitcnt lgkmcnt(8)
s_mul_i32 s51, s98, 12288
s_add_u32 s51, s51, s54
s_add_u32 s51, s51, 12582912
buffer_load_dwordx4 v[176:179], v247, s[40:43], s51 offen sc1
)"
R"(v_mfma_f32_16x16x32_bf16 v[52:55], v[220:223], v[156:159], v[52:55]
ds_read_b64_tr_b16 v[216:217], v234 offset:24832
ds_read_b64_tr_b16 v[218:219], v234 offset:30976
v_mfma_f32_16x16x32_bf16 v[12:15], v[220:223], v[152:155], v[12:15]
s_waitcnt lgkmcnt(8)
v_mfma_f32_16x16x32_bf16 v[16:19], v[224:227], v[152:155], v[16:19]
ds_read_b64_tr_b16 v[220:221], v235 offset:24832
ds_read_b64_tr_b16 v[222:223], v235 offset:30976
v_mfma_f32_16x16x32_bf16 v[56:59], v[224:227], v[156:159], v[56:59]
s_waitcnt lgkmcnt(8)
v_mfma_f32_16x16x32_bf16 v[60:63], v[228:231], v[156:159], v[60:63]
ds_read_b64_tr_b16 v[224:225], v240 offset:24576
ds_read_b64_tr_b16 v[226:227], v240 offset:30720
v_mfma_f32_16x16x32_bf16 v[20:23], v[228:231], v[152:155], v[20:23]
s_waitcnt lgkmcnt(8)
v_mfma_f32_16x16x32_bf16 v[24:27], v[208:211], v[152:155], v[24:27]
)"
R"(ds_read_b64_tr_b16 v[228:229], v241 offset:24576
ds_read_b64_tr_b16 v[230:231], v241 offset:30720
v_mfma_f32_16x16x32_bf16 v[64:67], v[208:211], v[156:159], v[64:67]
s_waitcnt lgkmcnt(8)
v_mfma_f32_16x16x32_bf16 v[68:71], v[212:215], v[156:159], v[68:71]
ds_read_b64_tr_b16 v[208:209], v242 offset:24576
ds_read_b64_tr_b16 v[210:211], v242 offset:30720
v_mfma_f32_16x16x32_bf16 v[28:31], v[212:215], v[152:155], v[28:31]
s_waitcnt lgkmcnt(8)
buffer_load_dwordx4 v[180:183], v247, s[40:43], s51 offen offset:1024 sc1
v_mfma_f32_16x16x32_bf16 v[32:35], v[216:219], v[152:155], v[32:35]
ds_read_b64_tr_b16 v[212:213], v243 offset:24576
ds_read_b64_tr_b16 v[214:215], v243 offset:30720
v_mfma_f32_16x16x32_bf16 v[72:75], v[216:219], v[156:159], v[72:75]
s_waitcnt lgkmcnt(8)
v_mfma_f32_16x16x32_bf16 v[76:79], v[220:223], v[156:159], v[76:79]
)"
R"(ds_read_b64_tr_b16 v[216:217], v240 offset:24704
ds_read_b64_tr_b16 v[218:219], v240 offset:30848
v_mfma_f32_16x16x32_bf16 v[36:39], v[220:223], v[152:155], v[36:39]
s_waitcnt lgkmcnt(8)
s_waitcnt vmcnt(4)
v_mfma_f32_16x16x32_bf16 v[0:3], v[224:227], v[160:163], v[0:3]
ds_read_b64_tr_b16 v[220:221], v241 offset:24704
ds_read_b64_tr_b16 v[222:223], v241 offset:30848
v_mfma_f32_16x16x32_bf16 v[40:43], v[224:227], v[164:167], v[40:43]
s_waitcnt lgkmcnt(8)
v_mfma_f32_16x16x32_bf16 v[44:47], v[228:231], v[164:167], v[44:47]
ds_read_b64_tr_b16 v[224:225], v242 offset:24704
ds_read_b64_tr_b16 v[226:227], v242 offset:30848
v_mfma_f32_16x16x32_bf16 v[4:7], v[228:231], v[160:163], v[4:7]
s_waitcnt lgkmcnt(8)
v_mfma_f32_16x16x32_bf16 v[8:11], v[208:211], v[160:163], v[8:11]
)"
R"(ds_read_b64_tr_b16 v[228:229], v243 offset:24704
ds_read_b64_tr_b16 v[230:231], v243 offset:30848
v_mfma_f32_16x16x32_bf16 v[48:51], v[208:211], v[164:167], v[48:51]
s_waitcnt lgkmcnt(8)
buffer_load_dwordx4 v[184:187], v247, s[40:43], s51 offen offset:2048 sc1
v_mfma_f32_16x16x32_bf16 v[52:55], v[212:215], v[164:167], v[52:55]
ds_read_b64_tr_b16 v[208:209], v240 offset:24832
ds_read_b64_tr_b16 v[210:211], v240 offset:30976
v_mfma_f32_16x16x32_bf16 v[12:15], v[212:215], v[160:163], v[12:15]
s_waitcnt lgkmcnt(8)
v_mfma_f32_16x16x32_bf16 v[16:19], v[216:219], v[160:163], v[16:19]
ds_read_b64_tr_b16 v[212:213], v241 offset:24832
ds_read_b64_tr_b16 v[214:215], v241 offset:30976
v_mfma_f32_16x16x32_bf16 v[56:59], v[216:219], v[164:167], v[56:59]
s_waitcnt lgkmcnt(8)
v_mfma_f32_16x16x32_bf16 v[60:63], v[220:223], v[164:167], v[60:63]
)"
R"(ds_read_b64_tr_b16 v[216:217], v232 offset:36864
ds_read_b64_tr_b16 v[218:219], v232 offset:43008
v_mfma_f32_16x16x32_bf16 v[20:23], v[220:223], v[160:163], v[20:23]
s_waitcnt lgkmcnt(8)
v_mfma_f32_16x16x32_bf16 v[24:27], v[224:227], v[160:163], v[24:27]
ds_read_b64_tr_b16 v[220:221], v233 offset:36864
ds_read_b64_tr_b16 v[222:223], v233 offset:43008
v_mfma_f32_16x16x32_bf16 v[64:67], v[224:227], v[164:167], v[64:67]
s_waitcnt lgkmcnt(8)
v_mfma_f32_16x16x32_bf16 v[68:71], v[228:231], v[164:167], v[68:71]
ds_read_b64_tr_b16 v[224:225], v238 offset:36864
ds_read_b64_tr_b16 v[226:227], v238 offset:43008
v_mfma_f32_16x16x32_bf16 v[28:31], v[228:231], v[160:163], v[28:31]
s_waitcnt lgkmcnt(8)
buffer_load_dwordx4 v[188:191], v247, s[40:43], s51 offen offset:3072 sc1
v_mfma_f32_16x16x32_bf16 v[32:35], v[208:211], v[160:163], v[32:35]
)"
R"(ds_read_b64_tr_b16 v[228:229], v239 offset:36864
ds_read_b64_tr_b16 v[230:231], v239 offset:43008
v_mfma_f32_16x16x32_bf16 v[72:75], v[208:211], v[164:167], v[72:75]
s_waitcnt lgkmcnt(8)
v_mfma_f32_16x16x32_bf16 v[76:79], v[212:215], v[164:167], v[76:79]
ds_read_b64_tr_b16 v[208:209], v232 offset:36992
ds_read_b64_tr_b16 v[210:211], v232 offset:43136
v_mfma_f32_16x16x32_bf16 v[36:39], v[212:215], v[160:163], v[36:39]
s_waitcnt lgkmcnt(8)
s_waitcnt vmcnt(4)
v_mfma_f32_16x16x32_bf16 v[0:3], v[216:219], v[168:171], v[0:3]
ds_read_b64_tr_b16 v[212:213], v233 offset:36992
ds_read_b64_tr_b16 v[214:215], v233 offset:43136
v_mfma_f32_16x16x32_bf16 v[40:43], v[216:219], v[172:175], v[40:43]
s_waitcnt lgkmcnt(8)
v_mfma_f32_16x16x32_bf16 v[44:47], v[220:223], v[172:175], v[44:47]
)"
R"(ds_read_b64_tr_b16 v[216:217], v238 offset:36992
ds_read_b64_tr_b16 v[218:219], v238 offset:43136
v_mfma_f32_16x16x32_bf16 v[4:7], v[220:223], v[168:171], v[4:7]
s_waitcnt lgkmcnt(8)
v_mfma_f32_16x16x32_bf16 v[8:11], v[224:227], v[168:171], v[8:11]
ds_read_b64_tr_b16 v[220:221], v239 offset:36992
ds_read_b64_tr_b16 v[222:223], v239 offset:43136
v_mfma_f32_16x16x32_bf16 v[48:51], v[224:227], v[172:175], v[48:51]
s_waitcnt lgkmcnt(8)
s_add_u32 s51, s51, 4096
buffer_load_dwordx4 v[192:195], v247, s[40:43], s51 offen sc1
v_mfma_f32_16x16x32_bf16 v[52:55], v[228:231], v[172:175], v[52:55]
ds_read_b64_tr_b16 v[224:225], v232 offset:37120
ds_read_b64_tr_b16 v[226:227], v232 offset:43264
v_mfma_f32_16x16x32_bf16 v[12:15], v[228:231], v[168:171], v[12:15]
s_waitcnt lgkmcnt(8)
)"
R"(v_mfma_f32_16x16x32_bf16 v[16:19], v[208:211], v[168:171], v[16:19]
ds_read_b64_tr_b16 v[228:229], v233 offset:37120
ds_read_b64_tr_b16 v[230:231], v233 offset:43264
v_mfma_f32_16x16x32_bf16 v[56:59], v[208:211], v[172:175], v[56:59]
s_waitcnt lgkmcnt(8)
v_mfma_f32_16x16x32_bf16 v[60:63], v[212:215], v[172:175], v[60:63]
ds_read_b64_tr_b16 v[208:209], v234 offset:12288
ds_read_b64_tr_b16 v[210:211], v234 offset:18432
v_mfma_f32_16x16x32_bf16 v[20:23], v[212:215], v[168:171], v[20:23]
s_waitcnt lgkmcnt(8)
v_mfma_f32_16x16x32_bf16 v[24:27], v[216:219], v[168:171], v[24:27]
ds_read_b64_tr_b16 v[212:213], v235 offset:12288
ds_read_b64_tr_b16 v[214:215], v235 offset:18432
v_mfma_f32_16x16x32_bf16 v[64:67], v[216:219], v[172:175], v[64:67]
s_waitcnt lgkmcnt(8)
v_mfma_f32_16x16x32_bf16 v[68:71], v[220:223], v[172:175], v[68:71]
)"
R"(ds_read_b64_tr_b16 v[216:217], v236 offset:12288
ds_read_b64_tr_b16 v[218:219], v236 offset:18432
v_mfma_f32_16x16x32_bf16 v[28:31], v[220:223], v[168:171], v[28:31]
s_waitcnt lgkmcnt(8)
buffer_load_dwordx4 v[196:199], v247, s[40:43], s51 offen offset:1024 sc1
v_mfma_f32_16x16x32_bf16 v[32:35], v[224:227], v[168:171], v[32:35]
ds_read_b64_tr_b16 v[220:221], v237 offset:12288
ds_read_b64_tr_b16 v[222:223], v237 offset:18432
v_mfma_f32_16x16x32_bf16 v[72:75], v[224:227], v[172:175], v[72:75]
s_waitcnt lgkmcnt(8)
v_mfma_f32_16x16x32_bf16 v[76:79], v[228:231], v[172:175], v[76:79]
ds_read_b64_tr_b16 v[224:225], v234 offset:12416
ds_read_b64_tr_b16 v[226:227], v234 offset:18560
v_mfma_f32_16x16x32_bf16 v[36:39], v[228:231], v[168:171], v[36:39]
s_waitcnt lgkmcnt(8)
s_barrier
)"
R"(s_waitcnt vmcnt(4)
v_mfma_f32_16x16x32_bf16 v[0:3], v[208:211], v[176:179], v[0:3]
ds_read_b64_tr_b16 v[228:229], v235 offset:12416
ds_read_b64_tr_b16 v[230:231], v235 offset:18560
v_mfma_f32_16x16x32_bf16 v[40:43], v[208:211], v[180:183], v[40:43]
s_waitcnt lgkmcnt(8)
v_mfma_f32_16x16x32_bf16 v[44:47], v[212:215], v[180:183], v[44:47]
ds_read_b64_tr_b16 v[208:209], v236 offset:12416
ds_read_b64_tr_b16 v[210:211], v236 offset:18560
v_mfma_f32_16x16x32_bf16 v[4:7], v[212:215], v[176:179], v[4:7]
s_waitcnt lgkmcnt(8)
v_mfma_f32_16x16x32_bf16 v[8:11], v[216:219], v[176:179], v[8:11]
ds_read_b64_tr_b16 v[212:213], v237 offset:12416
ds_read_b64_tr_b16 v[214:215], v237 offset:18560
v_mfma_f32_16x16x32_bf16 v[48:51], v[216:219], v[180:183], v[48:51]
s_waitcnt lgkmcnt(8)
)"
R"(buffer_load_dwordx4 v[200:203], v247, s[40:43], s51 offen offset:2048 sc1
v_mfma_f32_16x16x32_bf16 v[52:55], v[220:223], v[180:183], v[52:55]
ds_read_b64_tr_b16 v[216:217], v234 offset:12544
ds_read_b64_tr_b16 v[218:219], v234 offset:18688
v_mfma_f32_16x16x32_bf16 v[12:15], v[220:223], v[176:179], v[12:15]
s_waitcnt lgkmcnt(8)
v_mfma_f32_16x16x32_bf16 v[16:19], v[224:227], v[176:179], v[16:19]
ds_read_b64_tr_b16 v[220:221], v235 offset:12544
ds_read_b64_tr_b16 v[222:223], v235 offset:18688
v_mfma_f32_16x16x32_bf16 v[56:59], v[224:227], v[180:183], v[56:59]
s_waitcnt lgkmcnt(8)
v_mfma_f32_16x16x32_bf16 v[60:63], v[228:231], v[180:183], v[60:63]
ds_read_b64_tr_b16 v[224:225], v234 offset:49152
ds_read_b64_tr_b16 v[226:227], v234 offset:55296
v_mfma_f32_16x16x32_bf16 v[20:23], v[228:231], v[176:179], v[20:23]
s_waitcnt lgkmcnt(8)
)"
R"(v_mfma_f32_16x16x32_bf16 v[24:27], v[208:211], v[176:179], v[24:27]
ds_read_b64_tr_b16 v[228:229], v235 offset:49152
ds_read_b64_tr_b16 v[230:231], v235 offset:55296
v_mfma_f32_16x16x32_bf16 v[64:67], v[208:211], v[180:183], v[64:67]
s_waitcnt lgkmcnt(8)
v_mfma_f32_16x16x32_bf16 v[68:71], v[212:215], v[180:183], v[68:71]
ds_read_b64_tr_b16 v[208:209], v236 offset:49152
ds_read_b64_tr_b16 v[210:211], v236 offset:55296
v_mfma_f32_16x16x32_bf16 v[28:31], v[212:215], v[176:179], v[28:31]
s_waitcnt lgkmcnt(8)
buffer_load_dwordx4 v[204:207], v247, s[40:43], s51 offen offset:3072 sc1
v_mfma_f32_16x16x32_bf16 v[32:35], v[216:219], v[176:179], v[32:35]
ds_read_b64_tr_b16 v[212:213], v237 offset:49152
ds_read_b64_tr_b16 v[214:215], v237 offset:55296
v_mfma_f32_16x16x32_bf16 v[72:75], v[216:219], v[180:183], v[72:75]
s_waitcnt lgkmcnt(8)
)"
R"(v_mfma_f32_16x16x32_bf16 v[76:79], v[220:223], v[180:183], v[76:79]
ds_read_b64_tr_b16 v[216:217], v234 offset:49280
ds_read_b64_tr_b16 v[218:219], v234 offset:55424
v_mfma_f32_16x16x32_bf16 v[36:39], v[220:223], v[176:179], v[36:39]
s_waitcnt lgkmcnt(8)
s_waitcnt vmcnt(4)
v_mfma_f32_16x16x32_bf16 v[0:3], v[224:227], v[184:187], v[0:3]
ds_read_b64_tr_b16 v[220:221], v235 offset:49280
ds_read_b64_tr_b16 v[222:223], v235 offset:55424
v_mfma_f32_16x16x32_bf16 v[40:43], v[224:227], v[188:191], v[40:43]
s_waitcnt lgkmcnt(8)
v_mfma_f32_16x16x32_bf16 v[44:47], v[228:231], v[188:191], v[44:47]
ds_read_b64_tr_b16 v[224:225], v236 offset:49280
ds_read_b64_tr_b16 v[226:227], v236 offset:55424
v_mfma_f32_16x16x32_bf16 v[4:7], v[228:231], v[184:187], v[4:7]
s_waitcnt lgkmcnt(8)
)"
R"(v_mfma_f32_16x16x32_bf16 v[8:11], v[208:211], v[184:187], v[8:11]
ds_read_b64_tr_b16 v[228:229], v237 offset:49280
ds_read_b64_tr_b16 v[230:231], v237 offset:55424
v_mfma_f32_16x16x32_bf16 v[48:51], v[208:211], v[188:191], v[48:51]
s_waitcnt lgkmcnt(8)
s_add_u32 s51, s51, 4096
buffer_load_dwordx4 v[80:83], v247, s[40:43], s51 offen sc1
v_mfma_f32_16x16x32_bf16 v[52:55], v[212:215], v[188:191], v[52:55]
ds_read_b64_tr_b16 v[208:209], v234 offset:49408
ds_read_b64_tr_b16 v[210:211], v234 offset:55552
v_mfma_f32_16x16x32_bf16 v[12:15], v[212:215], v[184:187], v[12:15]
s_waitcnt lgkmcnt(8)
v_mfma_f32_16x16x32_bf16 v[16:19], v[216:219], v[184:187], v[16:19]
ds_read_b64_tr_b16 v[212:213], v235 offset:49408
ds_read_b64_tr_b16 v[214:215], v235 offset:55552
v_mfma_f32_16x16x32_bf16 v[56:59], v[216:219], v[188:191], v[56:59]
)"
R"(s_waitcnt lgkmcnt(8)
v_mfma_f32_16x16x32_bf16 v[60:63], v[220:223], v[188:191], v[60:63]
ds_read_b64_tr_b16 v[216:217], v232 offset:0
ds_read_b64_tr_b16 v[218:219], v232 offset:6144
v_mfma_f32_16x16x32_bf16 v[20:23], v[220:223], v[184:187], v[20:23]
s_waitcnt lgkmcnt(8)
v_mfma_f32_16x16x32_bf16 v[24:27], v[224:227], v[184:187], v[24:27]
ds_read_b64_tr_b16 v[220:221], v233 offset:0
ds_read_b64_tr_b16 v[222:223], v233 offset:6144
v_mfma_f32_16x16x32_bf16 v[64:67], v[224:227], v[188:191], v[64:67]
s_waitcnt lgkmcnt(8)
v_mfma_f32_16x16x32_bf16 v[68:71], v[228:231], v[188:191], v[68:71]
ds_read_b64_tr_b16 v[224:225], v238 offset:0
ds_read_b64_tr_b16 v[226:227], v238 offset:6144
v_mfma_f32_16x16x32_bf16 v[28:31], v[228:231], v[184:187], v[28:31]
s_waitcnt lgkmcnt(8)
)"
R"(buffer_load_dwordx4 v[84:87], v247, s[40:43], s51 offen offset:1024 sc1
v_mfma_f32_16x16x32_bf16 v[32:35], v[208:211], v[184:187], v[32:35]
ds_read_b64_tr_b16 v[228:229], v239 offset:0
ds_read_b64_tr_b16 v[230:231], v239 offset:6144
v_mfma_f32_16x16x32_bf16 v[72:75], v[208:211], v[188:191], v[72:75]
s_waitcnt lgkmcnt(8)
v_mfma_f32_16x16x32_bf16 v[76:79], v[212:215], v[188:191], v[76:79]
ds_read_b64_tr_b16 v[208:209], v232 offset:128
ds_read_b64_tr_b16 v[210:211], v232 offset:6272
v_mfma_f32_16x16x32_bf16 v[36:39], v[212:215], v[184:187], v[36:39]
s_waitcnt lgkmcnt(8)
s_waitcnt vmcnt(4)
v_mfma_f32_16x16x32_bf16 v[0:3], v[216:219], v[192:195], v[0:3]
ds_read_b64_tr_b16 v[212:213], v233 offset:128
ds_read_b64_tr_b16 v[214:215], v233 offset:6272
v_mfma_f32_16x16x32_bf16 v[40:43], v[216:219], v[196:199], v[40:43]
)"
R"(s_waitcnt lgkmcnt(8)
v_mfma_f32_16x16x32_bf16 v[44:47], v[220:223], v[196:199], v[44:47]
ds_read_b64_tr_b16 v[216:217], v238 offset:128
ds_read_b64_tr_b16 v[218:219], v238 offset:6272
v_mfma_f32_16x16x32_bf16 v[4:7], v[220:223], v[192:195], v[4:7]
s_waitcnt lgkmcnt(8)
v_mfma_f32_16x16x32_bf16 v[8:11], v[224:227], v[192:195], v[8:11]
ds_read_b64_tr_b16 v[220:221], v239 offset:128
ds_read_b64_tr_b16 v[222:223], v239 offset:6272
v_mfma_f32_16x16x32_bf16 v[48:51], v[224:227], v[196:199], v[48:51]
s_waitcnt lgkmcnt(8)
buffer_load_dwordx4 v[88:91], v247, s[40:43], s51 offen offset:2048 sc1
v_mfma_f32_16x16x32_bf16 v[52:55], v[228:231], v[196:199], v[52:55]
ds_read_b64_tr_b16 v[224:225], v232 offset:256
ds_read_b64_tr_b16 v[226:227], v232 offset:6400
v_mfma_f32_16x16x32_bf16 v[12:15], v[228:231], v[192:195], v[12:15]
)"
R"(s_waitcnt lgkmcnt(8)
v_mfma_f32_16x16x32_bf16 v[16:19], v[208:211], v[192:195], v[16:19]
ds_read_b64_tr_b16 v[228:229], v233 offset:256
ds_read_b64_tr_b16 v[230:231], v233 offset:6400
v_mfma_f32_16x16x32_bf16 v[56:59], v[208:211], v[196:199], v[56:59]
s_waitcnt lgkmcnt(8)
v_mfma_f32_16x16x32_bf16 v[60:63], v[212:215], v[196:199], v[60:63]
ds_read_b64_tr_b16 v[208:209], v232 offset:12288
ds_read_b64_tr_b16 v[210:211], v232 offset:18432
v_mfma_f32_16x16x32_bf16 v[20:23], v[212:215], v[192:195], v[20:23]
s_waitcnt lgkmcnt(8)
v_mfma_f32_16x16x32_bf16 v[24:27], v[216:219], v[192:195], v[24:27]
ds_read_b64_tr_b16 v[212:213], v233 offset:12288
ds_read_b64_tr_b16 v[214:215], v233 offset:18432
v_mfma_f32_16x16x32_bf16 v[64:67], v[216:219], v[196:199], v[64:67]
s_waitcnt lgkmcnt(8)
)"
R"(v_mfma_f32_16x16x32_bf16 v[68:71], v[220:223], v[196:199], v[68:71]
ds_read_b64_tr_b16 v[216:217], v238 offset:12288
ds_read_b64_tr_b16 v[218:219], v238 offset:18432
v_mfma_f32_16x16x32_bf16 v[28:31], v[220:223], v[192:195], v[28:31]
s_waitcnt lgkmcnt(8)
buffer_load_dwordx4 v[92:95], v247, s[40:43], s51 offen offset:3072 sc1
v_mfma_f32_16x16x32_bf16 v[32:35], v[224:227], v[192:195], v[32:35]
ds_read_b64_tr_b16 v[220:221], v239 offset:12288
ds_read_b64_tr_b16 v[222:223], v239 offset:18432
v_mfma_f32_16x16x32_bf16 v[72:75], v[224:227], v[196:199], v[72:75]
s_waitcnt lgkmcnt(8)
v_mfma_f32_16x16x32_bf16 v[76:79], v[228:231], v[196:199], v[76:79]
ds_read_b64_tr_b16 v[224:225], v232 offset:12416
ds_read_b64_tr_b16 v[226:227], v232 offset:18560
v_mfma_f32_16x16x32_bf16 v[36:39], v[228:231], v[192:195], v[36:39]
s_waitcnt lgkmcnt(8)
)"
R"(s_barrier
s_waitcnt vmcnt(4)
v_mfma_f32_16x16x32_bf16 v[0:3], v[208:211], v[200:203], v[0:3]
ds_read_b64_tr_b16 v[228:229], v233 offset:12416
ds_read_b64_tr_b16 v[230:231], v233 offset:18560
v_mfma_f32_16x16x32_bf16 v[40:43], v[208:211], v[204:207], v[40:43]
s_waitcnt lgkmcnt(8)
v_mfma_f32_16x16x32_bf16 v[44:47], v[212:215], v[204:207], v[44:47]
ds_read_b64_tr_b16 v[208:209], v238 offset:12416
ds_read_b64_tr_b16 v[210:211], v238 offset:18560
v_mfma_f32_16x16x32_bf16 v[4:7], v[212:215], v[200:203], v[4:7]
s_waitcnt lgkmcnt(8)
v_mfma_f32_16x16x32_bf16 v[8:11], v[216:219], v[200:203], v[8:11]
ds_read_b64_tr_b16 v[212:213], v239 offset:12416
ds_read_b64_tr_b16 v[214:215], v239 offset:18560
v_mfma_f32_16x16x32_bf16 v[48:51], v[216:219], v[204:207], v[48:51]
)"
R"(s_waitcnt lgkmcnt(8)
s_mul_i32 s51, s99, 12288
s_add_u32 s51, s51, s54
s_add_u32 s51, s51, 12582912
buffer_load_dwordx4 v[96:99], v247, s[40:43], s51 offen sc1
v_mfma_f32_16x16x32_bf16 v[52:55], v[220:223], v[204:207], v[52:55]
ds_read_b64_tr_b16 v[216:217], v232 offset:12544
ds_read_b64_tr_b16 v[218:219], v232 offset:18688
v_mfma_f32_16x16x32_bf16 v[12:15], v[220:223], v[200:203], v[12:15]
s_waitcnt lgkmcnt(8)
v_mfma_f32_16x16x32_bf16 v[16:19], v[224:227], v[200:203], v[16:19]
ds_read_b64_tr_b16 v[220:221], v233 offset:12544
ds_read_b64_tr_b16 v[222:223], v233 offset:18688
v_mfma_f32_16x16x32_bf16 v[56:59], v[224:227], v[204:207], v[56:59]
s_waitcnt lgkmcnt(8)
v_mfma_f32_16x16x32_bf16 v[60:63], v[228:231], v[204:207], v[60:63]
)"
R"(ds_read_b64_tr_b16 v[224:225], v240 offset:0
ds_read_b64_tr_b16 v[226:227], v240 offset:6144
v_mfma_f32_16x16x32_bf16 v[20:23], v[228:231], v[200:203], v[20:23]
s_waitcnt lgkmcnt(8)
v_mfma_f32_16x16x32_bf16 v[24:27], v[208:211], v[200:203], v[24:27]
ds_read_b64_tr_b16 v[228:229], v241 offset:0
ds_read_b64_tr_b16 v[230:231], v241 offset:6144
v_mfma_f32_16x16x32_bf16 v[64:67], v[208:211], v[204:207], v[64:67]
s_waitcnt lgkmcnt(8)
v_mfma_f32_16x16x32_bf16 v[68:71], v[212:215], v[204:207], v[68:71]
ds_read_b64_tr_b16 v[208:209], v242 offset:0
ds_read_b64_tr_b16 v[210:211], v242 offset:6144
v_mfma_f32_16x16x32_bf16 v[28:31], v[212:215], v[200:203], v[28:31]
s_waitcnt lgkmcnt(8)
buffer_load_dwordx4 v[100:103], v247, s[40:43], s51 offen offset:1024 sc1
v_mfma_f32_16x16x32_bf16 v[32:35], v[216:219], v[200:203], v[32:35]
)"
R"(ds_read_b64_tr_b16 v[212:213], v243 offset:0
ds_read_b64_tr_b16 v[214:215], v243 offset:6144
v_mfma_f32_16x16x32_bf16 v[72:75], v[216:219], v[204:207], v[72:75]
s_waitcnt lgkmcnt(8)
v_mfma_f32_16x16x32_bf16 v[76:79], v[220:223], v[204:207], v[76:79]
ds_read_b64_tr_b16 v[216:217], v240 offset:128
ds_read_b64_tr_b16 v[218:219], v240 offset:6272
v_mfma_f32_16x16x32_bf16 v[36:39], v[220:223], v[200:203], v[36:39]
s_waitcnt lgkmcnt(8)
s_waitcnt vmcnt(4)
v_mfma_f32_16x16x32_bf16 v[0:3], v[224:227], v[80:83], v[0:3]
ds_read_b64_tr_b16 v[220:221], v241 offset:128
ds_read_b64_tr_b16 v[222:223], v241 offset:6272
v_mfma_f32_16x16x32_bf16 v[40:43], v[224:227], v[84:87], v[40:43]
s_waitcnt lgkmcnt(8)
v_mfma_f32_16x16x32_bf16 v[44:47], v[228:231], v[84:87], v[44:47]
)"
R"(ds_read_b64_tr_b16 v[224:225], v242 offset:128
ds_read_b64_tr_b16 v[226:227], v242 offset:6272
v_mfma_f32_16x16x32_bf16 v[4:7], v[228:231], v[80:83], v[4:7]
s_waitcnt lgkmcnt(8)
v_mfma_f32_16x16x32_bf16 v[8:11], v[208:211], v[80:83], v[8:11]
ds_read_b64_tr_b16 v[228:229], v243 offset:128
ds_read_b64_tr_b16 v[230:231], v243 offset:6272
v_mfma_f32_16x16x32_bf16 v[48:51], v[208:211], v[84:87], v[48:51]
s_waitcnt lgkmcnt(8)
buffer_load_dwordx4 v[104:107], v247, s[40:43], s51 offen offset:2048 sc1
v_mfma_f32_16x16x32_bf16 v[52:55], v[212:215], v[84:87], v[52:55]
ds_read_b64_tr_b16 v[208:209], v240 offset:256
ds_read_b64_tr_b16 v[210:211], v240 offset:6400
v_mfma_f32_16x16x32_bf16 v[12:15], v[212:215], v[80:83], v[12:15]
s_waitcnt lgkmcnt(8)
v_mfma_f32_16x16x32_bf16 v[16:19], v[216:219], v[80:83], v[16:19]
)"
R"(ds_read_b64_tr_b16 v[212:213], v241 offset:256
ds_read_b64_tr_b16 v[214:215], v241 offset:6400
v_mfma_f32_16x16x32_bf16 v[56:59], v[216:219], v[84:87], v[56:59]
s_waitcnt lgkmcnt(8)
v_mfma_f32_16x16x32_bf16 v[60:63], v[220:223], v[84:87], v[60:63]
ds_read_b64_tr_b16 v[216:217], v234 offset:0
ds_read_b64_tr_b16 v[218:219], v234 offset:6144
v_mfma_f32_16x16x32_bf16 v[20:23], v[220:223], v[80:83], v[20:23]
s_waitcnt lgkmcnt(8)
v_mfma_f32_16x16x32_bf16 v[24:27], v[224:227], v[80:83], v[24:27]
ds_read_b64_tr_b16 v[220:221], v235 offset:0
ds_read_b64_tr_b16 v[222:223], v235 offset:6144
v_mfma_f32_16x16x32_bf16 v[64:67], v[224:227], v[84:87], v[64:67]
s_waitcnt lgkmcnt(8)
v_mfma_f32_16x16x32_bf16 v[68:71], v[228:231], v[84:87], v[68:71]
ds_read_b64_tr_b16 v[224:225], v236 offset:0
)"
R"(ds_read_b64_tr_b16 v[226:227], v236 offset:6144
v_mfma_f32_16x16x32_bf16 v[28:31], v[228:231], v[80:83], v[28:31]
s_waitcnt lgkmcnt(8)
buffer_load_dwordx4 v[108:111], v247, s[40:43], s51 offen offset:3072 sc1
v_mfma_f32_16x16x32_bf16 v[32:35], v[208:211], v[80:83], v[32:35]
ds_read_b64_tr_b16 v[228:229], v237 offset:0
ds_read_b64_tr_b16 v[230:231], v237 offset:6144
v_mfma_f32_16x16x32_bf16 v[72:75], v[208:211], v[84:87], v[72:75]
s_waitcnt lgkmcnt(8)
v_mfma_f32_16x16x32_bf16 v[76:79], v[212:215], v[84:87], v[76:79]
ds_read_b64_tr_b16 v[208:209], v234 offset:128
ds_read_b64_tr_b16 v[210:211], v234 offset:6272
v_mfma_f32_16x16x32_bf16 v[36:39], v[212:215], v[80:83], v[36:39]
s_waitcnt lgkmcnt(8)
s_waitcnt vmcnt(4)
v_mfma_f32_16x16x32_bf16 v[0:3], v[216:219], v[88:91], v[0:3]
)"
R"(ds_read_b64_tr_b16 v[212:213], v235 offset:128
ds_read_b64_tr_b16 v[214:215], v235 offset:6272
v_mfma_f32_16x16x32_bf16 v[40:43], v[216:219], v[92:95], v[40:43]
s_waitcnt lgkmcnt(8)
v_mfma_f32_16x16x32_bf16 v[44:47], v[220:223], v[92:95], v[44:47]
ds_read_b64_tr_b16 v[216:217], v236 offset:128
ds_read_b64_tr_b16 v[218:219], v236 offset:6272
v_mfma_f32_16x16x32_bf16 v[4:7], v[220:223], v[88:91], v[4:7]
s_waitcnt lgkmcnt(8)
v_mfma_f32_16x16x32_bf16 v[8:11], v[224:227], v[88:91], v[8:11]
ds_read_b64_tr_b16 v[220:221], v237 offset:128
ds_read_b64_tr_b16 v[222:223], v237 offset:6272
v_mfma_f32_16x16x32_bf16 v[48:51], v[224:227], v[92:95], v[48:51]
s_waitcnt lgkmcnt(8)
s_add_u32 s51, s51, 4096
buffer_load_dwordx4 v[112:115], v247, s[40:43], s51 offen sc1
)"
R"(v_mfma_f32_16x16x32_bf16 v[52:55], v[228:231], v[92:95], v[52:55]
ds_read_b64_tr_b16 v[224:225], v234 offset:256
ds_read_b64_tr_b16 v[226:227], v234 offset:6400
v_mfma_f32_16x16x32_bf16 v[12:15], v[228:231], v[88:91], v[12:15]
s_waitcnt lgkmcnt(8)
v_mfma_f32_16x16x32_bf16 v[16:19], v[208:211], v[88:91], v[16:19]
ds_read_b64_tr_b16 v[228:229], v235 offset:256
ds_read_b64_tr_b16 v[230:231], v235 offset:6400
v_mfma_f32_16x16x32_bf16 v[56:59], v[208:211], v[92:95], v[56:59]
s_waitcnt lgkmcnt(8)
v_mfma_f32_16x16x32_bf16 v[60:63], v[212:215], v[92:95], v[60:63]
ds_read_b64_tr_b16 v[208:209], v232 offset:24576
ds_read_b64_tr_b16 v[210:211], v232 offset:30720
v_mfma_f32_16x16x32_bf16 v[20:23], v[212:215], v[88:91], v[20:23]
s_waitcnt lgkmcnt(8)
v_mfma_f32_16x16x32_bf16 v[24:27], v[216:219], v[88:91], v[24:27]
)"
R"(ds_read_b64_tr_b16 v[212:213], v233 offset:24576
ds_read_b64_tr_b16 v[214:215], v233 offset:30720
v_mfma_f32_16x16x32_bf16 v[64:67], v[216:219], v[92:95], v[64:67]
s_waitcnt lgkmcnt(8)
v_mfma_f32_16x16x32_bf16 v[68:71], v[220:223], v[92:95], v[68:71]
ds_read_b64_tr_b16 v[216:217], v238 offset:24576
ds_read_b64_tr_b16 v[218:219], v238 offset:30720
v_mfma_f32_16x16x32_bf16 v[28:31], v[220:223], v[88:91], v[28:31]
s_waitcnt lgkmcnt(8)
buffer_load_dwordx4 v[116:119], v247, s[40:43], s51 offen offset:1024 sc1
v_mfma_f32_16x16x32_bf16 v[32:35], v[224:227], v[88:91], v[32:35]
ds_read_b64_tr_b16 v[220:221], v239 offset:24576
ds_read_b64_tr_b16 v[222:223], v239 offset:30720
v_mfma_f32_16x16x32_bf16 v[72:75], v[224:227], v[92:95], v[72:75]
s_waitcnt lgkmcnt(8)
v_mfma_f32_16x16x32_bf16 v[76:79], v[228:231], v[92:95], v[76:79]
)"
R"(ds_read_b64_tr_b16 v[224:225], v232 offset:24704
ds_read_b64_tr_b16 v[226:227], v232 offset:30848
v_mfma_f32_16x16x32_bf16 v[36:39], v[228:231], v[88:91], v[36:39]
s_waitcnt lgkmcnt(8)
s_barrier
s_waitcnt vmcnt(4)
v_mfma_f32_16x16x32_bf16 v[0:3], v[208:211], v[96:99], v[0:3]
ds_read_b64_tr_b16 v[228:229], v233 offset:24704
ds_read_b64_tr_b16 v[230:231], v233 offset:30848
v_mfma_f32_16x16x32_bf16 v[40:43], v[208:211], v[100:103], v[40:43]
s_waitcnt lgkmcnt(8)
v_mfma_f32_16x16x32_bf16 v[44:47], v[212:215], v[100:103], v[44:47]
ds_read_b64_tr_b16 v[208:209], v238 offset:24704
ds_read_b64_tr_b16 v[210:211], v238 offset:30848
v_mfma_f32_16x16x32_bf16 v[4:7], v[212:215], v[96:99], v[4:7]
s_waitcnt lgkmcnt(8)
)"
R"(v_mfma_f32_16x16x32_bf16 v[8:11], v[216:219], v[96:99], v[8:11]
ds_read_b64_tr_b16 v[212:213], v239 offset:24704
ds_read_b64_tr_b16 v[214:215], v239 offset:30848
v_mfma_f32_16x16x32_bf16 v[48:51], v[216:219], v[100:103], v[48:51]
s_waitcnt lgkmcnt(8)
buffer_load_dwordx4 v[120:123], v247, s[40:43], s51 offen offset:2048 sc1
v_mfma_f32_16x16x32_bf16 v[52:55], v[220:223], v[100:103], v[52:55]
ds_read_b64_tr_b16 v[216:217], v232 offset:24832
ds_read_b64_tr_b16 v[218:219], v232 offset:30976
v_mfma_f32_16x16x32_bf16 v[12:15], v[220:223], v[96:99], v[12:15]
s_waitcnt lgkmcnt(8)
v_mfma_f32_16x16x32_bf16 v[16:19], v[224:227], v[96:99], v[16:19]
ds_read_b64_tr_b16 v[220:221], v233 offset:24832
ds_read_b64_tr_b16 v[222:223], v233 offset:30976
v_mfma_f32_16x16x32_bf16 v[56:59], v[224:227], v[100:103], v[56:59]
s_waitcnt lgkmcnt(8)
)"
R"(v_mfma_f32_16x16x32_bf16 v[60:63], v[228:231], v[100:103], v[60:63]
ds_read_b64_tr_b16 v[224:225], v240 offset:12288
ds_read_b64_tr_b16 v[226:227], v240 offset:18432
v_mfma_f32_16x16x32_bf16 v[20:23], v[228:231], v[96:99], v[20:23]
s_waitcnt lgkmcnt(8)
v_mfma_f32_16x16x32_bf16 v[24:27], v[208:211], v[96:99], v[24:27]
ds_read_b64_tr_b16 v[228:229], v241 offset:12288
ds_read_b64_tr_b16 v[230:231], v241 offset:18432
v_mfma_f32_16x16x32_bf16 v[64:67], v[208:211], v[100:103], v[64:67]
s_waitcnt lgkmcnt(8)
v_mfma_f32_16x16x32_bf16 v[68:71], v[212:215], v[100:103], v[68:71]
ds_read_b64_tr_b16 v[208:209], v242 offset:12288
ds_read_b64_tr_b16 v[210:211], v242 offset:18432
v_mfma_f32_16x16x32_bf16 v[28:31], v[212:215], v[96:99], v[28:31]
s_waitcnt lgkmcnt(8)
buffer_load_dwordx4 v[124:127], v247, s[40:43], s51 offen offset:3072 sc1
)"
R"(v_mfma_f32_16x16x32_bf16 v[32:35], v[216:219], v[96:99], v[32:35]
ds_read_b64_tr_b16 v[212:213], v243 offset:12288
ds_read_b64_tr_b16 v[214:215], v243 offset:18432
v_mfma_f32_16x16x32_bf16 v[72:75], v[216:219], v[100:103], v[72:75]
s_waitcnt lgkmcnt(8)
v_mfma_f32_16x16x32_bf16 v[76:79], v[220:223], v[100:103], v[76:79]
ds_read_b64_tr_b16 v[216:217], v240 offset:12416
ds_read_b64_tr_b16 v[218:219], v240 offset:18560
v_mfma_f32_16x16x32_bf16 v[36:39], v[220:223], v[96:99], v[36:39]
s_waitcnt lgkmcnt(8)
s_waitcnt vmcnt(4)
v_mfma_f32_16x16x32_bf16 v[0:3], v[224:227], v[104:107], v[0:3]
ds_read_b64_tr_b16 v[220:221], v241 offset:12416
ds_read_b64_tr_b16 v[222:223], v241 offset:18560
v_mfma_f32_16x16x32_bf16 v[40:43], v[224:227], v[108:111], v[40:43]
s_waitcnt lgkmcnt(8)
)"
R"(v_mfma_f32_16x16x32_bf16 v[44:47], v[228:231], v[108:111], v[44:47]
ds_read_b64_tr_b16 v[224:225], v242 offset:12416
ds_read_b64_tr_b16 v[226:227], v242 offset:18560
v_mfma_f32_16x16x32_bf16 v[4:7], v[228:231], v[104:107], v[4:7]
s_waitcnt lgkmcnt(8)
v_mfma_f32_16x16x32_bf16 v[8:11], v[208:211], v[104:107], v[8:11]
ds_read_b64_tr_b16 v[228:229], v243 offset:12416
ds_read_b64_tr_b16 v[230:231], v243 offset:18560
v_mfma_f32_16x16x32_bf16 v[48:51], v[208:211], v[108:111], v[48:51]
s_waitcnt lgkmcnt(8)
s_add_u32 s51, s51, 4096
buffer_load_dwordx4 v[128:131], v247, s[40:43], s51 offen sc1
v_mfma_f32_16x16x32_bf16 v[52:55], v[212:215], v[108:111], v[52:55]
ds_read_b64_tr_b16 v[208:209], v240 offset:12544
ds_read_b64_tr_b16 v[210:211], v240 offset:18688
v_mfma_f32_16x16x32_bf16 v[12:15], v[212:215], v[104:107], v[12:15]
)"
R"(s_waitcnt lgkmcnt(8)
v_mfma_f32_16x16x32_bf16 v[16:19], v[216:219], v[104:107], v[16:19]
ds_read_b64_tr_b16 v[212:213], v241 offset:12544
ds_read_b64_tr_b16 v[214:215], v241 offset:18688
v_mfma_f32_16x16x32_bf16 v[56:59], v[216:219], v[108:111], v[56:59]
s_waitcnt lgkmcnt(8)
v_mfma_f32_16x16x32_bf16 v[60:63], v[220:223], v[108:111], v[60:63]
ds_read_b64_tr_b16 v[216:217], v240 offset:36864
ds_read_b64_tr_b16 v[218:219], v240 offset:43008
v_mfma_f32_16x16x32_bf16 v[20:23], v[220:223], v[104:107], v[20:23]
s_waitcnt lgkmcnt(8)
v_mfma_f32_16x16x32_bf16 v[24:27], v[224:227], v[104:107], v[24:27]
ds_read_b64_tr_b16 v[220:221], v241 offset:36864
ds_read_b64_tr_b16 v[222:223], v241 offset:43008
v_mfma_f32_16x16x32_bf16 v[64:67], v[224:227], v[108:111], v[64:67]
s_waitcnt lgkmcnt(8)
)"
R"(v_mfma_f32_16x16x32_bf16 v[68:71], v[228:231], v[108:111], v[68:71]
ds_read_b64_tr_b16 v[224:225], v242 offset:36864
ds_read_b64_tr_b16 v[226:227], v242 offset:43008
v_mfma_f32_16x16x32_bf16 v[28:31], v[228:231], v[104:107], v[28:31]
s_waitcnt lgkmcnt(8)
buffer_load_dwordx4 v[132:135], v247, s[40:43], s51 offen offset:1024 sc1
v_mfma_f32_16x16x32_bf16 v[32:35], v[208:211], v[104:107], v[32:35]
ds_read_b64_tr_b16 v[228:229], v243 offset:36864
ds_read_b64_tr_b16 v[230:231], v243 offset:43008
v_mfma_f32_16x16x32_bf16 v[72:75], v[208:211], v[108:111], v[72:75]
s_waitcnt lgkmcnt(8)
v_mfma_f32_16x16x32_bf16 v[76:79], v[212:215], v[108:111], v[76:79]
ds_read_b64_tr_b16 v[208:209], v240 offset:36992
ds_read_b64_tr_b16 v[210:211], v240 offset:43136
v_mfma_f32_16x16x32_bf16 v[36:39], v[212:215], v[104:107], v[36:39]
s_waitcnt lgkmcnt(8)
)"
R"(s_waitcnt vmcnt(4)
v_mfma_f32_16x16x32_bf16 v[0:3], v[216:219], v[112:115], v[0:3]
ds_read_b64_tr_b16 v[212:213], v241 offset:36992
ds_read_b64_tr_b16 v[214:215], v241 offset:43136
v_mfma_f32_16x16x32_bf16 v[40:43], v[216:219], v[116:119], v[40:43]
s_waitcnt lgkmcnt(8)
v_mfma_f32_16x16x32_bf16 v[44:47], v[220:223], v[116:119], v[44:47]
ds_read_b64_tr_b16 v[216:217], v242 offset:36992
ds_read_b64_tr_b16 v[218:219], v242 offset:43136
v_mfma_f32_16x16x32_bf16 v[4:7], v[220:223], v[112:115], v[4:7]
s_waitcnt lgkmcnt(8)
v_mfma_f32_16x16x32_bf16 v[8:11], v[224:227], v[112:115], v[8:11]
ds_read_b64_tr_b16 v[220:221], v243 offset:36992
ds_read_b64_tr_b16 v[222:223], v243 offset:43136
v_mfma_f32_16x16x32_bf16 v[48:51], v[224:227], v[116:119], v[48:51]
s_waitcnt lgkmcnt(8)
)"
R"(buffer_load_dwordx4 v[136:139], v247, s[40:43], s51 offen offset:2048 sc1
v_mfma_f32_16x16x32_bf16 v[52:55], v[228:231], v[116:119], v[52:55]
ds_read_b64_tr_b16 v[224:225], v240 offset:37120
ds_read_b64_tr_b16 v[226:227], v240 offset:43264
v_mfma_f32_16x16x32_bf16 v[12:15], v[228:231], v[112:115], v[12:15]
s_waitcnt lgkmcnt(8)
v_mfma_f32_16x16x32_bf16 v[16:19], v[208:211], v[112:115], v[16:19]
ds_read_b64_tr_b16 v[228:229], v241 offset:37120
ds_read_b64_tr_b16 v[230:231], v241 offset:43264
v_mfma_f32_16x16x32_bf16 v[56:59], v[208:211], v[116:119], v[56:59]
s_waitcnt lgkmcnt(8)
v_mfma_f32_16x16x32_bf16 v[60:63], v[212:215], v[116:119], v[60:63]
ds_read_b64_tr_b16 v[208:209], v234 offset:36864
ds_read_b64_tr_b16 v[210:211], v234 offset:43008
v_mfma_f32_16x16x32_bf16 v[20:23], v[212:215], v[112:115], v[20:23]
s_waitcnt lgkmcnt(8)
)"
R"(v_mfma_f32_16x16x32_bf16 v[24:27], v[216:219], v[112:115], v[24:27]
ds_read_b64_tr_b16 v[212:213], v235 offset:36864
ds_read_b64_tr_b16 v[214:215], v235 offset:43008
v_mfma_f32_16x16x32_bf16 v[64:67], v[216:219], v[116:119], v[64:67]
s_waitcnt lgkmcnt(8)
v_mfma_f32_16x16x32_bf16 v[68:71], v[220:223], v[116:119], v[68:71]
ds_read_b64_tr_b16 v[216:217], v236 offset:36864
ds_read_b64_tr_b16 v[218:219], v236 offset:43008
v_mfma_f32_16x16x32_bf16 v[28:31], v[220:223], v[112:115], v[28:31]
s_waitcnt lgkmcnt(8)
buffer_load_dwordx4 v[140:143], v247, s[40:43], s51 offen offset:3072 sc1
v_mfma_f32_16x16x32_bf16 v[32:35], v[224:227], v[112:115], v[32:35]
ds_read_b64_tr_b16 v[220:221], v237 offset:36864
ds_read_b64_tr_b16 v[222:223], v237 offset:43008
v_mfma_f32_16x16x32_bf16 v[72:75], v[224:227], v[116:119], v[72:75]
s_waitcnt lgkmcnt(8)
)"
R"(v_mfma_f32_16x16x32_bf16 v[76:79], v[228:231], v[116:119], v[76:79]
ds_read_b64_tr_b16 v[224:225], v234 offset:36992
ds_read_b64_tr_b16 v[226:227], v234 offset:43136
v_mfma_f32_16x16x32_bf16 v[36:39], v[228:231], v[112:115], v[36:39]
s_waitcnt lgkmcnt(8)
s_barrier
s_waitcnt vmcnt(4)
v_mfma_f32_16x16x32_bf16 v[0:3], v[208:211], v[120:123], v[0:3]
ds_read_b64_tr_b16 v[228:229], v235 offset:36992
ds_read_b64_tr_b16 v[230:231], v235 offset:43136
v_mfma_f32_16x16x32_bf16 v[40:43], v[208:211], v[124:127], v[40:43]
s_waitcnt lgkmcnt(8)
v_mfma_f32_16x16x32_bf16 v[44:47], v[212:215], v[124:127], v[44:47]
ds_read_b64_tr_b16 v[208:209], v236 offset:36992
ds_read_b64_tr_b16 v[210:211], v236 offset:43136
v_mfma_f32_16x16x32_bf16 v[4:7], v[212:215], v[120:123], v[4:7]
)"
R"(s_waitcnt lgkmcnt(8)
v_mfma_f32_16x16x32_bf16 v[8:11], v[216:219], v[120:123], v[8:11]
ds_read_b64_tr_b16 v[212:213], v237 offset:36992
ds_read_b64_tr_b16 v[214:215], v237 offset:43136
v_mfma_f32_16x16x32_bf16 v[48:51], v[216:219], v[124:127], v[48:51]
s_waitcnt lgkmcnt(8)
v_mfma_f32_16x16x32_bf16 v[52:55], v[220:223], v[124:127], v[52:55]
ds_read_b64_tr_b16 v[216:217], v234 offset:37120
ds_read_b64_tr_b16 v[218:219], v234 offset:43264
v_mfma_f32_16x16x32_bf16 v[12:15], v[220:223], v[120:123], v[12:15]
s_waitcnt lgkmcnt(8)
v_mfma_f32_16x16x32_bf16 v[16:19], v[224:227], v[120:123], v[16:19]
ds_read_b64_tr_b16 v[220:221], v235 offset:37120
ds_read_b64_tr_b16 v[222:223], v235 offset:43264
v_mfma_f32_16x16x32_bf16 v[56:59], v[224:227], v[124:127], v[56:59]
s_waitcnt lgkmcnt(8)
)"
R"(v_mfma_f32_16x16x32_bf16 v[60:63], v[228:231], v[124:127], v[60:63]
ds_read_b64_tr_b16 v[224:225], v234 offset:24576
ds_read_b64_tr_b16 v[226:227], v234 offset:30720
v_mfma_f32_16x16x32_bf16 v[20:23], v[228:231], v[120:123], v[20:23]
s_waitcnt lgkmcnt(8)
v_mfma_f32_16x16x32_bf16 v[24:27], v[208:211], v[120:123], v[24:27]
ds_read_b64_tr_b16 v[228:229], v235 offset:24576
ds_read_b64_tr_b16 v[230:231], v235 offset:30720
v_mfma_f32_16x16x32_bf16 v[64:67], v[208:211], v[124:127], v[64:67]
s_waitcnt lgkmcnt(8)
v_mfma_f32_16x16x32_bf16 v[68:71], v[212:215], v[124:127], v[68:71]
ds_read_b64_tr_b16 v[208:209], v236 offset:24576
ds_read_b64_tr_b16 v[210:211], v236 offset:30720
v_mfma_f32_16x16x32_bf16 v[28:31], v[212:215], v[120:123], v[28:31]
s_waitcnt lgkmcnt(8)
v_mfma_f32_16x16x32_bf16 v[32:35], v[216:219], v[120:123], v[32:35]
)"
R"(ds_read_b64_tr_b16 v[212:213], v237 offset:24576
ds_read_b64_tr_b16 v[214:215], v237 offset:30720
v_mfma_f32_16x16x32_bf16 v[72:75], v[216:219], v[124:127], v[72:75]
s_waitcnt lgkmcnt(8)
v_mfma_f32_16x16x32_bf16 v[76:79], v[220:223], v[124:127], v[76:79]
ds_read_b64_tr_b16 v[216:217], v234 offset:24704
ds_read_b64_tr_b16 v[218:219], v234 offset:30848
v_mfma_f32_16x16x32_bf16 v[36:39], v[220:223], v[120:123], v[36:39]
s_waitcnt lgkmcnt(8)
s_waitcnt vmcnt(2)
v_mfma_f32_16x16x32_bf16 v[0:3], v[224:227], v[128:131], v[0:3]
ds_read_b64_tr_b16 v[220:221], v235 offset:24704
ds_read_b64_tr_b16 v[222:223], v235 offset:30848
v_mfma_f32_16x16x32_bf16 v[40:43], v[224:227], v[132:135], v[40:43]
s_waitcnt lgkmcnt(8)
v_mfma_f32_16x16x32_bf16 v[44:47], v[228:231], v[132:135], v[44:47]
)"
R"(ds_read_b64_tr_b16 v[224:225], v236 offset:24704
ds_read_b64_tr_b16 v[226:227], v236 offset:30848
v_mfma_f32_16x16x32_bf16 v[4:7], v[228:231], v[128:131], v[4:7]
s_waitcnt lgkmcnt(8)
v_mfma_f32_16x16x32_bf16 v[8:11], v[208:211], v[128:131], v[8:11]
ds_read_b64_tr_b16 v[228:229], v237 offset:24704
ds_read_b64_tr_b16 v[230:231], v237 offset:30848
v_mfma_f32_16x16x32_bf16 v[48:51], v[208:211], v[132:135], v[48:51]
s_waitcnt lgkmcnt(8)
v_mfma_f32_16x16x32_bf16 v[52:55], v[212:215], v[132:135], v[52:55]
ds_read_b64_tr_b16 v[208:209], v234 offset:24832
ds_read_b64_tr_b16 v[210:211], v234 offset:30976
v_mfma_f32_16x16x32_bf16 v[12:15], v[212:215], v[128:131], v[12:15]
s_waitcnt lgkmcnt(8)
v_mfma_f32_16x16x32_bf16 v[16:19], v[216:219], v[128:131], v[16:19]
ds_read_b64_tr_b16 v[212:213], v235 offset:24832
)"
R"(ds_read_b64_tr_b16 v[214:215], v235 offset:30976
v_mfma_f32_16x16x32_bf16 v[56:59], v[216:219], v[132:135], v[56:59]
s_waitcnt lgkmcnt(8)
v_mfma_f32_16x16x32_bf16 v[60:63], v[220:223], v[132:135], v[60:63]
ds_read_b64_tr_b16 v[216:217], v240 offset:24576
ds_read_b64_tr_b16 v[218:219], v240 offset:30720
v_mfma_f32_16x16x32_bf16 v[20:23], v[220:223], v[128:131], v[20:23]
s_waitcnt lgkmcnt(8)
v_mfma_f32_16x16x32_bf16 v[24:27], v[224:227], v[128:131], v[24:27]
ds_read_b64_tr_b16 v[220:221], v241 offset:24576
ds_read_b64_tr_b16 v[222:223], v241 offset:30720
v_mfma_f32_16x16x32_bf16 v[64:67], v[224:227], v[132:135], v[64:67]
s_waitcnt lgkmcnt(8)
v_mfma_f32_16x16x32_bf16 v[68:71], v[228:231], v[132:135], v[68:71]
ds_read_b64_tr_b16 v[224:225], v242 offset:24576
ds_read_b64_tr_b16 v[226:227], v242 offset:30720
)"
R"(v_mfma_f32_16x16x32_bf16 v[28:31], v[228:231], v[128:131], v[28:31]
s_waitcnt lgkmcnt(8)
v_mfma_f32_16x16x32_bf16 v[32:35], v[208:211], v[128:131], v[32:35]
ds_read_b64_tr_b16 v[228:229], v243 offset:24576
ds_read_b64_tr_b16 v[230:231], v243 offset:30720
v_mfma_f32_16x16x32_bf16 v[72:75], v[208:211], v[132:135], v[72:75]
s_waitcnt lgkmcnt(8)
v_mfma_f32_16x16x32_bf16 v[76:79], v[212:215], v[132:135], v[76:79]
ds_read_b64_tr_b16 v[208:209], v240 offset:24704
ds_read_b64_tr_b16 v[210:211], v240 offset:30848
v_mfma_f32_16x16x32_bf16 v[36:39], v[212:215], v[128:131], v[36:39]
s_waitcnt lgkmcnt(8)
s_waitcnt vmcnt(0)
v_mfma_f32_16x16x32_bf16 v[0:3], v[216:219], v[136:139], v[0:3]
ds_read_b64_tr_b16 v[212:213], v241 offset:24704
ds_read_b64_tr_b16 v[214:215], v241 offset:30848
)"
R"(v_mfma_f32_16x16x32_bf16 v[40:43], v[216:219], v[140:143], v[40:43]
s_waitcnt lgkmcnt(8)
v_mfma_f32_16x16x32_bf16 v[44:47], v[220:223], v[140:143], v[44:47]
ds_read_b64_tr_b16 v[216:217], v242 offset:24704
ds_read_b64_tr_b16 v[218:219], v242 offset:30848
v_mfma_f32_16x16x32_bf16 v[4:7], v[220:223], v[136:139], v[4:7]
s_waitcnt lgkmcnt(8)
v_mfma_f32_16x16x32_bf16 v[8:11], v[224:227], v[136:139], v[8:11]
ds_read_b64_tr_b16 v[220:221], v243 offset:24704
ds_read_b64_tr_b16 v[222:223], v243 offset:30848
v_mfma_f32_16x16x32_bf16 v[48:51], v[224:227], v[140:143], v[48:51]
s_waitcnt lgkmcnt(8)
v_mfma_f32_16x16x32_bf16 v[52:55], v[228:231], v[140:143], v[52:55]
ds_read_b64_tr_b16 v[224:225], v240 offset:24832
ds_read_b64_tr_b16 v[226:227], v240 offset:30976
v_mfma_f32_16x16x32_bf16 v[12:15], v[228:231], v[136:139], v[12:15]
)"
R"(s_waitcnt lgkmcnt(8)
v_mfma_f32_16x16x32_bf16 v[16:19], v[208:211], v[136:139], v[16:19]
ds_read_b64_tr_b16 v[228:229], v241 offset:24832
ds_read_b64_tr_b16 v[230:231], v241 offset:30976
v_mfma_f32_16x16x32_bf16 v[56:59], v[208:211], v[140:143], v[56:59]
s_waitcnt lgkmcnt(8)
v_mfma_f32_16x16x32_bf16 v[60:63], v[212:215], v[140:143], v[60:63]
v_mfma_f32_16x16x32_bf16 v[20:23], v[212:215], v[136:139], v[20:23]
s_waitcnt lgkmcnt(6)
v_mfma_f32_16x16x32_bf16 v[24:27], v[216:219], v[136:139], v[24:27]
v_mfma_f32_16x16x32_bf16 v[64:67], v[216:219], v[140:143], v[64:67]
s_waitcnt lgkmcnt(4)
v_mfma_f32_16x16x32_bf16 v[68:71], v[220:223], v[140:143], v[68:71]
v_mfma_f32_16x16x32_bf16 v[28:31], v[220:223], v[136:139], v[28:31]
s_waitcnt lgkmcnt(2)
v_mfma_f32_16x16x32_bf16 v[32:35], v[224:227], v[136:139], v[32:35]
)"
R"(v_mfma_f32_16x16x32_bf16 v[72:75], v[224:227], v[140:143], v[72:75]
s_waitcnt lgkmcnt(0)
v_mfma_f32_16x16x32_bf16 v[76:79], v[228:231], v[140:143], v[76:79]
v_mfma_f32_16x16x32_bf16 v[36:39], v[228:231], v[136:139], v[36:39]
s_and_b32 s68, s67, 0xfffffffc
s_lshl_b32 s68, s68, 2
s_add_u32 s68, s68, s64
s_lshl_b32 s68, s68, 2
s_add_u32 s68, s68, s60
s_mul_i32 s68, s68, 2560
s_add_u32 s68, s68, 25165824
v_lshrrev_b32 v200, 1, v247
s_nop 7
s_cmp_eq_u32 s64, 0
s_cbranch_scc1 Lown_0
v_cvt_pk_bf16_f32 v116, v0, v1
)"
R"(v_cvt_pk_bf16_f32 v117, v2, v3
v_cvt_pk_bf16_f32 v118, v4, v5
v_cvt_pk_bf16_f32 v119, v6, v7
v_cvt_pk_bf16_f32 v120, v8, v9
v_cvt_pk_bf16_f32 v121, v10, v11
v_cvt_pk_bf16_f32 v122, v12, v13
v_cvt_pk_bf16_f32 v123, v14, v15
v_cvt_pk_bf16_f32 v124, v16, v17
v_cvt_pk_bf16_f32 v125, v18, v19
buffer_store_dwordx4 v[116:119], v247, s[40:43], s68 offen sc1
buffer_store_dwordx4 v[120:123], v247, s[40:43], s68 offen offset:1024 sc1
buffer_store_dwordx2 v[124:125], v200, s[40:43], s68 offen offset:2048 sc1
s_branch Lnx_0
Lown_0:
v_mov_b32 v96, v0
v_mov_b32 v97, v1
)"
R"(v_mov_b32 v98, v2
v_mov_b32 v99, v3
v_mov_b32 v100, v4
v_mov_b32 v101, v5
v_mov_b32 v102, v6
v_mov_b32 v103, v7
v_mov_b32 v104, v8
v_mov_b32 v105, v9
v_mov_b32 v106, v10
v_mov_b32 v107, v11
v_mov_b32 v108, v12
v_mov_b32 v109, v13
v_mov_b32 v110, v14
v_mov_b32 v111, v15
v_mov_b32 v112, v16
v_mov_b32 v113, v17
)"
R"(v_mov_b32 v114, v18
v_mov_b32 v115, v19
Lnx_0:
s_add_u32 s68, s68, 40960
s_cmp_eq_u32 s64, 1
s_cbranch_scc1 Lown_1
v_cvt_pk_bf16_f32 v126, v20, v21
v_cvt_pk_bf16_f32 v127, v22, v23
v_cvt_pk_bf16_f32 v128, v24, v25
v_cvt_pk_bf16_f32 v129, v26, v27
v_cvt_pk_bf16_f32 v130, v28, v29
v_cvt_pk_bf16_f32 v131, v30, v31
v_cvt_pk_bf16_f32 v132, v32, v33
v_cvt_pk_bf16_f32 v133, v34, v35
v_cvt_pk_bf16_f32 v134, v36, v37
v_cvt_pk_bf16_f32 v135, v38, v39
)"
R"(buffer_store_dwordx4 v[126:129], v247, s[40:43], s68 offen sc1
buffer_store_dwordx4 v[130:133], v247, s[40:43], s68 offen offset:1024 sc1
buffer_store_dwordx2 v[134:135], v200, s[40:43], s68 offen offset:2048 sc1
s_branch Lnx_1
Lown_1:
v_mov_b32 v96, v20
v_mov_b32 v97, v21
v_mov_b32 v98, v22
v_mov_b32 v99, v23
v_mov_b32 v100, v24
v_mov_b32 v101, v25
v_mov_b32 v102, v26
v_mov_b32 v103, v27
v_mov_b32 v104, v28
v_mov_b32 v105, v29
v_mov_b32 v106, v30
)"
R"(v_mov_b32 v107, v31
v_mov_b32 v108, v32
v_mov_b32 v109, v33
v_mov_b32 v110, v34
v_mov_b32 v111, v35
v_mov_b32 v112, v36
v_mov_b32 v113, v37
v_mov_b32 v114, v38
v_mov_b32 v115, v39
Lnx_1:
s_add_u32 s68, s68, 40960
s_cmp_eq_u32 s64, 2
s_cbranch_scc1 Lown_2
v_cvt_pk_bf16_f32 v116, v40, v41
v_cvt_pk_bf16_f32 v117, v42, v43
v_cvt_pk_bf16_f32 v118, v44, v45
)"
R"(v_cvt_pk_bf16_f32 v119, v46, v47
v_cvt_pk_bf16_f32 v120, v48, v49
v_cvt_pk_bf16_f32 v121, v50, v51
v_cvt_pk_bf16_f32 v122, v52, v53
v_cvt_pk_bf16_f32 v123, v54, v55
v_cvt_pk_bf16_f32 v124, v56, v57
v_cvt_pk_bf16_f32 v125, v58, v59
buffer_store_dwordx4 v[116:119], v247, s[40:43], s68 offen sc1
buffer_store_dwordx4 v[120:123], v247, s[40:43], s68 offen offset:1024 sc1
buffer_store_dwordx2 v[124:125], v200, s[40:43], s68 offen offset:2048 sc1
s_branch Lnx_2
Lown_2:
v_mov_b32 v96, v40
v_mov_b32 v97, v41
v_mov_b32 v98, v42
v_mov_b32 v99, v43
)"
R"(v_mov_b32 v100, v44
v_mov_b32 v101, v45
v_mov_b32 v102, v46
v_mov_b32 v103, v47
v_mov_b32 v104, v48
v_mov_b32 v105, v49
v_mov_b32 v106, v50
v_mov_b32 v107, v51
v_mov_b32 v108, v52
v_mov_b32 v109, v53
v_mov_b32 v110, v54
v_mov_b32 v111, v55
v_mov_b32 v112, v56
v_mov_b32 v113, v57
v_mov_b32 v114, v58
v_mov_b32 v115, v59
)"
R"(Lnx_2:
s_add_u32 s68, s68, 40960
s_cmp_eq_u32 s64, 3
s_cbranch_scc1 Lown_3
v_cvt_pk_bf16_f32 v126, v60, v61
v_cvt_pk_bf16_f32 v127, v62, v63
v_cvt_pk_bf16_f32 v128, v64, v65
v_cvt_pk_bf16_f32 v129, v66, v67
v_cvt_pk_bf16_f32 v130, v68, v69
v_cvt_pk_bf16_f32 v131, v70, v71
v_cvt_pk_bf16_f32 v132, v72, v73
v_cvt_pk_bf16_f32 v133, v74, v75
v_cvt_pk_bf16_f32 v134, v76, v77
v_cvt_pk_bf16_f32 v135, v78, v79
buffer_store_dwordx4 v[126:129], v247, s[40:43], s68 offen sc1
buffer_store_dwordx4 v[130:133], v247, s[40:43], s68 offen offset:1024 sc1
)"
R"(buffer_store_dwordx2 v[134:135], v200, s[40:43], s68 offen offset:2048 sc1
s_branch Lnx_3
Lown_3:
v_mov_b32 v96, v60
v_mov_b32 v97, v61
v_mov_b32 v98, v62
v_mov_b32 v99, v63
v_mov_b32 v100, v64
v_mov_b32 v101, v65
v_mov_b32 v102, v66
v_mov_b32 v103, v67
v_mov_b32 v104, v68
v_mov_b32 v105, v69
v_mov_b32 v106, v70
v_mov_b32 v107, v71
v_mov_b32 v108, v72
)"
R"(v_mov_b32 v109, v73
v_mov_b32 v110, v74
v_mov_b32 v111, v75
v_mov_b32 v112, v76
v_mov_b32 v113, v77
v_mov_b32 v114, v78
v_mov_b32 v115, v79
Lnx_3:
s_lshl_b32 s70, s67, 4
s_add_u32 s70, s70, s60
s_mul_i32 s70, s70, 2560
s_add_u32 s70, s70, 25165824
s_add_u32 s71, s64, 1
s_and_b32 s71, s71, 3
s_mul_i32 s71, s71, 10240
s_add_u32 s74, s70, s71
)"
R"(s_add_u32 s71, s64, 2
s_and_b32 s71, s71, 3
s_mul_i32 s71, s71, 10240
s_add_u32 s75, s70, s71
s_add_u32 s71, s64, 3
s_and_b32 s71, s71, 3
s_mul_i32 s71, s71, 10240
s_add_u32 s76, s70, s71
v_bfe_u32 v201, v247, 4, 4
v_mul_u32_u24 v201, 2560, v201
v_lshrrev_b32 v202, 8, v247
v_lshl_add_u32 v201, v202, 3, v201
s_lshl_b32 s72, s62, 2
s_add_u32 s72, s72, s60
s_lshl_b32 s72, s72, 1
s_lshr_b32 s73, s64, 1
)"
R"(s_add_u32 s72, s72, s73
s_mul_i32 s72, s72, 40960
s_mul_i32 s73, s65, 1280
s_add_u32 s72, s72, s73
s_mul_i32 s73, s63, 320
s_add_u32 s72, s72, s73
s_and_b32 s73, s64, 1
s_mul_i32 s73, s73, 160
s_add_u32 s72, s72, s73
s_waitcnt vmcnt(0)
s_barrier
s_barrier
buffer_load_dwordx4 v[128:131], v247, s[40:43], s74 offen sc1
buffer_load_dwordx4 v[132:135], v247, s[40:43], s74 offen offset:1024 sc1
buffer_load_dwordx2 v[136:137], v200, s[40:43], s74 offen offset:2048 sc1
buffer_load_dwordx4 v[140:143], v247, s[40:43], s75 offen sc1
)"
R"(buffer_load_dwordx4 v[144:147], v247, s[40:43], s75 offen offset:1024 sc1
buffer_load_dwordx2 v[148:149], v200, s[40:43], s75 offen offset:2048 sc1
buffer_load_dwordx4 v[152:155], v247, s[40:43], s76 offen sc1
buffer_load_dwordx4 v[156:159], v247, s[40:43], s76 offen offset:1024 sc1
buffer_load_dwordx2 v[160:161], v200, s[40:43], s76 offen offset:2048 sc1
v_mov_b32 v203, 0xffff0000
s_waitcnt vmcnt(6)
v_lshlrev_b32 v204, 16, v128
v_and_b32 v205, v203, v128
v_pk_add_f32 v[96:97], v[96:97], v[204:205]
v_lshlrev_b32 v206, 16, v129
v_and_b32 v207, v203, v129
v_pk_add_f32 v[98:99], v[98:99], v[206:207]
v_lshlrev_b32 v204, 16, v130
v_and_b32 v205, v203, v130
v_pk_add_f32 v[100:101], v[100:101], v[204:205]
)"
R"(v_lshlrev_b32 v206, 16, v131
v_and_b32 v207, v203, v131
v_pk_add_f32 v[102:103], v[102:103], v[206:207]
v_lshlrev_b32 v204, 16, v132
v_and_b32 v205, v203, v132
v_pk_add_f32 v[104:105], v[104:105], v[204:205]
v_lshlrev_b32 v206, 16, v133
v_and_b32 v207, v203, v133
v_pk_add_f32 v[106:107], v[106:107], v[206:207]
v_lshlrev_b32 v204, 16, v134
v_and_b32 v205, v203, v134
v_pk_add_f32 v[108:109], v[108:109], v[204:205]
v_lshlrev_b32 v206, 16, v135
v_and_b32 v207, v203, v135
v_pk_add_f32 v[110:111], v[110:111], v[206:207]
v_lshlrev_b32 v204, 16, v136
)"
R"(v_and_b32 v205, v203, v136
v_pk_add_f32 v[112:113], v[112:113], v[204:205]
v_lshlrev_b32 v206, 16, v137
v_and_b32 v207, v203, v137
v_pk_add_f32 v[114:115], v[114:115], v[206:207]
s_waitcnt vmcnt(3)
v_lshlrev_b32 v204, 16, v140
v_and_b32 v205, v203, v140
v_pk_add_f32 v[96:97], v[96:97], v[204:205]
v_lshlrev_b32 v206, 16, v141
v_and_b32 v207, v203, v141
v_pk_add_f32 v[98:99], v[98:99], v[206:207]
v_lshlrev_b32 v204, 16, v142
v_and_b32 v205, v203, v142
v_pk_add_f32 v[100:101], v[100:101], v[204:205]
v_lshlrev_b32 v206, 16, v143
)"
R"(v_and_b32 v207, v203, v143
v_pk_add_f32 v[102:103], v[102:103], v[206:207]
v_lshlrev_b32 v204, 16, v144
v_and_b32 v205, v203, v144
v_pk_add_f32 v[104:105], v[104:105], v[204:205]
v_lshlrev_b32 v206, 16, v145
v_and_b32 v207, v203, v145
v_pk_add_f32 v[106:107], v[106:107], v[206:207]
v_lshlrev_b32 v204, 16, v146
v_and_b32 v205, v203, v146
v_pk_add_f32 v[108:109], v[108:109], v[204:205]
v_lshlrev_b32 v206, 16, v147
v_and_b32 v207, v203, v147
v_pk_add_f32 v[110:111], v[110:111], v[206:207]
v_lshlrev_b32 v204, 16, v148
v_and_b32 v205, v203, v148
)"
R"(v_pk_add_f32 v[112:113], v[112:113], v[204:205]
v_lshlrev_b32 v206, 16, v149
v_and_b32 v207, v203, v149
v_pk_add_f32 v[114:115], v[114:115], v[206:207]
s_waitcnt vmcnt(0)
v_lshlrev_b32 v204, 16, v152
v_and_b32 v205, v203, v152
v_pk_add_f32 v[96:97], v[96:97], v[204:205]
v_lshlrev_b32 v206, 16, v153
v_and_b32 v207, v203, v153
v_pk_add_f32 v[98:99], v[98:99], v[206:207]
v_lshlrev_b32 v204, 16, v154
v_and_b32 v205, v203, v154
v_pk_add_f32 v[100:101], v[100:101], v[204:205]
v_lshlrev_b32 v206, 16, v155
v_and_b32 v207, v203, v155
)"
R"(v_pk_add_f32 v[102:103], v[102:103], v[206:207]
v_lshlrev_b32 v204, 16, v156
v_and_b32 v205, v203, v156
v_pk_add_f32 v[104:105], v[104:105], v[204:205]
v_lshlrev_b32 v206, 16, v157
v_and_b32 v207, v203, v157
v_pk_add_f32 v[106:107], v[106:107], v[206:207]
v_lshlrev_b32 v204, 16, v158
v_and_b32 v205, v203, v158
v_pk_add_f32 v[108:109], v[108:109], v[204:205]
v_lshlrev_b32 v206, 16, v159
v_and_b32 v207, v203, v159
v_pk_add_f32 v[110:111], v[110:111], v[206:207]
v_lshlrev_b32 v204, 16, v160
v_and_b32 v205, v203, v160
v_pk_add_f32 v[112:113], v[112:113], v[204:205]
)"
R"(v_lshlrev_b32 v206, 16, v161
v_and_b32 v207, v203, v161
v_pk_add_f32 v[114:115], v[114:115], v[206:207]
v_cvt_pk_bf16_f32 v208, v96, v97
v_cvt_pk_bf16_f32 v209, v98, v99
buffer_store_dwordx2 v[208:209], v201, s[44:47], s72 offen
v_cvt_pk_bf16_f32 v210, v100, v101
v_cvt_pk_bf16_f32 v211, v102, v103
buffer_store_dwordx2 v[210:211], v201, s[44:47], s72 offen offset:32
v_cvt_pk_bf16_f32 v212, v104, v105
v_cvt_pk_bf16_f32 v213, v106, v107
buffer_store_dwordx2 v[212:213], v201, s[44:47], s72 offen offset:64
v_cvt_pk_bf16_f32 v214, v108, v109
v_cvt_pk_bf16_f32 v215, v110, v111
buffer_store_dwordx2 v[214:215], v201, s[44:47], s72 offen offset:96
v_cvt_pk_bf16_f32 v216, v112, v113
)"
R"(v_cvt_pk_bf16_f32 v217, v114, v115
buffer_store_dwordx2 v[216:217], v201, s[44:47], s72 offen offset:128
s_endpgm
Lteam_B:
v_lshrrev_b32 v2, 3, v1
s_lshl_b32 s68, s60, 5
v_add_u32 v2, s68, v2
v_mul_u32_u24 v2, 1280, v2
v_and_b32 v3, 7, v1
v_lshrrev_b32 v4, 4, v1
v_xor_b32 v3, v3, v4
v_lshl_add_u32 v160, v3, 4, v2
v_xor_b32 v3, 64, v160
v_add_u32 v161, 9216, v3
v_add_u32 v162, 18432, v160
v_add_u32 v163, 27648, v3
)"
R"(s_lshr_b32 s68, s62, 1
s_mul_i32 s68, s68, 3
s_mul_i32 s69, s63, 5
s_add_u32 s85, s68, s69
s_sub_u32 s69, s85, 10
s_cmp_ge_u32 s85, 10
s_cselect_b32 s85, s69, s85
s_sub_u32 s69, s85, 10
s_cmp_ge_u32 s85, 10
s_cselect_b32 s85, s69, s85
s_lshl_b32 s51, s85, 7
v_add_u32 v3, 0, v1
v_mul_u32_u24 v2, 0xaab, v3
v_lshrrev_b32 v2, 16, v2
v_mul_u32_u24 v6, 24, v2
v_sub_u32 v3, v3, v6
)"
R"(s_lshl_b32 s68, s60, 3
v_add_u32 v4, s68, v2
v_and_b32 v6, 6, v2
v_xor_b32 v5, v3, v6
v_and_b32 v7, 2, v4
v_lshrrev_b32 v8, 1, v4
v_and_b32 v8, 4, v8
v_or_b32 v7, v7, v8
v_xor_b32 v7, v3, v7
v_mul_u32_u24 v6, 12288, v4
v_lshl_add_u32 v6, v7, 4, v6
v_mov_b32 v252, v6
v_add_u32 v3, 64, v1
v_mul_u32_u24 v2, 0xaab, v3
v_lshrrev_b32 v2, 16, v2
v_mul_u32_u24 v6, 24, v2
)"
R"(v_sub_u32 v3, v3, v6
s_lshl_b32 s68, s60, 3
v_add_u32 v4, s68, v2
v_and_b32 v6, 6, v2
v_xor_b32 v5, v3, v6
v_and_b32 v7, 2, v4
v_lshrrev_b32 v8, 1, v4
v_and_b32 v8, 4, v8
v_or_b32 v7, v7, v8
v_xor_b32 v7, v3, v7
v_mul_u32_u24 v6, 12288, v4
v_lshl_add_u32 v6, v7, 4, v6
v_subrev_u32 v253, 1024, v6
v_add_u32 v3, 128, v1
v_mul_u32_u24 v2, 0xaab, v3
v_lshrrev_b32 v2, 16, v2
)"
R"(v_mul_u32_u24 v6, 24, v2
v_sub_u32 v3, v3, v6
s_lshl_b32 s68, s60, 3
v_add_u32 v4, s68, v2
v_and_b32 v6, 6, v2
v_xor_b32 v5, v3, v6
v_and_b32 v7, 2, v4
v_lshrrev_b32 v8, 1, v4
v_and_b32 v8, 4, v8
v_or_b32 v7, v7, v8
v_xor_b32 v7, v3, v7
v_mul_u32_u24 v6, 12288, v4
v_lshl_add_u32 v6, v7, 4, v6
v_subrev_u32 v254, 2048, v6
s_lshl_b32 s68, s64, 3
s_add_u32 s68, s68, s63
)"
R"(s_mul_i32 s52, s68, 384
s_add_u32 s53, s52, 7864320
s_mul_i32 s68, s85, 786432
s_add_u32 s50, s52, s68
s_waitcnt lgkmcnt(0)
s_memrealtime s[88:89]
s_memtime s[74:75]
s_and_b32 s29, s29, 0xffff
s_mov_b32 s30, 7864320
s_mov_b32 s31, 0x20000
s_and_b32 s37, s37, 0xffff
s_mov_b32 s38, 163840
s_mov_b32 s39, 0x20000
s_mul_i32 s68, s62, 163840
s_add_u32 s36, s36, s68
s_addc_u32 s37, s37, 0
)"
R"(s_add_u32 m0, s84, 65536
s_nop 0
buffer_load_dwordx4 v160, s[36:39], s51 offen lds
buffer_load_dwordx4 v161, s[36:39], s51 offen offset:1024 lds
buffer_load_dwordx4 v162, s[36:39], s51 offen offset:2048 lds
buffer_load_dwordx4 v163, s[36:39], s51 offen offset:3072 lds
s_add_u32 s51, s51, 128
s_cmp_eq_u32 s51, 1280
s_cselect_b32 s51, 0, s51
s_add_u32 m0, s56, 98304
s_nop 0
buffer_load_dwordx4 v252, s[28:31], s50 offen lds
buffer_load_dwordx4 v253, s[28:31], s50 offen offset:1024 lds
buffer_load_dwordx4 v254, s[28:31], s50 offen offset:2048 lds
s_add_u32 s50, s50, 393216
s_cmp_eq_u32 s50, s53
)"
R"(s_cselect_b32 s50, s52, s50
s_add_u32 m0, s56, 110592
s_nop 0
buffer_load_dwordx4 v252, s[28:31], s50 offen lds
buffer_load_dwordx4 v253, s[28:31], s50 offen offset:1024 lds
buffer_load_dwordx4 v254, s[28:31], s50 offen offset:2048 lds
s_add_u32 s50, s50, 393216
s_cmp_eq_u32 s50, s53
s_cselect_b32 s50, s52, s50
s_and_b32 s21, s21, 0xffff
s_mov_b32 s22, 100663296
s_mov_b32 s23, 0x20000
s_and_b32 s25, s25, 0xffff
s_mov_b32 s26, 100663296
s_mov_b32 s27, 0x20000
s_and_b32 s33, s33, 0xffff
)"
R"(s_mov_b32 s34, 7864320
s_mov_b32 s35, 0x20000
s_and_b32 s41, s41, 0xffff
s_mov_b32 s42, 35848192
s_mov_b32 s43, 0x20000
s_and_b32 s45, s45, 0xffff
s_mov_b32 s46, 2621440
s_mov_b32 s47, 0x20000
s_mov_b32 s57, 0x3dd53b95
s_waitcnt lgkmcnt(0)
s_mov_b32 s75, s88
s_add_u32 s94, s88, 830
s_mov_b32 s95, s94
v_and_b32 v2, 3, v1
s_lshl_b32 s68, s62, 3
s_add_u32 s68, s68, s61
)"
R"(v_lshl_add_u32 v3, v2, 6, s68
s_lshl_b32 s68, s66, 3
s_lshl_b32 s69, s65, 2
s_add_u32 s68, s68, s69
v_add_u32 v4, s68, v2
v_mov_b32 v5, s63
v_mov_b32 v6, s64
v_cmp_gt_u32 vcc, 8, v1
v_and_b32 v7, 12, v1
s_nop 0
v_cndmask_b32 v3, v4, v3, vcc
v_cndmask_b32 v5, v6, v5, vcc
v_add_u32 v5, v5, v7
v_lshlrev_b32 v5, 2, v5
v_lshl_add_u32 v5, v3, 8, v5
v_add_u32 v250, 35651584, v5
)"
R"(s_lshl_b32 s68, s67, 8
s_add_u32 s68, s68, 35651584
v_lshl_add_u32 v249, v1, 2, s68
s_cmp_eq_u32 s59, 4
s_cbranch_scc0 Lw4_1
s_mov_b64 exec, 0xfff
buffer_load_dword v248, v249, s[40:43], 0 offen offset:128 sc1
s_mov_b64 exec, -1
Lw4_1:
v_add_u32 v3, 0, v1
v_mul_u32_u24 v2, 0xaab, v3
v_lshrrev_b32 v2, 16, v2
v_mul_u32_u24 v6, 24, v2
v_sub_u32 v3, v3, v6
s_lshl_b32 s68, s60, 3
v_add_u32 v4, s68, v2
)"
R"(v_and_b32 v6, 6, v2
v_xor_b32 v5, v3, v6
v_mul_u32_u24 v6, 384, v4
v_lshl_add_u32 v6, v5, 4, v6
v_mov_b32 v179, v6
v_mul_u32_u24 v6, 1280, v4
v_lshl_add_u32 v6, v5, 4, v6
v_mov_b32 v7, v6
v_cmp_gt_u32 vcc, 20, v5
v_mov_b32 v8, 0x80000000
s_nop 0
v_cndmask_b32 v244, v8, v7, vcc
v_bfe_u32 v7, v4, 1, 3
v_xor_b32 v7, v3, v7
v_mul_u32_u24 v6, 384, v4
v_lshl_add_u32 v6, v7, 4, v6
)"
R"(v_mov_b32 v176, v6
v_add_u32 v3, 64, v1
v_mul_u32_u24 v2, 0xaab, v3
v_lshrrev_b32 v2, 16, v2
v_mul_u32_u24 v6, 24, v2
v_sub_u32 v3, v3, v6
s_lshl_b32 s68, s60, 3
v_add_u32 v4, s68, v2
v_and_b32 v6, 6, v2
v_xor_b32 v5, v3, v6
v_mul_u32_u24 v6, 384, v4
v_lshl_add_u32 v6, v5, 4, v6
v_subrev_u32 v180, 1024, v6
v_mul_u32_u24 v6, 1280, v4
v_lshl_add_u32 v6, v5, 4, v6
v_subrev_u32 v7, 1024, v6
)"
R"(v_cmp_gt_u32 vcc, 20, v5
v_mov_b32 v8, 0x80000000
s_nop 0
v_cndmask_b32 v245, v8, v7, vcc
v_bfe_u32 v7, v4, 1, 3
v_xor_b32 v7, v3, v7
v_mul_u32_u24 v6, 384, v4
v_lshl_add_u32 v6, v7, 4, v6
v_subrev_u32 v177, 1024, v6
v_add_u32 v3, 128, v1
v_mul_u32_u24 v2, 0xaab, v3
v_lshrrev_b32 v2, 16, v2
v_mul_u32_u24 v6, 24, v2
v_sub_u32 v3, v3, v6
s_lshl_b32 s68, s60, 3
v_add_u32 v4, s68, v2
)"
R"(v_and_b32 v6, 6, v2
v_xor_b32 v5, v3, v6
v_mul_u32_u24 v6, 384, v4
v_lshl_add_u32 v6, v5, 4, v6
v_subrev_u32 v181, 2048, v6
v_mul_u32_u24 v6, 1280, v4
v_lshl_add_u32 v6, v5, 4, v6
v_subrev_u32 v7, 2048, v6
v_cmp_gt_u32 vcc, 20, v5
v_mov_b32 v8, 0x80000000
s_nop 0
v_cndmask_b32 v246, v8, v7, vcc
v_bfe_u32 v7, v4, 1, 3
v_xor_b32 v7, v3, v7
v_mul_u32_u24 v6, 384, v4
v_lshl_add_u32 v6, v7, 4, v6
)"
R"(v_subrev_u32 v178, 2048, v6
s_lshl_b32 s68, s62, 5
s_lshl_b32 s69, s64, 3
s_add_u32 s68, s68, s69
s_add_u32 s68, s68, s63
s_mul_i32 s68, s68, 393216
s_add_u32 s48, s68, 0
s_add_u32 s49, s68, 0
v_lshrrev_b32 v2, 4, v1
v_bfe_u32 v3, v1, 2, 2
v_lshl_add_u32 v4, v2, 3, v3
v_mul_u32_u24 v4, 384, v4
v_lshrrev_b32 v5, 1, v3
v_and_b32 v6, 1, v2
v_lshl_add_u32 v5, v6, 1, v5
v_lshl_add_u32 v4, v5, 5, v4
)"
R"(v_and_b32 v5, 3, v1
v_lshl_add_u32 v4, v5, 3, v4
v_add_u32 v166, 98304, v4
v_xor_b32 v167, 32, v166
v_xor_b32 v168, 64, v166
v_xor_b32 v169, 96, v166
v_and_b32 v2, 15, v1
v_lshrrev_b32 v3, 4, v1
v_lshrrev_b32 v4, 1, v2
v_xor_b32 v4, v3, v4
v_mul_u32_u24 v2, 128, v2
v_lshl_add_u32 v164, v4, 4, v2
s_lshl_b32 s68, s60, 12
s_add_u32 s68, s68, 147456
v_add_u32 v164, s68, v164
v_xor_b32 v165, 64, v164
)"
R"(v_subrev_u32 v170, 81920, v164
v_subrev_u32 v171, 81920, v165
v_and_b32 v2, 15, v1
v_lshrrev_b32 v3, 4, v1
v_lshrrev_b32 v4, 1, v2
v_xor_b32 v4, v3, v4
v_mul_u32_u24 v2, 384, v2
v_lshl_add_u32 v232, v4, 4, v2
v_xor_b32 v233, 64, v232
v_lshrrev_b32 v2, 4, v1
v_bfe_u32 v3, v1, 2, 2
v_lshl_add_u32 v4, v2, 2, v3
v_mul_u32_u24 v4, 384, v4
v_lshrrev_b32 v5, 1, v3
v_and_b32 v6, 1, v2
v_lshl_add_u32 v5, v6, 1, v5
)"
R"(v_lshl_add_u32 v4, v5, 5, v4
v_and_b32 v5, 3, v1
v_lshl_add_u32 v4, v5, 3, v4
v_add_u32 v234, 49152, v4
v_xor_b32 v235, 32, v234
v_xor_b32 v236, 64, v234
v_xor_b32 v237, 96, v234
v_lshlrev_b32 v247, 4, v1
s_lshl_b32 s68, s62, 3
s_add_u32 s68, s68, s61
s_lshl_b32 s68, s68, 2
s_add_u32 s68, s68, s60
s_mul_i32 s68, s68, 49152
s_add_u32 s83, s68, 12582912
s_mul_i32 s68, s63, 12288
s_add_u32 s83, s83, s68
)"
R"(s_cmp_eq_u32 s65, 1
s_cselect_b64 vcc, -1, 0
s_setprio 0
s_add_u32 m0, s84, 81920
s_nop 0
buffer_load_dwordx4 v160, s[36:39], s51 offen lds
buffer_load_dwordx4 v161, s[36:39], s51 offen offset:1024 lds
buffer_load_dwordx4 v162, s[36:39], s51 offen offset:2048 lds
buffer_load_dwordx4 v163, s[36:39], s51 offen offset:3072 lds
s_add_u32 s51, s51, 128
s_cmp_eq_u32 s51, 1280
s_cselect_b32 s51, 0, s51
s_add_u32 m0, s56, 122880
s_nop 0
buffer_load_dwordx4 v252, s[28:31], s50 offen lds
buffer_load_dwordx4 v253, s[28:31], s50 offen offset:1024 lds
)"
R"(buffer_load_dwordx4 v254, s[28:31], s50 offen offset:2048 lds
s_add_u32 s50, s50, 393216
s_cmp_eq_u32 s50, s53
s_cselect_b32 s50, s52, s50
s_memtime s[76:77]
s_memrealtime s[88:89]
s_waitcnt lgkmcnt(0)
s_sub_u32 s76, s76, s74
s_sub_u32 s88, s88, s75
s_mul_i32 s88, s88, 19
s_cmp_gt_u32 s76, s88
s_cselect_b32 s73, -1, 0
s_cmp_eq_u32 s60, 0
s_cselect_b32 s73, s73, 0
s_waitcnt vmcnt(7)
s_barrier
)"
R"(ds_read_b128 v[96:99], v170 offset:0
ds_read_b128 v[100:103], v170 offset:2048
ds_read_b128 v[104:107], v171 offset:0
ds_read_b128 v[108:111], v171 offset:2048
s_add_u32 m0, s56, 135168
s_nop 0
buffer_load_dwordx4 v252, s[28:31], s50 offen lds
buffer_load_dwordx4 v253, s[28:31], s50 offen offset:1024 lds
buffer_load_dwordx4 v254, s[28:31], s50 offen offset:2048 lds
s_add_u32 s50, s50, 393216
s_cmp_eq_u32 s50, s53
s_cselect_b32 s50, s52, s50
s_add_u32 m0, s56, 147456
s_nop 0
buffer_load_dwordx4 v252, s[28:31], s50 offen lds
buffer_load_dwordx4 v253, s[28:31], s50 offen offset:1024 lds
)"
R"(buffer_load_dwordx4 v254, s[28:31], s50 offen offset:2048 lds
s_add_u32 s50, s50, 393216
s_cmp_eq_u32 s50, s53
s_cselect_b32 s50, s52, s50
s_waitcnt lgkmcnt(0)
s_waitcnt vmcnt(6)
s_barrier
ds_read_b128 v[112:115], v170 offset:16384
ds_read_b128 v[116:119], v170 offset:18432
ds_read_b128 v[120:123], v171 offset:16384
ds_read_b128 v[124:127], v171 offset:18432
s_cmp_eq_u32 s59, 4
s_cbranch_scc0 Lw4_2
s_mov_b64 exec, 0xfff
v_add_u32 v248, 1, v248
buffer_store_dword v248, v250, s[40:43], 0 offen sc1
)"
R"(s_mov_b64 exec, -1
Lw4_2:
s_add_u32 m0, s84, 65536
s_nop 0
buffer_load_dwordx4 v160, s[36:39], s51 offen lds
buffer_load_dwordx4 v161, s[36:39], s51 offen offset:1024 lds
buffer_load_dwordx4 v162, s[36:39], s51 offen offset:2048 lds
buffer_load_dwordx4 v163, s[36:39], s51 offen offset:3072 lds
s_add_u32 s51, s51, 128
s_cmp_eq_u32 s51, 1280
s_cselect_b32 s51, 0, s51
s_add_u32 m0, s56, 98304
s_nop 0
buffer_load_dwordx4 v252, s[28:31], s50 offen lds
buffer_load_dwordx4 v253, s[28:31], s50 offen offset:1024 lds
buffer_load_dwordx4 v254, s[28:31], s50 offen offset:2048 lds
)"
R"(s_add_u32 s50, s50, 393216
s_cmp_eq_u32 s50, s53
s_cselect_b32 s50, s52, s50
s_waitcnt lgkmcnt(0)
s_waitcnt vmcnt(10)
s_barrier
s_add_u32 m0, s84, 81920
s_nop 0
buffer_load_dwordx4 v160, s[36:39], s51 offen lds
buffer_load_dwordx4 v161, s[36:39], s51 offen offset:1024 lds
buffer_load_dwordx4 v162, s[36:39], s51 offen offset:2048 lds
buffer_load_dwordx4 v163, s[36:39], s51 offen offset:3072 lds
s_add_u32 s51, s51, 128
s_cmp_eq_u32 s51, 1280
s_cselect_b32 s51, 0, s51
s_add_u32 m0, s56, 110592
)"
R"(s_nop 0
buffer_load_dwordx4 v252, s[28:31], s50 offen lds
buffer_load_dwordx4 v253, s[28:31], s50 offen offset:1024 lds
buffer_load_dwordx4 v254, s[28:31], s50 offen offset:2048 lds
s_add_u32 s50, s50, 393216
s_cmp_eq_u32 s50, s53
s_cselect_b32 s50, s52, s50
s_waitcnt vmcnt(10)
s_barrier
ds_read_b128 v[128:131], v170 offset:0
ds_read_b128 v[132:135], v170 offset:2048
ds_read_b128 v[136:139], v171 offset:0
ds_read_b128 v[140:143], v171 offset:2048
s_add_u32 m0, s56, 122880
s_nop 0
buffer_load_dwordx4 v252, s[28:31], s50 offen lds
)"
R"(buffer_load_dwordx4 v253, s[28:31], s50 offen offset:1024 lds
buffer_load_dwordx4 v254, s[28:31], s50 offen offset:2048 lds
s_add_u32 s50, s50, 393216
s_cmp_eq_u32 s50, s53
s_cselect_b32 s50, s52, s50
s_waitcnt lgkmcnt(0)
s_waitcnt vmcnt(10)
s_barrier
s_add_u32 m0, s84, 65536
s_nop 0
buffer_load_dwordx4 v160, s[36:39], s51 offen lds
buffer_load_dwordx4 v161, s[36:39], s51 offen offset:1024 lds
buffer_load_dwordx4 v162, s[36:39], s51 offen offset:2048 lds
buffer_load_dwordx4 v163, s[36:39], s51 offen offset:3072 lds
s_add_u32 s51, s51, 128
s_cmp_eq_u32 s51, 1280
)"
R"(s_cselect_b32 s51, 0, s51
s_add_u32 m0, s56, 135168
s_nop 0
buffer_load_dwordx4 v252, s[28:31], s50 offen lds
buffer_load_dwordx4 v253, s[28:31], s50 offen offset:1024 lds
buffer_load_dwordx4 v254, s[28:31], s50 offen offset:2048 lds
s_add_u32 s50, s50, 393216
s_cmp_eq_u32 s50, s53
s_cselect_b32 s50, s52, s50
s_waitcnt vmcnt(10)
s_barrier
s_add_u32 m0, s56, 147456
s_nop 0
buffer_load_dwordx4 v252, s[28:31], s50 offen lds
buffer_load_dwordx4 v253, s[28:31], s50 offen offset:1024 lds
buffer_load_dwordx4 v254, s[28:31], s50 offen offset:2048 lds
)"
R"(s_add_u32 s50, s50, 393216
s_cmp_eq_u32 s50, s53
s_cselect_b32 s50, s52, s50
s_add_u32 m0, s56, 0
s_nop 0
buffer_load_dwordx4 v176, s[20:23], s48 offen nt lds
buffer_load_dwordx4 v177, s[20:23], s48 offen offset:1024 nt lds
buffer_load_dwordx4 v178, s[20:23], s48 offen offset:2048 nt lds
s_add_u32 s48, s48, 12288
s_waitcnt vmcnt(13)
s_barrier
s_add_u32 m0, s84, 81920
s_nop 0
buffer_load_dwordx4 v160, s[36:39], s51 offen lds
buffer_load_dwordx4 v161, s[36:39], s51 offen offset:1024 lds
buffer_load_dwordx4 v162, s[36:39], s51 offen offset:2048 lds
)"
R"(buffer_load_dwordx4 v163, s[36:39], s51 offen offset:3072 lds
s_add_u32 s51, s51, 128
s_cmp_eq_u32 s51, 1280
s_cselect_b32 s51, 0, s51
s_add_u32 m0, s56, 98304
s_nop 0
buffer_load_dwordx4 v252, s[28:31], s50 offen lds
buffer_load_dwordx4 v253, s[28:31], s50 offen offset:1024 lds
buffer_load_dwordx4 v254, s[28:31], s50 offen offset:2048 lds
s_add_u32 s50, s50, 393216
s_cmp_eq_u32 s50, s53
s_cselect_b32 s50, s52, s50
s_waitcnt vmcnt(13)
s_barrier
s_add_u32 m0, s56, 110592
s_nop 0
)"
R"(buffer_load_dwordx4 v252, s[28:31], s50 offen lds
buffer_load_dwordx4 v253, s[28:31], s50 offen offset:1024 lds
buffer_load_dwordx4 v254, s[28:31], s50 offen offset:2048 lds
s_add_u32 s50, s50, 393216
s_cmp_eq_u32 s50, s53
s_cselect_b32 s50, s52, s50
s_waitcnt vmcnt(6)
s_barrier
s_add_u32 m0, s84, 65536
s_nop 0
buffer_load_dwordx4 v160, s[36:39], s51 offen lds
buffer_load_dwordx4 v161, s[36:39], s51 offen offset:1024 lds
buffer_load_dwordx4 v162, s[36:39], s51 offen offset:2048 lds
buffer_load_dwordx4 v163, s[36:39], s51 offen offset:3072 lds
s_add_u32 s51, s51, 128
s_cmp_eq_u32 s51, 1280
)"
R"(s_cselect_b32 s51, 0, s51
s_add_u32 m0, s56, 122880
s_nop 0
buffer_load_dwordx4 v252, s[28:31], s50 offen lds
buffer_load_dwordx4 v253, s[28:31], s50 offen offset:1024 lds
buffer_load_dwordx4 v254, s[28:31], s50 offen offset:2048 lds
s_add_u32 s50, s50, 393216
s_cmp_eq_u32 s50, s53
s_cselect_b32 s50, s52, s50
s_waitcnt vmcnt(10)
s_barrier
s_add_u32 m0, s84, 81920
s_nop 0
buffer_load_dwordx4 v160, s[36:39], s51 offen lds
buffer_load_dwordx4 v161, s[36:39], s51 offen offset:1024 lds
buffer_load_dwordx4 v162, s[36:39], s51 offen offset:2048 lds
)"
R"(buffer_load_dwordx4 v163, s[36:39], s51 offen offset:3072 lds
s_add_u32 s51, s51, 128
s_cmp_eq_u32 s51, 1280
s_cselect_b32 s51, 0, s51
s_add_u32 m0, s56, 135168
s_nop 0
buffer_load_dwordx4 v252, s[28:31], s50 offen lds
buffer_load_dwordx4 v253, s[28:31], s50 offen offset:1024 lds
buffer_load_dwordx4 v254, s[28:31], s50 offen offset:2048 lds
s_add_u32 s50, s50, 393216
s_cmp_eq_u32 s50, s53
s_cselect_b32 s50, s52, s50
s_waitcnt vmcnt(10)
s_barrier
s_add_u32 m0, s56, 147456
s_nop 0
)"
R"(buffer_load_dwordx4 v252, s[28:31], s50 offen lds
buffer_load_dwordx4 v253, s[28:31], s50 offen offset:1024 lds
buffer_load_dwordx4 v254, s[28:31], s50 offen offset:2048 lds
s_add_u32 s50, s50, 393216
s_cmp_eq_u32 s50, s53
s_cselect_b32 s50, s52, s50
s_add_u32 m0, s56, 12288
s_nop 0
buffer_load_dwordx4 v176, s[20:23], s48 offen nt lds
buffer_load_dwordx4 v177, s[20:23], s48 offen offset:1024 nt lds
buffer_load_dwordx4 v178, s[20:23], s48 offen offset:2048 nt lds
s_add_u32 s48, s48, 12288
s_waitcnt vmcnt(9)
s_barrier
s_add_u32 m0, s84, 65536
s_nop 0
)"
R"(buffer_load_dwordx4 v160, s[36:39], s51 offen lds
buffer_load_dwordx4 v161, s[36:39], s51 offen offset:1024 lds
buffer_load_dwordx4 v162, s[36:39], s51 offen offset:2048 lds
buffer_load_dwordx4 v163, s[36:39], s51 offen offset:3072 lds
s_add_u32 s51, s51, 128
s_cmp_eq_u32 s51, 1280
s_cselect_b32 s51, 0, s51
s_add_u32 m0, s56, 98304
s_nop 0
buffer_load_dwordx4 v252, s[28:31], s50 offen lds
buffer_load_dwordx4 v253, s[28:31], s50 offen offset:1024 lds
buffer_load_dwordx4 v254, s[28:31], s50 offen offset:2048 lds
s_add_u32 s50, s50, 393216
s_cmp_eq_u32 s50, s53
s_cselect_b32 s50, s52, s50
s_waitcnt vmcnt(13)
)"
R"(s_barrier
s_add_u32 m0, s84, 81920
s_nop 0
buffer_load_dwordx4 v160, s[36:39], s51 offen lds
buffer_load_dwordx4 v161, s[36:39], s51 offen offset:1024 lds
buffer_load_dwordx4 v162, s[36:39], s51 offen offset:2048 lds
buffer_load_dwordx4 v163, s[36:39], s51 offen offset:3072 lds
s_add_u32 s51, s51, 128
s_cmp_eq_u32 s51, 1280
s_cselect_b32 s51, 0, s51
s_add_u32 m0, s56, 110592
s_nop 0
buffer_load_dwordx4 v252, s[28:31], s50 offen lds
buffer_load_dwordx4 v253, s[28:31], s50 offen offset:1024 lds
buffer_load_dwordx4 v254, s[28:31], s50 offen offset:2048 lds
s_add_u32 s50, s50, 393216
)"
R"(s_cmp_eq_u32 s50, s53
s_cselect_b32 s50, s52, s50
s_waitcnt vmcnt(10)
s_barrier
s_add_u32 m0, s56, 122880
s_nop 0
buffer_load_dwordx4 v252, s[28:31], s50 offen lds
buffer_load_dwordx4 v253, s[28:31], s50 offen offset:1024 lds
buffer_load_dwordx4 v254, s[28:31], s50 offen offset:2048 lds
s_add_u32 s50, s50, 393216
s_cmp_eq_u32 s50, s53
s_cselect_b32 s50, s52, s50
s_add_u32 m0, s56, 49152
s_nop 0
buffer_load_dwordx4 v179, s[24:27], s49 offen nt lds
buffer_load_dwordx4 v180, s[24:27], s49 offen offset:1024 nt lds
)"
R"(buffer_load_dwordx4 v181, s[24:27], s49 offen offset:2048 nt lds
s_add_u32 s49, s49, 12288
s_waitcnt vmcnt(9)
s_barrier
s_add_u32 m0, s56, 135168
s_nop 0
buffer_load_dwordx4 v252, s[28:31], s50 offen lds
buffer_load_dwordx4 v253, s[28:31], s50 offen offset:1024 lds
buffer_load_dwordx4 v254, s[28:31], s50 offen offset:2048 lds
s_add_u32 s50, s50, 393216
s_cmp_eq_u32 s50, s53
s_cselect_b32 s50, s52, s50
s_waitcnt vmcnt(9)
s_barrier
s_add_u32 m0, s56, 24576
s_nop 0
)"
R"(buffer_load_dwordx4 v176, s[20:23], s48 offen nt lds
buffer_load_dwordx4 v177, s[20:23], s48 offen offset:1024 nt lds
buffer_load_dwordx4 v178, s[20:23], s48 offen offset:2048 nt lds
s_add_u32 s48, s48, 12288
s_waitcnt vmcnt(9)
s_barrier
s_add_u32 m0, s56, 98304
s_nop 0
buffer_load_dwordx4 v252, s[28:31], s50 offen lds
buffer_load_dwordx4 v253, s[28:31], s50 offen offset:1024 lds
buffer_load_dwordx4 v254, s[28:31], s50 offen offset:2048 lds
s_add_u32 s50, s50, 393216
s_cmp_eq_u32 s50, s53
s_cselect_b32 s50, s52, s50
s_add_u32 s50, s50, 1536
s_add_u32 s52, s52, 1536
)"
R"(s_add_u32 s53, s53, 1536
s_waitcnt vmcnt(6)
s_barrier
s_add_u32 m0, s56, 110592
s_nop 0
buffer_load_dwordx4 v252, s[28:31], s50 offen lds
buffer_load_dwordx4 v253, s[28:31], s50 offen offset:1024 lds
buffer_load_dwordx4 v254, s[28:31], s50 offen offset:2048 lds
s_add_u32 s50, s50, 393216
s_cmp_eq_u32 s50, s53
s_cselect_b32 s50, s52, s50
s_add_u32 m0, s56, 61440
s_nop 0
buffer_load_dwordx4 v179, s[24:27], s49 offen nt lds
buffer_load_dwordx4 v180, s[24:27], s49 offen offset:1024 nt lds
buffer_load_dwordx4 v181, s[24:27], s49 offen offset:2048 nt lds
)"
R"(s_add_u32 s49, s49, 12288
s_waitcnt vmcnt(6)
s_barrier
s_add_u32 m0, s56, 122880
s_nop 0
buffer_load_dwordx4 v252, s[28:31], s50 offen lds
buffer_load_dwordx4 v253, s[28:31], s50 offen offset:1024 lds
buffer_load_dwordx4 v254, s[28:31], s50 offen offset:2048 lds
s_add_u32 s50, s50, 393216
s_cmp_eq_u32 s50, s53
s_cselect_b32 s50, s52, s50
s_barrier
s_add_u32 m0, s56, 135168
s_nop 0
buffer_load_dwordx4 v252, s[28:31], s50 offen lds
buffer_load_dwordx4 v253, s[28:31], s50 offen offset:1024 lds
)"
R"(buffer_load_dwordx4 v254, s[28:31], s50 offen offset:2048 lds
s_add_u32 s50, s50, 393216
s_cmp_eq_u32 s50, s53
s_cselect_b32 s50, s52, s50
s_add_u32 m0, s56, 36864
s_nop 0
buffer_load_dwordx4 v176, s[20:23], s48 offen nt lds
buffer_load_dwordx4 v177, s[20:23], s48 offen offset:1024 nt lds
buffer_load_dwordx4 v178, s[20:23], s48 offen offset:2048 nt lds
s_add_u32 s48, s48, 12288
s_add_u32 m0, s56, 73728
s_nop 0
buffer_load_dwordx4 v179, s[24:27], s49 offen nt lds
buffer_load_dwordx4 v180, s[24:27], s49 offen offset:1024 nt lds
buffer_load_dwordx4 v181, s[24:27], s49 offen offset:2048 nt lds
s_add_u32 s49, s49, 12288
)"
R"(s_add_u32 m0, s56, 86016
s_nop 0
buffer_load_dwordx4 v179, s[24:27], s49 offen nt lds
buffer_load_dwordx4 v180, s[24:27], s49 offen offset:1024 nt lds
buffer_load_dwordx4 v181, s[24:27], s49 offen offset:2048 nt lds
s_add_u32 s49, s49, 12288
s_add_u32 s51, s51, 384
s_sub_u32 s68, s51, 1280
s_cmp_ge_u32 s51, 1280
s_cselect_b32 s51, s68, s51
s_setprio 2
s_cmp_eq_u32 s73, -1
s_cbranch_scc0 LpeB2_1_0
LplB2_1_0:
s_memrealtime s[88:89]
s_waitcnt lgkmcnt(0)
)"
R"(s_sub_u32 s89, s88, s94
s_cmp_lt_i32 s89, 0
s_cbranch_scc1 LplB2_1_0
s_add_u32 s95, s95, 95
s_sub_u32 s89, s88, s95
s_add_u32 s88, s88, 92
s_cmp_gt_i32 s89, 755
s_cselect_b32 s68, 20, 0
s_add_u32 s88, s88, s68
s_sub_u32 s89, s88, s95
s_cmp_gt_i32 s89, 0
s_cselect_b32 s94, s88, s95
LpeB2_1_0:
s_waitcnt vmcnt(12)
s_barrier
ds_read_b64_tr_b16 v[184:185], v166 offset:12288
)"
R"(ds_read_b64_tr_b16 v[186:187], v166 offset:13824
ds_read_b64_tr_b16 v[188:189], v167 offset:12288
ds_read_b64_tr_b16 v[190:191], v167 offset:13824
ds_read_b64_tr_b16 v[192:193], v168 offset:12288
ds_read_b64_tr_b16 v[194:195], v168 offset:13824
ds_read_b64_tr_b16 v[196:197], v169 offset:12288
ds_read_b64_tr_b16 v[198:199], v169 offset:13824
ds_read_b64_tr_b16 v[200:201], v166 offset:12416
ds_read_b64_tr_b16 v[202:203], v166 offset:13952
s_waitcnt lgkmcnt(8)
v_mfma_f32_16x16x32_bf16 v[0:3], v[184:187], v[96:99], 0
ds_read_b64_tr_b16 v[204:205], v167 offset:12416
ds_read_b64_tr_b16 v[206:207], v167 offset:13952
v_mfma_f32_16x16x32_bf16 v[4:7], v[184:187], v[100:103], 0
s_waitcnt lgkmcnt(8)
v_mfma_f32_16x16x32_bf16 v[12:15], v[188:191], v[100:103], 0
)"
R"(ds_read_b64_tr_b16 v[208:209], v168 offset:12416
ds_read_b64_tr_b16 v[210:211], v168 offset:13952
v_mfma_f32_16x16x32_bf16 v[8:11], v[188:191], v[96:99], 0
s_waitcnt lgkmcnt(8)
v_mfma_f32_16x16x32_bf16 v[16:19], v[192:195], v[96:99], 0
ds_read_b64_tr_b16 v[212:213], v169 offset:12416
ds_read_b64_tr_b16 v[214:215], v169 offset:13952
v_mfma_f32_16x16x32_bf16 v[20:23], v[192:195], v[100:103], 0
s_waitcnt lgkmcnt(8)
v_mfma_f32_16x16x32_bf16 v[28:31], v[196:199], v[100:103], 0
v_mfma_f32_16x16x32_bf16 v[24:27], v[196:199], v[96:99], 0
s_waitcnt lgkmcnt(6)
v_mfma_f32_16x16x32_bf16 v[32:35], v[200:203], v[96:99], 0
v_mfma_f32_16x16x32_bf16 v[36:39], v[200:203], v[100:103], 0
s_waitcnt lgkmcnt(4)
v_mfma_f32_16x16x32_bf16 v[44:47], v[204:207], v[100:103], 0
)"
R"(v_mfma_f32_16x16x32_bf16 v[40:43], v[204:207], v[96:99], 0
s_waitcnt lgkmcnt(2)
v_mfma_f32_16x16x32_bf16 v[48:51], v[208:211], v[96:99], 0
v_mfma_f32_16x16x32_bf16 v[52:55], v[208:211], v[100:103], 0
s_waitcnt lgkmcnt(0)
v_mfma_f32_16x16x32_bf16 v[60:63], v[212:215], v[100:103], 0
v_mfma_f32_16x16x32_bf16 v[56:59], v[212:215], v[96:99], 0
s_add_u32 m0, s56, 98304
s_nop 0
buffer_load_dwordx4 v252, s[28:31], s50 offen lds
buffer_load_dwordx4 v253, s[28:31], s50 offen offset:1024 lds
buffer_load_dwordx4 v254, s[28:31], s50 offen offset:2048 lds
s_add_u32 s50, s50, 393216
s_cmp_eq_u32 s50, s53
s_cselect_b32 s50, s52, s50
ds_read_b64_tr_b16 v[184:185], v166 offset:12544
)"
R"(ds_read_b64_tr_b16 v[186:187], v166 offset:14080
ds_read_b64_tr_b16 v[188:189], v167 offset:12544
ds_read_b64_tr_b16 v[190:191], v167 offset:14080
ds_read_b64_tr_b16 v[192:193], v168 offset:12544
ds_read_b64_tr_b16 v[194:195], v168 offset:14080
ds_read_b64_tr_b16 v[196:197], v169 offset:12544
ds_read_b64_tr_b16 v[198:199], v169 offset:14080
ds_read_b64_tr_b16 v[200:201], v166 offset:24576
ds_read_b64_tr_b16 v[202:203], v166 offset:26112
s_add_u32 m0, s56, 0
s_nop 0
buffer_load_dwordx4 v176, s[20:23], s48 offen nt lds
buffer_load_dwordx4 v177, s[20:23], s48 offen offset:1024 nt lds
buffer_load_dwordx4 v178, s[20:23], s48 offen offset:2048 nt lds
s_add_u32 s48, s48, 12288
ds_read_b64_tr_b16 v[204:205], v167 offset:24576
)"
R"(ds_read_b64_tr_b16 v[206:207], v167 offset:26112
ds_read_b64_tr_b16 v[208:209], v168 offset:24576
ds_read_b64_tr_b16 v[210:211], v168 offset:26112
ds_read_b64_tr_b16 v[212:213], v169 offset:24576
s_waitcnt lgkmcnt(11)
ds_read_b64_tr_b16 v[214:215], v169 offset:26112
s_waitcnt lgkmcnt(0)
s_cmp_eq_u32 s73, -1
s_cbranch_scc0 LpeB2_1_1
LplB2_1_1:
s_memrealtime s[88:89]
s_waitcnt lgkmcnt(0)
s_sub_u32 s89, s88, s94
s_cmp_lt_i32 s89, 0
s_cbranch_scc1 LplB2_1_1
s_add_u32 s95, s95, 95
)"
R"(s_sub_u32 s89, s88, s95
s_add_u32 s88, s88, 92
s_cmp_gt_i32 s89, 755
s_cselect_b32 s68, 20, 0
s_add_u32 s88, s88, s68
s_sub_u32 s89, s88, s95
s_cmp_gt_i32 s89, 0
s_cselect_b32 s94, s88, s95
LpeB2_1_1:
s_barrier
s_add_u32 m0, s56, 110592
s_nop 0
buffer_load_dwordx4 v252, s[28:31], s50 offen lds
buffer_load_dwordx4 v253, s[28:31], s50 offen offset:1024 lds
buffer_load_dwordx4 v254, s[28:31], s50 offen offset:2048 lds
s_add_u32 s50, s50, 393216
)"
R"(s_cmp_eq_u32 s50, s53
s_cselect_b32 s50, s52, s50
v_mfma_f32_16x16x32_bf16 v[64:67], v[184:187], v[96:99], 0
v_mfma_f32_16x16x32_bf16 v[68:71], v[184:187], v[100:103], 0
v_mfma_f32_16x16x32_bf16 v[76:79], v[188:191], v[100:103], 0
ds_read_b64_tr_b16 v[184:185], v166 offset:24704
ds_read_b64_tr_b16 v[186:187], v166 offset:26240
v_mfma_f32_16x16x32_bf16 v[72:75], v[188:191], v[96:99], 0
v_mfma_f32_16x16x32_bf16 v[80:83], v[192:195], v[96:99], 0
ds_read_b64_tr_b16 v[188:189], v167 offset:24704
ds_read_b64_tr_b16 v[190:191], v167 offset:26240
v_mfma_f32_16x16x32_bf16 v[84:87], v[192:195], v[100:103], 0
v_mfma_f32_16x16x32_bf16 v[92:95], v[196:199], v[100:103], 0
ds_read_b64_tr_b16 v[192:193], v168 offset:24704
ds_read_b64_tr_b16 v[194:195], v168 offset:26240
v_mfma_f32_16x16x32_bf16 v[88:91], v[196:199], v[96:99], 0
)"
R"(v_mfma_f32_16x16x32_bf16 v[0:3], v[200:203], v[104:107], v[0:3]
ds_read_b64_tr_b16 v[196:197], v169 offset:24704
ds_read_b64_tr_b16 v[198:199], v169 offset:26240
v_mfma_f32_16x16x32_bf16 v[4:7], v[200:203], v[108:111], v[4:7]
v_mfma_f32_16x16x32_bf16 v[12:15], v[204:207], v[108:111], v[12:15]
ds_read_b64_tr_b16 v[200:201], v166 offset:24832
ds_read_b64_tr_b16 v[202:203], v166 offset:26368
v_mfma_f32_16x16x32_bf16 v[8:11], v[204:207], v[104:107], v[8:11]
v_mfma_f32_16x16x32_bf16 v[16:19], v[208:211], v[104:107], v[16:19]
ds_read_b64_tr_b16 v[204:205], v167 offset:24832
ds_read_b64_tr_b16 v[206:207], v167 offset:26368
v_mfma_f32_16x16x32_bf16 v[20:23], v[208:211], v[108:111], v[20:23]
v_mfma_f32_16x16x32_bf16 v[28:31], v[212:215], v[108:111], v[28:31]
ds_read_b64_tr_b16 v[208:209], v168 offset:24832
ds_read_b64_tr_b16 v[210:211], v168 offset:26368
v_mfma_f32_16x16x32_bf16 v[24:27], v[212:215], v[104:107], v[24:27]
)"
R"(s_add_u32 m0, s56, 12288
s_nop 0
buffer_load_dwordx4 v176, s[20:23], s48 offen nt lds
buffer_load_dwordx4 v177, s[20:23], s48 offen offset:1024 nt lds
buffer_load_dwordx4 v178, s[20:23], s48 offen offset:2048 nt lds
s_add_u32 s48, s48, 12288
ds_read_b64_tr_b16 v[212:213], v169 offset:24832
s_waitcnt lgkmcnt(11)
ds_read_b64_tr_b16 v[214:215], v169 offset:26368
s_add_u32 m0, s56, 49152
s_nop 0
buffer_load_dwordx4 v179, s[24:27], s49 offen nt lds
buffer_load_dwordx4 v180, s[24:27], s49 offen offset:1024 nt lds
buffer_load_dwordx4 v181, s[24:27], s49 offen offset:2048 nt lds
s_add_u32 s49, s49, 12288
s_waitcnt lgkmcnt(0)
)"
R"(s_cmp_eq_u32 s73, -1
s_cbranch_scc0 LpeB2_1_2
LplB2_1_2:
s_memrealtime s[88:89]
s_waitcnt lgkmcnt(0)
s_sub_u32 s89, s88, s94
s_cmp_lt_i32 s89, 0
s_cbranch_scc1 LplB2_1_2
s_add_u32 s95, s95, 95
s_sub_u32 s89, s88, s95
s_add_u32 s88, s88, 92
s_cmp_gt_i32 s89, 755
s_cselect_b32 s68, 20, 0
s_add_u32 s88, s88, s68
s_sub_u32 s89, s88, s95
s_cmp_gt_i32 s89, 0
)"
R"(s_cselect_b32 s94, s88, s95
LpeB2_1_2:
s_waitcnt vmcnt(18)
s_barrier
s_add_u32 m0, s56, 122880
s_nop 0
buffer_load_dwordx4 v252, s[28:31], s50 offen lds
buffer_load_dwordx4 v253, s[28:31], s50 offen offset:1024 lds
buffer_load_dwordx4 v254, s[28:31], s50 offen offset:2048 lds
s_add_u32 s50, s50, 393216
s_cmp_eq_u32 s50, s53
s_cselect_b32 s50, s52, s50
v_mfma_f32_16x16x32_bf16 v[32:35], v[184:187], v[104:107], v[32:35]
v_mfma_f32_16x16x32_bf16 v[36:39], v[184:187], v[108:111], v[36:39]
v_mfma_f32_16x16x32_bf16 v[44:47], v[188:191], v[108:111], v[44:47]
ds_read_b64_tr_b16 v[184:185], v166 offset:36864
)"
R"(ds_read_b64_tr_b16 v[186:187], v166 offset:38400
v_mfma_f32_16x16x32_bf16 v[40:43], v[188:191], v[104:107], v[40:43]
v_mfma_f32_16x16x32_bf16 v[48:51], v[192:195], v[104:107], v[48:51]
ds_read_b64_tr_b16 v[188:189], v167 offset:36864
ds_read_b64_tr_b16 v[190:191], v167 offset:38400
v_mfma_f32_16x16x32_bf16 v[52:55], v[192:195], v[108:111], v[52:55]
v_mfma_f32_16x16x32_bf16 v[60:63], v[196:199], v[108:111], v[60:63]
ds_read_b64_tr_b16 v[192:193], v168 offset:36864
ds_read_b64_tr_b16 v[194:195], v168 offset:38400
v_mfma_f32_16x16x32_bf16 v[56:59], v[196:199], v[104:107], v[56:59]
v_mfma_f32_16x16x32_bf16 v[64:67], v[200:203], v[104:107], v[64:67]
ds_read_b64_tr_b16 v[196:197], v169 offset:36864
ds_read_b64_tr_b16 v[198:199], v169 offset:38400
v_mfma_f32_16x16x32_bf16 v[68:71], v[200:203], v[108:111], v[68:71]
v_mfma_f32_16x16x32_bf16 v[76:79], v[204:207], v[108:111], v[76:79]
ds_read_b64_tr_b16 v[200:201], v166 offset:36992
)"
R"(ds_read_b64_tr_b16 v[202:203], v166 offset:38528
v_mfma_f32_16x16x32_bf16 v[72:75], v[204:207], v[104:107], v[72:75]
v_mfma_f32_16x16x32_bf16 v[80:83], v[208:211], v[104:107], v[80:83]
ds_read_b64_tr_b16 v[204:205], v167 offset:36992
ds_read_b64_tr_b16 v[206:207], v167 offset:38528
v_mfma_f32_16x16x32_bf16 v[84:87], v[208:211], v[108:111], v[84:87]
v_mfma_f32_16x16x32_bf16 v[92:95], v[212:215], v[108:111], v[92:95]
ds_read_b64_tr_b16 v[208:209], v168 offset:36992
ds_read_b64_tr_b16 v[210:211], v168 offset:38528
v_mfma_f32_16x16x32_bf16 v[88:91], v[212:215], v[104:107], v[88:91]
s_add_u32 m0, s56, 24576
s_nop 0
buffer_load_dwordx4 v176, s[20:23], s48 offen nt lds
buffer_load_dwordx4 v177, s[20:23], s48 offen offset:1024 nt lds
buffer_load_dwordx4 v178, s[20:23], s48 offen offset:2048 nt lds
s_add_u32 s48, s48, 12288
)"
R"(ds_read_b64_tr_b16 v[212:213], v169 offset:36992
s_waitcnt lgkmcnt(11)
ds_read_b64_tr_b16 v[214:215], v169 offset:38528
s_add_u32 m0, s56, 61440
s_nop 0
buffer_load_dwordx4 v179, s[24:27], s49 offen nt lds
buffer_load_dwordx4 v180, s[24:27], s49 offen offset:1024 nt lds
buffer_load_dwordx4 v181, s[24:27], s49 offen offset:2048 nt lds
s_add_u32 s49, s49, 12288
s_waitcnt lgkmcnt(0)
s_cmp_eq_u32 s73, -1
s_cbranch_scc0 LpeB2_1_3
LplB2_1_3:
s_memrealtime s[88:89]
s_waitcnt lgkmcnt(0)
s_sub_u32 s89, s88, s94
)"
R"(s_cmp_lt_i32 s89, 0
s_cbranch_scc1 LplB2_1_3
s_add_u32 s95, s95, 95
s_sub_u32 s89, s88, s95
s_add_u32 s88, s88, 92
s_cmp_gt_i32 s89, 755
s_cselect_b32 s68, 20, 0
s_add_u32 s88, s88, s68
s_sub_u32 s89, s88, s95
s_cmp_gt_i32 s89, 0
s_cselect_b32 s94, s88, s95
LpeB2_1_3:
s_waitcnt vmcnt(18)
s_barrier
s_add_u32 m0, s56, 36864
s_nop 0
)"
R"(buffer_load_dwordx4 v176, s[20:23], s48 offen nt lds
buffer_load_dwordx4 v177, s[20:23], s48 offen offset:1024 nt lds
buffer_load_dwordx4 v178, s[20:23], s48 offen offset:2048 nt lds
s_add_u32 s48, s48, 12288
v_mfma_f32_16x16x32_bf16 v[0:3], v[184:187], v[112:115], v[0:3]
v_mfma_f32_16x16x32_bf16 v[4:7], v[184:187], v[116:119], v[4:7]
v_mfma_f32_16x16x32_bf16 v[12:15], v[188:191], v[116:119], v[12:15]
ds_read_b64_tr_b16 v[184:185], v166 offset:37120
ds_read_b64_tr_b16 v[186:187], v166 offset:38656
v_mfma_f32_16x16x32_bf16 v[8:11], v[188:191], v[112:115], v[8:11]
v_mfma_f32_16x16x32_bf16 v[16:19], v[192:195], v[112:115], v[16:19]
ds_read_b64_tr_b16 v[188:189], v167 offset:37120
ds_read_b64_tr_b16 v[190:191], v167 offset:38656
v_mfma_f32_16x16x32_bf16 v[20:23], v[192:195], v[116:119], v[20:23]
v_mfma_f32_16x16x32_bf16 v[28:31], v[196:199], v[116:119], v[28:31]
ds_read_b64_tr_b16 v[192:193], v168 offset:37120
)"
R"(ds_read_b64_tr_b16 v[194:195], v168 offset:38656
v_mfma_f32_16x16x32_bf16 v[24:27], v[196:199], v[112:115], v[24:27]
v_mfma_f32_16x16x32_bf16 v[32:35], v[200:203], v[112:115], v[32:35]
ds_read_b64_tr_b16 v[196:197], v169 offset:37120
ds_read_b64_tr_b16 v[198:199], v169 offset:38656
v_mfma_f32_16x16x32_bf16 v[36:39], v[200:203], v[116:119], v[36:39]
v_mfma_f32_16x16x32_bf16 v[44:47], v[204:207], v[116:119], v[44:47]
ds_read_b64_tr_b16 v[200:201], v166 offset:0
ds_read_b64_tr_b16 v[202:203], v166 offset:1536
v_mfma_f32_16x16x32_bf16 v[40:43], v[204:207], v[112:115], v[40:43]
v_mfma_f32_16x16x32_bf16 v[48:51], v[208:211], v[112:115], v[48:51]
ds_read_b64_tr_b16 v[204:205], v167 offset:0
ds_read_b64_tr_b16 v[206:207], v167 offset:1536
v_mfma_f32_16x16x32_bf16 v[52:55], v[208:211], v[116:119], v[52:55]
v_mfma_f32_16x16x32_bf16 v[60:63], v[212:215], v[116:119], v[60:63]
ds_read_b64_tr_b16 v[208:209], v168 offset:0
)"
R"(ds_read_b64_tr_b16 v[210:211], v168 offset:1536
v_mfma_f32_16x16x32_bf16 v[56:59], v[212:215], v[112:115], v[56:59]
s_add_u32 m0, s56, 73728
s_nop 0
buffer_load_dwordx4 v179, s[24:27], s49 offen nt lds
ds_read_b64_tr_b16 v[212:213], v169 offset:0
s_waitcnt lgkmcnt(11)
ds_read_b64_tr_b16 v[214:215], v169 offset:1536
buffer_load_dwordx4 v180, s[24:27], s49 offen offset:1024 nt lds
buffer_load_dwordx4 v181, s[24:27], s49 offen offset:2048 nt lds
s_add_u32 s49, s49, 12288
s_waitcnt lgkmcnt(0)
s_cmp_eq_u32 s73, -1
s_cbranch_scc0 LpeB3_1_0
LplB3_1_0:
s_memrealtime s[88:89]
)"
R"(s_waitcnt lgkmcnt(0)
s_sub_u32 s89, s88, s94
s_cmp_lt_i32 s89, 0
s_cbranch_scc1 LplB3_1_0
s_add_u32 s95, s95, 95
s_sub_u32 s89, s88, s95
s_add_u32 s88, s88, 92
s_cmp_gt_i32 s89, 755
s_cselect_b32 s68, 20, 0
s_add_u32 s88, s88, s68
s_sub_u32 s89, s88, s95
s_cmp_gt_i32 s89, 0
s_cselect_b32 s94, s88, s95
LpeB3_1_0:
s_waitcnt vmcnt(15)
s_barrier
)"
R"(s_add_u32 m0, s56, 135168
s_nop 0
buffer_load_dwordx4 v252, s[28:31], s50 offen lds
buffer_load_dwordx4 v253, s[28:31], s50 offen offset:1024 lds
buffer_load_dwordx4 v254, s[28:31], s50 offen offset:2048 lds
s_add_u32 s50, s50, 393216
s_cmp_eq_u32 s50, s53
s_cselect_b32 s50, s52, s50
v_mfma_f32_16x16x32_bf16 v[64:67], v[184:187], v[112:115], v[64:67]
v_mfma_f32_16x16x32_bf16 v[68:71], v[184:187], v[116:119], v[68:71]
v_mfma_f32_16x16x32_bf16 v[76:79], v[188:191], v[116:119], v[76:79]
ds_read_b64_tr_b16 v[184:185], v166 offset:128
ds_read_b64_tr_b16 v[186:187], v166 offset:1664
v_mfma_f32_16x16x32_bf16 v[72:75], v[188:191], v[112:115], v[72:75]
v_mfma_f32_16x16x32_bf16 v[80:83], v[192:195], v[112:115], v[80:83]
ds_read_b64_tr_b16 v[188:189], v167 offset:128
)"
R"(ds_read_b64_tr_b16 v[190:191], v167 offset:1664
v_mfma_f32_16x16x32_bf16 v[84:87], v[192:195], v[116:119], v[84:87]
v_mfma_f32_16x16x32_bf16 v[92:95], v[196:199], v[116:119], v[92:95]
ds_read_b64_tr_b16 v[192:193], v168 offset:128
ds_read_b64_tr_b16 v[194:195], v168 offset:1664
v_mfma_f32_16x16x32_bf16 v[88:91], v[196:199], v[112:115], v[88:91]
v_mfma_f32_16x16x32_bf16 v[0:3], v[200:203], v[120:123], v[0:3]
ds_read_b64_tr_b16 v[196:197], v169 offset:128
ds_read_b64_tr_b16 v[198:199], v169 offset:1664
v_mfma_f32_16x16x32_bf16 v[4:7], v[200:203], v[124:127], v[4:7]
v_mfma_f32_16x16x32_bf16 v[12:15], v[204:207], v[124:127], v[12:15]
ds_read_b64_tr_b16 v[200:201], v166 offset:256
ds_read_b64_tr_b16 v[202:203], v166 offset:1792
v_mfma_f32_16x16x32_bf16 v[8:11], v[204:207], v[120:123], v[8:11]
v_mfma_f32_16x16x32_bf16 v[16:19], v[208:211], v[120:123], v[16:19]
ds_read_b64_tr_b16 v[204:205], v167 offset:256
)"
R"(ds_read_b64_tr_b16 v[206:207], v167 offset:1792
v_mfma_f32_16x16x32_bf16 v[20:23], v[208:211], v[124:127], v[20:23]
v_mfma_f32_16x16x32_bf16 v[28:31], v[212:215], v[124:127], v[28:31]
ds_read_b64_tr_b16 v[208:209], v168 offset:256
ds_read_b64_tr_b16 v[210:211], v168 offset:1792
v_mfma_f32_16x16x32_bf16 v[24:27], v[212:215], v[120:123], v[24:27]
s_add_u32 m0, s84, 147456
s_nop 0
buffer_load_dwordx4 v160, s[36:39], s51 offen lds
buffer_load_dwordx4 v161, s[36:39], s51 offen offset:1024 lds
buffer_load_dwordx4 v162, s[36:39], s51 offen offset:2048 lds
buffer_load_dwordx4 v163, s[36:39], s51 offen offset:3072 lds
s_add_u32 s51, s51, 128
s_cmp_eq_u32 s51, 1280
s_cselect_b32 s51, 0, s51
s_add_u32 m0, s56, 0
)"
R"(s_nop 0
buffer_load_dwordx4 v176, s[20:23], s48 offen nt lds
ds_read_b64_tr_b16 v[212:213], v169 offset:256
s_waitcnt lgkmcnt(11)
ds_read_b64_tr_b16 v[214:215], v169 offset:1792
buffer_load_dwordx4 v177, s[20:23], s48 offen offset:1024 nt lds
buffer_load_dwordx4 v178, s[20:23], s48 offen offset:2048 nt lds
s_add_u32 s48, s48, 12288
s_add_u32 m0, s56, 86016
s_nop 0
buffer_load_dwordx4 v179, s[24:27], s49 offen nt lds
buffer_load_dwordx4 v180, s[24:27], s49 offen offset:1024 nt lds
buffer_load_dwordx4 v181, s[24:27], s49 offen offset:2048 nt lds
s_add_u32 s49, s49, 12288
s_waitcnt lgkmcnt(0)
s_cmp_eq_u32 s73, -1
)"
R"(s_cbranch_scc0 LpeB3_1_1
LplB3_1_1:
s_memrealtime s[88:89]
s_waitcnt lgkmcnt(0)
s_sub_u32 s89, s88, s94
s_cmp_lt_i32 s89, 0
s_cbranch_scc1 LplB3_1_1
s_add_u32 s95, s95, 95
s_sub_u32 s89, s88, s95
s_add_u32 s88, s88, 92
s_cmp_gt_i32 s89, 755
s_cselect_b32 s68, 20, 0
s_add_u32 s88, s88, s68
s_sub_u32 s89, s88, s95
s_cmp_gt_i32 s89, 0
s_cselect_b32 s94, s88, s95
)"
R"(LpeB3_1_1:
s_waitcnt vmcnt(19)
s_barrier
s_add_u32 m0, s56, 98304
s_nop 0
buffer_load_dwordx4 v252, s[28:31], s50 offen lds
buffer_load_dwordx4 v253, s[28:31], s50 offen offset:1024 lds
buffer_load_dwordx4 v254, s[28:31], s50 offen offset:2048 lds
s_add_u32 s50, s50, 393216
s_cmp_eq_u32 s50, s53
s_cselect_b32 s50, s52, s50
v_mfma_f32_16x16x32_bf16 v[32:35], v[184:187], v[120:123], v[32:35]
v_mfma_f32_16x16x32_bf16 v[36:39], v[184:187], v[124:127], v[36:39]
v_mfma_f32_16x16x32_bf16 v[44:47], v[188:191], v[124:127], v[44:47]
ds_read_b64_tr_b16 v[184:185], v166 offset:12288
ds_read_b64_tr_b16 v[186:187], v166 offset:13824
)"
R"(v_mfma_f32_16x16x32_bf16 v[40:43], v[188:191], v[120:123], v[40:43]
v_mfma_f32_16x16x32_bf16 v[48:51], v[192:195], v[120:123], v[48:51]
ds_read_b64_tr_b16 v[188:189], v167 offset:12288
ds_read_b64_tr_b16 v[190:191], v167 offset:13824
v_mfma_f32_16x16x32_bf16 v[52:55], v[192:195], v[124:127], v[52:55]
v_mfma_f32_16x16x32_bf16 v[60:63], v[196:199], v[124:127], v[60:63]
ds_read_b64_tr_b16 v[192:193], v168 offset:12288
ds_read_b64_tr_b16 v[194:195], v168 offset:13824
v_mfma_f32_16x16x32_bf16 v[56:59], v[196:199], v[120:123], v[56:59]
v_mfma_f32_16x16x32_bf16 v[64:67], v[200:203], v[120:123], v[64:67]
ds_read_b64_tr_b16 v[196:197], v169 offset:12288
ds_read_b64_tr_b16 v[198:199], v169 offset:13824
v_mfma_f32_16x16x32_bf16 v[68:71], v[200:203], v[124:127], v[68:71]
v_mfma_f32_16x16x32_bf16 v[76:79], v[204:207], v[124:127], v[76:79]
ds_read_b64_tr_b16 v[200:201], v166 offset:12416
ds_read_b64_tr_b16 v[202:203], v166 offset:13952
)"
R"(v_mfma_f32_16x16x32_bf16 v[72:75], v[204:207], v[120:123], v[72:75]
v_mfma_f32_16x16x32_bf16 v[80:83], v[208:211], v[120:123], v[80:83]
ds_read_b64_tr_b16 v[204:205], v167 offset:12416
ds_read_b64_tr_b16 v[206:207], v167 offset:13952
v_mfma_f32_16x16x32_bf16 v[84:87], v[208:211], v[124:127], v[84:87]
v_mfma_f32_16x16x32_bf16 v[92:95], v[212:215], v[124:127], v[92:95]
ds_read_b64_tr_b16 v[208:209], v168 offset:12416
ds_read_b64_tr_b16 v[210:211], v168 offset:13952
v_mfma_f32_16x16x32_bf16 v[88:91], v[212:215], v[120:123], v[88:91]
s_add_u32 m0, s56, 12288
s_nop 0
buffer_load_dwordx4 v176, s[20:23], s48 offen nt lds
buffer_load_dwordx4 v177, s[20:23], s48 offen offset:1024 nt lds
buffer_load_dwordx4 v178, s[20:23], s48 offen offset:2048 nt lds
s_add_u32 s48, s48, 12288
ds_read_b64_tr_b16 v[212:213], v169 offset:12416
)"
R"(s_waitcnt lgkmcnt(11)
ds_read_b64_tr_b16 v[214:215], v169 offset:13952
s_add_u32 m0, s56, 49152
s_nop 0
buffer_load_dwordx4 v179, s[24:27], s49 offen nt lds
buffer_load_dwordx4 v180, s[24:27], s49 offen offset:1024 nt lds
buffer_load_dwordx4 v181, s[24:27], s49 offen offset:2048 nt lds
s_add_u32 s49, s49, 12288
s_waitcnt lgkmcnt(0)
s_cmp_eq_u32 s73, -1
s_cbranch_scc0 LpeB3_1_2
LplB3_1_2:
s_memrealtime s[88:89]
s_waitcnt lgkmcnt(0)
s_sub_u32 s89, s88, s94
s_cmp_lt_i32 s89, 0
)"
R"(s_cbranch_scc1 LplB3_1_2
s_add_u32 s95, s95, 95
s_sub_u32 s89, s88, s95
s_add_u32 s88, s88, 92
s_cmp_gt_i32 s89, 755
s_cselect_b32 s68, 20, 0
s_add_u32 s88, s88, s68
s_sub_u32 s89, s88, s95
s_cmp_gt_i32 s89, 0
s_cselect_b32 s94, s88, s95
LpeB3_1_2:
s_waitcnt vmcnt(22)
s_barrier
s_add_u32 m0, s56, 24576
s_nop 0
buffer_load_dwordx4 v176, s[20:23], s48 offen nt lds
)"
R"(buffer_load_dwordx4 v177, s[20:23], s48 offen offset:1024 nt lds
buffer_load_dwordx4 v178, s[20:23], s48 offen offset:2048 nt lds
s_add_u32 s48, s48, 12288
v_mfma_f32_16x16x32_bf16 v[0:3], v[184:187], v[128:131], v[0:3]
v_mfma_f32_16x16x32_bf16 v[4:7], v[184:187], v[132:135], v[4:7]
v_mfma_f32_16x16x32_bf16 v[12:15], v[188:191], v[132:135], v[12:15]
ds_read_b64_tr_b16 v[184:185], v166 offset:12544
ds_read_b64_tr_b16 v[186:187], v166 offset:14080
v_mfma_f32_16x16x32_bf16 v[8:11], v[188:191], v[128:131], v[8:11]
v_mfma_f32_16x16x32_bf16 v[16:19], v[192:195], v[128:131], v[16:19]
ds_read_b64_tr_b16 v[188:189], v167 offset:12544
ds_read_b64_tr_b16 v[190:191], v167 offset:14080
v_mfma_f32_16x16x32_bf16 v[20:23], v[192:195], v[132:135], v[20:23]
v_mfma_f32_16x16x32_bf16 v[28:31], v[196:199], v[132:135], v[28:31]
ds_read_b64_tr_b16 v[192:193], v168 offset:12544
ds_read_b64_tr_b16 v[194:195], v168 offset:14080
)"
R"(v_mfma_f32_16x16x32_bf16 v[24:27], v[196:199], v[128:131], v[24:27]
v_mfma_f32_16x16x32_bf16 v[32:35], v[200:203], v[128:131], v[32:35]
ds_read_b64_tr_b16 v[196:197], v169 offset:12544
ds_read_b64_tr_b16 v[198:199], v169 offset:14080
v_mfma_f32_16x16x32_bf16 v[36:39], v[200:203], v[132:135], v[36:39]
v_mfma_f32_16x16x32_bf16 v[44:47], v[204:207], v[132:135], v[44:47]
ds_read_b64_tr_b16 v[200:201], v166 offset:24576
ds_read_b64_tr_b16 v[202:203], v166 offset:26112
v_mfma_f32_16x16x32_bf16 v[40:43], v[204:207], v[128:131], v[40:43]
v_mfma_f32_16x16x32_bf16 v[48:51], v[208:211], v[128:131], v[48:51]
ds_read_b64_tr_b16 v[204:205], v167 offset:24576
ds_read_b64_tr_b16 v[206:207], v167 offset:26112
v_mfma_f32_16x16x32_bf16 v[52:55], v[208:211], v[132:135], v[52:55]
v_mfma_f32_16x16x32_bf16 v[60:63], v[212:215], v[132:135], v[60:63]
ds_read_b64_tr_b16 v[208:209], v168 offset:24576
ds_read_b64_tr_b16 v[210:211], v168 offset:26112
)"
R"(v_mfma_f32_16x16x32_bf16 v[56:59], v[212:215], v[128:131], v[56:59]
s_add_u32 m0, s56, 61440
s_nop 0
buffer_load_dwordx4 v179, s[24:27], s49 offen nt lds
ds_read_b64_tr_b16 v[212:213], v169 offset:24576
s_waitcnt lgkmcnt(11)
ds_read_b64_tr_b16 v[214:215], v169 offset:26112
buffer_load_dwordx4 v180, s[24:27], s49 offen offset:1024 nt lds
buffer_load_dwordx4 v181, s[24:27], s49 offen offset:2048 nt lds
s_add_u32 s49, s49, 12288
s_waitcnt lgkmcnt(0)
s_cmp_eq_u32 s73, -1
s_cbranch_scc0 LpeB3_1_3
LplB3_1_3:
s_memrealtime s[88:89]
s_waitcnt lgkmcnt(0)
)"
R"(s_sub_u32 s89, s88, s94
s_cmp_lt_i32 s89, 0
s_cbranch_scc1 LplB3_1_3
s_add_u32 s95, s95, 95
s_sub_u32 s89, s88, s95
s_add_u32 s88, s88, 92
s_cmp_gt_i32 s89, 755
s_cselect_b32 s68, 20, 0
s_add_u32 s88, s88, s68
s_sub_u32 s89, s88, s95
s_cmp_gt_i32 s89, 0
s_cselect_b32 s94, s88, s95
LpeB3_1_3:
s_waitcnt vmcnt(15)
s_barrier
s_add_u32 m0, s56, 110592
)"
R"(s_nop 0
buffer_load_dwordx4 v252, s[28:31], s50 offen lds
buffer_load_dwordx4 v253, s[28:31], s50 offen offset:1024 lds
buffer_load_dwordx4 v254, s[28:31], s50 offen offset:2048 lds
s_add_u32 s50, s50, 393216
s_cmp_eq_u32 s50, s53
s_cselect_b32 s50, s52, s50
v_mfma_f32_16x16x32_bf16 v[64:67], v[184:187], v[128:131], v[64:67]
v_mfma_f32_16x16x32_bf16 v[68:71], v[184:187], v[132:135], v[68:71]
v_mfma_f32_16x16x32_bf16 v[76:79], v[188:191], v[132:135], v[76:79]
ds_read_b64_tr_b16 v[184:185], v166 offset:24704
ds_read_b64_tr_b16 v[186:187], v166 offset:26240
v_mfma_f32_16x16x32_bf16 v[72:75], v[188:191], v[128:131], v[72:75]
v_mfma_f32_16x16x32_bf16 v[80:83], v[192:195], v[128:131], v[80:83]
ds_read_b64_tr_b16 v[188:189], v167 offset:24704
ds_read_b64_tr_b16 v[190:191], v167 offset:26240
)"
R"(v_mfma_f32_16x16x32_bf16 v[84:87], v[192:195], v[132:135], v[84:87]
v_mfma_f32_16x16x32_bf16 v[92:95], v[196:199], v[132:135], v[92:95]
ds_read_b64_tr_b16 v[192:193], v168 offset:24704
ds_read_b64_tr_b16 v[194:195], v168 offset:26240
v_mfma_f32_16x16x32_bf16 v[88:91], v[196:199], v[128:131], v[88:91]
v_mfma_f32_16x16x32_bf16 v[0:3], v[200:203], v[136:139], v[0:3]
ds_read_b64_tr_b16 v[196:197], v169 offset:24704
ds_read_b64_tr_b16 v[198:199], v169 offset:26240
v_mfma_f32_16x16x32_bf16 v[4:7], v[200:203], v[140:143], v[4:7]
v_mfma_f32_16x16x32_bf16 v[12:15], v[204:207], v[140:143], v[12:15]
ds_read_b64_tr_b16 v[200:201], v166 offset:24832
ds_read_b64_tr_b16 v[202:203], v166 offset:26368
v_mfma_f32_16x16x32_bf16 v[8:11], v[204:207], v[136:139], v[8:11]
v_mfma_f32_16x16x32_bf16 v[16:19], v[208:211], v[136:139], v[16:19]
ds_read_b64_tr_b16 v[204:205], v167 offset:24832
ds_read_b64_tr_b16 v[206:207], v167 offset:26368
)"
R"(v_mfma_f32_16x16x32_bf16 v[20:23], v[208:211], v[140:143], v[20:23]
v_mfma_f32_16x16x32_bf16 v[28:31], v[212:215], v[140:143], v[28:31]
ds_read_b64_tr_b16 v[208:209], v168 offset:24832
ds_read_b64_tr_b16 v[210:211], v168 offset:26368
v_mfma_f32_16x16x32_bf16 v[24:27], v[212:215], v[136:139], v[24:27]
s_add_u32 m0, s56, 36864
s_nop 0
buffer_load_dwordx4 v176, s[20:23], s48 offen nt lds
buffer_load_dwordx4 v177, s[20:23], s48 offen offset:1024 nt lds
buffer_load_dwordx4 v178, s[20:23], s48 offen offset:2048 nt lds
s_add_u32 s48, s48, 12288
ds_read_b64_tr_b16 v[212:213], v169 offset:24832
s_waitcnt lgkmcnt(11)
ds_read_b64_tr_b16 v[214:215], v169 offset:26368
s_add_u32 m0, s56, 73728
s_nop 0
)"
R"(buffer_load_dwordx4 v179, s[24:27], s49 offen nt lds
buffer_load_dwordx4 v180, s[24:27], s49 offen offset:1024 nt lds
buffer_load_dwordx4 v181, s[24:27], s49 offen offset:2048 nt lds
s_add_u32 s49, s49, 12288
s_waitcnt lgkmcnt(0)
s_cmp_eq_u32 s73, -1
s_cbranch_scc0 LpeB4_1_0
LplB4_1_0:
s_memrealtime s[88:89]
s_waitcnt lgkmcnt(0)
s_sub_u32 s89, s88, s94
s_cmp_lt_i32 s89, 0
s_cbranch_scc1 LplB4_1_0
s_add_u32 s95, s95, 95
s_sub_u32 s89, s88, s95
s_add_u32 s88, s88, 92
)"
R"(s_cmp_gt_i32 s89, 755
s_cselect_b32 s68, 20, 0
s_add_u32 s88, s88, s68
s_sub_u32 s89, s88, s95
s_cmp_gt_i32 s89, 0
s_cselect_b32 s94, s88, s95
LpeB4_1_0:
s_waitcnt vmcnt(15)
s_barrier
s_add_u32 m0, s56, 122880
s_nop 0
buffer_load_dwordx4 v252, s[28:31], s50 offen lds
buffer_load_dwordx4 v253, s[28:31], s50 offen offset:1024 lds
buffer_load_dwordx4 v254, s[28:31], s50 offen offset:2048 lds
s_add_u32 s50, s50, 393216
s_cmp_eq_u32 s50, s53
)"
R"(s_cselect_b32 s50, s52, s50
v_mfma_f32_16x16x32_bf16 v[32:35], v[184:187], v[136:139], v[32:35]
v_mfma_f32_16x16x32_bf16 v[36:39], v[184:187], v[140:143], v[36:39]
v_mfma_f32_16x16x32_bf16 v[44:47], v[188:191], v[140:143], v[44:47]
ds_read_b64_tr_b16 v[184:185], v166 offset:36864
ds_read_b64_tr_b16 v[186:187], v166 offset:38400
v_mfma_f32_16x16x32_bf16 v[40:43], v[188:191], v[136:139], v[40:43]
v_mfma_f32_16x16x32_bf16 v[48:51], v[192:195], v[136:139], v[48:51]
ds_read_b64_tr_b16 v[188:189], v167 offset:36864
ds_read_b64_tr_b16 v[190:191], v167 offset:38400
v_mfma_f32_16x16x32_bf16 v[52:55], v[192:195], v[140:143], v[52:55]
v_mfma_f32_16x16x32_bf16 v[60:63], v[196:199], v[140:143], v[60:63]
ds_read_b64_tr_b16 v[192:193], v168 offset:36864
ds_read_b64_tr_b16 v[194:195], v168 offset:38400
v_mfma_f32_16x16x32_bf16 v[56:59], v[196:199], v[136:139], v[56:59]
v_mfma_f32_16x16x32_bf16 v[64:67], v[200:203], v[136:139], v[64:67]
)"
R"(ds_read_b64_tr_b16 v[196:197], v169 offset:36864
ds_read_b64_tr_b16 v[198:199], v169 offset:38400
v_mfma_f32_16x16x32_bf16 v[68:71], v[200:203], v[140:143], v[68:71]
v_mfma_f32_16x16x32_bf16 v[76:79], v[204:207], v[140:143], v[76:79]
ds_read_b64_tr_b16 v[200:201], v166 offset:36992
ds_read_b64_tr_b16 v[202:203], v166 offset:38528
v_mfma_f32_16x16x32_bf16 v[72:75], v[204:207], v[136:139], v[72:75]
v_mfma_f32_16x16x32_bf16 v[80:83], v[208:211], v[136:139], v[80:83]
ds_read_b64_tr_b16 v[204:205], v167 offset:36992
ds_read_b64_tr_b16 v[206:207], v167 offset:38528
v_mfma_f32_16x16x32_bf16 v[84:87], v[208:211], v[140:143], v[84:87]
v_mfma_f32_16x16x32_bf16 v[92:95], v[212:215], v[140:143], v[92:95]
ds_read_b64_tr_b16 v[208:209], v168 offset:36992
ds_read_b64_tr_b16 v[210:211], v168 offset:38528
v_mfma_f32_16x16x32_bf16 v[88:91], v[212:215], v[136:139], v[88:91]
ds_read_b128 v[144:147], v164 offset:0
)"
R"(s_waitcnt lgkmcnt(11)
ds_read_b128 v[148:151], v164 offset:2048
ds_read_b128 v[152:155], v165 offset:0
ds_read_b128 v[156:159], v165 offset:2048
s_waitcnt lgkmcnt(0)
s_add_u32 m0, s84, 147456
s_nop 0
buffer_load_dwordx4 v160, s[36:39], s51 offen lds
buffer_load_dwordx4 v161, s[36:39], s51 offen offset:1024 lds
buffer_load_dwordx4 v162, s[36:39], s51 offen offset:2048 lds
buffer_load_dwordx4 v163, s[36:39], s51 offen offset:3072 lds
s_add_u32 s51, s51, 128
s_cmp_eq_u32 s51, 1280
s_cselect_b32 s51, 0, s51
s_add_u32 m0, s56, 0
s_nop 0
)"
R"(buffer_load_dwordx4 v176, s[20:23], s48 offen nt lds
ds_read_b64_tr_b16 v[212:213], v169 offset:36992
ds_read_b64_tr_b16 v[214:215], v169 offset:38528
buffer_load_dwordx4 v177, s[20:23], s48 offen offset:1024 nt lds
buffer_load_dwordx4 v178, s[20:23], s48 offen offset:2048 nt lds
s_add_u32 s48, s48, 12288
s_add_u32 m0, s56, 86016
s_nop 0
buffer_load_dwordx4 v179, s[24:27], s49 offen nt lds
buffer_load_dwordx4 v180, s[24:27], s49 offen offset:1024 nt lds
buffer_load_dwordx4 v181, s[24:27], s49 offen offset:2048 nt lds
s_add_u32 s49, s49, 12288
s_waitcnt lgkmcnt(0)
s_cmp_eq_u32 s73, -1
s_cbranch_scc0 LpeB4_1_1
LplB4_1_1:
)"
R"(s_memrealtime s[88:89]
s_waitcnt lgkmcnt(0)
s_sub_u32 s89, s88, s94
s_cmp_lt_i32 s89, 0
s_cbranch_scc1 LplB4_1_1
s_add_u32 s95, s95, 95
s_sub_u32 s89, s88, s95
s_add_u32 s88, s88, 92
s_cmp_gt_i32 s89, 755
s_cselect_b32 s68, 20, 0
s_add_u32 s88, s88, s68
s_sub_u32 s89, s88, s95
s_cmp_gt_i32 s89, 0
s_cselect_b32 s94, s88, s95
LpeB4_1_1:
s_waitcnt vmcnt(22)
)"
R"(s_barrier
s_add_u32 m0, s56, 12288
s_nop 0
buffer_load_dwordx4 v176, s[20:23], s48 offen nt lds
buffer_load_dwordx4 v177, s[20:23], s48 offen offset:1024 nt lds
buffer_load_dwordx4 v178, s[20:23], s48 offen offset:2048 nt lds
s_add_u32 s48, s48, 12288
v_mfma_f32_16x16x32_bf16 v[0:3], v[184:187], v[144:147], v[0:3]
v_mfma_f32_16x16x32_bf16 v[4:7], v[184:187], v[148:151], v[4:7]
v_mfma_f32_16x16x32_bf16 v[12:15], v[188:191], v[148:151], v[12:15]
ds_read_b64_tr_b16 v[184:185], v166 offset:37120
ds_read_b64_tr_b16 v[186:187], v166 offset:38656
v_mfma_f32_16x16x32_bf16 v[8:11], v[188:191], v[144:147], v[8:11]
v_mfma_f32_16x16x32_bf16 v[16:19], v[192:195], v[144:147], v[16:19]
ds_read_b64_tr_b16 v[188:189], v167 offset:37120
ds_read_b64_tr_b16 v[190:191], v167 offset:38656
)"
R"(v_mfma_f32_16x16x32_bf16 v[20:23], v[192:195], v[148:151], v[20:23]
v_mfma_f32_16x16x32_bf16 v[28:31], v[196:199], v[148:151], v[28:31]
ds_read_b64_tr_b16 v[192:193], v168 offset:37120
ds_read_b64_tr_b16 v[194:195], v168 offset:38656
v_mfma_f32_16x16x32_bf16 v[24:27], v[196:199], v[144:147], v[24:27]
v_mfma_f32_16x16x32_bf16 v[32:35], v[200:203], v[144:147], v[32:35]
ds_read_b64_tr_b16 v[196:197], v169 offset:37120
ds_read_b64_tr_b16 v[198:199], v169 offset:38656
v_mfma_f32_16x16x32_bf16 v[36:39], v[200:203], v[148:151], v[36:39]
v_mfma_f32_16x16x32_bf16 v[44:47], v[204:207], v[148:151], v[44:47]
ds_read_b64_tr_b16 v[200:201], v166 offset:0
ds_read_b64_tr_b16 v[202:203], v166 offset:1536
v_mfma_f32_16x16x32_bf16 v[40:43], v[204:207], v[144:147], v[40:43]
v_mfma_f32_16x16x32_bf16 v[48:51], v[208:211], v[144:147], v[48:51]
ds_read_b64_tr_b16 v[204:205], v167 offset:0
ds_read_b64_tr_b16 v[206:207], v167 offset:1536
)"
R"(v_mfma_f32_16x16x32_bf16 v[52:55], v[208:211], v[148:151], v[52:55]
v_mfma_f32_16x16x32_bf16 v[60:63], v[212:215], v[148:151], v[60:63]
ds_read_b64_tr_b16 v[208:209], v168 offset:0
ds_read_b64_tr_b16 v[210:211], v168 offset:1536
v_mfma_f32_16x16x32_bf16 v[56:59], v[212:215], v[144:147], v[56:59]
s_add_u32 m0, s56, 49152
s_nop 0
buffer_load_dwordx4 v179, s[24:27], s49 offen nt lds
ds_read_b64_tr_b16 v[212:213], v169 offset:0
s_waitcnt lgkmcnt(11)
ds_read_b64_tr_b16 v[214:215], v169 offset:1536
buffer_load_dwordx4 v180, s[24:27], s49 offen offset:1024 nt lds
buffer_load_dwordx4 v181, s[24:27], s49 offen offset:2048 nt lds
s_add_u32 s49, s49, 12288
s_waitcnt lgkmcnt(0)
s_cmp_eq_u32 s73, -1
)"
R"(s_cbranch_scc0 LpeB4_1_2
LplB4_1_2:
s_memrealtime s[88:89]
s_waitcnt lgkmcnt(0)
s_sub_u32 s89, s88, s94
s_cmp_lt_i32 s89, 0
s_cbranch_scc1 LplB4_1_2
s_add_u32 s95, s95, 95
s_sub_u32 s89, s88, s95
s_add_u32 s88, s88, 92
s_cmp_gt_i32 s89, 755
s_cselect_b32 s68, 20, 0
s_add_u32 s88, s88, s68
s_sub_u32 s89, s88, s95
s_cmp_gt_i32 s89, 0
s_cselect_b32 s94, s88, s95
)"
R"(LpeB4_1_2:
s_waitcnt vmcnt(19)
s_barrier
s_add_u32 m0, s56, 135168
s_nop 0
buffer_load_dwordx4 v252, s[28:31], s50 offen lds
buffer_load_dwordx4 v253, s[28:31], s50 offen offset:1024 lds
buffer_load_dwordx4 v254, s[28:31], s50 offen offset:2048 lds
s_add_u32 s50, s50, 393216
s_cmp_eq_u32 s50, s53
s_cselect_b32 s50, s52, s50
v_mfma_f32_16x16x32_bf16 v[64:67], v[184:187], v[144:147], v[64:67]
v_mfma_f32_16x16x32_bf16 v[68:71], v[184:187], v[148:151], v[68:71]
v_mfma_f32_16x16x32_bf16 v[76:79], v[188:191], v[148:151], v[76:79]
ds_read_b64_tr_b16 v[184:185], v166 offset:128
ds_read_b64_tr_b16 v[186:187], v166 offset:1664
)"
R"(v_mfma_f32_16x16x32_bf16 v[72:75], v[188:191], v[144:147], v[72:75]
v_mfma_f32_16x16x32_bf16 v[80:83], v[192:195], v[144:147], v[80:83]
ds_read_b64_tr_b16 v[188:189], v167 offset:128
ds_read_b64_tr_b16 v[190:191], v167 offset:1664
v_mfma_f32_16x16x32_bf16 v[84:87], v[192:195], v[148:151], v[84:87]
v_mfma_f32_16x16x32_bf16 v[92:95], v[196:199], v[148:151], v[92:95]
ds_read_b64_tr_b16 v[192:193], v168 offset:128
ds_read_b64_tr_b16 v[194:195], v168 offset:1664
v_mfma_f32_16x16x32_bf16 v[88:91], v[196:199], v[144:147], v[88:91]
v_mfma_f32_16x16x32_bf16 v[0:3], v[200:203], v[152:155], v[0:3]
ds_read_b64_tr_b16 v[196:197], v169 offset:128
ds_read_b64_tr_b16 v[198:199], v169 offset:1664
v_mfma_f32_16x16x32_bf16 v[4:7], v[200:203], v[156:159], v[4:7]
v_mfma_f32_16x16x32_bf16 v[12:15], v[204:207], v[156:159], v[12:15]
ds_read_b64_tr_b16 v[200:201], v166 offset:256
ds_read_b64_tr_b16 v[202:203], v166 offset:1792
)"
R"(v_mfma_f32_16x16x32_bf16 v[8:11], v[204:207], v[152:155], v[8:11]
v_mfma_f32_16x16x32_bf16 v[16:19], v[208:211], v[152:155], v[16:19]
ds_read_b64_tr_b16 v[204:205], v167 offset:256
ds_read_b64_tr_b16 v[206:207], v167 offset:1792
v_mfma_f32_16x16x32_bf16 v[20:23], v[208:211], v[156:159], v[20:23]
v_mfma_f32_16x16x32_bf16 v[28:31], v[212:215], v[156:159], v[28:31]
ds_read_b64_tr_b16 v[208:209], v168 offset:256
ds_read_b64_tr_b16 v[210:211], v168 offset:1792
v_mfma_f32_16x16x32_bf16 v[24:27], v[212:215], v[152:155], v[24:27]
s_add_u32 m0, s56, 24576
s_nop 0
buffer_load_dwordx4 v176, s[20:23], s48 offen nt lds
buffer_load_dwordx4 v177, s[20:23], s48 offen offset:1024 nt lds
buffer_load_dwordx4 v178, s[20:23], s48 offen offset:2048 nt lds
s_add_u32 s48, s48, 12288
ds_read_b64_tr_b16 v[212:213], v169 offset:256
)"
R"(s_waitcnt lgkmcnt(11)
ds_read_b64_tr_b16 v[214:215], v169 offset:1792
s_add_u32 m0, s56, 61440
s_nop 0
buffer_load_dwordx4 v179, s[24:27], s49 offen nt lds
buffer_load_dwordx4 v180, s[24:27], s49 offen offset:1024 nt lds
buffer_load_dwordx4 v181, s[24:27], s49 offen offset:2048 nt lds
s_add_u32 s49, s49, 12288
s_waitcnt lgkmcnt(0)
s_cmp_eq_u32 s73, -1
s_cbranch_scc0 LpeB4_1_3
LplB4_1_3:
s_memrealtime s[88:89]
s_waitcnt lgkmcnt(0)
s_sub_u32 s89, s88, s94
s_cmp_lt_i32 s89, 0
)"
R"(s_cbranch_scc1 LplB4_1_3
s_add_u32 s95, s95, 95
s_sub_u32 s89, s88, s95
s_add_u32 s88, s88, 92
s_cmp_gt_i32 s89, 755
s_cselect_b32 s68, 20, 0
s_add_u32 s88, s88, s68
s_sub_u32 s89, s88, s95
s_cmp_gt_i32 s89, 0
s_cselect_b32 s94, s88, s95
LpeB4_1_3:
s_waitcnt vmcnt(15)
s_barrier
s_add_u32 m0, s56, 98304
s_nop 0
buffer_load_dwordx4 v252, s[28:31], s50 offen lds
)"
R"(buffer_load_dwordx4 v253, s[28:31], s50 offen offset:1024 lds
buffer_load_dwordx4 v254, s[28:31], s50 offen offset:2048 lds
s_add_u32 s50, s50, 393216
s_cmp_eq_u32 s50, s53
s_cselect_b32 s50, s52, s50
v_mfma_f32_16x16x32_bf16 v[32:35], v[184:187], v[152:155], v[32:35]
v_mfma_f32_16x16x32_bf16 v[36:39], v[184:187], v[156:159], v[36:39]
v_mfma_f32_16x16x32_bf16 v[44:47], v[188:191], v[156:159], v[44:47]
ds_read_b64_tr_b16 v[184:185], v166 offset:12288
ds_read_b64_tr_b16 v[186:187], v166 offset:13824
v_mfma_f32_16x16x32_bf16 v[40:43], v[188:191], v[152:155], v[40:43]
v_mfma_f32_16x16x32_bf16 v[48:51], v[192:195], v[152:155], v[48:51]
ds_read_b64_tr_b16 v[188:189], v167 offset:12288
ds_read_b64_tr_b16 v[190:191], v167 offset:13824
v_mfma_f32_16x16x32_bf16 v[52:55], v[192:195], v[156:159], v[52:55]
v_mfma_f32_16x16x32_bf16 v[60:63], v[196:199], v[156:159], v[60:63]
)"
R"(ds_read_b64_tr_b16 v[192:193], v168 offset:12288
ds_read_b64_tr_b16 v[194:195], v168 offset:13824
v_mfma_f32_16x16x32_bf16 v[56:59], v[196:199], v[152:155], v[56:59]
v_mfma_f32_16x16x32_bf16 v[64:67], v[200:203], v[152:155], v[64:67]
ds_read_b64_tr_b16 v[196:197], v169 offset:12288
ds_read_b64_tr_b16 v[198:199], v169 offset:13824
v_mfma_f32_16x16x32_bf16 v[68:71], v[200:203], v[156:159], v[68:71]
v_mfma_f32_16x16x32_bf16 v[76:79], v[204:207], v[156:159], v[76:79]
ds_read_b64_tr_b16 v[200:201], v166 offset:12416
ds_read_b64_tr_b16 v[202:203], v166 offset:13952
v_mfma_f32_16x16x32_bf16 v[72:75], v[204:207], v[152:155], v[72:75]
v_mfma_f32_16x16x32_bf16 v[80:83], v[208:211], v[152:155], v[80:83]
ds_read_b64_tr_b16 v[204:205], v167 offset:12416
ds_read_b64_tr_b16 v[206:207], v167 offset:13952
v_mfma_f32_16x16x32_bf16 v[84:87], v[208:211], v[156:159], v[84:87]
v_mfma_f32_16x16x32_bf16 v[92:95], v[212:215], v[156:159], v[92:95]
)"
R"(ds_read_b64_tr_b16 v[208:209], v168 offset:12416
ds_read_b64_tr_b16 v[210:211], v168 offset:13952
v_mfma_f32_16x16x32_bf16 v[88:91], v[212:215], v[152:155], v[88:91]
ds_read_b128 v[144:147], v164 offset:0
s_waitcnt lgkmcnt(11)
ds_read_b128 v[148:151], v164 offset:2048
ds_read_b128 v[152:155], v165 offset:0
ds_read_b128 v[156:159], v165 offset:2048
s_waitcnt lgkmcnt(0)
s_add_u32 m0, s84, 147456
s_nop 0
buffer_load_dwordx4 v160, s[36:39], s51 offen lds
buffer_load_dwordx4 v161, s[36:39], s51 offen offset:1024 lds
buffer_load_dwordx4 v162, s[36:39], s51 offen offset:2048 lds
buffer_load_dwordx4 v163, s[36:39], s51 offen offset:3072 lds
s_add_u32 s51, s51, 128
)"
R"(s_cmp_eq_u32 s51, 1280
s_cselect_b32 s51, 0, s51
s_add_u32 m0, s56, 36864
s_nop 0
buffer_load_dwordx4 v176, s[20:23], s48 offen nt lds
ds_read_b64_tr_b16 v[212:213], v169 offset:12416
ds_read_b64_tr_b16 v[214:215], v169 offset:13952
buffer_load_dwordx4 v177, s[20:23], s48 offen offset:1024 nt lds
buffer_load_dwordx4 v178, s[20:23], s48 offen offset:2048 nt lds
s_add_u32 s48, s48, 12288
s_add_u32 m0, s56, 73728
s_nop 0
buffer_load_dwordx4 v179, s[24:27], s49 offen nt lds
buffer_load_dwordx4 v180, s[24:27], s49 offen offset:1024 nt lds
buffer_load_dwordx4 v181, s[24:27], s49 offen offset:2048 nt lds
s_add_u32 s49, s49, 12288
)"
R"(s_waitcnt lgkmcnt(0)
s_cmp_eq_u32 s73, -1
s_cbranch_scc0 LpeB5_1_0
LplB5_1_0:
s_memrealtime s[88:89]
s_waitcnt lgkmcnt(0)
s_sub_u32 s89, s88, s94
s_cmp_lt_i32 s89, 0
s_cbranch_scc1 LplB5_1_0
s_add_u32 s95, s95, 95
s_sub_u32 s89, s88, s95
s_add_u32 s88, s88, 92
s_cmp_gt_i32 s89, 755
s_cselect_b32 s68, 20, 0
s_add_u32 s88, s88, s68
s_sub_u32 s89, s88, s95
)"
R"(s_cmp_gt_i32 s89, 0
s_cselect_b32 s94, s88, s95
LpeB5_1_0:
s_waitcnt vmcnt(22)
s_barrier
s_add_u32 m0, s56, 0
s_nop 0
buffer_load_dwordx4 v176, s[20:23], s48 offen nt lds
buffer_load_dwordx4 v177, s[20:23], s48 offen offset:1024 nt lds
buffer_load_dwordx4 v178, s[20:23], s48 offen offset:2048 nt lds
s_add_u32 s48, s48, 12288
v_mfma_f32_16x16x32_bf16 v[0:3], v[184:187], v[144:147], v[0:3]
v_mfma_f32_16x16x32_bf16 v[4:7], v[184:187], v[148:151], v[4:7]
v_mfma_f32_16x16x32_bf16 v[12:15], v[188:191], v[148:151], v[12:15]
ds_read_b64_tr_b16 v[184:185], v166 offset:12544
ds_read_b64_tr_b16 v[186:187], v166 offset:14080
)"
R"(v_mfma_f32_16x16x32_bf16 v[8:11], v[188:191], v[144:147], v[8:11]
v_mfma_f32_16x16x32_bf16 v[16:19], v[192:195], v[144:147], v[16:19]
ds_read_b64_tr_b16 v[188:189], v167 offset:12544
ds_read_b64_tr_b16 v[190:191], v167 offset:14080
v_mfma_f32_16x16x32_bf16 v[20:23], v[192:195], v[148:151], v[20:23]
v_mfma_f32_16x16x32_bf16 v[28:31], v[196:199], v[148:151], v[28:31]
ds_read_b64_tr_b16 v[192:193], v168 offset:12544
ds_read_b64_tr_b16 v[194:195], v168 offset:14080
v_mfma_f32_16x16x32_bf16 v[24:27], v[196:199], v[144:147], v[24:27]
v_mfma_f32_16x16x32_bf16 v[32:35], v[200:203], v[144:147], v[32:35]
ds_read_b64_tr_b16 v[196:197], v169 offset:12544
ds_read_b64_tr_b16 v[198:199], v169 offset:14080
v_mfma_f32_16x16x32_bf16 v[36:39], v[200:203], v[148:151], v[36:39]
v_mfma_f32_16x16x32_bf16 v[44:47], v[204:207], v[148:151], v[44:47]
ds_read_b64_tr_b16 v[200:201], v166 offset:24576
ds_read_b64_tr_b16 v[202:203], v166 offset:26112
)"
R"(v_mfma_f32_16x16x32_bf16 v[40:43], v[204:207], v[144:147], v[40:43]
v_mfma_f32_16x16x32_bf16 v[48:51], v[208:211], v[144:147], v[48:51]
ds_read_b64_tr_b16 v[204:205], v167 offset:24576
ds_read_b64_tr_b16 v[206:207], v167 offset:26112
v_mfma_f32_16x16x32_bf16 v[52:55], v[208:211], v[148:151], v[52:55]
v_mfma_f32_16x16x32_bf16 v[60:63], v[212:215], v[148:151], v[60:63]
ds_read_b64_tr_b16 v[208:209], v168 offset:24576
ds_read_b64_tr_b16 v[210:211], v168 offset:26112
v_mfma_f32_16x16x32_bf16 v[56:59], v[212:215], v[144:147], v[56:59]
s_add_u32 m0, s56, 86016
s_nop 0
buffer_load_dwordx4 v179, s[24:27], s49 offen nt lds
ds_read_b64_tr_b16 v[212:213], v169 offset:24576
s_waitcnt lgkmcnt(11)
ds_read_b64_tr_b16 v[214:215], v169 offset:26112
buffer_load_dwordx4 v180, s[24:27], s49 offen offset:1024 nt lds
)"
R"(buffer_load_dwordx4 v181, s[24:27], s49 offen offset:2048 nt lds
s_add_u32 s49, s49, 12288
s_waitcnt lgkmcnt(0)
s_cmp_eq_u32 s73, -1
s_cbranch_scc0 LpeB5_1_1
LplB5_1_1:
s_memrealtime s[88:89]
s_waitcnt lgkmcnt(0)
s_sub_u32 s89, s88, s94
s_cmp_lt_i32 s89, 0
s_cbranch_scc1 LplB5_1_1
s_add_u32 s95, s95, 95
s_sub_u32 s89, s88, s95
s_add_u32 s88, s88, 92
s_cmp_gt_i32 s89, 755
s_cselect_b32 s68, 20, 0
)"
R"(s_add_u32 s88, s88, s68
s_sub_u32 s89, s88, s95
s_cmp_gt_i32 s89, 0
s_cselect_b32 s94, s88, s95
LpeB5_1_1:
s_waitcnt vmcnt(19)
s_barrier
s_add_u32 m0, s56, 110592
s_nop 0
buffer_load_dwordx4 v252, s[28:31], s50 offen lds
buffer_load_dwordx4 v253, s[28:31], s50 offen offset:1024 lds
buffer_load_dwordx4 v254, s[28:31], s50 offen offset:2048 lds
s_add_u32 s50, s50, 393216
s_cmp_eq_u32 s50, s53
s_cselect_b32 s50, s52, s50
v_mfma_f32_16x16x32_bf16 v[64:67], v[184:187], v[144:147], v[64:67]
)"
R"(v_mfma_f32_16x16x32_bf16 v[68:71], v[184:187], v[148:151], v[68:71]
v_mfma_f32_16x16x32_bf16 v[76:79], v[188:191], v[148:151], v[76:79]
ds_read_b64_tr_b16 v[184:185], v166 offset:24704
ds_read_b64_tr_b16 v[186:187], v166 offset:26240
v_mfma_f32_16x16x32_bf16 v[72:75], v[188:191], v[144:147], v[72:75]
v_mfma_f32_16x16x32_bf16 v[80:83], v[192:195], v[144:147], v[80:83]
ds_read_b64_tr_b16 v[188:189], v167 offset:24704
ds_read_b64_tr_b16 v[190:191], v167 offset:26240
v_mfma_f32_16x16x32_bf16 v[84:87], v[192:195], v[148:151], v[84:87]
v_mfma_f32_16x16x32_bf16 v[92:95], v[196:199], v[148:151], v[92:95]
ds_read_b64_tr_b16 v[192:193], v168 offset:24704
ds_read_b64_tr_b16 v[194:195], v168 offset:26240
v_mfma_f32_16x16x32_bf16 v[88:91], v[196:199], v[144:147], v[88:91]
v_mfma_f32_16x16x32_bf16 v[0:3], v[200:203], v[152:155], v[0:3]
ds_read_b64_tr_b16 v[196:197], v169 offset:24704
ds_read_b64_tr_b16 v[198:199], v169 offset:26240
)"
R"(v_mfma_f32_16x16x32_bf16 v[4:7], v[200:203], v[156:159], v[4:7]
v_mfma_f32_16x16x32_bf16 v[12:15], v[204:207], v[156:159], v[12:15]
ds_read_b64_tr_b16 v[200:201], v166 offset:24832
ds_read_b64_tr_b16 v[202:203], v166 offset:26368
v_mfma_f32_16x16x32_bf16 v[8:11], v[204:207], v[152:155], v[8:11]
v_mfma_f32_16x16x32_bf16 v[16:19], v[208:211], v[152:155], v[16:19]
ds_read_b64_tr_b16 v[204:205], v167 offset:24832
ds_read_b64_tr_b16 v[206:207], v167 offset:26368
v_mfma_f32_16x16x32_bf16 v[20:23], v[208:211], v[156:159], v[20:23]
v_mfma_f32_16x16x32_bf16 v[28:31], v[212:215], v[156:159], v[28:31]
ds_read_b64_tr_b16 v[208:209], v168 offset:24832
ds_read_b64_tr_b16 v[210:211], v168 offset:26368
v_mfma_f32_16x16x32_bf16 v[24:27], v[212:215], v[152:155], v[24:27]
s_add_u32 m0, s56, 12288
s_nop 0
buffer_load_dwordx4 v176, s[20:23], s48 offen nt lds
)"
R"(buffer_load_dwordx4 v177, s[20:23], s48 offen offset:1024 nt lds
buffer_load_dwordx4 v178, s[20:23], s48 offen offset:2048 nt lds
s_add_u32 s48, s48, 12288
ds_read_b64_tr_b16 v[212:213], v169 offset:24832
s_waitcnt lgkmcnt(11)
ds_read_b64_tr_b16 v[214:215], v169 offset:26368
s_add_u32 m0, s56, 49152
s_nop 0
buffer_load_dwordx4 v179, s[24:27], s49 offen nt lds
buffer_load_dwordx4 v180, s[24:27], s49 offen offset:1024 nt lds
buffer_load_dwordx4 v181, s[24:27], s49 offen offset:2048 nt lds
s_add_u32 s49, s49, 12288
s_waitcnt lgkmcnt(0)
s_cmp_eq_u32 s73, -1
s_cbranch_scc0 LpeB5_1_2
LplB5_1_2:
)"
R"(s_memrealtime s[88:89]
s_waitcnt lgkmcnt(0)
s_sub_u32 s89, s88, s94
s_cmp_lt_i32 s89, 0
s_cbranch_scc1 LplB5_1_2
s_add_u32 s95, s95, 95
s_sub_u32 s89, s88, s95
s_add_u32 s88, s88, 92
s_cmp_gt_i32 s89, 755
s_cselect_b32 s68, 20, 0
s_add_u32 s88, s88, s68
s_sub_u32 s89, s88, s95
s_cmp_gt_i32 s89, 0
s_cselect_b32 s94, s88, s95
LpeB5_1_2:
s_waitcnt vmcnt(15)
)"
R"(s_barrier
s_add_u32 m0, s56, 122880
s_nop 0
buffer_load_dwordx4 v252, s[28:31], s50 offen lds
buffer_load_dwordx4 v253, s[28:31], s50 offen offset:1024 lds
buffer_load_dwordx4 v254, s[28:31], s50 offen offset:2048 lds
s_add_u32 s50, s50, 393216
s_cmp_eq_u32 s50, s53
s_cselect_b32 s50, s52, s50
v_mfma_f32_16x16x32_bf16 v[32:35], v[184:187], v[152:155], v[32:35]
v_mfma_f32_16x16x32_bf16 v[36:39], v[184:187], v[156:159], v[36:39]
v_mfma_f32_16x16x32_bf16 v[44:47], v[188:191], v[156:159], v[44:47]
ds_read_b64_tr_b16 v[184:185], v166 offset:36864
ds_read_b64_tr_b16 v[186:187], v166 offset:38400
v_mfma_f32_16x16x32_bf16 v[40:43], v[188:191], v[152:155], v[40:43]
v_mfma_f32_16x16x32_bf16 v[48:51], v[192:195], v[152:155], v[48:51]
)"
R"(ds_read_b64_tr_b16 v[188:189], v167 offset:36864
ds_read_b64_tr_b16 v[190:191], v167 offset:38400
v_mfma_f32_16x16x32_bf16 v[52:55], v[192:195], v[156:159], v[52:55]
v_mfma_f32_16x16x32_bf16 v[60:63], v[196:199], v[156:159], v[60:63]
ds_read_b64_tr_b16 v[192:193], v168 offset:36864
ds_read_b64_tr_b16 v[194:195], v168 offset:38400
v_mfma_f32_16x16x32_bf16 v[56:59], v[196:199], v[152:155], v[56:59]
v_mfma_f32_16x16x32_bf16 v[64:67], v[200:203], v[152:155], v[64:67]
ds_read_b64_tr_b16 v[196:197], v169 offset:36864
ds_read_b64_tr_b16 v[198:199], v169 offset:38400
v_mfma_f32_16x16x32_bf16 v[68:71], v[200:203], v[156:159], v[68:71]
v_mfma_f32_16x16x32_bf16 v[76:79], v[204:207], v[156:159], v[76:79]
ds_read_b64_tr_b16 v[200:201], v166 offset:36992
ds_read_b64_tr_b16 v[202:203], v166 offset:38528
v_mfma_f32_16x16x32_bf16 v[72:75], v[204:207], v[152:155], v[72:75]
v_mfma_f32_16x16x32_bf16 v[80:83], v[208:211], v[152:155], v[80:83]
)"
R"(ds_read_b64_tr_b16 v[204:205], v167 offset:36992
ds_read_b64_tr_b16 v[206:207], v167 offset:38528
v_mfma_f32_16x16x32_bf16 v[84:87], v[208:211], v[156:159], v[84:87]
v_mfma_f32_16x16x32_bf16 v[92:95], v[212:215], v[156:159], v[92:95]
ds_read_b64_tr_b16 v[208:209], v168 offset:36992
ds_read_b64_tr_b16 v[210:211], v168 offset:38528
v_mfma_f32_16x16x32_bf16 v[88:91], v[212:215], v[152:155], v[88:91]
ds_read_b128 v[144:147], v164 offset:0
s_waitcnt lgkmcnt(11)
ds_read_b128 v[148:151], v164 offset:2048
ds_read_b128 v[152:155], v165 offset:0
ds_read_b128 v[156:159], v165 offset:2048
s_waitcnt lgkmcnt(0)
s_add_u32 m0, s84, 147456
s_nop 0
buffer_load_dwordx4 v160, s[36:39], s51 offen lds
)"
R"(buffer_load_dwordx4 v161, s[36:39], s51 offen offset:1024 lds
buffer_load_dwordx4 v162, s[36:39], s51 offen offset:2048 lds
buffer_load_dwordx4 v163, s[36:39], s51 offen offset:3072 lds
s_add_u32 s51, s51, 128
s_cmp_eq_u32 s51, 1280
s_cselect_b32 s51, 0, s51
s_add_u32 m0, s56, 24576
s_nop 0
buffer_load_dwordx4 v176, s[20:23], s48 offen nt lds
ds_read_b64_tr_b16 v[212:213], v169 offset:36992
ds_read_b64_tr_b16 v[214:215], v169 offset:38528
buffer_load_dwordx4 v177, s[20:23], s48 offen offset:1024 nt lds
buffer_load_dwordx4 v178, s[20:23], s48 offen offset:2048 nt lds
s_add_u32 s48, s48, 12288
s_add_u32 m0, s56, 61440
s_nop 0
)"
R"(buffer_load_dwordx4 v179, s[24:27], s49 offen nt lds
buffer_load_dwordx4 v180, s[24:27], s49 offen offset:1024 nt lds
buffer_load_dwordx4 v181, s[24:27], s49 offen offset:2048 nt lds
s_add_u32 s49, s49, 12288
s_waitcnt lgkmcnt(0)
s_cmp_eq_u32 s73, -1
s_cbranch_scc0 LpeB5_1_3
LplB5_1_3:
s_memrealtime s[88:89]
s_waitcnt lgkmcnt(0)
s_sub_u32 s89, s88, s94
s_cmp_lt_i32 s89, 0
s_cbranch_scc1 LplB5_1_3
s_add_u32 s95, s95, 95
s_sub_u32 s89, s88, s95
s_add_u32 s88, s88, 92
)"
R"(s_cmp_gt_i32 s89, 755
s_cselect_b32 s68, 20, 0
s_add_u32 s88, s88, s68
s_sub_u32 s89, s88, s95
s_cmp_gt_i32 s89, 0
s_cselect_b32 s94, s88, s95
LpeB5_1_3:
s_waitcnt vmcnt(22)
s_barrier
s_add_u32 m0, s56, 36864
s_nop 0
buffer_load_dwordx4 v176, s[20:23], s48 offen nt lds
buffer_load_dwordx4 v177, s[20:23], s48 offen offset:1024 nt lds
buffer_load_dwordx4 v178, s[20:23], s48 offen offset:2048 nt lds
s_add_u32 s48, s48, 12288
v_mfma_f32_16x16x32_bf16 v[0:3], v[184:187], v[144:147], v[0:3]
)"
R"(v_mfma_f32_16x16x32_bf16 v[4:7], v[184:187], v[148:151], v[4:7]
v_mfma_f32_16x16x32_bf16 v[12:15], v[188:191], v[148:151], v[12:15]
ds_read_b64_tr_b16 v[184:185], v166 offset:37120
ds_read_b64_tr_b16 v[186:187], v166 offset:38656
v_mfma_f32_16x16x32_bf16 v[8:11], v[188:191], v[144:147], v[8:11]
v_mfma_f32_16x16x32_bf16 v[16:19], v[192:195], v[144:147], v[16:19]
ds_read_b64_tr_b16 v[188:189], v167 offset:37120
ds_read_b64_tr_b16 v[190:191], v167 offset:38656
v_mfma_f32_16x16x32_bf16 v[20:23], v[192:195], v[148:151], v[20:23]
v_mfma_f32_16x16x32_bf16 v[28:31], v[196:199], v[148:151], v[28:31]
ds_read_b64_tr_b16 v[192:193], v168 offset:37120
ds_read_b64_tr_b16 v[194:195], v168 offset:38656
v_mfma_f32_16x16x32_bf16 v[24:27], v[196:199], v[144:147], v[24:27]
v_mfma_f32_16x16x32_bf16 v[32:35], v[200:203], v[144:147], v[32:35]
ds_read_b64_tr_b16 v[196:197], v169 offset:37120
ds_read_b64_tr_b16 v[198:199], v169 offset:38656
)"
R"(v_mfma_f32_16x16x32_bf16 v[36:39], v[200:203], v[148:151], v[36:39]
v_mfma_f32_16x16x32_bf16 v[44:47], v[204:207], v[148:151], v[44:47]
ds_read_b64_tr_b16 v[200:201], v166 offset:0
ds_read_b64_tr_b16 v[202:203], v166 offset:1536
v_mfma_f32_16x16x32_bf16 v[40:43], v[204:207], v[144:147], v[40:43]
v_mfma_f32_16x16x32_bf16 v[48:51], v[208:211], v[144:147], v[48:51]
ds_read_b64_tr_b16 v[204:205], v167 offset:0
ds_read_b64_tr_b16 v[206:207], v167 offset:1536
v_mfma_f32_16x16x32_bf16 v[52:55], v[208:211], v[148:151], v[52:55]
v_mfma_f32_16x16x32_bf16 v[60:63], v[212:215], v[148:151], v[60:63]
ds_read_b64_tr_b16 v[208:209], v168 offset:0
ds_read_b64_tr_b16 v[210:211], v168 offset:1536
v_mfma_f32_16x16x32_bf16 v[56:59], v[212:215], v[144:147], v[56:59]
s_add_u32 m0, s56, 73728
s_nop 0
buffer_load_dwordx4 v179, s[24:27], s49 offen nt lds
)"
R"(ds_read_b64_tr_b16 v[212:213], v169 offset:0
s_waitcnt lgkmcnt(11)
ds_read_b64_tr_b16 v[214:215], v169 offset:1536
buffer_load_dwordx4 v180, s[24:27], s49 offen offset:1024 nt lds
buffer_load_dwordx4 v181, s[24:27], s49 offen offset:2048 nt lds
s_add_u32 s49, s49, 12288
s_waitcnt lgkmcnt(0)
s_cmp_eq_u32 s73, -1
s_cbranch_scc0 LpeB6_1_0
LplB6_1_0:
s_memrealtime s[88:89]
s_waitcnt lgkmcnt(0)
s_sub_u32 s89, s88, s94
s_cmp_lt_i32 s89, 0
s_cbranch_scc1 LplB6_1_0
s_add_u32 s95, s95, 95
)"
R"(s_sub_u32 s89, s88, s95
s_add_u32 s88, s88, 92
s_cmp_gt_i32 s89, 755
s_cselect_b32 s68, 20, 0
s_add_u32 s88, s88, s68
s_sub_u32 s89, s88, s95
s_cmp_gt_i32 s89, 0
s_cselect_b32 s94, s88, s95
LpeB6_1_0:
s_waitcnt vmcnt(19)
s_barrier
s_add_u32 m0, s56, 135168
s_nop 0
buffer_load_dwordx4 v252, s[28:31], s50 offen lds
buffer_load_dwordx4 v253, s[28:31], s50 offen offset:1024 lds
buffer_load_dwordx4 v254, s[28:31], s50 offen offset:2048 lds
)"
R"(s_add_u32 s50, s50, 393216
s_cmp_eq_u32 s50, s53
s_cselect_b32 s50, s52, s50
v_mfma_f32_16x16x32_bf16 v[64:67], v[184:187], v[144:147], v[64:67]
v_mfma_f32_16x16x32_bf16 v[68:71], v[184:187], v[148:151], v[68:71]
v_mfma_f32_16x16x32_bf16 v[76:79], v[188:191], v[148:151], v[76:79]
ds_read_b64_tr_b16 v[184:185], v166 offset:128
ds_read_b64_tr_b16 v[186:187], v166 offset:1664
v_mfma_f32_16x16x32_bf16 v[72:75], v[188:191], v[144:147], v[72:75]
v_mfma_f32_16x16x32_bf16 v[80:83], v[192:195], v[144:147], v[80:83]
ds_read_b64_tr_b16 v[188:189], v167 offset:128
ds_read_b64_tr_b16 v[190:191], v167 offset:1664
v_mfma_f32_16x16x32_bf16 v[84:87], v[192:195], v[148:151], v[84:87]
v_mfma_f32_16x16x32_bf16 v[92:95], v[196:199], v[148:151], v[92:95]
ds_read_b64_tr_b16 v[192:193], v168 offset:128
ds_read_b64_tr_b16 v[194:195], v168 offset:1664
)"
R"(v_mfma_f32_16x16x32_bf16 v[88:91], v[196:199], v[144:147], v[88:91]
v_mfma_f32_16x16x32_bf16 v[0:3], v[200:203], v[152:155], v[0:3]
ds_read_b64_tr_b16 v[196:197], v169 offset:128
ds_read_b64_tr_b16 v[198:199], v169 offset:1664
v_mfma_f32_16x16x32_bf16 v[4:7], v[200:203], v[156:159], v[4:7]
v_mfma_f32_16x16x32_bf16 v[12:15], v[204:207], v[156:159], v[12:15]
ds_read_b64_tr_b16 v[200:201], v166 offset:256
ds_read_b64_tr_b16 v[202:203], v166 offset:1792
v_mfma_f32_16x16x32_bf16 v[8:11], v[204:207], v[152:155], v[8:11]
v_mfma_f32_16x16x32_bf16 v[16:19], v[208:211], v[152:155], v[16:19]
ds_read_b64_tr_b16 v[204:205], v167 offset:256
ds_read_b64_tr_b16 v[206:207], v167 offset:1792
v_mfma_f32_16x16x32_bf16 v[20:23], v[208:211], v[156:159], v[20:23]
v_mfma_f32_16x16x32_bf16 v[28:31], v[212:215], v[156:159], v[28:31]
ds_read_b64_tr_b16 v[208:209], v168 offset:256
ds_read_b64_tr_b16 v[210:211], v168 offset:1792
)"
R"(v_mfma_f32_16x16x32_bf16 v[24:27], v[212:215], v[152:155], v[24:27]
s_add_u32 m0, s56, 0
s_nop 0
buffer_load_dwordx4 v176, s[20:23], s48 offen nt lds
buffer_load_dwordx4 v177, s[20:23], s48 offen offset:1024 nt lds
buffer_load_dwordx4 v178, s[20:23], s48 offen offset:2048 nt lds
s_add_u32 s48, s48, 12288
ds_read_b64_tr_b16 v[212:213], v169 offset:256
s_waitcnt lgkmcnt(11)
ds_read_b64_tr_b16 v[214:215], v169 offset:1792
s_add_u32 m0, s56, 86016
s_nop 0
buffer_load_dwordx4 v179, s[24:27], s49 offen nt lds
buffer_load_dwordx4 v180, s[24:27], s49 offen offset:1024 nt lds
buffer_load_dwordx4 v181, s[24:27], s49 offen offset:2048 nt lds
s_add_u32 s49, s49, 12288
)"
R"(s_waitcnt lgkmcnt(0)
s_cmp_eq_u32 s73, -1
s_cbranch_scc0 LpeB6_1_1
LplB6_1_1:
s_memrealtime s[88:89]
s_waitcnt lgkmcnt(0)
s_sub_u32 s89, s88, s94
s_cmp_lt_i32 s89, 0
s_cbranch_scc1 LplB6_1_1
s_add_u32 s95, s95, 95
s_sub_u32 s89, s88, s95
s_add_u32 s88, s88, 92
s_cmp_gt_i32 s89, 755
s_cselect_b32 s68, 20, 0
s_add_u32 s88, s88, s68
s_sub_u32 s89, s88, s95
)"
R"(s_cmp_gt_i32 s89, 0
s_cselect_b32 s94, s88, s95
LpeB6_1_1:
s_waitcnt vmcnt(15)
s_barrier
s_add_u32 m0, s56, 98304
s_nop 0
buffer_load_dwordx4 v252, s[28:31], s50 offen lds
buffer_load_dwordx4 v253, s[28:31], s50 offen offset:1024 lds
buffer_load_dwordx4 v254, s[28:31], s50 offen offset:2048 lds
s_add_u32 s50, s50, 393216
s_cmp_eq_u32 s50, s53
s_cselect_b32 s50, s52, s50
v_mfma_f32_16x16x32_bf16 v[32:35], v[184:187], v[152:155], v[32:35]
v_mfma_f32_16x16x32_bf16 v[36:39], v[184:187], v[156:159], v[36:39]
v_mfma_f32_16x16x32_bf16 v[44:47], v[188:191], v[156:159], v[44:47]
)"
R"(ds_read_b64_tr_b16 v[184:185], v166 offset:12288
ds_read_b64_tr_b16 v[186:187], v166 offset:13824
v_mfma_f32_16x16x32_bf16 v[40:43], v[188:191], v[152:155], v[40:43]
v_mfma_f32_16x16x32_bf16 v[48:51], v[192:195], v[152:155], v[48:51]
ds_read_b64_tr_b16 v[188:189], v167 offset:12288
ds_read_b64_tr_b16 v[190:191], v167 offset:13824
v_mfma_f32_16x16x32_bf16 v[52:55], v[192:195], v[156:159], v[52:55]
v_mfma_f32_16x16x32_bf16 v[60:63], v[196:199], v[156:159], v[60:63]
ds_read_b64_tr_b16 v[192:193], v168 offset:12288
ds_read_b64_tr_b16 v[194:195], v168 offset:13824
v_mfma_f32_16x16x32_bf16 v[56:59], v[196:199], v[152:155], v[56:59]
v_mfma_f32_16x16x32_bf16 v[64:67], v[200:203], v[152:155], v[64:67]
ds_read_b64_tr_b16 v[196:197], v169 offset:12288
ds_read_b64_tr_b16 v[198:199], v169 offset:13824
v_mfma_f32_16x16x32_bf16 v[68:71], v[200:203], v[156:159], v[68:71]
v_mfma_f32_16x16x32_bf16 v[76:79], v[204:207], v[156:159], v[76:79]
)"
R"(ds_read_b64_tr_b16 v[200:201], v166 offset:12416
ds_read_b64_tr_b16 v[202:203], v166 offset:13952
v_mfma_f32_16x16x32_bf16 v[72:75], v[204:207], v[152:155], v[72:75]
v_mfma_f32_16x16x32_bf16 v[80:83], v[208:211], v[152:155], v[80:83]
ds_read_b64_tr_b16 v[204:205], v167 offset:12416
ds_read_b64_tr_b16 v[206:207], v167 offset:13952
v_mfma_f32_16x16x32_bf16 v[84:87], v[208:211], v[156:159], v[84:87]
v_mfma_f32_16x16x32_bf16 v[92:95], v[212:215], v[156:159], v[92:95]
ds_read_b64_tr_b16 v[208:209], v168 offset:12416
ds_read_b64_tr_b16 v[210:211], v168 offset:13952
v_mfma_f32_16x16x32_bf16 v[88:91], v[212:215], v[152:155], v[88:91]
ds_read_b128 v[144:147], v164 offset:0
s_waitcnt lgkmcnt(11)
ds_read_b128 v[148:151], v164 offset:2048
ds_read_b128 v[152:155], v165 offset:0
ds_read_b128 v[156:159], v165 offset:2048
)"
R"(s_waitcnt lgkmcnt(0)
s_add_u32 m0, s84, 147456
s_nop 0
buffer_load_dwordx4 v160, s[36:39], s51 offen lds
buffer_load_dwordx4 v161, s[36:39], s51 offen offset:1024 lds
buffer_load_dwordx4 v162, s[36:39], s51 offen offset:2048 lds
buffer_load_dwordx4 v163, s[36:39], s51 offen offset:3072 lds
s_add_u32 s51, s51, 128
s_cmp_eq_u32 s51, 1280
s_cselect_b32 s51, 0, s51
s_add_u32 m0, s56, 12288
s_nop 0
buffer_load_dwordx4 v176, s[20:23], s48 offen nt lds
ds_read_b64_tr_b16 v[212:213], v169 offset:12416
ds_read_b64_tr_b16 v[214:215], v169 offset:13952
buffer_load_dwordx4 v177, s[20:23], s48 offen offset:1024 nt lds
)"
R"(buffer_load_dwordx4 v178, s[20:23], s48 offen offset:2048 nt lds
s_add_u32 s48, s48, 12288
s_add_u32 m0, s56, 49152
s_nop 0
buffer_load_dwordx4 v179, s[24:27], s49 offen nt lds
buffer_load_dwordx4 v180, s[24:27], s49 offen offset:1024 nt lds
buffer_load_dwordx4 v181, s[24:27], s49 offen offset:2048 nt lds
s_add_u32 s49, s49, 12288
s_waitcnt lgkmcnt(0)
s_cmp_eq_u32 s73, -1
s_cbranch_scc0 LpeB6_1_2
LplB6_1_2:
s_memrealtime s[88:89]
s_waitcnt lgkmcnt(0)
s_sub_u32 s89, s88, s94
s_cmp_lt_i32 s89, 0
)"
R"(s_cbranch_scc1 LplB6_1_2
s_add_u32 s95, s95, 95
s_sub_u32 s89, s88, s95
s_add_u32 s88, s88, 92
s_cmp_gt_i32 s89, 755
s_cselect_b32 s68, 20, 0
s_add_u32 s88, s88, s68
s_sub_u32 s89, s88, s95
s_cmp_gt_i32 s89, 0
s_cselect_b32 s94, s88, s95
LpeB6_1_2:
s_waitcnt vmcnt(22)
s_barrier
s_add_u32 m0, s56, 24576
s_nop 0
buffer_load_dwordx4 v176, s[20:23], s48 offen nt lds
)"
R"(buffer_load_dwordx4 v177, s[20:23], s48 offen offset:1024 nt lds
buffer_load_dwordx4 v178, s[20:23], s48 offen offset:2048 nt lds
s_add_u32 s48, s48, 12288
v_mfma_f32_16x16x32_bf16 v[0:3], v[184:187], v[144:147], v[0:3]
v_mfma_f32_16x16x32_bf16 v[4:7], v[184:187], v[148:151], v[4:7]
v_mfma_f32_16x16x32_bf16 v[12:15], v[188:191], v[148:151], v[12:15]
ds_read_b64_tr_b16 v[184:185], v166 offset:12544
ds_read_b64_tr_b16 v[186:187], v166 offset:14080
v_mfma_f32_16x16x32_bf16 v[8:11], v[188:191], v[144:147], v[8:11]
v_mfma_f32_16x16x32_bf16 v[16:19], v[192:195], v[144:147], v[16:19]
ds_read_b64_tr_b16 v[188:189], v167 offset:12544
ds_read_b64_tr_b16 v[190:191], v167 offset:14080
v_mfma_f32_16x16x32_bf16 v[20:23], v[192:195], v[148:151], v[20:23]
v_mfma_f32_16x16x32_bf16 v[28:31], v[196:199], v[148:151], v[28:31]
ds_read_b64_tr_b16 v[192:193], v168 offset:12544
ds_read_b64_tr_b16 v[194:195], v168 offset:14080
)"
R"(v_mfma_f32_16x16x32_bf16 v[24:27], v[196:199], v[144:147], v[24:27]
v_mfma_f32_16x16x32_bf16 v[32:35], v[200:203], v[144:147], v[32:35]
ds_read_b64_tr_b16 v[196:197], v169 offset:12544
ds_read_b64_tr_b16 v[198:199], v169 offset:14080
v_mfma_f32_16x16x32_bf16 v[36:39], v[200:203], v[148:151], v[36:39]
v_mfma_f32_16x16x32_bf16 v[44:47], v[204:207], v[148:151], v[44:47]
ds_read_b64_tr_b16 v[200:201], v166 offset:24576
ds_read_b64_tr_b16 v[202:203], v166 offset:26112
v_mfma_f32_16x16x32_bf16 v[40:43], v[204:207], v[144:147], v[40:43]
v_mfma_f32_16x16x32_bf16 v[48:51], v[208:211], v[144:147], v[48:51]
ds_read_b64_tr_b16 v[204:205], v167 offset:24576
ds_read_b64_tr_b16 v[206:207], v167 offset:26112
v_mfma_f32_16x16x32_bf16 v[52:55], v[208:211], v[148:151], v[52:55]
v_mfma_f32_16x16x32_bf16 v[60:63], v[212:215], v[148:151], v[60:63]
ds_read_b64_tr_b16 v[208:209], v168 offset:24576
ds_read_b64_tr_b16 v[210:211], v168 offset:26112
)"
R"(v_mfma_f32_16x16x32_bf16 v[56:59], v[212:215], v[144:147], v[56:59]
s_add_u32 m0, s56, 61440
s_nop 0
buffer_load_dwordx4 v179, s[24:27], s49 offen nt lds
ds_read_b64_tr_b16 v[212:213], v169 offset:24576
s_waitcnt lgkmcnt(11)
ds_read_b64_tr_b16 v[214:215], v169 offset:26112
buffer_load_dwordx4 v180, s[24:27], s49 offen offset:1024 nt lds
buffer_load_dwordx4 v181, s[24:27], s49 offen offset:2048 nt lds
s_add_u32 s49, s49, 12288
s_waitcnt lgkmcnt(0)
s_cmp_eq_u32 s73, -1
s_cbranch_scc0 LpeB6_1_3
LplB6_1_3:
s_memrealtime s[88:89]
s_waitcnt lgkmcnt(0)
)"
R"(s_sub_u32 s89, s88, s94
s_cmp_lt_i32 s89, 0
s_cbranch_scc1 LplB6_1_3
s_add_u32 s95, s95, 95
s_sub_u32 s89, s88, s95
s_add_u32 s88, s88, 92
s_cmp_gt_i32 s89, 755
s_cselect_b32 s68, 20, 0
s_add_u32 s88, s88, s68
s_sub_u32 s89, s88, s95
s_cmp_gt_i32 s89, 0
s_cselect_b32 s94, s88, s95
LpeB6_1_3:
s_waitcnt vmcnt(19)
s_barrier
s_add_u32 m0, s56, 110592
)"
R"(s_nop 0
buffer_load_dwordx4 v252, s[28:31], s50 offen lds
buffer_load_dwordx4 v253, s[28:31], s50 offen offset:1024 lds
buffer_load_dwordx4 v254, s[28:31], s50 offen offset:2048 lds
s_add_u32 s50, s50, 393216
s_cmp_eq_u32 s50, s53
s_cselect_b32 s50, s52, s50
v_mfma_f32_16x16x32_bf16 v[64:67], v[184:187], v[144:147], v[64:67]
v_mfma_f32_16x16x32_bf16 v[68:71], v[184:187], v[148:151], v[68:71]
v_mfma_f32_16x16x32_bf16 v[76:79], v[188:191], v[148:151], v[76:79]
ds_read_b64_tr_b16 v[184:185], v166 offset:24704
ds_read_b64_tr_b16 v[186:187], v166 offset:26240
v_mfma_f32_16x16x32_bf16 v[72:75], v[188:191], v[144:147], v[72:75]
v_mfma_f32_16x16x32_bf16 v[80:83], v[192:195], v[144:147], v[80:83]
ds_read_b64_tr_b16 v[188:189], v167 offset:24704
ds_read_b64_tr_b16 v[190:191], v167 offset:26240
)"
R"(v_mfma_f32_16x16x32_bf16 v[84:87], v[192:195], v[148:151], v[84:87]
v_mfma_f32_16x16x32_bf16 v[92:95], v[196:199], v[148:151], v[92:95]
ds_read_b64_tr_b16 v[192:193], v168 offset:24704
ds_read_b64_tr_b16 v[194:195], v168 offset:26240
v_mfma_f32_16x16x32_bf16 v[88:91], v[196:199], v[144:147], v[88:91]
v_mfma_f32_16x16x32_bf16 v[0:3], v[200:203], v[152:155], v[0:3]
ds_read_b64_tr_b16 v[196:197], v169 offset:24704
ds_read_b64_tr_b16 v[198:199], v169 offset:26240
v_mfma_f32_16x16x32_bf16 v[4:7], v[200:203], v[156:159], v[4:7]
v_mfma_f32_16x16x32_bf16 v[12:15], v[204:207], v[156:159], v[12:15]
ds_read_b64_tr_b16 v[200:201], v166 offset:24832
ds_read_b64_tr_b16 v[202:203], v166 offset:26368
v_mfma_f32_16x16x32_bf16 v[8:11], v[204:207], v[152:155], v[8:11]
v_mfma_f32_16x16x32_bf16 v[16:19], v[208:211], v[152:155], v[16:19]
ds_read_b64_tr_b16 v[204:205], v167 offset:24832
ds_read_b64_tr_b16 v[206:207], v167 offset:26368
)"
R"(v_mfma_f32_16x16x32_bf16 v[20:23], v[208:211], v[156:159], v[20:23]
v_mfma_f32_16x16x32_bf16 v[28:31], v[212:215], v[156:159], v[28:31]
ds_read_b64_tr_b16 v[208:209], v168 offset:24832
ds_read_b64_tr_b16 v[210:211], v168 offset:26368
v_mfma_f32_16x16x32_bf16 v[24:27], v[212:215], v[152:155], v[24:27]
s_add_u32 m0, s56, 36864
s_nop 0
buffer_load_dwordx4 v176, s[20:23], s48 offen nt lds
buffer_load_dwordx4 v177, s[20:23], s48 offen offset:1024 nt lds
buffer_load_dwordx4 v178, s[20:23], s48 offen offset:2048 nt lds
s_add_u32 s48, s48, 12288
ds_read_b64_tr_b16 v[212:213], v169 offset:24832
s_waitcnt lgkmcnt(11)
ds_read_b64_tr_b16 v[214:215], v169 offset:26368
s_add_u32 m0, s56, 73728
s_nop 0
)"
R"(buffer_load_dwordx4 v179, s[24:27], s49 offen nt lds
buffer_load_dwordx4 v180, s[24:27], s49 offen offset:1024 nt lds
buffer_load_dwordx4 v181, s[24:27], s49 offen offset:2048 nt lds
s_add_u32 s49, s49, 12288
s_waitcnt lgkmcnt(0)
s_cmp_eq_u32 s73, -1
s_cbranch_scc0 LpeB7_1_0
LplB7_1_0:
s_memrealtime s[88:89]
s_waitcnt lgkmcnt(0)
s_sub_u32 s89, s88, s94
s_cmp_lt_i32 s89, 0
s_cbranch_scc1 LplB7_1_0
s_add_u32 s95, s95, 95
s_sub_u32 s89, s88, s95
s_add_u32 s88, s88, 92
)"
R"(s_cmp_gt_i32 s89, 755
s_cselect_b32 s68, 20, 0
s_add_u32 s88, s88, s68
s_sub_u32 s89, s88, s95
s_cmp_gt_i32 s89, 0
s_cselect_b32 s94, s88, s95
LpeB7_1_0:
s_waitcnt vmcnt(15)
s_barrier
s_add_u32 m0, s56, 122880
s_nop 0
buffer_load_dwordx4 v252, s[28:31], s50 offen lds
buffer_load_dwordx4 v253, s[28:31], s50 offen offset:1024 lds
buffer_load_dwordx4 v254, s[28:31], s50 offen offset:2048 lds
s_add_u32 s50, s50, 393216
s_cmp_eq_u32 s50, s53
)"
R"(s_cselect_b32 s50, s52, s50
v_mfma_f32_16x16x32_bf16 v[32:35], v[184:187], v[152:155], v[32:35]
v_mfma_f32_16x16x32_bf16 v[36:39], v[184:187], v[156:159], v[36:39]
v_mfma_f32_16x16x32_bf16 v[44:47], v[188:191], v[156:159], v[44:47]
ds_read_b64_tr_b16 v[184:185], v166 offset:36864
ds_read_b64_tr_b16 v[186:187], v166 offset:38400
v_mfma_f32_16x16x32_bf16 v[40:43], v[188:191], v[152:155], v[40:43]
v_mfma_f32_16x16x32_bf16 v[48:51], v[192:195], v[152:155], v[48:51]
ds_read_b64_tr_b16 v[188:189], v167 offset:36864
ds_read_b64_tr_b16 v[190:191], v167 offset:38400
v_mfma_f32_16x16x32_bf16 v[52:55], v[192:195], v[156:159], v[52:55]
v_mfma_f32_16x16x32_bf16 v[60:63], v[196:199], v[156:159], v[60:63]
ds_read_b64_tr_b16 v[192:193], v168 offset:36864
ds_read_b64_tr_b16 v[194:195], v168 offset:38400
v_mfma_f32_16x16x32_bf16 v[56:59], v[196:199], v[152:155], v[56:59]
v_mfma_f32_16x16x32_bf16 v[64:67], v[200:203], v[152:155], v[64:67]
)"
R"(ds_read_b64_tr_b16 v[196:197], v169 offset:36864
ds_read_b64_tr_b16 v[198:199], v169 offset:38400
v_mfma_f32_16x16x32_bf16 v[68:71], v[200:203], v[156:159], v[68:71]
v_mfma_f32_16x16x32_bf16 v[76:79], v[204:207], v[156:159], v[76:79]
ds_read_b64_tr_b16 v[200:201], v166 offset:36992
ds_read_b64_tr_b16 v[202:203], v166 offset:38528
v_mfma_f32_16x16x32_bf16 v[72:75], v[204:207], v[152:155], v[72:75]
v_mfma_f32_16x16x32_bf16 v[80:83], v[208:211], v[152:155], v[80:83]
ds_read_b64_tr_b16 v[204:205], v167 offset:36992
ds_read_b64_tr_b16 v[206:207], v167 offset:38528
v_mfma_f32_16x16x32_bf16 v[84:87], v[208:211], v[156:159], v[84:87]
v_mfma_f32_16x16x32_bf16 v[92:95], v[212:215], v[156:159], v[92:95]
ds_read_b64_tr_b16 v[208:209], v168 offset:36992
ds_read_b64_tr_b16 v[210:211], v168 offset:38528
v_mfma_f32_16x16x32_bf16 v[88:91], v[212:215], v[152:155], v[88:91]
ds_read_b128 v[144:147], v164 offset:0
)"
R"(s_waitcnt lgkmcnt(11)
ds_read_b128 v[148:151], v164 offset:2048
ds_read_b128 v[152:155], v165 offset:0
ds_read_b128 v[156:159], v165 offset:2048
s_waitcnt lgkmcnt(0)
s_add_u32 m0, s84, 147456
s_nop 0
buffer_load_dwordx4 v160, s[36:39], s51 offen lds
buffer_load_dwordx4 v161, s[36:39], s51 offen offset:1024 lds
buffer_load_dwordx4 v162, s[36:39], s51 offen offset:2048 lds
buffer_load_dwordx4 v163, s[36:39], s51 offen offset:3072 lds
s_add_u32 s51, s51, 128
s_cmp_eq_u32 s51, 1280
s_cselect_b32 s51, 0, s51
s_add_u32 m0, s56, 0
s_nop 0
)"
R"(buffer_load_dwordx4 v176, s[20:23], s48 offen nt lds
ds_read_b64_tr_b16 v[212:213], v169 offset:36992
ds_read_b64_tr_b16 v[214:215], v169 offset:38528
buffer_load_dwordx4 v177, s[20:23], s48 offen offset:1024 nt lds
buffer_load_dwordx4 v178, s[20:23], s48 offen offset:2048 nt lds
s_add_u32 s48, s48, 12288
s_add_u32 m0, s56, 86016
s_nop 0
buffer_load_dwordx4 v179, s[24:27], s49 offen nt lds
buffer_load_dwordx4 v180, s[24:27], s49 offen offset:1024 nt lds
buffer_load_dwordx4 v181, s[24:27], s49 offen offset:2048 nt lds
s_add_u32 s49, s49, 12288
s_waitcnt lgkmcnt(0)
s_cmp_eq_u32 s73, -1
s_cbranch_scc0 LpeB7_1_1
LplB7_1_1:
)"
R"(s_memrealtime s[88:89]
s_waitcnt lgkmcnt(0)
s_sub_u32 s89, s88, s94
s_cmp_lt_i32 s89, 0
s_cbranch_scc1 LplB7_1_1
s_add_u32 s95, s95, 95
s_sub_u32 s89, s88, s95
s_add_u32 s88, s88, 92
s_cmp_gt_i32 s89, 755
s_cselect_b32 s68, 20, 0
s_add_u32 s88, s88, s68
s_sub_u32 s89, s88, s95
s_cmp_gt_i32 s89, 0
s_cselect_b32 s94, s88, s95
LpeB7_1_1:
s_waitcnt vmcnt(22)
)"
R"(s_barrier
s_add_u32 m0, s56, 12288
s_nop 0
buffer_load_dwordx4 v176, s[20:23], s48 offen nt lds
buffer_load_dwordx4 v177, s[20:23], s48 offen offset:1024 nt lds
buffer_load_dwordx4 v178, s[20:23], s48 offen offset:2048 nt lds
s_add_u32 s48, s48, 12288
v_mfma_f32_16x16x32_bf16 v[0:3], v[184:187], v[144:147], v[0:3]
v_mfma_f32_16x16x32_bf16 v[4:7], v[184:187], v[148:151], v[4:7]
v_mfma_f32_16x16x32_bf16 v[12:15], v[188:191], v[148:151], v[12:15]
ds_read_b64_tr_b16 v[184:185], v166 offset:37120
ds_read_b64_tr_b16 v[186:187], v166 offset:38656
v_mfma_f32_16x16x32_bf16 v[8:11], v[188:191], v[144:147], v[8:11]
v_mfma_f32_16x16x32_bf16 v[16:19], v[192:195], v[144:147], v[16:19]
ds_read_b64_tr_b16 v[188:189], v167 offset:37120
ds_read_b64_tr_b16 v[190:191], v167 offset:38656
)"
R"(v_mfma_f32_16x16x32_bf16 v[20:23], v[192:195], v[148:151], v[20:23]
v_mfma_f32_16x16x32_bf16 v[28:31], v[196:199], v[148:151], v[28:31]
ds_read_b64_tr_b16 v[192:193], v168 offset:37120
ds_read_b64_tr_b16 v[194:195], v168 offset:38656
v_mfma_f32_16x16x32_bf16 v[24:27], v[196:199], v[144:147], v[24:27]
v_mfma_f32_16x16x32_bf16 v[32:35], v[200:203], v[144:147], v[32:35]
ds_read_b64_tr_b16 v[196:197], v169 offset:37120
ds_read_b64_tr_b16 v[198:199], v169 offset:38656
v_mfma_f32_16x16x32_bf16 v[36:39], v[200:203], v[148:151], v[36:39]
v_mfma_f32_16x16x32_bf16 v[44:47], v[204:207], v[148:151], v[44:47]
ds_read_b64_tr_b16 v[200:201], v166 offset:0
ds_read_b64_tr_b16 v[202:203], v166 offset:1536
v_mfma_f32_16x16x32_bf16 v[40:43], v[204:207], v[144:147], v[40:43]
v_mfma_f32_16x16x32_bf16 v[48:51], v[208:211], v[144:147], v[48:51]
ds_read_b64_tr_b16 v[204:205], v167 offset:0
ds_read_b64_tr_b16 v[206:207], v167 offset:1536
)"
R"(v_mfma_f32_16x16x32_bf16 v[52:55], v[208:211], v[148:151], v[52:55]
v_mfma_f32_16x16x32_bf16 v[60:63], v[212:215], v[148:151], v[60:63]
ds_read_b64_tr_b16 v[208:209], v168 offset:0
ds_read_b64_tr_b16 v[210:211], v168 offset:1536
v_mfma_f32_16x16x32_bf16 v[56:59], v[212:215], v[144:147], v[56:59]
s_add_u32 m0, s56, 49152
s_nop 0
buffer_load_dwordx4 v179, s[24:27], s49 offen nt lds
ds_read_b64_tr_b16 v[212:213], v169 offset:0
s_waitcnt lgkmcnt(11)
ds_read_b64_tr_b16 v[214:215], v169 offset:1536
buffer_load_dwordx4 v180, s[24:27], s49 offen offset:1024 nt lds
buffer_load_dwordx4 v181, s[24:27], s49 offen offset:2048 nt lds
s_add_u32 s49, s49, 12288
s_waitcnt lgkmcnt(0)
s_cmp_eq_u32 s73, -1
)"
R"(s_cbranch_scc0 LpeB7_1_2
LplB7_1_2:
s_memrealtime s[88:89]
s_waitcnt lgkmcnt(0)
s_sub_u32 s89, s88, s94
s_cmp_lt_i32 s89, 0
s_cbranch_scc1 LplB7_1_2
s_add_u32 s95, s95, 95
s_sub_u32 s89, s88, s95
s_add_u32 s88, s88, 92
s_cmp_gt_i32 s89, 755
s_cselect_b32 s68, 20, 0
s_add_u32 s88, s88, s68
s_sub_u32 s89, s88, s95
s_cmp_gt_i32 s89, 0
s_cselect_b32 s94, s88, s95
)"
R"(LpeB7_1_2:
s_waitcnt vmcnt(19)
s_barrier
s_add_u32 m0, s56, 135168
s_nop 0
buffer_load_dwordx4 v252, s[28:31], s50 offen lds
buffer_load_dwordx4 v253, s[28:31], s50 offen offset:1024 lds
buffer_load_dwordx4 v254, s[28:31], s50 offen offset:2048 lds
s_add_u32 s50, s50, 393216
s_cmp_eq_u32 s50, s53
s_cselect_b32 s50, s52, s50
v_mfma_f32_16x16x32_bf16 v[64:67], v[184:187], v[144:147], v[64:67]
v_mfma_f32_16x16x32_bf16 v[68:71], v[184:187], v[148:151], v[68:71]
v_mfma_f32_16x16x32_bf16 v[76:79], v[188:191], v[148:151], v[76:79]
ds_read_b64_tr_b16 v[184:185], v166 offset:128
ds_read_b64_tr_b16 v[186:187], v166 offset:1664
)"
R"(v_mfma_f32_16x16x32_bf16 v[72:75], v[188:191], v[144:147], v[72:75]
v_mfma_f32_16x16x32_bf16 v[80:83], v[192:195], v[144:147], v[80:83]
ds_read_b64_tr_b16 v[188:189], v167 offset:128
ds_read_b64_tr_b16 v[190:191], v167 offset:1664
v_mfma_f32_16x16x32_bf16 v[84:87], v[192:195], v[148:151], v[84:87]
v_mfma_f32_16x16x32_bf16 v[92:95], v[196:199], v[148:151], v[92:95]
ds_read_b64_tr_b16 v[192:193], v168 offset:128
ds_read_b64_tr_b16 v[194:195], v168 offset:1664
v_mfma_f32_16x16x32_bf16 v[88:91], v[196:199], v[144:147], v[88:91]
v_mfma_f32_16x16x32_bf16 v[0:3], v[200:203], v[152:155], v[0:3]
ds_read_b64_tr_b16 v[196:197], v169 offset:128
ds_read_b64_tr_b16 v[198:199], v169 offset:1664
v_mfma_f32_16x16x32_bf16 v[4:7], v[200:203], v[156:159], v[4:7]
v_mfma_f32_16x16x32_bf16 v[12:15], v[204:207], v[156:159], v[12:15]
ds_read_b64_tr_b16 v[200:201], v166 offset:256
ds_read_b64_tr_b16 v[202:203], v166 offset:1792
)"
R"(v_mfma_f32_16x16x32_bf16 v[8:11], v[204:207], v[152:155], v[8:11]
v_mfma_f32_16x16x32_bf16 v[16:19], v[208:211], v[152:155], v[16:19]
ds_read_b64_tr_b16 v[204:205], v167 offset:256
ds_read_b64_tr_b16 v[206:207], v167 offset:1792
v_mfma_f32_16x16x32_bf16 v[20:23], v[208:211], v[156:159], v[20:23]
v_mfma_f32_16x16x32_bf16 v[28:31], v[212:215], v[156:159], v[28:31]
ds_read_b64_tr_b16 v[208:209], v168 offset:256
ds_read_b64_tr_b16 v[210:211], v168 offset:1792
v_mfma_f32_16x16x32_bf16 v[24:27], v[212:215], v[152:155], v[24:27]
s_add_u32 m0, s56, 24576
s_nop 0
buffer_load_dwordx4 v176, s[20:23], s48 offen nt lds
buffer_load_dwordx4 v177, s[20:23], s48 offen offset:1024 nt lds
buffer_load_dwordx4 v178, s[20:23], s48 offen offset:2048 nt lds
s_add_u32 s48, s48, 12288
ds_read_b64_tr_b16 v[212:213], v169 offset:256
)"
R"(s_waitcnt lgkmcnt(11)
ds_read_b64_tr_b16 v[214:215], v169 offset:1792
s_add_u32 m0, s56, 61440
s_nop 0
buffer_load_dwordx4 v179, s[24:27], s49 offen nt lds
buffer_load_dwordx4 v180, s[24:27], s49 offen offset:1024 nt lds
buffer_load_dwordx4 v181, s[24:27], s49 offen offset:2048 nt lds
s_add_u32 s49, s49, 12288
s_waitcnt lgkmcnt(0)
s_cmp_eq_u32 s73, -1
s_cbranch_scc0 LpeB7_1_3
LplB7_1_3:
s_memrealtime s[88:89]
s_waitcnt lgkmcnt(0)
s_sub_u32 s89, s88, s94
s_cmp_lt_i32 s89, 0
)"
R"(s_cbranch_scc1 LplB7_1_3
s_add_u32 s95, s95, 95
s_sub_u32 s89, s88, s95
s_add_u32 s88, s88, 92
s_cmp_gt_i32 s89, 755
s_cselect_b32 s68, 20, 0
s_add_u32 s88, s88, s68
s_sub_u32 s89, s88, s95
s_cmp_gt_i32 s89, 0
s_cselect_b32 s94, s88, s95
LpeB7_1_3:
s_waitcnt vmcnt(15)
s_barrier
s_add_u32 m0, s56, 98304
s_nop 0
buffer_load_dwordx4 v252, s[28:31], s50 offen lds
)"
R"(buffer_load_dwordx4 v253, s[28:31], s50 offen offset:1024 lds
buffer_load_dwordx4 v254, s[28:31], s50 offen offset:2048 lds
s_add_u32 s50, s50, 393216
s_cmp_eq_u32 s50, s53
s_cselect_b32 s50, s52, s50
v_mfma_f32_16x16x32_bf16 v[32:35], v[184:187], v[152:155], v[32:35]
v_mfma_f32_16x16x32_bf16 v[36:39], v[184:187], v[156:159], v[36:39]
v_mfma_f32_16x16x32_bf16 v[44:47], v[188:191], v[156:159], v[44:47]
ds_read_b64_tr_b16 v[184:185], v166 offset:12288
ds_read_b64_tr_b16 v[186:187], v166 offset:13824
v_mfma_f32_16x16x32_bf16 v[40:43], v[188:191], v[152:155], v[40:43]
v_mfma_f32_16x16x32_bf16 v[48:51], v[192:195], v[152:155], v[48:51]
ds_read_b64_tr_b16 v[188:189], v167 offset:12288
ds_read_b64_tr_b16 v[190:191], v167 offset:13824
v_mfma_f32_16x16x32_bf16 v[52:55], v[192:195], v[156:159], v[52:55]
v_mfma_f32_16x16x32_bf16 v[60:63], v[196:199], v[156:159], v[60:63]
)"
R"(ds_read_b64_tr_b16 v[192:193], v168 offset:12288
ds_read_b64_tr_b16 v[194:195], v168 offset:13824
v_mfma_f32_16x16x32_bf16 v[56:59], v[196:199], v[152:155], v[56:59]
v_mfma_f32_16x16x32_bf16 v[64:67], v[200:203], v[152:155], v[64:67]
ds_read_b64_tr_b16 v[196:197], v169 offset:12288
ds_read_b64_tr_b16 v[198:199], v169 offset:13824
v_mfma_f32_16x16x32_bf16 v[68:71], v[200:203], v[156:159], v[68:71]
v_mfma_f32_16x16x32_bf16 v[76:79], v[204:207], v[156:159], v[76:79]
ds_read_b64_tr_b16 v[200:201], v166 offset:12416
ds_read_b64_tr_b16 v[202:203], v166 offset:13952
v_mfma_f32_16x16x32_bf16 v[72:75], v[204:207], v[152:155], v[72:75]
v_mfma_f32_16x16x32_bf16 v[80:83], v[208:211], v[152:155], v[80:83]
ds_read_b64_tr_b16 v[204:205], v167 offset:12416
ds_read_b64_tr_b16 v[206:207], v167 offset:13952
v_mfma_f32_16x16x32_bf16 v[84:87], v[208:211], v[156:159], v[84:87]
v_mfma_f32_16x16x32_bf16 v[92:95], v[212:215], v[156:159], v[92:95]
)"
R"(ds_read_b64_tr_b16 v[208:209], v168 offset:12416
ds_read_b64_tr_b16 v[210:211], v168 offset:13952
v_mfma_f32_16x16x32_bf16 v[88:91], v[212:215], v[152:155], v[88:91]
ds_read_b128 v[144:147], v164 offset:0
s_waitcnt lgkmcnt(11)
ds_read_b128 v[148:151], v164 offset:2048
ds_read_b128 v[152:155], v165 offset:0
ds_read_b128 v[156:159], v165 offset:2048
s_waitcnt lgkmcnt(0)
s_add_u32 m0, s84, 147456
s_nop 0
buffer_load_dwordx4 v160, s[36:39], s51 offen lds
buffer_load_dwordx4 v161, s[36:39], s51 offen offset:1024 lds
buffer_load_dwordx4 v162, s[36:39], s51 offen offset:2048 lds
buffer_load_dwordx4 v163, s[36:39], s51 offen offset:3072 lds
s_add_u32 s51, s51, 128
)"
R"(s_cmp_eq_u32 s51, 1280
s_cselect_b32 s51, 0, s51
s_add_u32 m0, s56, 36864
s_nop 0
buffer_load_dwordx4 v176, s[20:23], s48 offen nt lds
ds_read_b64_tr_b16 v[212:213], v169 offset:12416
ds_read_b64_tr_b16 v[214:215], v169 offset:13952
buffer_load_dwordx4 v177, s[20:23], s48 offen offset:1024 nt lds
buffer_load_dwordx4 v178, s[20:23], s48 offen offset:2048 nt lds
s_add_u32 s48, s48, 12288
s_add_u32 m0, s56, 73728
s_nop 0
buffer_load_dwordx4 v179, s[24:27], s49 offen nt lds
buffer_load_dwordx4 v180, s[24:27], s49 offen offset:1024 nt lds
buffer_load_dwordx4 v181, s[24:27], s49 offen offset:2048 nt lds
s_add_u32 s49, s49, 12288
)"
R"(s_waitcnt lgkmcnt(0)
s_cmp_eq_u32 s73, -1
s_cbranch_scc0 LpeB8_1_0
LplB8_1_0:
s_memrealtime s[88:89]
s_waitcnt lgkmcnt(0)
s_sub_u32 s89, s88, s94
s_cmp_lt_i32 s89, 0
s_cbranch_scc1 LplB8_1_0
s_add_u32 s95, s95, 95
s_sub_u32 s89, s88, s95
s_add_u32 s88, s88, 92
s_cmp_gt_i32 s89, 755
s_cselect_b32 s68, 20, 0
s_add_u32 s88, s88, s68
s_sub_u32 s89, s88, s95
)"
R"(s_cmp_gt_i32 s89, 0
s_cselect_b32 s94, s88, s95
LpeB8_1_0:
s_waitcnt vmcnt(22)
s_barrier
s_add_u32 m0, s56, 0
s_nop 0
buffer_load_dwordx4 v176, s[20:23], s48 offen nt lds
buffer_load_dwordx4 v177, s[20:23], s48 offen offset:1024 nt lds
buffer_load_dwordx4 v178, s[20:23], s48 offen offset:2048 nt lds
s_add_u32 s48, s48, 12288
v_mfma_f32_16x16x32_bf16 v[0:3], v[184:187], v[144:147], v[0:3]
v_mfma_f32_16x16x32_bf16 v[4:7], v[184:187], v[148:151], v[4:7]
v_mfma_f32_16x16x32_bf16 v[12:15], v[188:191], v[148:151], v[12:15]
ds_read_b64_tr_b16 v[184:185], v166 offset:12544
ds_read_b64_tr_b16 v[186:187], v166 offset:14080
)"
R"(v_mfma_f32_16x16x32_bf16 v[8:11], v[188:191], v[144:147], v[8:11]
v_mfma_f32_16x16x32_bf16 v[16:19], v[192:195], v[144:147], v[16:19]
ds_read_b64_tr_b16 v[188:189], v167 offset:12544
ds_read_b64_tr_b16 v[190:191], v167 offset:14080
v_mfma_f32_16x16x32_bf16 v[20:23], v[192:195], v[148:151], v[20:23]
v_mfma_f32_16x16x32_bf16 v[28:31], v[196:199], v[148:151], v[28:31]
ds_read_b64_tr_b16 v[192:193], v168 offset:12544
ds_read_b64_tr_b16 v[194:195], v168 offset:14080
v_mfma_f32_16x16x32_bf16 v[24:27], v[196:199], v[144:147], v[24:27]
v_mfma_f32_16x16x32_bf16 v[32:35], v[200:203], v[144:147], v[32:35]
ds_read_b64_tr_b16 v[196:197], v169 offset:12544
ds_read_b64_tr_b16 v[198:199], v169 offset:14080
v_mfma_f32_16x16x32_bf16 v[36:39], v[200:203], v[148:151], v[36:39]
v_mfma_f32_16x16x32_bf16 v[44:47], v[204:207], v[148:151], v[44:47]
ds_read_b64_tr_b16 v[200:201], v166 offset:24576
ds_read_b64_tr_b16 v[202:203], v166 offset:26112
)"
R"(v_mfma_f32_16x16x32_bf16 v[40:43], v[204:207], v[144:147], v[40:43]
v_mfma_f32_16x16x32_bf16 v[48:51], v[208:211], v[144:147], v[48:51]
ds_read_b64_tr_b16 v[204:205], v167 offset:24576
ds_read_b64_tr_b16 v[206:207], v167 offset:26112
v_mfma_f32_16x16x32_bf16 v[52:55], v[208:211], v[148:151], v[52:55]
v_mfma_f32_16x16x32_bf16 v[60:63], v[212:215], v[148:151], v[60:63]
ds_read_b64_tr_b16 v[208:209], v168 offset:24576
ds_read_b64_tr_b16 v[210:211], v168 offset:26112
v_mfma_f32_16x16x32_bf16 v[56:59], v[212:215], v[144:147], v[56:59]
s_add_u32 m0, s56, 86016
s_nop 0
buffer_load_dwordx4 v179, s[24:27], s49 offen nt lds
ds_read_b64_tr_b16 v[212:213], v169 offset:24576
s_waitcnt lgkmcnt(11)
ds_read_b64_tr_b16 v[214:215], v169 offset:26112
buffer_load_dwordx4 v180, s[24:27], s49 offen offset:1024 nt lds
)"
R"(buffer_load_dwordx4 v181, s[24:27], s49 offen offset:2048 nt lds
s_add_u32 s49, s49, 12288
s_waitcnt lgkmcnt(0)
s_cmp_eq_u32 s73, -1
s_cbranch_scc0 LpeB8_1_1
LplB8_1_1:
s_memrealtime s[88:89]
s_waitcnt lgkmcnt(0)
s_sub_u32 s89, s88, s94
s_cmp_lt_i32 s89, 0
s_cbranch_scc1 LplB8_1_1
s_add_u32 s95, s95, 95
s_sub_u32 s89, s88, s95
s_add_u32 s88, s88, 92
s_cmp_gt_i32 s89, 755
s_cselect_b32 s68, 20, 0
)"
R"(s_add_u32 s88, s88, s68
s_sub_u32 s89, s88, s95
s_cmp_gt_i32 s89, 0
s_cselect_b32 s94, s88, s95
LpeB8_1_1:
s_waitcnt vmcnt(19)
s_barrier
s_add_u32 m0, s56, 12288
s_nop 0
buffer_load_dwordx4 v176, s[20:23], s48 offen nt lds
buffer_load_dwordx4 v177, s[20:23], s48 offen offset:1024 nt lds
buffer_load_dwordx4 v178, s[20:23], s48 offen offset:2048 nt lds
s_add_u32 s48, s48, 12288
v_mfma_f32_16x16x32_bf16 v[64:67], v[184:187], v[144:147], v[64:67]
v_mfma_f32_16x16x32_bf16 v[68:71], v[184:187], v[148:151], v[68:71]
v_mfma_f32_16x16x32_bf16 v[76:79], v[188:191], v[148:151], v[76:79]
)"
R"(ds_read_b64_tr_b16 v[184:185], v166 offset:24704
ds_read_b64_tr_b16 v[186:187], v166 offset:26240
v_mfma_f32_16x16x32_bf16 v[72:75], v[188:191], v[144:147], v[72:75]
v_mfma_f32_16x16x32_bf16 v[80:83], v[192:195], v[144:147], v[80:83]
ds_read_b64_tr_b16 v[188:189], v167 offset:24704
ds_read_b64_tr_b16 v[190:191], v167 offset:26240
v_mfma_f32_16x16x32_bf16 v[84:87], v[192:195], v[148:151], v[84:87]
v_mfma_f32_16x16x32_bf16 v[92:95], v[196:199], v[148:151], v[92:95]
ds_read_b64_tr_b16 v[192:193], v168 offset:24704
ds_read_b64_tr_b16 v[194:195], v168 offset:26240
v_mfma_f32_16x16x32_bf16 v[88:91], v[196:199], v[144:147], v[88:91]
v_mfma_f32_16x16x32_bf16 v[0:3], v[200:203], v[152:155], v[0:3]
ds_read_b64_tr_b16 v[196:197], v169 offset:24704
ds_read_b64_tr_b16 v[198:199], v169 offset:26240
v_mfma_f32_16x16x32_bf16 v[4:7], v[200:203], v[156:159], v[4:7]
v_mfma_f32_16x16x32_bf16 v[12:15], v[204:207], v[156:159], v[12:15]
)"
R"(ds_read_b64_tr_b16 v[200:201], v166 offset:24832
ds_read_b64_tr_b16 v[202:203], v166 offset:26368
v_mfma_f32_16x16x32_bf16 v[8:11], v[204:207], v[152:155], v[8:11]
v_mfma_f32_16x16x32_bf16 v[16:19], v[208:211], v[152:155], v[16:19]
ds_read_b64_tr_b16 v[204:205], v167 offset:24832
ds_read_b64_tr_b16 v[206:207], v167 offset:26368
v_mfma_f32_16x16x32_bf16 v[20:23], v[208:211], v[156:159], v[20:23]
v_mfma_f32_16x16x32_bf16 v[28:31], v[212:215], v[156:159], v[28:31]
ds_read_b64_tr_b16 v[208:209], v168 offset:24832
ds_read_b64_tr_b16 v[210:211], v168 offset:26368
v_mfma_f32_16x16x32_bf16 v[24:27], v[212:215], v[152:155], v[24:27]
s_add_u32 m0, s56, 49152
s_nop 0
buffer_load_dwordx4 v179, s[24:27], s49 offen nt lds
ds_read_b64_tr_b16 v[212:213], v169 offset:24832
s_waitcnt lgkmcnt(11)
)"
R"(ds_read_b64_tr_b16 v[214:215], v169 offset:26368
buffer_load_dwordx4 v180, s[24:27], s49 offen offset:1024 nt lds
buffer_load_dwordx4 v181, s[24:27], s49 offen offset:2048 nt lds
s_add_u32 s49, s49, 12288
s_waitcnt lgkmcnt(0)
s_cmp_eq_u32 s73, -1
s_cbranch_scc0 LpeB8_1_2
LplB8_1_2:
s_memrealtime s[88:89]
s_waitcnt lgkmcnt(0)
s_sub_u32 s89, s88, s94
s_cmp_lt_i32 s89, 0
s_cbranch_scc1 LplB8_1_2
s_add_u32 s95, s95, 95
s_sub_u32 s89, s88, s95
s_add_u32 s88, s88, 92
)"
R"(s_cmp_gt_i32 s89, 755
s_cselect_b32 s68, 20, 0
s_add_u32 s88, s88, s68
s_sub_u32 s89, s88, s95
s_cmp_gt_i32 s89, 0
s_cselect_b32 s94, s88, s95
LpeB8_1_2:
s_waitcnt vmcnt(12)
s_barrier
s_add_u32 m0, s56, 24576
s_nop 0
buffer_load_dwordx4 v176, s[20:23], s48 offen nt lds
buffer_load_dwordx4 v177, s[20:23], s48 offen offset:1024 nt lds
buffer_load_dwordx4 v178, s[20:23], s48 offen offset:2048 nt lds
s_add_u32 s48, s48, 12288
v_mfma_f32_16x16x32_bf16 v[32:35], v[184:187], v[152:155], v[32:35]
)"
R"(v_mfma_f32_16x16x32_bf16 v[36:39], v[184:187], v[156:159], v[36:39]
v_mfma_f32_16x16x32_bf16 v[44:47], v[188:191], v[156:159], v[44:47]
ds_read_b64_tr_b16 v[184:185], v166 offset:36864
ds_read_b64_tr_b16 v[186:187], v166 offset:38400
v_mfma_f32_16x16x32_bf16 v[40:43], v[188:191], v[152:155], v[40:43]
v_mfma_f32_16x16x32_bf16 v[48:51], v[192:195], v[152:155], v[48:51]
ds_read_b64_tr_b16 v[188:189], v167 offset:36864
ds_read_b64_tr_b16 v[190:191], v167 offset:38400
v_mfma_f32_16x16x32_bf16 v[52:55], v[192:195], v[156:159], v[52:55]
v_mfma_f32_16x16x32_bf16 v[60:63], v[196:199], v[156:159], v[60:63]
ds_read_b64_tr_b16 v[192:193], v168 offset:36864
ds_read_b64_tr_b16 v[194:195], v168 offset:38400
v_mfma_f32_16x16x32_bf16 v[56:59], v[196:199], v[152:155], v[56:59]
v_mfma_f32_16x16x32_bf16 v[64:67], v[200:203], v[152:155], v[64:67]
ds_read_b64_tr_b16 v[196:197], v169 offset:36864
ds_read_b64_tr_b16 v[198:199], v169 offset:38400
)"
R"(v_mfma_f32_16x16x32_bf16 v[68:71], v[200:203], v[156:159], v[68:71]
v_mfma_f32_16x16x32_bf16 v[76:79], v[204:207], v[156:159], v[76:79]
ds_read_b64_tr_b16 v[200:201], v166 offset:36992
ds_read_b64_tr_b16 v[202:203], v166 offset:38528
v_mfma_f32_16x16x32_bf16 v[72:75], v[204:207], v[152:155], v[72:75]
v_mfma_f32_16x16x32_bf16 v[80:83], v[208:211], v[152:155], v[80:83]
ds_read_b64_tr_b16 v[204:205], v167 offset:36992
ds_read_b64_tr_b16 v[206:207], v167 offset:38528
v_mfma_f32_16x16x32_bf16 v[84:87], v[208:211], v[156:159], v[84:87]
v_mfma_f32_16x16x32_bf16 v[92:95], v[212:215], v[156:159], v[92:95]
ds_read_b64_tr_b16 v[208:209], v168 offset:36992
ds_read_b64_tr_b16 v[210:211], v168 offset:38528
v_mfma_f32_16x16x32_bf16 v[88:91], v[212:215], v[152:155], v[88:91]
ds_read_b128 v[144:147], v164 offset:0
s_waitcnt lgkmcnt(11)
ds_read_b128 v[148:151], v164 offset:2048
)"
R"(ds_read_b128 v[152:155], v165 offset:0
ds_read_b128 v[156:159], v165 offset:2048
s_waitcnt lgkmcnt(0)
s_add_u32 m0, s56, 61440
s_nop 0
buffer_load_dwordx4 v179, s[24:27], s49 offen nt lds
ds_read_b64_tr_b16 v[212:213], v169 offset:36992
ds_read_b64_tr_b16 v[214:215], v169 offset:38528
buffer_load_dwordx4 v180, s[24:27], s49 offen offset:1024 nt lds
buffer_load_dwordx4 v181, s[24:27], s49 offen offset:2048 nt lds
s_add_u32 s49, s49, 12288
s_waitcnt lgkmcnt(0)
s_cmp_eq_u32 s73, -1
s_cbranch_scc0 LpeB8_1_3
LplB8_1_3:
s_memrealtime s[88:89]
)"
R"(s_waitcnt lgkmcnt(0)
s_sub_u32 s89, s88, s94
s_cmp_lt_i32 s89, 0
s_cbranch_scc1 LplB8_1_3
s_add_u32 s95, s95, 95
s_sub_u32 s89, s88, s95
s_add_u32 s88, s88, 92
s_cmp_gt_i32 s89, 755
s_cselect_b32 s68, 20, 0
s_add_u32 s88, s88, s68
s_sub_u32 s89, s88, s95
s_cmp_gt_i32 s89, 0
s_cselect_b32 s94, s88, s95
LpeB8_1_3:
s_waitcnt vmcnt(12)
s_barrier
)"
R"(s_add_u32 m0, s56, 36864
s_nop 0
buffer_load_dwordx4 v176, s[20:23], s48 offen nt lds
buffer_load_dwordx4 v177, s[20:23], s48 offen offset:1024 nt lds
buffer_load_dwordx4 v178, s[20:23], s48 offen offset:2048 nt lds
s_add_u32 s48, s48, 12288
v_mfma_f32_16x16x32_bf16 v[0:3], v[184:187], v[144:147], v[0:3]
v_mfma_f32_16x16x32_bf16 v[4:7], v[184:187], v[148:151], v[4:7]
v_mfma_f32_16x16x32_bf16 v[12:15], v[188:191], v[148:151], v[12:15]
ds_read_b64_tr_b16 v[184:185], v166 offset:37120
ds_read_b64_tr_b16 v[186:187], v166 offset:38656
v_mfma_f32_16x16x32_bf16 v[8:11], v[188:191], v[144:147], v[8:11]
v_mfma_f32_16x16x32_bf16 v[16:19], v[192:195], v[144:147], v[16:19]
ds_read_b64_tr_b16 v[188:189], v167 offset:37120
ds_read_b64_tr_b16 v[190:191], v167 offset:38656
v_mfma_f32_16x16x32_bf16 v[20:23], v[192:195], v[148:151], v[20:23]
)"
R"(v_mfma_f32_16x16x32_bf16 v[28:31], v[196:199], v[148:151], v[28:31]
ds_read_b64_tr_b16 v[192:193], v168 offset:37120
ds_read_b64_tr_b16 v[194:195], v168 offset:38656
v_mfma_f32_16x16x32_bf16 v[24:27], v[196:199], v[144:147], v[24:27]
v_mfma_f32_16x16x32_bf16 v[32:35], v[200:203], v[144:147], v[32:35]
ds_read_b64_tr_b16 v[196:197], v169 offset:37120
ds_read_b64_tr_b16 v[198:199], v169 offset:38656
v_mfma_f32_16x16x32_bf16 v[36:39], v[200:203], v[148:151], v[36:39]
v_mfma_f32_16x16x32_bf16 v[44:47], v[204:207], v[148:151], v[44:47]
ds_read_b64_tr_b16 v[200:201], v166 offset:0
ds_read_b64_tr_b16 v[202:203], v166 offset:1536
v_mfma_f32_16x16x32_bf16 v[40:43], v[204:207], v[144:147], v[40:43]
v_mfma_f32_16x16x32_bf16 v[48:51], v[208:211], v[144:147], v[48:51]
ds_read_b64_tr_b16 v[204:205], v167 offset:0
ds_read_b64_tr_b16 v[206:207], v167 offset:1536
v_mfma_f32_16x16x32_bf16 v[52:55], v[208:211], v[148:151], v[52:55]
)"
R"(v_mfma_f32_16x16x32_bf16 v[60:63], v[212:215], v[148:151], v[60:63]
ds_read_b64_tr_b16 v[208:209], v168 offset:0
ds_read_b64_tr_b16 v[210:211], v168 offset:1536
v_mfma_f32_16x16x32_bf16 v[56:59], v[212:215], v[144:147], v[56:59]
s_add_u32 m0, s56, 73728
s_nop 0
buffer_load_dwordx4 v179, s[24:27], s49 offen nt lds
ds_read_b64_tr_b16 v[212:213], v169 offset:0
s_waitcnt lgkmcnt(11)
ds_read_b64_tr_b16 v[214:215], v169 offset:1536
buffer_load_dwordx4 v180, s[24:27], s49 offen offset:1024 nt lds
buffer_load_dwordx4 v181, s[24:27], s49 offen offset:2048 nt lds
s_add_u32 s49, s49, 12288
s_waitcnt lgkmcnt(0)
s_cmp_eq_u32 s73, -1
s_cbranch_scc0 LpeB9_1_0
)"
R"(LplB9_1_0:
s_memrealtime s[88:89]
s_waitcnt lgkmcnt(0)
s_sub_u32 s89, s88, s94
s_cmp_lt_i32 s89, 0
s_cbranch_scc1 LplB9_1_0
s_add_u32 s95, s95, 95
s_sub_u32 s89, s88, s95
s_add_u32 s88, s88, 92
s_cmp_gt_i32 s89, 755
s_cselect_b32 s68, 20, 0
s_add_u32 s88, s88, s68
s_sub_u32 s89, s88, s95
s_cmp_gt_i32 s89, 0
s_cselect_b32 s94, s88, s95
LpeB9_1_0:
)"
R"(s_waitcnt vmcnt(12)
s_barrier
s_add_u32 s48, s48, 1179648
s_add_u32 m0, s56, 0
s_nop 0
buffer_load_dwordx4 v176, s[20:23], s48 offen nt lds
buffer_load_dwordx4 v177, s[20:23], s48 offen offset:1024 nt lds
buffer_load_dwordx4 v178, s[20:23], s48 offen offset:2048 nt lds
s_add_u32 s48, s48, 12288
v_mfma_f32_16x16x32_bf16 v[64:67], v[184:187], v[144:147], v[64:67]
v_mfma_f32_16x16x32_bf16 v[68:71], v[184:187], v[148:151], v[68:71]
v_mfma_f32_16x16x32_bf16 v[76:79], v[188:191], v[148:151], v[76:79]
ds_read_b64_tr_b16 v[184:185], v166 offset:128
ds_read_b64_tr_b16 v[186:187], v166 offset:1664
v_mfma_f32_16x16x32_bf16 v[72:75], v[188:191], v[144:147], v[72:75]
v_mfma_f32_16x16x32_bf16 v[80:83], v[192:195], v[144:147], v[80:83]
)"
R"(ds_read_b64_tr_b16 v[188:189], v167 offset:128
ds_read_b64_tr_b16 v[190:191], v167 offset:1664
v_mfma_f32_16x16x32_bf16 v[84:87], v[192:195], v[148:151], v[84:87]
v_mfma_f32_16x16x32_bf16 v[92:95], v[196:199], v[148:151], v[92:95]
ds_read_b64_tr_b16 v[192:193], v168 offset:128
ds_read_b64_tr_b16 v[194:195], v168 offset:1664
v_mfma_f32_16x16x32_bf16 v[88:91], v[196:199], v[144:147], v[88:91]
v_mfma_f32_16x16x32_bf16 v[0:3], v[200:203], v[152:155], v[0:3]
ds_read_b64_tr_b16 v[196:197], v169 offset:128
ds_read_b64_tr_b16 v[198:199], v169 offset:1664
v_mfma_f32_16x16x32_bf16 v[4:7], v[200:203], v[156:159], v[4:7]
v_mfma_f32_16x16x32_bf16 v[12:15], v[204:207], v[156:159], v[12:15]
ds_read_b64_tr_b16 v[200:201], v166 offset:256
ds_read_b64_tr_b16 v[202:203], v166 offset:1792
v_mfma_f32_16x16x32_bf16 v[8:11], v[204:207], v[152:155], v[8:11]
v_mfma_f32_16x16x32_bf16 v[16:19], v[208:211], v[152:155], v[16:19]
)"
R"(ds_read_b64_tr_b16 v[204:205], v167 offset:256
ds_read_b64_tr_b16 v[206:207], v167 offset:1792
v_mfma_f32_16x16x32_bf16 v[20:23], v[208:211], v[156:159], v[20:23]
v_mfma_f32_16x16x32_bf16 v[28:31], v[212:215], v[156:159], v[28:31]
ds_read_b64_tr_b16 v[208:209], v168 offset:256
ds_read_b64_tr_b16 v[210:211], v168 offset:1792
v_mfma_f32_16x16x32_bf16 v[24:27], v[212:215], v[152:155], v[24:27]
s_add_u32 m0, s56, 86016
s_nop 0
buffer_load_dwordx4 v179, s[24:27], s49 offen nt lds
ds_read_b64_tr_b16 v[212:213], v169 offset:256
s_waitcnt lgkmcnt(11)
ds_read_b64_tr_b16 v[214:215], v169 offset:1792
buffer_load_dwordx4 v180, s[24:27], s49 offen offset:1024 nt lds
buffer_load_dwordx4 v181, s[24:27], s49 offen offset:2048 nt lds
s_add_u32 s49, s49, 12288
)"
R"(s_waitcnt lgkmcnt(0)
s_cmp_eq_u32 s73, -1
s_cbranch_scc0 LpeB9_1_1
LplB9_1_1:
s_memrealtime s[88:89]
s_waitcnt lgkmcnt(0)
s_sub_u32 s89, s88, s94
s_cmp_lt_i32 s89, 0
s_cbranch_scc1 LplB9_1_1
s_add_u32 s95, s95, 95
s_sub_u32 s89, s88, s95
s_add_u32 s88, s88, 92
s_cmp_gt_i32 s89, 755
s_cselect_b32 s68, 20, 0
s_add_u32 s88, s88, s68
s_sub_u32 s89, s88, s95
)"
R"(s_cmp_gt_i32 s89, 0
s_cselect_b32 s94, s88, s95
LpeB9_1_1:
s_waitcnt vmcnt(12)
s_barrier
s_add_u32 m0, s56, 12288
s_nop 0
buffer_load_dwordx4 v176, s[20:23], s48 offen nt lds
buffer_load_dwordx4 v177, s[20:23], s48 offen offset:1024 nt lds
buffer_load_dwordx4 v178, s[20:23], s48 offen offset:2048 nt lds
s_add_u32 s48, s48, 12288
v_mfma_f32_16x16x32_bf16 v[32:35], v[184:187], v[152:155], v[32:35]
v_mfma_f32_16x16x32_bf16 v[36:39], v[184:187], v[156:159], v[36:39]
v_mfma_f32_16x16x32_bf16 v[44:47], v[188:191], v[156:159], v[44:47]
v_mfma_f32_16x16x32_bf16 v[40:43], v[188:191], v[152:155], v[40:43]
v_mfma_f32_16x16x32_bf16 v[48:51], v[192:195], v[152:155], v[48:51]
)"
R"(v_mfma_f32_16x16x32_bf16 v[52:55], v[192:195], v[156:159], v[52:55]
v_mfma_f32_16x16x32_bf16 v[60:63], v[196:199], v[156:159], v[60:63]
v_mfma_f32_16x16x32_bf16 v[56:59], v[196:199], v[152:155], v[56:59]
v_mfma_f32_16x16x32_bf16 v[64:67], v[200:203], v[152:155], v[64:67]
v_mfma_f32_16x16x32_bf16 v[68:71], v[200:203], v[156:159], v[68:71]
v_mfma_f32_16x16x32_bf16 v[76:79], v[204:207], v[156:159], v[76:79]
v_mfma_f32_16x16x32_bf16 v[72:75], v[204:207], v[152:155], v[72:75]
v_mfma_f32_16x16x32_bf16 v[80:83], v[208:211], v[152:155], v[80:83]
v_mfma_f32_16x16x32_bf16 v[84:87], v[208:211], v[156:159], v[84:87]
v_mfma_f32_16x16x32_bf16 v[92:95], v[212:215], v[156:159], v[92:95]
v_mfma_f32_16x16x32_bf16 v[88:91], v[212:215], v[152:155], v[88:91]
s_add_u32 s49, s49, 1179648
s_add_u32 m0, s56, 49152
s_nop 0
buffer_load_dwordx4 v179, s[24:27], s49 offen nt lds
buffer_load_dwordx4 v180, s[24:27], s49 offen offset:1024 nt lds
)"
R"(buffer_load_dwordx4 v181, s[24:27], s49 offen offset:2048 nt lds
s_add_u32 s49, s49, 12288
s_cmp_eq_u32 s73, -1
s_cbranch_scc0 LpeB9_1_2
LplB9_1_2:
s_memrealtime s[88:89]
s_waitcnt lgkmcnt(0)
s_sub_u32 s89, s88, s94
s_cmp_lt_i32 s89, 0
s_cbranch_scc1 LplB9_1_2
s_add_u32 s95, s95, 95
s_sub_u32 s89, s88, s95
s_add_u32 s88, s88, 92
s_cmp_gt_i32 s89, 755
s_cselect_b32 s68, 20, 0
s_add_u32 s88, s88, s68
)"
R"(s_sub_u32 s89, s88, s95
s_cmp_gt_i32 s89, 0
s_cselect_b32 s94, s88, s95
LpeB9_1_2:
s_waitcnt vmcnt(12)
s_barrier
s_add_u32 m0, s56, 24576
s_nop 0
buffer_load_dwordx4 v176, s[20:23], s48 offen nt lds
buffer_load_dwordx4 v177, s[20:23], s48 offen offset:1024 nt lds
buffer_load_dwordx4 v178, s[20:23], s48 offen offset:2048 nt lds
s_add_u32 s48, s48, 12288
s_add_u32 m0, s56, 61440
s_nop 0
buffer_load_dwordx4 v179, s[24:27], s49 offen nt lds
buffer_load_dwordx4 v180, s[24:27], s49 offen offset:1024 nt lds
)"
R"(buffer_load_dwordx4 v181, s[24:27], s49 offen offset:2048 nt lds
s_add_u32 s49, s49, 12288
v_mov_b32 v242, s57
v_pk_mul_f32 v[0:1], v[0:1], v[242:243] op_sel_hi:[1,0]
v_pk_mul_f32 v[2:3], v[2:3], v[242:243] op_sel_hi:[1,0]
v_pk_mul_f32 v[8:9], v[8:9], v[242:243] op_sel_hi:[1,0]
v_pk_mul_f32 v[10:11], v[10:11], v[242:243] op_sel_hi:[1,0]
v_pk_mul_f32 v[4:5], v[4:5], v[242:243] op_sel_hi:[1,0]
v_pk_mul_f32 v[6:7], v[6:7], v[242:243] op_sel_hi:[1,0]
v_pk_mul_f32 v[12:13], v[12:13], v[242:243] op_sel_hi:[1,0]
v_pk_mul_f32 v[14:15], v[14:15], v[242:243] op_sel_hi:[1,0]
v_cvt_pk_bf16_f32 v96, v0, v1
v_cvt_pk_bf16_f32 v97, v2, v3
v_cvt_pk_bf16_f32 v98, v8, v9
v_cvt_pk_bf16_f32 v99, v10, v11
v_cvt_pk_bf16_f32 v100, v4, v5
)"
R"(v_cvt_pk_bf16_f32 v101, v6, v7
v_cvt_pk_bf16_f32 v102, v12, v13
v_cvt_pk_bf16_f32 v103, v14, v15
v_permlane32_swap_b32 v96, v98
v_permlane32_swap_b32 v97, v99
v_permlane32_swap_b32 v100, v102
v_permlane32_swap_b32 v101, v103
v_permlane16_swap_b32 v96, v98
v_permlane16_swap_b32 v97, v99
v_permlane16_swap_b32 v100, v102
v_permlane16_swap_b32 v101, v103
v_pk_mul_f32 v[16:17], v[16:17], v[242:243] op_sel_hi:[1,0]
v_pk_mul_f32 v[18:19], v[18:19], v[242:243] op_sel_hi:[1,0]
v_pk_mul_f32 v[24:25], v[24:25], v[242:243] op_sel_hi:[1,0]
v_pk_mul_f32 v[26:27], v[26:27], v[242:243] op_sel_hi:[1,0]
v_pk_mul_f32 v[20:21], v[20:21], v[242:243] op_sel_hi:[1,0]
)"
R"(v_pk_mul_f32 v[22:23], v[22:23], v[242:243] op_sel_hi:[1,0]
v_pk_mul_f32 v[28:29], v[28:29], v[242:243] op_sel_hi:[1,0]
v_pk_mul_f32 v[30:31], v[30:31], v[242:243] op_sel_hi:[1,0]
v_cvt_pk_bf16_f32 v104, v16, v17
v_cvt_pk_bf16_f32 v105, v18, v19
v_cvt_pk_bf16_f32 v106, v24, v25
v_cvt_pk_bf16_f32 v107, v26, v27
v_cvt_pk_bf16_f32 v108, v20, v21
v_cvt_pk_bf16_f32 v109, v22, v23
v_cvt_pk_bf16_f32 v110, v28, v29
v_cvt_pk_bf16_f32 v111, v30, v31
v_permlane32_swap_b32 v104, v106
v_permlane32_swap_b32 v105, v107
v_permlane32_swap_b32 v108, v110
v_permlane32_swap_b32 v109, v111
v_permlane16_swap_b32 v104, v106
)"
R"(v_permlane16_swap_b32 v105, v107
v_permlane16_swap_b32 v108, v110
v_permlane16_swap_b32 v109, v111
v_pk_mul_f32 v[32:33], v[32:33], v[242:243] op_sel_hi:[1,0]
v_pk_mul_f32 v[34:35], v[34:35], v[242:243] op_sel_hi:[1,0]
v_pk_mul_f32 v[40:41], v[40:41], v[242:243] op_sel_hi:[1,0]
v_pk_mul_f32 v[42:43], v[42:43], v[242:243] op_sel_hi:[1,0]
v_pk_mul_f32 v[36:37], v[36:37], v[242:243] op_sel_hi:[1,0]
v_pk_mul_f32 v[38:39], v[38:39], v[242:243] op_sel_hi:[1,0]
v_pk_mul_f32 v[44:45], v[44:45], v[242:243] op_sel_hi:[1,0]
v_pk_mul_f32 v[46:47], v[46:47], v[242:243] op_sel_hi:[1,0]
v_cvt_pk_bf16_f32 v112, v32, v33
v_cvt_pk_bf16_f32 v113, v34, v35
v_cvt_pk_bf16_f32 v114, v40, v41
v_cvt_pk_bf16_f32 v115, v42, v43
v_cvt_pk_bf16_f32 v116, v36, v37
)"
R"(v_cvt_pk_bf16_f32 v117, v38, v39
v_cvt_pk_bf16_f32 v118, v44, v45
v_cvt_pk_bf16_f32 v119, v46, v47
v_permlane32_swap_b32 v112, v114
v_permlane32_swap_b32 v113, v115
v_permlane32_swap_b32 v116, v118
v_permlane32_swap_b32 v117, v119
v_permlane16_swap_b32 v112, v114
v_permlane16_swap_b32 v113, v115
v_permlane16_swap_b32 v116, v118
v_permlane16_swap_b32 v117, v119
v_pk_mul_f32 v[48:49], v[48:49], v[242:243] op_sel_hi:[1,0]
v_pk_mul_f32 v[50:51], v[50:51], v[242:243] op_sel_hi:[1,0]
v_pk_mul_f32 v[56:57], v[56:57], v[242:243] op_sel_hi:[1,0]
v_pk_mul_f32 v[58:59], v[58:59], v[242:243] op_sel_hi:[1,0]
v_pk_mul_f32 v[52:53], v[52:53], v[242:243] op_sel_hi:[1,0]
)"
R"(v_pk_mul_f32 v[54:55], v[54:55], v[242:243] op_sel_hi:[1,0]
v_pk_mul_f32 v[60:61], v[60:61], v[242:243] op_sel_hi:[1,0]
v_pk_mul_f32 v[62:63], v[62:63], v[242:243] op_sel_hi:[1,0]
v_cvt_pk_bf16_f32 v120, v48, v49
v_cvt_pk_bf16_f32 v121, v50, v51
v_cvt_pk_bf16_f32 v122, v56, v57
v_cvt_pk_bf16_f32 v123, v58, v59
v_cvt_pk_bf16_f32 v124, v52, v53
v_cvt_pk_bf16_f32 v125, v54, v55
v_cvt_pk_bf16_f32 v126, v60, v61
v_cvt_pk_bf16_f32 v127, v62, v63
v_permlane32_swap_b32 v120, v122
v_permlane32_swap_b32 v121, v123
v_permlane32_swap_b32 v124, v126
v_permlane32_swap_b32 v125, v127
v_permlane16_swap_b32 v120, v122
)"
R"(v_permlane16_swap_b32 v121, v123
v_permlane16_swap_b32 v124, v126
v_permlane16_swap_b32 v125, v127
v_pk_mul_f32 v[64:65], v[64:65], v[242:243] op_sel_hi:[1,0]
v_pk_mul_f32 v[66:67], v[66:67], v[242:243] op_sel_hi:[1,0]
v_pk_mul_f32 v[72:73], v[72:73], v[242:243] op_sel_hi:[1,0]
v_pk_mul_f32 v[74:75], v[74:75], v[242:243] op_sel_hi:[1,0]
v_pk_mul_f32 v[68:69], v[68:69], v[242:243] op_sel_hi:[1,0]
v_pk_mul_f32 v[70:71], v[70:71], v[242:243] op_sel_hi:[1,0]
v_pk_mul_f32 v[76:77], v[76:77], v[242:243] op_sel_hi:[1,0]
v_pk_mul_f32 v[78:79], v[78:79], v[242:243] op_sel_hi:[1,0]
v_cvt_pk_bf16_f32 v128, v64, v65
v_cvt_pk_bf16_f32 v129, v66, v67
v_cvt_pk_bf16_f32 v130, v72, v73
v_cvt_pk_bf16_f32 v131, v74, v75
v_cvt_pk_bf16_f32 v132, v68, v69
)"
R"(v_cvt_pk_bf16_f32 v133, v70, v71
v_cvt_pk_bf16_f32 v134, v76, v77
v_cvt_pk_bf16_f32 v135, v78, v79
v_permlane32_swap_b32 v128, v130
v_permlane32_swap_b32 v129, v131
v_permlane32_swap_b32 v132, v134
v_permlane32_swap_b32 v133, v135
v_permlane16_swap_b32 v128, v130
v_permlane16_swap_b32 v129, v131
v_permlane16_swap_b32 v132, v134
v_permlane16_swap_b32 v133, v135
v_pk_mul_f32 v[80:81], v[80:81], v[242:243] op_sel_hi:[1,0]
v_pk_mul_f32 v[82:83], v[82:83], v[242:243] op_sel_hi:[1,0]
v_pk_mul_f32 v[88:89], v[88:89], v[242:243] op_sel_hi:[1,0]
v_pk_mul_f32 v[90:91], v[90:91], v[242:243] op_sel_hi:[1,0]
v_pk_mul_f32 v[84:85], v[84:85], v[242:243] op_sel_hi:[1,0]
)"
R"(v_pk_mul_f32 v[86:87], v[86:87], v[242:243] op_sel_hi:[1,0]
v_pk_mul_f32 v[92:93], v[92:93], v[242:243] op_sel_hi:[1,0]
v_pk_mul_f32 v[94:95], v[94:95], v[242:243] op_sel_hi:[1,0]
v_cvt_pk_bf16_f32 v136, v80, v81
v_cvt_pk_bf16_f32 v137, v82, v83
v_cvt_pk_bf16_f32 v138, v88, v89
v_cvt_pk_bf16_f32 v139, v90, v91
v_cvt_pk_bf16_f32 v140, v84, v85
v_cvt_pk_bf16_f32 v141, v86, v87
v_cvt_pk_bf16_f32 v142, v92, v93
v_cvt_pk_bf16_f32 v143, v94, v95
v_permlane32_swap_b32 v136, v138
v_permlane32_swap_b32 v137, v139
v_permlane32_swap_b32 v140, v142
v_permlane32_swap_b32 v141, v143
v_permlane16_swap_b32 v136, v138
)"
R"(v_permlane16_swap_b32 v137, v139
v_permlane16_swap_b32 v140, v142
v_permlane16_swap_b32 v141, v143
s_cmp_eq_u32 s73, -1
s_cbranch_scc0 LpeB9_1_3
LplB9_1_3:
s_memrealtime s[88:89]
s_waitcnt lgkmcnt(0)
s_sub_u32 s89, s88, s94
s_cmp_lt_i32 s89, 0
s_cbranch_scc1 LplB9_1_3
s_add_u32 s95, s95, 95
s_sub_u32 s89, s88, s95
s_add_u32 s88, s88, 92
s_cmp_gt_i32 s89, 755
s_cselect_b32 s68, 20, 0
)"
R"(s_add_u32 s88, s88, s68
s_sub_u32 s89, s88, s95
s_cmp_gt_i32 s89, 0
s_cselect_b32 s94, s88, s95
LpeB9_1_3:
s_waitcnt vmcnt(12)
s_barrier
s_add_u32 m0, s56, 36864
s_nop 0
buffer_load_dwordx4 v176, s[20:23], s48 offen nt lds
buffer_load_dwordx4 v177, s[20:23], s48 offen offset:1024 nt lds
buffer_load_dwordx4 v178, s[20:23], s48 offen offset:2048 nt lds
s_add_u32 s48, s48, 12288
s_add_u32 m0, s56, 73728
s_nop 0
buffer_load_dwordx4 v179, s[24:27], s49 offen nt lds
)"
R"(buffer_load_dwordx4 v180, s[24:27], s49 offen offset:1024 nt lds
buffer_load_dwordx4 v181, s[24:27], s49 offen offset:2048 nt lds
s_add_u32 s49, s49, 12288
ds_read_b128 v[184:187], v232 offset:0
ds_read_b128 v[188:191], v232 offset:6144
ds_read_b128 v[192:195], v233 offset:0
ds_read_b128 v[196:199], v233 offset:6144
ds_read_b128 v[200:203], v232 offset:128
s_waitcnt lgkmcnt(4)
v_mfma_f32_16x16x32_bf16 v[144:147], v[184:187], v[96:99], 0
v_mfma_f32_16x16x32_bf16 v[152:155], v[184:187], v[100:103], 0
ds_read_b128 v[184:187], v232 offset:6272
s_waitcnt lgkmcnt(4)
v_mfma_f32_16x16x32_bf16 v[156:159], v[188:191], v[100:103], 0
v_mfma_f32_16x16x32_bf16 v[148:151], v[188:191], v[96:99], 0
ds_read_b128 v[188:191], v233 offset:128
)"
R"(s_waitcnt lgkmcnt(4)
v_mfma_f32_16x16x32_bf16 v[144:147], v[192:195], v[104:107], v[144:147]
v_mfma_f32_16x16x32_bf16 v[152:155], v[192:195], v[108:111], v[152:155]
ds_read_b128 v[192:195], v233 offset:6272
s_waitcnt lgkmcnt(4)
v_mfma_f32_16x16x32_bf16 v[156:159], v[196:199], v[108:111], v[156:159]
v_mfma_f32_16x16x32_bf16 v[148:151], v[196:199], v[104:107], v[148:151]
ds_read_b128 v[196:199], v232 offset:256
s_waitcnt lgkmcnt(4)
v_mfma_f32_16x16x32_bf16 v[144:147], v[200:203], v[112:115], v[144:147]
v_mfma_f32_16x16x32_bf16 v[152:155], v[200:203], v[116:119], v[152:155]
ds_read_b128 v[200:203], v232 offset:6400
s_waitcnt lgkmcnt(4)
v_mfma_f32_16x16x32_bf16 v[156:159], v[184:187], v[116:119], v[156:159]
v_mfma_f32_16x16x32_bf16 v[148:151], v[184:187], v[112:115], v[148:151]
ds_read_b128 v[184:187], v233 offset:256
)"
R"(s_waitcnt lgkmcnt(4)
v_mfma_f32_16x16x32_bf16 v[144:147], v[188:191], v[120:123], v[144:147]
v_mfma_f32_16x16x32_bf16 v[152:155], v[188:191], v[124:127], v[152:155]
ds_read_b128 v[188:191], v233 offset:6400
s_waitcnt lgkmcnt(4)
v_mfma_f32_16x16x32_bf16 v[156:159], v[192:195], v[124:127], v[156:159]
v_mfma_f32_16x16x32_bf16 v[148:151], v[192:195], v[120:123], v[148:151]
s_waitcnt lgkmcnt(3)
v_mfma_f32_16x16x32_bf16 v[144:147], v[196:199], v[128:131], v[144:147]
v_mfma_f32_16x16x32_bf16 v[152:155], v[196:199], v[132:135], v[152:155]
s_waitcnt lgkmcnt(2)
v_mfma_f32_16x16x32_bf16 v[156:159], v[200:203], v[132:135], v[156:159]
v_mfma_f32_16x16x32_bf16 v[148:151], v[200:203], v[128:131], v[148:151]
s_waitcnt lgkmcnt(1)
v_mfma_f32_16x16x32_bf16 v[144:147], v[184:187], v[136:139], v[144:147]
v_mfma_f32_16x16x32_bf16 v[152:155], v[184:187], v[140:143], v[152:155]
)"
R"(s_waitcnt lgkmcnt(0)
v_mfma_f32_16x16x32_bf16 v[156:159], v[188:191], v[140:143], v[156:159]
v_mfma_f32_16x16x32_bf16 v[148:151], v[188:191], v[136:139], v[148:151]
v_mov_b32 v238, 0
v_mov_b32 v239, 0
v_mov_b32 v240, 0
v_mov_b32 v241, 0
v_max3_f32 v242, v144, v145, v146
s_nop 2
v_max3_f32 v242, v242, v147, v148
v_max3_f32 v242, v242, v149, v150
v_max_f32 v242, v242, v151
v_max3_f32 v243, v152, v153, v154
v_max3_f32 v243, v243, v155, v156
v_max3_f32 v243, v243, v157, v158
v_max_f32 v243, v243, v159
)"
R"(v_mov_b32 v212, v242
v_mov_b32 v213, v243
s_nop 0
v_permlane32_swap_b32 v242, v212
v_permlane32_swap_b32 v243, v213
v_max_f32 v242, v242, v212
v_max_f32 v243, v243, v213
v_mov_b32 v212, v242
v_mov_b32 v213, v243
s_nop 0
v_permlane16_swap_b32 v242, v212
v_permlane16_swap_b32 v243, v213
v_max_f32 v242, v242, v212
v_max_f32 v243, v243, v213
v_sub_f32 v224, 0, v242
v_sub_f32 v225, 0, v242
)"
R"(v_sub_f32 v226, 0, v242
v_sub_f32 v227, 0, v242
v_sub_f32 v228, 0, v243
v_sub_f32 v229, 0, v243
v_sub_f32 v230, 0, v243
v_sub_f32 v231, 0, v243
v_sub_f32 v144, v144, v242
v_sub_f32 v145, v145, v242
v_sub_f32 v146, v146, v242
v_sub_f32 v147, v147, v242
v_sub_f32 v148, v148, v242
v_sub_f32 v149, v149, v242
v_sub_f32 v150, v150, v242
v_sub_f32 v151, v151, v242
v_sub_f32 v152, v152, v243
v_sub_f32 v153, v153, v243
)"
R"(v_sub_f32 v154, v154, v243
v_sub_f32 v155, v155, v243
v_sub_f32 v156, v156, v243
v_sub_f32 v157, v157, v243
v_sub_f32 v158, v158, v243
v_sub_f32 v159, v159, v243
s_setprio 0
s_waitcnt vmcnt(12)
s_barrier
ds_read_b128 v[184:187], v232 offset:12288
ds_read_b128 v[188:191], v232 offset:18432
ds_read_b128 v[192:195], v233 offset:12288
ds_read_b128 v[196:199], v233 offset:18432
ds_read_b128 v[200:203], v232 offset:12416
s_waitcnt lgkmcnt(4)
v_mfma_f32_16x16x32_bf16 v[160:163], v[184:187], v[96:99], v[224:227]
)"
R"(v_exp_f32 v144, v144
v_mfma_f32_16x16x32_bf16 v[168:171], v[184:187], v[100:103], v[228:231]
s_waitcnt lgkmcnt(3)
v_exp_f32 v145, v145
v_mfma_f32_16x16x32_bf16 v[172:175], v[188:191], v[100:103], v[228:231]
v_exp_f32 v146, v146
v_mfma_f32_16x16x32_bf16 v[164:167], v[188:191], v[96:99], v[224:227]
s_waitcnt lgkmcnt(2)
ds_read_b128 v[184:187], v232 offset:18560
ds_read_b128 v[188:191], v233 offset:12416
v_mfma_f32_16x16x32_bf16 v[160:163], v[192:195], v[104:107], v[160:163]
v_exp_f32 v147, v147
v_mfma_f32_16x16x32_bf16 v[168:171], v[192:195], v[108:111], v[168:171]
s_waitcnt lgkmcnt(3)
ds_read_b64_tr_b16 v[204:205], v234 offset:0
v_cvt_pk_bf16_f32 v176, v144, v145
)"
R"(v_cvt_pk_bf16_f32 v177, v146, v147
v_mfma_f32_16x16x32_bf16 v[172:175], v[196:199], v[108:111], v[172:175]
v_exp_f32 v148, v148
v_mfma_f32_16x16x32_bf16 v[164:167], v[196:199], v[104:107], v[164:167]
s_waitcnt lgkmcnt(3)
ds_read_b128 v[192:195], v233 offset:18560
ds_read_b128 v[196:199], v232 offset:12544
v_mfma_f32_16x16x32_bf16 v[160:163], v[200:203], v[112:115], v[160:163]
v_exp_f32 v149, v149
v_mfma_f32_16x16x32_bf16 v[168:171], v[200:203], v[116:119], v[168:171]
s_waitcnt lgkmcnt(4)
ds_read_b64_tr_b16 v[206:207], v234 offset:6144
v_exp_f32 v150, v150
v_mfma_f32_16x16x32_bf16 v[172:175], v[184:187], v[116:119], v[172:175]
v_exp_f32 v151, v151
v_mfma_f32_16x16x32_bf16 v[164:167], v[184:187], v[112:115], v[164:167]
)"
R"(s_waitcnt lgkmcnt(4)
ds_read_b128 v[200:203], v232 offset:18688
ds_read_b128 v[184:187], v233 offset:12544
v_mfma_f32_16x16x32_bf16 v[160:163], v[188:191], v[120:123], v[160:163]
v_exp_f32 v152, v152
v_mfma_f32_16x16x32_bf16 v[168:171], v[188:191], v[124:127], v[168:171]
s_waitcnt lgkmcnt(4)
ds_read_b64_tr_b16 v[208:209], v235 offset:0
v_cvt_pk_bf16_f32 v178, v148, v149
v_cvt_pk_bf16_f32 v179, v150, v151
v_mfma_f32_16x16x32_bf16 v[172:175], v[192:195], v[124:127], v[172:175]
v_exp_f32 v153, v153
v_mfma_f32_16x16x32_bf16 v[164:167], v[192:195], v[120:123], v[164:167]
s_waitcnt lgkmcnt(4)
ds_read_b128 v[188:191], v233 offset:18688
ds_read_b64_tr_b16 v[210:211], v235 offset:6144
)"
R"(v_mfma_f32_16x16x32_bf16 v[160:163], v[196:199], v[128:131], v[160:163]
v_exp_f32 v154, v154
v_mfma_f32_16x16x32_bf16 v[168:171], v[196:199], v[132:135], v[168:171]
s_waitcnt lgkmcnt(4)
ds_read_b64_tr_b16 v[212:213], v236 offset:0
v_exp_f32 v155, v155
v_mfma_f32_16x16x32_bf16 v[172:175], v[200:203], v[132:135], v[172:175]
v_exp_f32 v156, v156
v_mfma_f32_16x16x32_bf16 v[164:167], v[200:203], v[128:131], v[164:167]
s_waitcnt lgkmcnt(4)
ds_read_b64_tr_b16 v[214:215], v236 offset:6144
v_cvt_pk_bf16_f32 v180, v152, v153
v_cvt_pk_bf16_f32 v181, v154, v155
v_mfma_f32_16x16x32_bf16 v[160:163], v[184:187], v[136:139], v[160:163]
v_exp_f32 v157, v157
v_mfma_f32_16x16x32_bf16 v[168:171], v[184:187], v[140:143], v[168:171]
)"
R"(s_waitcnt lgkmcnt(3)
ds_read_b64_tr_b16 v[216:217], v237 offset:0
v_exp_f32 v158, v158
v_mfma_f32_16x16x32_bf16 v[172:175], v[188:191], v[140:143], v[172:175]
v_exp_f32 v159, v159
v_mfma_f32_16x16x32_bf16 v[164:167], v[188:191], v[136:139], v[164:167]
ds_read_b64_tr_b16 v[218:219], v237 offset:6144
v_cvt_pk_bf16_f32 v182, v156, v157
v_cvt_pk_bf16_f32 v183, v158, v159
v_mfma_f32_16x16x32_bf16 v[0:3], v[204:207], v[176:179], 0
ds_read_b64_tr_b16 v[220:221], v234 offset:128
ds_read_b64_tr_b16 v[222:223], v234 offset:6272
v_mfma_f32_16x16x32_bf16 v[4:7], v[204:207], v[180:183], 0
s_waitcnt lgkmcnt(6)
ds_read_b64_tr_b16 v[184:185], v235 offset:128
ds_read_b64_tr_b16 v[186:187], v235 offset:6272
)"
R"(v_mfma_f32_16x16x32_bf16 v[12:15], v[208:211], v[180:183], 0
ds_read_b64_tr_b16 v[188:189], v236 offset:128
v_add_f32 v238, v238, v144
v_mfma_f32_16x16x32_bf16 v[8:11], v[208:211], v[176:179], 0
s_waitcnt lgkmcnt(7)
ds_read_b64_tr_b16 v[190:191], v236 offset:6272
v_add_f32 v240, v240, v152
v_mfma_f32_16x16x32_bf16 v[16:19], v[212:215], v[176:179], 0
ds_read_b64_tr_b16 v[192:193], v237 offset:128
v_max3_f32 v242, v160, v161, v162
v_mfma_f32_16x16x32_bf16 v[20:23], v[212:215], v[180:183], 0
s_waitcnt lgkmcnt(7)
ds_read_b64_tr_b16 v[194:195], v237 offset:6272
v_max3_f32 v243, v167, v168, v169
v_mfma_f32_16x16x32_bf16 v[28:31], v[216:219], v[180:183], 0
ds_read_b64_tr_b16 v[196:197], v234 offset:256
)"
R"(v_max3_f32 v242, v242, v163, v164
v_mfma_f32_16x16x32_bf16 v[24:27], v[216:219], v[176:179], 0
s_waitcnt lgkmcnt(7)
ds_read_b64_tr_b16 v[198:199], v234 offset:6400
ds_read_b64_tr_b16 v[200:201], v235 offset:256
v_mfma_f32_16x16x32_bf16 v[32:35], v[220:223], v[176:179], 0
ds_read_b64_tr_b16 v[202:203], v235 offset:6400
ds_read_b64_tr_b16 v[204:205], v236 offset:256
v_mfma_f32_16x16x32_bf16 v[36:39], v[220:223], v[180:183], 0
s_waitcnt lgkmcnt(9)
ds_read_b64_tr_b16 v[206:207], v236 offset:6400
ds_read_b64_tr_b16 v[208:209], v237 offset:256
v_mfma_f32_16x16x32_bf16 v[44:47], v[184:187], v[180:183], 0
ds_read_b64_tr_b16 v[210:211], v237 offset:6400
v_max3_f32 v243, v243, v170, v171
v_mfma_f32_16x16x32_bf16 v[40:43], v[184:187], v[176:179], 0
)"
R"(s_waitcnt lgkmcnt(10)
v_max3_f32 v242, v242, v165, v166
v_max3_f32 v243, v243, v172, v173
v_mfma_f32_16x16x32_bf16 v[48:51], v[188:191], v[176:179], 0
v_max3_f32 v242, v242, v174, v175
v_add_f32 v239, v239, v145
v_mfma_f32_16x16x32_bf16 v[52:55], v[188:191], v[180:183], 0
s_waitcnt lgkmcnt(8)
v_max_f32 v242, v242, v243
v_add_f32 v241, v241, v153
v_mfma_f32_16x16x32_bf16 v[60:63], v[192:195], v[180:183], 0
v_cmp_lt_f32 vcc, 0x41000000, v242
v_add_f32 v238, v238, v146
v_mfma_f32_16x16x32_bf16 v[56:59], v[192:195], v[176:179], 0
s_waitcnt lgkmcnt(0)
v_add_f32 v240, v240, v154
)"
R"(v_add_f32 v239, v239, v147
s_waitcnt vmcnt(6)
s_barrier
v_mfma_f32_16x16x32_bf16 v[64:67], v[196:199], v[176:179], 0
ds_read_b128 v[184:187], v232 offset:24576
ds_read_b128 v[188:191], v232 offset:30720
v_mfma_f32_16x16x32_bf16 v[68:71], v[196:199], v[180:183], 0
ds_read_b128 v[192:195], v233 offset:24576
v_add_f32 v241, v241, v155
v_mfma_f32_16x16x32_bf16 v[76:79], v[200:203], v[180:183], 0
ds_read_b128 v[196:199], v233 offset:30720
v_add_f32 v238, v238, v148
v_mfma_f32_16x16x32_bf16 v[72:75], v[200:203], v[176:179], 0
ds_read_b128 v[200:203], v232 offset:24704
v_add_f32 v240, v240, v156
v_mfma_f32_16x16x32_bf16 v[80:83], v[204:207], v[176:179], 0
)"
R"(v_add_f32 v239, v239, v149
v_add_f32 v241, v241, v157
v_mfma_f32_16x16x32_bf16 v[84:87], v[204:207], v[180:183], 0
v_add_f32 v238, v238, v150
v_add_f32 v240, v240, v158
v_mfma_f32_16x16x32_bf16 v[92:95], v[208:211], v[180:183], 0
v_add_f32 v239, v239, v151
v_add_f32 v241, v241, v159
v_mfma_f32_16x16x32_bf16 v[88:91], v[208:211], v[176:179], 0
s_waitcnt lgkmcnt(4)
s_cbranch_vccz LkB10_0
s_getpc_b64 s[80:81]
s_branch Lrare_1
LkB10_0:
v_mfma_f32_16x16x32_bf16 v[144:147], v[184:187], v[96:99], v[224:227]
v_exp_f32 v160, v160
)"
R"(v_mfma_f32_16x16x32_bf16 v[152:155], v[184:187], v[100:103], v[228:231]
s_waitcnt lgkmcnt(3)
v_exp_f32 v161, v161
v_mfma_f32_16x16x32_bf16 v[156:159], v[188:191], v[100:103], v[228:231]
v_exp_f32 v162, v162
v_mfma_f32_16x16x32_bf16 v[148:151], v[188:191], v[96:99], v[224:227]
s_waitcnt lgkmcnt(2)
ds_read_b128 v[184:187], v232 offset:30848
ds_read_b128 v[188:191], v233 offset:24704
v_mfma_f32_16x16x32_bf16 v[144:147], v[192:195], v[104:107], v[144:147]
v_exp_f32 v163, v163
v_mfma_f32_16x16x32_bf16 v[152:155], v[192:195], v[108:111], v[152:155]
s_waitcnt lgkmcnt(3)
ds_read_b64_tr_b16 v[204:205], v234 offset:12288
v_cvt_pk_bf16_f32 v176, v160, v161
v_cvt_pk_bf16_f32 v177, v162, v163
)"
R"(v_mfma_f32_16x16x32_bf16 v[156:159], v[196:199], v[108:111], v[156:159]
v_exp_f32 v164, v164
v_mfma_f32_16x16x32_bf16 v[148:151], v[196:199], v[104:107], v[148:151]
s_waitcnt lgkmcnt(3)
ds_read_b128 v[192:195], v233 offset:30848
ds_read_b128 v[196:199], v232 offset:24832
v_mfma_f32_16x16x32_bf16 v[144:147], v[200:203], v[112:115], v[144:147]
v_exp_f32 v165, v165
v_mfma_f32_16x16x32_bf16 v[152:155], v[200:203], v[116:119], v[152:155]
s_waitcnt lgkmcnt(4)
ds_read_b64_tr_b16 v[206:207], v234 offset:18432
v_exp_f32 v166, v166
v_mfma_f32_16x16x32_bf16 v[156:159], v[184:187], v[116:119], v[156:159]
v_exp_f32 v167, v167
v_mfma_f32_16x16x32_bf16 v[148:151], v[184:187], v[112:115], v[148:151]
s_waitcnt lgkmcnt(4)
)"
R"(ds_read_b128 v[200:203], v232 offset:30976
ds_read_b128 v[184:187], v233 offset:24832
v_mfma_f32_16x16x32_bf16 v[144:147], v[188:191], v[120:123], v[144:147]
v_exp_f32 v168, v168
v_mfma_f32_16x16x32_bf16 v[152:155], v[188:191], v[124:127], v[152:155]
s_waitcnt lgkmcnt(4)
ds_read_b64_tr_b16 v[208:209], v235 offset:12288
v_cvt_pk_bf16_f32 v178, v164, v165
v_cvt_pk_bf16_f32 v179, v166, v167
v_mfma_f32_16x16x32_bf16 v[156:159], v[192:195], v[124:127], v[156:159]
v_exp_f32 v169, v169
v_mfma_f32_16x16x32_bf16 v[148:151], v[192:195], v[120:123], v[148:151]
s_waitcnt lgkmcnt(4)
ds_read_b128 v[188:191], v233 offset:30976
ds_read_b64_tr_b16 v[210:211], v235 offset:18432
v_mfma_f32_16x16x32_bf16 v[144:147], v[196:199], v[128:131], v[144:147]
)"
R"(v_exp_f32 v170, v170
v_mfma_f32_16x16x32_bf16 v[152:155], v[196:199], v[132:135], v[152:155]
s_waitcnt lgkmcnt(4)
ds_read_b64_tr_b16 v[212:213], v236 offset:12288
v_exp_f32 v171, v171
v_mfma_f32_16x16x32_bf16 v[156:159], v[200:203], v[132:135], v[156:159]
v_exp_f32 v172, v172
v_mfma_f32_16x16x32_bf16 v[148:151], v[200:203], v[128:131], v[148:151]
s_waitcnt lgkmcnt(4)
ds_read_b64_tr_b16 v[214:215], v236 offset:18432
v_cvt_pk_bf16_f32 v180, v168, v169
v_cvt_pk_bf16_f32 v181, v170, v171
v_mfma_f32_16x16x32_bf16 v[144:147], v[184:187], v[136:139], v[144:147]
v_exp_f32 v173, v173
v_mfma_f32_16x16x32_bf16 v[152:155], v[184:187], v[140:143], v[152:155]
s_waitcnt lgkmcnt(3)
)"
R"(ds_read_b64_tr_b16 v[216:217], v237 offset:12288
v_exp_f32 v174, v174
v_mfma_f32_16x16x32_bf16 v[156:159], v[188:191], v[140:143], v[156:159]
v_exp_f32 v175, v175
v_mfma_f32_16x16x32_bf16 v[148:151], v[188:191], v[136:139], v[148:151]
ds_read_b64_tr_b16 v[218:219], v237 offset:18432
v_cvt_pk_bf16_f32 v182, v172, v173
v_cvt_pk_bf16_f32 v183, v174, v175
v_mfma_f32_16x16x32_bf16 v[0:3], v[204:207], v[176:179], v[0:3]
ds_read_b64_tr_b16 v[220:221], v234 offset:12416
ds_read_b64_tr_b16 v[222:223], v234 offset:18560
v_mfma_f32_16x16x32_bf16 v[4:7], v[204:207], v[180:183], v[4:7]
s_waitcnt lgkmcnt(6)
ds_read_b64_tr_b16 v[184:185], v235 offset:12416
ds_read_b64_tr_b16 v[186:187], v235 offset:18560
v_mfma_f32_16x16x32_bf16 v[12:15], v[208:211], v[180:183], v[12:15]
)"
R"(ds_read_b64_tr_b16 v[188:189], v236 offset:12416
v_add_f32 v238, v238, v160
v_mfma_f32_16x16x32_bf16 v[8:11], v[208:211], v[176:179], v[8:11]
s_waitcnt lgkmcnt(7)
ds_read_b64_tr_b16 v[190:191], v236 offset:18560
v_add_f32 v240, v240, v168
v_mfma_f32_16x16x32_bf16 v[16:19], v[212:215], v[176:179], v[16:19]
ds_read_b64_tr_b16 v[192:193], v237 offset:12416
v_max3_f32 v242, v144, v145, v146
v_mfma_f32_16x16x32_bf16 v[20:23], v[212:215], v[180:183], v[20:23]
s_waitcnt lgkmcnt(7)
ds_read_b64_tr_b16 v[194:195], v237 offset:18560
v_max3_f32 v243, v151, v152, v153
v_mfma_f32_16x16x32_bf16 v[28:31], v[216:219], v[180:183], v[28:31]
ds_read_b64_tr_b16 v[196:197], v234 offset:12544
v_max3_f32 v242, v242, v147, v148
)"
R"(v_mfma_f32_16x16x32_bf16 v[24:27], v[216:219], v[176:179], v[24:27]
s_waitcnt lgkmcnt(7)
ds_read_b64_tr_b16 v[198:199], v234 offset:18688
ds_read_b64_tr_b16 v[200:201], v235 offset:12544
v_mfma_f32_16x16x32_bf16 v[32:35], v[220:223], v[176:179], v[32:35]
ds_read_b64_tr_b16 v[202:203], v235 offset:18688
ds_read_b64_tr_b16 v[204:205], v236 offset:12544
v_mfma_f32_16x16x32_bf16 v[36:39], v[220:223], v[180:183], v[36:39]
s_waitcnt lgkmcnt(9)
ds_read_b64_tr_b16 v[206:207], v236 offset:18688
ds_read_b64_tr_b16 v[208:209], v237 offset:12544
v_mfma_f32_16x16x32_bf16 v[44:47], v[184:187], v[180:183], v[44:47]
ds_read_b64_tr_b16 v[210:211], v237 offset:18688
v_max3_f32 v243, v243, v154, v155
v_mfma_f32_16x16x32_bf16 v[40:43], v[184:187], v[176:179], v[40:43]
s_waitcnt lgkmcnt(10)
)"
R"(v_max3_f32 v242, v242, v149, v150
v_max3_f32 v243, v243, v156, v157
v_mfma_f32_16x16x32_bf16 v[48:51], v[188:191], v[176:179], v[48:51]
v_max3_f32 v242, v242, v158, v159
v_add_f32 v239, v239, v161
v_mfma_f32_16x16x32_bf16 v[52:55], v[188:191], v[180:183], v[52:55]
s_waitcnt lgkmcnt(8)
v_max_f32 v242, v242, v243
v_add_f32 v241, v241, v169
v_mfma_f32_16x16x32_bf16 v[60:63], v[192:195], v[180:183], v[60:63]
v_cmp_lt_f32 vcc, 0x41000000, v242
v_add_f32 v238, v238, v162
v_mfma_f32_16x16x32_bf16 v[56:59], v[192:195], v[176:179], v[56:59]
s_waitcnt lgkmcnt(0)
v_add_f32 v240, v240, v170
v_add_f32 v239, v239, v163
)"
R"(s_waitcnt vmcnt(0)
s_barrier
v_mfma_f32_16x16x32_bf16 v[64:67], v[196:199], v[176:179], v[64:67]
ds_read_b128 v[184:187], v232 offset:36864
ds_read_b128 v[188:191], v232 offset:43008
v_mfma_f32_16x16x32_bf16 v[68:71], v[196:199], v[180:183], v[68:71]
ds_read_b128 v[192:195], v233 offset:36864
v_add_f32 v241, v241, v171
v_mfma_f32_16x16x32_bf16 v[76:79], v[200:203], v[180:183], v[76:79]
ds_read_b128 v[196:199], v233 offset:43008
v_add_f32 v238, v238, v164
v_mfma_f32_16x16x32_bf16 v[72:75], v[200:203], v[176:179], v[72:75]
ds_read_b128 v[200:203], v232 offset:36992
v_add_f32 v240, v240, v172
v_mfma_f32_16x16x32_bf16 v[80:83], v[204:207], v[176:179], v[80:83]
v_add_f32 v239, v239, v165
)"
R"(v_add_f32 v241, v241, v173
v_mfma_f32_16x16x32_bf16 v[84:87], v[204:207], v[180:183], v[84:87]
v_add_f32 v238, v238, v166
v_add_f32 v240, v240, v174
v_mfma_f32_16x16x32_bf16 v[92:95], v[208:211], v[180:183], v[92:95]
v_add_f32 v239, v239, v167
v_add_f32 v241, v241, v175
v_mfma_f32_16x16x32_bf16 v[88:91], v[208:211], v[176:179], v[88:91]
s_waitcnt lgkmcnt(4)
s_cbranch_vccz LkB10_1
s_getpc_b64 s[80:81]
s_branch Lrare_0
LkB10_1:
s_cmp_eq_u32 s59, 4
s_cbranch_scc0 Lw4_3
s_mov_b64 exec, 0xfff
)"
R"(buffer_load_dword v251, v249, s[40:43], 0 offen sc1
s_mov_b64 exec, -1
Lw4_3:
v_mfma_f32_16x16x32_bf16 v[160:163], v[184:187], v[96:99], v[224:227]
v_exp_f32 v144, v144
v_mfma_f32_16x16x32_bf16 v[168:171], v[184:187], v[100:103], v[228:231]
s_waitcnt lgkmcnt(3)
v_exp_f32 v145, v145
v_mfma_f32_16x16x32_bf16 v[172:175], v[188:191], v[100:103], v[228:231]
v_exp_f32 v146, v146
v_mfma_f32_16x16x32_bf16 v[164:167], v[188:191], v[96:99], v[224:227]
s_waitcnt lgkmcnt(2)
ds_read_b128 v[184:187], v232 offset:43136
ds_read_b128 v[188:191], v233 offset:36992
v_mfma_f32_16x16x32_bf16 v[160:163], v[192:195], v[104:107], v[160:163]
v_exp_f32 v147, v147
)"
R"(v_mfma_f32_16x16x32_bf16 v[168:171], v[192:195], v[108:111], v[168:171]
s_waitcnt lgkmcnt(3)
ds_read_b64_tr_b16 v[204:205], v234 offset:24576
v_cvt_pk_bf16_f32 v176, v144, v145
v_cvt_pk_bf16_f32 v177, v146, v147
v_mfma_f32_16x16x32_bf16 v[172:175], v[196:199], v[108:111], v[172:175]
v_exp_f32 v148, v148
v_mfma_f32_16x16x32_bf16 v[164:167], v[196:199], v[104:107], v[164:167]
s_waitcnt lgkmcnt(3)
ds_read_b128 v[192:195], v233 offset:43136
ds_read_b128 v[196:199], v232 offset:37120
v_mfma_f32_16x16x32_bf16 v[160:163], v[200:203], v[112:115], v[160:163]
v_exp_f32 v149, v149
v_mfma_f32_16x16x32_bf16 v[168:171], v[200:203], v[116:119], v[168:171]
s_waitcnt lgkmcnt(4)
ds_read_b64_tr_b16 v[206:207], v234 offset:30720
)"
R"(v_exp_f32 v150, v150
v_mfma_f32_16x16x32_bf16 v[172:175], v[184:187], v[116:119], v[172:175]
v_exp_f32 v151, v151
v_mfma_f32_16x16x32_bf16 v[164:167], v[184:187], v[112:115], v[164:167]
s_waitcnt lgkmcnt(4)
ds_read_b128 v[200:203], v232 offset:43264
ds_read_b128 v[184:187], v233 offset:37120
v_mfma_f32_16x16x32_bf16 v[160:163], v[188:191], v[120:123], v[160:163]
v_exp_f32 v152, v152
v_mfma_f32_16x16x32_bf16 v[168:171], v[188:191], v[124:127], v[168:171]
s_waitcnt lgkmcnt(4)
ds_read_b64_tr_b16 v[208:209], v235 offset:24576
v_cvt_pk_bf16_f32 v178, v148, v149
v_cvt_pk_bf16_f32 v179, v150, v151
v_mfma_f32_16x16x32_bf16 v[172:175], v[192:195], v[124:127], v[172:175]
v_exp_f32 v153, v153
)"
R"(v_mfma_f32_16x16x32_bf16 v[164:167], v[192:195], v[120:123], v[164:167]
s_waitcnt lgkmcnt(4)
ds_read_b128 v[188:191], v233 offset:43264
ds_read_b64_tr_b16 v[210:211], v235 offset:30720
v_mfma_f32_16x16x32_bf16 v[160:163], v[196:199], v[128:131], v[160:163]
v_exp_f32 v154, v154
v_mfma_f32_16x16x32_bf16 v[168:171], v[196:199], v[132:135], v[168:171]
s_waitcnt lgkmcnt(4)
ds_read_b64_tr_b16 v[212:213], v236 offset:24576
v_exp_f32 v155, v155
v_mfma_f32_16x16x32_bf16 v[172:175], v[200:203], v[132:135], v[172:175]
v_exp_f32 v156, v156
v_mfma_f32_16x16x32_bf16 v[164:167], v[200:203], v[128:131], v[164:167]
s_waitcnt lgkmcnt(4)
ds_read_b64_tr_b16 v[214:215], v236 offset:30720
v_cvt_pk_bf16_f32 v180, v152, v153
)"
R"(v_cvt_pk_bf16_f32 v181, v154, v155
v_mfma_f32_16x16x32_bf16 v[160:163], v[184:187], v[136:139], v[160:163]
v_exp_f32 v157, v157
v_mfma_f32_16x16x32_bf16 v[168:171], v[184:187], v[140:143], v[168:171]
s_waitcnt lgkmcnt(3)
ds_read_b64_tr_b16 v[216:217], v237 offset:24576
v_exp_f32 v158, v158
v_mfma_f32_16x16x32_bf16 v[172:175], v[188:191], v[140:143], v[172:175]
v_exp_f32 v159, v159
v_mfma_f32_16x16x32_bf16 v[164:167], v[188:191], v[136:139], v[164:167]
ds_read_b64_tr_b16 v[218:219], v237 offset:30720
v_cvt_pk_bf16_f32 v182, v156, v157
v_cvt_pk_bf16_f32 v183, v158, v159
v_mfma_f32_16x16x32_bf16 v[0:3], v[204:207], v[176:179], v[0:3]
ds_read_b64_tr_b16 v[220:221], v234 offset:24704
ds_read_b64_tr_b16 v[222:223], v234 offset:30848
)"
R"(v_mfma_f32_16x16x32_bf16 v[4:7], v[204:207], v[180:183], v[4:7]
s_waitcnt lgkmcnt(6)
ds_read_b64_tr_b16 v[184:185], v235 offset:24704
ds_read_b64_tr_b16 v[186:187], v235 offset:30848
v_mfma_f32_16x16x32_bf16 v[12:15], v[208:211], v[180:183], v[12:15]
ds_read_b64_tr_b16 v[188:189], v236 offset:24704
v_add_f32 v238, v238, v144
v_mfma_f32_16x16x32_bf16 v[8:11], v[208:211], v[176:179], v[8:11]
s_waitcnt lgkmcnt(7)
ds_read_b64_tr_b16 v[190:191], v236 offset:30848
v_add_f32 v240, v240, v152
v_mfma_f32_16x16x32_bf16 v[16:19], v[212:215], v[176:179], v[16:19]
ds_read_b64_tr_b16 v[192:193], v237 offset:24704
v_max3_f32 v242, v160, v161, v162
v_mfma_f32_16x16x32_bf16 v[20:23], v[212:215], v[180:183], v[20:23]
s_waitcnt lgkmcnt(7)
)"
R"(ds_read_b64_tr_b16 v[194:195], v237 offset:30848
v_max3_f32 v243, v167, v168, v169
v_mfma_f32_16x16x32_bf16 v[28:31], v[216:219], v[180:183], v[28:31]
ds_read_b64_tr_b16 v[196:197], v234 offset:24832
v_max3_f32 v242, v242, v163, v164
v_mfma_f32_16x16x32_bf16 v[24:27], v[216:219], v[176:179], v[24:27]
s_waitcnt lgkmcnt(7)
ds_read_b64_tr_b16 v[198:199], v234 offset:30976
ds_read_b64_tr_b16 v[200:201], v235 offset:24832
v_mfma_f32_16x16x32_bf16 v[32:35], v[220:223], v[176:179], v[32:35]
ds_read_b64_tr_b16 v[202:203], v235 offset:30976
ds_read_b64_tr_b16 v[204:205], v236 offset:24832
v_mfma_f32_16x16x32_bf16 v[36:39], v[220:223], v[180:183], v[36:39]
s_waitcnt lgkmcnt(9)
ds_read_b64_tr_b16 v[206:207], v236 offset:30976
ds_read_b64_tr_b16 v[208:209], v237 offset:24832
)"
R"(v_mfma_f32_16x16x32_bf16 v[44:47], v[184:187], v[180:183], v[44:47]
ds_read_b64_tr_b16 v[210:211], v237 offset:30976
v_max3_f32 v243, v243, v170, v171
v_mfma_f32_16x16x32_bf16 v[40:43], v[184:187], v[176:179], v[40:43]
s_waitcnt lgkmcnt(10)
v_max3_f32 v242, v242, v165, v166
v_max3_f32 v243, v243, v172, v173
v_mfma_f32_16x16x32_bf16 v[48:51], v[188:191], v[176:179], v[48:51]
v_max3_f32 v242, v242, v174, v175
v_add_f32 v239, v239, v145
v_mfma_f32_16x16x32_bf16 v[52:55], v[188:191], v[180:183], v[52:55]
s_waitcnt lgkmcnt(8)
v_max_f32 v242, v242, v243
v_add_f32 v241, v241, v153
v_mfma_f32_16x16x32_bf16 v[60:63], v[192:195], v[180:183], v[60:63]
v_cmp_lt_f32 vcc, 0x41000000, v242
)"
R"(v_add_f32 v238, v238, v146
v_mfma_f32_16x16x32_bf16 v[56:59], v[192:195], v[176:179], v[56:59]
s_waitcnt lgkmcnt(0)
v_add_f32 v240, v240, v154
v_add_f32 v239, v239, v147
s_barrier
v_mfma_f32_16x16x32_bf16 v[64:67], v[196:199], v[176:179], v[64:67]
ds_read_b128 v[184:187], v232 offset:0
ds_read_b128 v[188:191], v232 offset:6144
v_mfma_f32_16x16x32_bf16 v[68:71], v[196:199], v[180:183], v[68:71]
ds_read_b128 v[192:195], v233 offset:0
v_add_f32 v241, v241, v155
v_mfma_f32_16x16x32_bf16 v[76:79], v[200:203], v[180:183], v[76:79]
ds_read_b128 v[196:199], v233 offset:6144
v_add_f32 v238, v238, v148
v_mfma_f32_16x16x32_bf16 v[72:75], v[200:203], v[176:179], v[72:75]
)"
R"(ds_read_b128 v[200:203], v232 offset:128
v_add_f32 v240, v240, v156
v_mfma_f32_16x16x32_bf16 v[80:83], v[204:207], v[176:179], v[80:83]
v_add_f32 v239, v239, v149
v_add_f32 v241, v241, v157
v_mfma_f32_16x16x32_bf16 v[84:87], v[204:207], v[180:183], v[84:87]
v_add_f32 v238, v238, v150
v_add_f32 v240, v240, v158
v_mfma_f32_16x16x32_bf16 v[92:95], v[208:211], v[180:183], v[92:95]
v_add_f32 v239, v239, v151
v_add_f32 v241, v241, v159
v_mfma_f32_16x16x32_bf16 v[88:91], v[208:211], v[176:179], v[88:91]
s_waitcnt lgkmcnt(4)
s_cbranch_vccz LkB10_2
s_getpc_b64 s[80:81]
s_branch Lrare_1
)"
R"(LkB10_2:
s_cmp_eq_u32 s59, 4
s_cbranch_scc0 Lw4_4
s_mov_b64 exec, 0xf
s_waitcnt vmcnt(0)
buffer_store_dword v251, v250, s[40:43], 0 offen offset:128 sc1
s_mov_b64 exec, -1
Lw4_4:
v_mfma_f32_16x16x32_bf16 v[144:147], v[184:187], v[96:99], v[224:227]
v_exp_f32 v160, v160
v_mfma_f32_16x16x32_bf16 v[152:155], v[184:187], v[100:103], v[228:231]
s_waitcnt lgkmcnt(3)
v_exp_f32 v161, v161
v_mfma_f32_16x16x32_bf16 v[156:159], v[188:191], v[100:103], v[228:231]
v_exp_f32 v162, v162
v_mfma_f32_16x16x32_bf16 v[148:151], v[188:191], v[96:99], v[224:227]
)"
R"(s_waitcnt lgkmcnt(2)
ds_read_b128 v[184:187], v232 offset:6272
ds_read_b128 v[188:191], v233 offset:128
v_mfma_f32_16x16x32_bf16 v[144:147], v[192:195], v[104:107], v[144:147]
v_exp_f32 v163, v163
v_mfma_f32_16x16x32_bf16 v[152:155], v[192:195], v[108:111], v[152:155]
s_waitcnt lgkmcnt(3)
ds_read_b64_tr_b16 v[204:205], v234 offset:36864
v_cvt_pk_bf16_f32 v176, v160, v161
v_cvt_pk_bf16_f32 v177, v162, v163
v_mfma_f32_16x16x32_bf16 v[156:159], v[196:199], v[108:111], v[156:159]
v_exp_f32 v164, v164
v_mfma_f32_16x16x32_bf16 v[148:151], v[196:199], v[104:107], v[148:151]
s_waitcnt lgkmcnt(3)
ds_read_b128 v[192:195], v233 offset:6272
ds_read_b128 v[196:199], v232 offset:256
)"
R"(v_mfma_f32_16x16x32_bf16 v[144:147], v[200:203], v[112:115], v[144:147]
v_exp_f32 v165, v165
v_mfma_f32_16x16x32_bf16 v[152:155], v[200:203], v[116:119], v[152:155]
s_waitcnt lgkmcnt(4)
ds_read_b64_tr_b16 v[206:207], v234 offset:43008
v_exp_f32 v166, v166
v_mfma_f32_16x16x32_bf16 v[156:159], v[184:187], v[116:119], v[156:159]
v_exp_f32 v167, v167
v_mfma_f32_16x16x32_bf16 v[148:151], v[184:187], v[112:115], v[148:151]
s_waitcnt lgkmcnt(4)
ds_read_b128 v[200:203], v232 offset:6400
ds_read_b128 v[184:187], v233 offset:256
v_mfma_f32_16x16x32_bf16 v[144:147], v[188:191], v[120:123], v[144:147]
v_exp_f32 v168, v168
v_mfma_f32_16x16x32_bf16 v[152:155], v[188:191], v[124:127], v[152:155]
s_waitcnt lgkmcnt(4)
)"
R"(ds_read_b64_tr_b16 v[208:209], v235 offset:36864
v_cvt_pk_bf16_f32 v178, v164, v165
v_cvt_pk_bf16_f32 v179, v166, v167
v_mfma_f32_16x16x32_bf16 v[156:159], v[192:195], v[124:127], v[156:159]
v_exp_f32 v169, v169
v_mfma_f32_16x16x32_bf16 v[148:151], v[192:195], v[120:123], v[148:151]
s_waitcnt lgkmcnt(4)
ds_read_b128 v[188:191], v233 offset:6400
ds_read_b64_tr_b16 v[210:211], v235 offset:43008
v_mfma_f32_16x16x32_bf16 v[144:147], v[196:199], v[128:131], v[144:147]
v_exp_f32 v170, v170
v_mfma_f32_16x16x32_bf16 v[152:155], v[196:199], v[132:135], v[152:155]
s_waitcnt lgkmcnt(4)
ds_read_b64_tr_b16 v[212:213], v236 offset:36864
v_exp_f32 v171, v171
v_mfma_f32_16x16x32_bf16 v[156:159], v[200:203], v[132:135], v[156:159]
)"
R"(v_exp_f32 v172, v172
v_mfma_f32_16x16x32_bf16 v[148:151], v[200:203], v[128:131], v[148:151]
s_waitcnt lgkmcnt(4)
ds_read_b64_tr_b16 v[214:215], v236 offset:43008
v_cvt_pk_bf16_f32 v180, v168, v169
v_cvt_pk_bf16_f32 v181, v170, v171
v_mfma_f32_16x16x32_bf16 v[144:147], v[184:187], v[136:139], v[144:147]
v_exp_f32 v173, v173
v_mfma_f32_16x16x32_bf16 v[152:155], v[184:187], v[140:143], v[152:155]
s_waitcnt lgkmcnt(3)
ds_read_b64_tr_b16 v[216:217], v237 offset:36864
v_exp_f32 v174, v174
v_mfma_f32_16x16x32_bf16 v[156:159], v[188:191], v[140:143], v[156:159]
v_exp_f32 v175, v175
v_mfma_f32_16x16x32_bf16 v[148:151], v[188:191], v[136:139], v[148:151]
ds_read_b64_tr_b16 v[218:219], v237 offset:43008
)"
R"(v_cvt_pk_bf16_f32 v182, v172, v173
v_cvt_pk_bf16_f32 v183, v174, v175
v_mfma_f32_16x16x32_bf16 v[0:3], v[204:207], v[176:179], v[0:3]
ds_read_b64_tr_b16 v[220:221], v234 offset:36992
ds_read_b64_tr_b16 v[222:223], v234 offset:43136
v_mfma_f32_16x16x32_bf16 v[4:7], v[204:207], v[180:183], v[4:7]
s_waitcnt lgkmcnt(6)
ds_read_b64_tr_b16 v[184:185], v235 offset:36992
ds_read_b64_tr_b16 v[186:187], v235 offset:43136
v_mfma_f32_16x16x32_bf16 v[12:15], v[208:211], v[180:183], v[12:15]
ds_read_b64_tr_b16 v[188:189], v236 offset:36992
v_add_f32 v238, v238, v160
v_mfma_f32_16x16x32_bf16 v[8:11], v[208:211], v[176:179], v[8:11]
s_waitcnt lgkmcnt(7)
ds_read_b64_tr_b16 v[190:191], v236 offset:43136
v_add_f32 v240, v240, v168
)"
R"(v_mfma_f32_16x16x32_bf16 v[16:19], v[212:215], v[176:179], v[16:19]
ds_read_b64_tr_b16 v[192:193], v237 offset:36992
v_max3_f32 v242, v144, v145, v146
v_mfma_f32_16x16x32_bf16 v[20:23], v[212:215], v[180:183], v[20:23]
s_waitcnt lgkmcnt(7)
ds_read_b64_tr_b16 v[194:195], v237 offset:43136
v_max3_f32 v243, v151, v152, v153
v_mfma_f32_16x16x32_bf16 v[28:31], v[216:219], v[180:183], v[28:31]
ds_read_b64_tr_b16 v[196:197], v234 offset:37120
v_max3_f32 v242, v242, v147, v148
v_mfma_f32_16x16x32_bf16 v[24:27], v[216:219], v[176:179], v[24:27]
s_waitcnt lgkmcnt(7)
ds_read_b64_tr_b16 v[198:199], v234 offset:43264
ds_read_b64_tr_b16 v[200:201], v235 offset:37120
v_mfma_f32_16x16x32_bf16 v[32:35], v[220:223], v[176:179], v[32:35]
ds_read_b64_tr_b16 v[202:203], v235 offset:43264
)"
R"(ds_read_b64_tr_b16 v[204:205], v236 offset:37120
v_mfma_f32_16x16x32_bf16 v[36:39], v[220:223], v[180:183], v[36:39]
s_waitcnt lgkmcnt(9)
ds_read_b64_tr_b16 v[206:207], v236 offset:43264
ds_read_b64_tr_b16 v[208:209], v237 offset:37120
v_mfma_f32_16x16x32_bf16 v[44:47], v[184:187], v[180:183], v[44:47]
ds_read_b64_tr_b16 v[210:211], v237 offset:43264
v_max3_f32 v243, v243, v154, v155
v_mfma_f32_16x16x32_bf16 v[40:43], v[184:187], v[176:179], v[40:43]
s_waitcnt lgkmcnt(10)
v_max3_f32 v242, v242, v149, v150
v_max3_f32 v243, v243, v156, v157
v_mfma_f32_16x16x32_bf16 v[48:51], v[188:191], v[176:179], v[48:51]
v_max3_f32 v242, v242, v158, v159
v_add_f32 v239, v239, v161
v_mfma_f32_16x16x32_bf16 v[52:55], v[188:191], v[180:183], v[52:55]
)"
R"(s_waitcnt lgkmcnt(8)
v_max_f32 v242, v242, v243
v_add_f32 v241, v241, v169
v_mfma_f32_16x16x32_bf16 v[60:63], v[192:195], v[180:183], v[60:63]
v_cmp_lt_f32 vcc, 0x41000000, v242
v_add_f32 v238, v238, v162
v_mfma_f32_16x16x32_bf16 v[56:59], v[192:195], v[176:179], v[56:59]
s_waitcnt lgkmcnt(0)
v_add_f32 v240, v240, v170
v_add_f32 v239, v239, v163
s_barrier
v_mfma_f32_16x16x32_bf16 v[64:67], v[196:199], v[176:179], v[64:67]
ds_read_b128 v[184:187], v232 offset:12288
ds_read_b128 v[188:191], v232 offset:18432
v_mfma_f32_16x16x32_bf16 v[68:71], v[196:199], v[180:183], v[68:71]
ds_read_b128 v[192:195], v233 offset:12288
)"
R"(v_add_f32 v241, v241, v171
v_mfma_f32_16x16x32_bf16 v[76:79], v[200:203], v[180:183], v[76:79]
ds_read_b128 v[196:199], v233 offset:18432
v_add_f32 v238, v238, v164
v_mfma_f32_16x16x32_bf16 v[72:75], v[200:203], v[176:179], v[72:75]
ds_read_b128 v[200:203], v232 offset:12416
v_add_f32 v240, v240, v172
v_mfma_f32_16x16x32_bf16 v[80:83], v[204:207], v[176:179], v[80:83]
v_add_f32 v239, v239, v165
v_add_f32 v241, v241, v173
v_mfma_f32_16x16x32_bf16 v[84:87], v[204:207], v[180:183], v[84:87]
v_add_f32 v238, v238, v166
v_add_f32 v240, v240, v174
v_mfma_f32_16x16x32_bf16 v[92:95], v[208:211], v[180:183], v[92:95]
v_add_f32 v239, v239, v167
v_add_f32 v241, v241, v175
)"
R"(v_mfma_f32_16x16x32_bf16 v[88:91], v[208:211], v[176:179], v[88:91]
s_waitcnt lgkmcnt(4)
s_cbranch_vccz LkB11_3
s_getpc_b64 s[80:81]
s_branch Lrare_0
LkB11_3:
v_mfma_f32_16x16x32_bf16 v[160:163], v[184:187], v[96:99], v[224:227]
v_exp_f32 v144, v144
v_mfma_f32_16x16x32_bf16 v[168:171], v[184:187], v[100:103], v[228:231]
s_waitcnt lgkmcnt(3)
v_exp_f32 v145, v145
v_mfma_f32_16x16x32_bf16 v[172:175], v[188:191], v[100:103], v[228:231]
v_exp_f32 v146, v146
v_mfma_f32_16x16x32_bf16 v[164:167], v[188:191], v[96:99], v[224:227]
s_waitcnt lgkmcnt(2)
ds_read_b128 v[184:187], v232 offset:18560
)"
R"(ds_read_b128 v[188:191], v233 offset:12416
v_mfma_f32_16x16x32_bf16 v[160:163], v[192:195], v[104:107], v[160:163]
v_exp_f32 v147, v147
v_mfma_f32_16x16x32_bf16 v[168:171], v[192:195], v[108:111], v[168:171]
s_waitcnt lgkmcnt(3)
ds_read_b64_tr_b16 v[204:205], v234 offset:0
v_cvt_pk_bf16_f32 v176, v144, v145
v_cvt_pk_bf16_f32 v177, v146, v147
v_mfma_f32_16x16x32_bf16 v[172:175], v[196:199], v[108:111], v[172:175]
v_exp_f32 v148, v148
v_mfma_f32_16x16x32_bf16 v[164:167], v[196:199], v[104:107], v[164:167]
s_waitcnt lgkmcnt(3)
ds_read_b128 v[192:195], v233 offset:18560
ds_read_b128 v[196:199], v232 offset:12544
v_mfma_f32_16x16x32_bf16 v[160:163], v[200:203], v[112:115], v[160:163]
v_exp_f32 v149, v149
)"
R"(v_mfma_f32_16x16x32_bf16 v[168:171], v[200:203], v[116:119], v[168:171]
s_waitcnt lgkmcnt(4)
ds_read_b64_tr_b16 v[206:207], v234 offset:6144
v_exp_f32 v150, v150
v_mfma_f32_16x16x32_bf16 v[172:175], v[184:187], v[116:119], v[172:175]
v_exp_f32 v151, v151
v_mfma_f32_16x16x32_bf16 v[164:167], v[184:187], v[112:115], v[164:167]
s_waitcnt lgkmcnt(4)
ds_read_b128 v[200:203], v232 offset:18688
ds_read_b128 v[184:187], v233 offset:12544
v_mfma_f32_16x16x32_bf16 v[160:163], v[188:191], v[120:123], v[160:163]
v_exp_f32 v152, v152
v_mfma_f32_16x16x32_bf16 v[168:171], v[188:191], v[124:127], v[168:171]
s_waitcnt lgkmcnt(4)
ds_read_b64_tr_b16 v[208:209], v235 offset:0
v_cvt_pk_bf16_f32 v178, v148, v149
)"
R"(v_cvt_pk_bf16_f32 v179, v150, v151
v_mfma_f32_16x16x32_bf16 v[172:175], v[192:195], v[124:127], v[172:175]
v_exp_f32 v153, v153
v_mfma_f32_16x16x32_bf16 v[164:167], v[192:195], v[120:123], v[164:167]
s_waitcnt lgkmcnt(4)
ds_read_b128 v[188:191], v233 offset:18688
ds_read_b64_tr_b16 v[210:211], v235 offset:6144
v_mfma_f32_16x16x32_bf16 v[160:163], v[196:199], v[128:131], v[160:163]
v_exp_f32 v154, v154
v_mfma_f32_16x16x32_bf16 v[168:171], v[196:199], v[132:135], v[168:171]
s_waitcnt lgkmcnt(4)
ds_read_b64_tr_b16 v[212:213], v236 offset:0
v_exp_f32 v155, v155
v_mfma_f32_16x16x32_bf16 v[172:175], v[200:203], v[132:135], v[172:175]
v_exp_f32 v156, v156
v_mfma_f32_16x16x32_bf16 v[164:167], v[200:203], v[128:131], v[164:167]
)"
R"(s_waitcnt lgkmcnt(4)
ds_read_b64_tr_b16 v[214:215], v236 offset:6144
v_cvt_pk_bf16_f32 v180, v152, v153
v_cvt_pk_bf16_f32 v181, v154, v155
v_mfma_f32_16x16x32_bf16 v[160:163], v[184:187], v[136:139], v[160:163]
v_exp_f32 v157, v157
v_mfma_f32_16x16x32_bf16 v[168:171], v[184:187], v[140:143], v[168:171]
s_waitcnt lgkmcnt(3)
ds_read_b64_tr_b16 v[216:217], v237 offset:0
v_exp_f32 v158, v158
v_mfma_f32_16x16x32_bf16 v[172:175], v[188:191], v[140:143], v[172:175]
v_exp_f32 v159, v159
v_mfma_f32_16x16x32_bf16 v[164:167], v[188:191], v[136:139], v[164:167]
ds_read_b64_tr_b16 v[218:219], v237 offset:6144
v_cvt_pk_bf16_f32 v182, v156, v157
v_cvt_pk_bf16_f32 v183, v158, v159
)"
R"(v_mfma_f32_16x16x32_bf16 v[0:3], v[204:207], v[176:179], v[0:3]
ds_read_b64_tr_b16 v[220:221], v234 offset:128
ds_read_b64_tr_b16 v[222:223], v234 offset:6272
v_mfma_f32_16x16x32_bf16 v[4:7], v[204:207], v[180:183], v[4:7]
s_waitcnt lgkmcnt(6)
ds_read_b64_tr_b16 v[184:185], v235 offset:128
ds_read_b64_tr_b16 v[186:187], v235 offset:6272
v_mfma_f32_16x16x32_bf16 v[12:15], v[208:211], v[180:183], v[12:15]
ds_read_b64_tr_b16 v[188:189], v236 offset:128
v_add_f32 v238, v238, v144
v_mfma_f32_16x16x32_bf16 v[8:11], v[208:211], v[176:179], v[8:11]
s_waitcnt lgkmcnt(7)
ds_read_b64_tr_b16 v[190:191], v236 offset:6272
v_add_f32 v240, v240, v152
v_mfma_f32_16x16x32_bf16 v[16:19], v[212:215], v[176:179], v[16:19]
ds_read_b64_tr_b16 v[192:193], v237 offset:128
)"
R"(v_max3_f32 v242, v160, v161, v162
v_mfma_f32_16x16x32_bf16 v[20:23], v[212:215], v[180:183], v[20:23]
s_waitcnt lgkmcnt(7)
ds_read_b64_tr_b16 v[194:195], v237 offset:6272
v_max3_f32 v243, v167, v168, v169
v_mfma_f32_16x16x32_bf16 v[28:31], v[216:219], v[180:183], v[28:31]
ds_read_b64_tr_b16 v[196:197], v234 offset:256
v_max3_f32 v242, v242, v163, v164
v_mfma_f32_16x16x32_bf16 v[24:27], v[216:219], v[176:179], v[24:27]
s_waitcnt lgkmcnt(7)
ds_read_b64_tr_b16 v[198:199], v234 offset:6400
ds_read_b64_tr_b16 v[200:201], v235 offset:256
v_mfma_f32_16x16x32_bf16 v[32:35], v[220:223], v[176:179], v[32:35]
ds_read_b64_tr_b16 v[202:203], v235 offset:6400
ds_read_b64_tr_b16 v[204:205], v236 offset:256
v_mfma_f32_16x16x32_bf16 v[36:39], v[220:223], v[180:183], v[36:39]
)"
R"(s_waitcnt lgkmcnt(9)
ds_read_b64_tr_b16 v[206:207], v236 offset:6400
ds_read_b64_tr_b16 v[208:209], v237 offset:256
v_mfma_f32_16x16x32_bf16 v[44:47], v[184:187], v[180:183], v[44:47]
ds_read_b64_tr_b16 v[210:211], v237 offset:6400
v_max3_f32 v243, v243, v170, v171
v_mfma_f32_16x16x32_bf16 v[40:43], v[184:187], v[176:179], v[40:43]
s_waitcnt lgkmcnt(10)
v_max3_f32 v242, v242, v165, v166
v_max3_f32 v243, v243, v172, v173
v_mfma_f32_16x16x32_bf16 v[48:51], v[188:191], v[176:179], v[48:51]
v_max3_f32 v242, v242, v174, v175
v_add_f32 v239, v239, v145
v_mfma_f32_16x16x32_bf16 v[52:55], v[188:191], v[180:183], v[52:55]
s_waitcnt lgkmcnt(8)
v_max_f32 v242, v242, v243
)"
R"(v_add_f32 v241, v241, v153
v_mfma_f32_16x16x32_bf16 v[60:63], v[192:195], v[180:183], v[60:63]
v_cmp_lt_f32 vcc, 0x41000000, v242
v_add_f32 v238, v238, v146
v_mfma_f32_16x16x32_bf16 v[56:59], v[192:195], v[176:179], v[56:59]
s_waitcnt lgkmcnt(0)
v_add_f32 v240, v240, v154
v_add_f32 v239, v239, v147
s_barrier
v_mfma_f32_16x16x32_bf16 v[64:67], v[196:199], v[176:179], v[64:67]
ds_read_b128 v[184:187], v232 offset:24576
ds_read_b128 v[188:191], v232 offset:30720
v_mfma_f32_16x16x32_bf16 v[68:71], v[196:199], v[180:183], v[68:71]
ds_read_b128 v[192:195], v233 offset:24576
v_add_f32 v241, v241, v155
v_mfma_f32_16x16x32_bf16 v[76:79], v[200:203], v[180:183], v[76:79]
)"
R"(ds_read_b128 v[196:199], v233 offset:30720
v_add_f32 v238, v238, v148
v_mfma_f32_16x16x32_bf16 v[72:75], v[200:203], v[176:179], v[72:75]
ds_read_b128 v[200:203], v232 offset:24704
v_add_f32 v240, v240, v156
v_mfma_f32_16x16x32_bf16 v[80:83], v[204:207], v[176:179], v[80:83]
v_add_f32 v239, v239, v149
v_add_f32 v241, v241, v157
v_mfma_f32_16x16x32_bf16 v[84:87], v[204:207], v[180:183], v[84:87]
v_add_f32 v238, v238, v150
v_add_f32 v240, v240, v158
v_mfma_f32_16x16x32_bf16 v[92:95], v[208:211], v[180:183], v[92:95]
v_add_f32 v239, v239, v151
v_add_f32 v241, v241, v159
v_mfma_f32_16x16x32_bf16 v[88:91], v[208:211], v[176:179], v[88:91]
s_waitcnt lgkmcnt(4)
)"
R"(s_cbranch_vccz LkB11_0
s_getpc_b64 s[80:81]
s_branch Lrare_1
LkB11_0:
s_cmp_eq_u32 s59, 4
s_cbranch_scc0 Lw4_5
s_mov_b64 exec, 0xf
buffer_load_dword v252, v249, s[40:43], 0 offen offset:128 sc1
s_mov_b64 exec, -1
Lw4_5:
v_mfma_f32_16x16x32_bf16 v[144:147], v[184:187], v[96:99], v[224:227]
v_exp_f32 v160, v160
v_mfma_f32_16x16x32_bf16 v[152:155], v[184:187], v[100:103], v[228:231]
s_waitcnt lgkmcnt(3)
v_exp_f32 v161, v161
v_mfma_f32_16x16x32_bf16 v[156:159], v[188:191], v[100:103], v[228:231]
)"
R"(v_exp_f32 v162, v162
v_mfma_f32_16x16x32_bf16 v[148:151], v[188:191], v[96:99], v[224:227]
s_waitcnt lgkmcnt(2)
ds_read_b128 v[184:187], v232 offset:30848
ds_read_b128 v[188:191], v233 offset:24704
v_mfma_f32_16x16x32_bf16 v[144:147], v[192:195], v[104:107], v[144:147]
v_exp_f32 v163, v163
v_mfma_f32_16x16x32_bf16 v[152:155], v[192:195], v[108:111], v[152:155]
s_waitcnt lgkmcnt(3)
ds_read_b64_tr_b16 v[204:205], v234 offset:12288
v_cvt_pk_bf16_f32 v176, v160, v161
v_cvt_pk_bf16_f32 v177, v162, v163
v_mfma_f32_16x16x32_bf16 v[156:159], v[196:199], v[108:111], v[156:159]
v_exp_f32 v164, v164
v_mfma_f32_16x16x32_bf16 v[148:151], v[196:199], v[104:107], v[148:151]
s_waitcnt lgkmcnt(3)
)"
R"(ds_read_b128 v[192:195], v233 offset:30848
ds_read_b128 v[196:199], v232 offset:24832
v_mfma_f32_16x16x32_bf16 v[144:147], v[200:203], v[112:115], v[144:147]
v_exp_f32 v165, v165
v_mfma_f32_16x16x32_bf16 v[152:155], v[200:203], v[116:119], v[152:155]
s_waitcnt lgkmcnt(4)
ds_read_b64_tr_b16 v[206:207], v234 offset:18432
v_exp_f32 v166, v166
v_mfma_f32_16x16x32_bf16 v[156:159], v[184:187], v[116:119], v[156:159]
v_exp_f32 v167, v167
v_mfma_f32_16x16x32_bf16 v[148:151], v[184:187], v[112:115], v[148:151]
s_waitcnt lgkmcnt(4)
ds_read_b128 v[200:203], v232 offset:30976
ds_read_b128 v[184:187], v233 offset:24832
v_mfma_f32_16x16x32_bf16 v[144:147], v[188:191], v[120:123], v[144:147]
v_exp_f32 v168, v168
)"
R"(v_mfma_f32_16x16x32_bf16 v[152:155], v[188:191], v[124:127], v[152:155]
s_waitcnt lgkmcnt(4)
ds_read_b64_tr_b16 v[208:209], v235 offset:12288
v_cvt_pk_bf16_f32 v178, v164, v165
v_cvt_pk_bf16_f32 v179, v166, v167
v_mfma_f32_16x16x32_bf16 v[156:159], v[192:195], v[124:127], v[156:159]
v_exp_f32 v169, v169
v_mfma_f32_16x16x32_bf16 v[148:151], v[192:195], v[120:123], v[148:151]
s_waitcnt lgkmcnt(4)
ds_read_b128 v[188:191], v233 offset:30976
ds_read_b64_tr_b16 v[210:211], v235 offset:18432
v_mfma_f32_16x16x32_bf16 v[144:147], v[196:199], v[128:131], v[144:147]
v_exp_f32 v170, v170
v_mfma_f32_16x16x32_bf16 v[152:155], v[196:199], v[132:135], v[152:155]
s_waitcnt lgkmcnt(4)
ds_read_b64_tr_b16 v[212:213], v236 offset:12288
)"
R"(v_exp_f32 v171, v171
v_mfma_f32_16x16x32_bf16 v[156:159], v[200:203], v[132:135], v[156:159]
v_exp_f32 v172, v172
v_mfma_f32_16x16x32_bf16 v[148:151], v[200:203], v[128:131], v[148:151]
s_waitcnt lgkmcnt(4)
ds_read_b64_tr_b16 v[214:215], v236 offset:18432
v_cvt_pk_bf16_f32 v180, v168, v169
v_cvt_pk_bf16_f32 v181, v170, v171
v_mfma_f32_16x16x32_bf16 v[144:147], v[184:187], v[136:139], v[144:147]
v_exp_f32 v173, v173
v_mfma_f32_16x16x32_bf16 v[152:155], v[184:187], v[140:143], v[152:155]
s_waitcnt lgkmcnt(3)
ds_read_b64_tr_b16 v[216:217], v237 offset:12288
v_exp_f32 v174, v174
v_mfma_f32_16x16x32_bf16 v[156:159], v[188:191], v[140:143], v[156:159]
v_exp_f32 v175, v175
)"
R"(v_mfma_f32_16x16x32_bf16 v[148:151], v[188:191], v[136:139], v[148:151]
ds_read_b64_tr_b16 v[218:219], v237 offset:18432
v_cvt_pk_bf16_f32 v182, v172, v173
v_cvt_pk_bf16_f32 v183, v174, v175
v_mfma_f32_16x16x32_bf16 v[0:3], v[204:207], v[176:179], v[0:3]
ds_read_b64_tr_b16 v[220:221], v234 offset:12416
ds_read_b64_tr_b16 v[222:223], v234 offset:18560
v_mfma_f32_16x16x32_bf16 v[4:7], v[204:207], v[180:183], v[4:7]
s_waitcnt lgkmcnt(6)
ds_read_b64_tr_b16 v[184:185], v235 offset:12416
ds_read_b64_tr_b16 v[186:187], v235 offset:18560
v_mfma_f32_16x16x32_bf16 v[12:15], v[208:211], v[180:183], v[12:15]
ds_read_b64_tr_b16 v[188:189], v236 offset:12416
v_add_f32 v238, v238, v160
v_mfma_f32_16x16x32_bf16 v[8:11], v[208:211], v[176:179], v[8:11]
s_waitcnt lgkmcnt(7)
)"
R"(ds_read_b64_tr_b16 v[190:191], v236 offset:18560
v_add_f32 v240, v240, v168
v_mfma_f32_16x16x32_bf16 v[16:19], v[212:215], v[176:179], v[16:19]
ds_read_b64_tr_b16 v[192:193], v237 offset:12416
v_max3_f32 v242, v144, v145, v146
v_mfma_f32_16x16x32_bf16 v[20:23], v[212:215], v[180:183], v[20:23]
s_waitcnt lgkmcnt(7)
ds_read_b64_tr_b16 v[194:195], v237 offset:18560
v_max3_f32 v243, v151, v152, v153
v_mfma_f32_16x16x32_bf16 v[28:31], v[216:219], v[180:183], v[28:31]
ds_read_b64_tr_b16 v[196:197], v234 offset:12544
v_max3_f32 v242, v242, v147, v148
v_mfma_f32_16x16x32_bf16 v[24:27], v[216:219], v[176:179], v[24:27]
s_waitcnt lgkmcnt(7)
ds_read_b64_tr_b16 v[198:199], v234 offset:18688
ds_read_b64_tr_b16 v[200:201], v235 offset:12544
)"
R"(v_mfma_f32_16x16x32_bf16 v[32:35], v[220:223], v[176:179], v[32:35]
ds_read_b64_tr_b16 v[202:203], v235 offset:18688
ds_read_b64_tr_b16 v[204:205], v236 offset:12544
v_mfma_f32_16x16x32_bf16 v[36:39], v[220:223], v[180:183], v[36:39]
s_waitcnt lgkmcnt(9)
ds_read_b64_tr_b16 v[206:207], v236 offset:18688
ds_read_b64_tr_b16 v[208:209], v237 offset:12544
v_mfma_f32_16x16x32_bf16 v[44:47], v[184:187], v[180:183], v[44:47]
ds_read_b64_tr_b16 v[210:211], v237 offset:18688
v_max3_f32 v243, v243, v154, v155
v_mfma_f32_16x16x32_bf16 v[40:43], v[184:187], v[176:179], v[40:43]
s_waitcnt lgkmcnt(10)
v_max3_f32 v242, v242, v149, v150
v_max3_f32 v243, v243, v156, v157
v_mfma_f32_16x16x32_bf16 v[48:51], v[188:191], v[176:179], v[48:51]
v_max3_f32 v242, v242, v158, v159
)"
R"(v_add_f32 v239, v239, v161
v_mfma_f32_16x16x32_bf16 v[52:55], v[188:191], v[180:183], v[52:55]
s_waitcnt lgkmcnt(8)
v_max_f32 v242, v242, v243
v_add_f32 v241, v241, v169
v_mfma_f32_16x16x32_bf16 v[60:63], v[192:195], v[180:183], v[60:63]
v_cmp_lt_f32 vcc, 0x41000000, v242
v_add_f32 v238, v238, v162
v_mfma_f32_16x16x32_bf16 v[56:59], v[192:195], v[176:179], v[56:59]
s_waitcnt lgkmcnt(0)
v_add_f32 v240, v240, v170
v_add_f32 v239, v239, v163
s_barrier
v_mfma_f32_16x16x32_bf16 v[64:67], v[196:199], v[176:179], v[64:67]
ds_read_b128 v[184:187], v232 offset:36864
ds_read_b128 v[188:191], v232 offset:43008
)"
R"(v_mfma_f32_16x16x32_bf16 v[68:71], v[196:199], v[180:183], v[68:71]
ds_read_b128 v[192:195], v233 offset:36864
v_add_f32 v241, v241, v171
v_mfma_f32_16x16x32_bf16 v[76:79], v[200:203], v[180:183], v[76:79]
ds_read_b128 v[196:199], v233 offset:43008
v_add_f32 v238, v238, v164
v_mfma_f32_16x16x32_bf16 v[72:75], v[200:203], v[176:179], v[72:75]
ds_read_b128 v[200:203], v232 offset:36992
v_add_f32 v240, v240, v172
v_mfma_f32_16x16x32_bf16 v[80:83], v[204:207], v[176:179], v[80:83]
v_add_f32 v239, v239, v165
v_add_f32 v241, v241, v173
v_mfma_f32_16x16x32_bf16 v[84:87], v[204:207], v[180:183], v[84:87]
v_add_f32 v238, v238, v166
v_add_f32 v240, v240, v174
v_mfma_f32_16x16x32_bf16 v[92:95], v[208:211], v[180:183], v[92:95]
)"
R"(v_add_f32 v239, v239, v167
v_add_f32 v241, v241, v175
v_mfma_f32_16x16x32_bf16 v[88:91], v[208:211], v[176:179], v[88:91]
s_waitcnt lgkmcnt(4)
s_cbranch_vccz LkB11_1
s_getpc_b64 s[80:81]
s_branch Lrare_0
LkB11_1:
s_cmp_eq_u32 s59, 4
s_cbranch_scc0 Lw4_6
s_mov_b64 exec, 0xf
s_mov_b32 s82, 65536
s_branch Lpc_7
Lp_7:
buffer_load_dword v252, v249, s[40:43], 0 offen offset:128 sc1
Lpc_7:
)"
R"(s_waitcnt vmcnt(0)
v_cmp_ne_u32 vcc, v252, v248
s_nop 1
s_cbranch_vccz Lpd_7
s_sub_u32 s82, s82, 1
s_cmp_lg_u32 s82, 0
s_cbranch_scc1 Lp_7
Lpd_7:
s_mov_b64 exec, 0xfff
buffer_load_dword v251, v249, s[40:43], 0 offen sc1
s_mov_b64 exec, -1
Lw4_6:
v_mfma_f32_16x16x32_bf16 v[160:163], v[184:187], v[96:99], v[224:227]
v_exp_f32 v144, v144
v_mfma_f32_16x16x32_bf16 v[168:171], v[184:187], v[100:103], v[228:231]
s_waitcnt lgkmcnt(3)
)"
R"(v_exp_f32 v145, v145
v_mfma_f32_16x16x32_bf16 v[172:175], v[188:191], v[100:103], v[228:231]
v_exp_f32 v146, v146
v_mfma_f32_16x16x32_bf16 v[164:167], v[188:191], v[96:99], v[224:227]
s_waitcnt lgkmcnt(2)
ds_read_b128 v[184:187], v232 offset:43136
ds_read_b128 v[188:191], v233 offset:36992
v_mfma_f32_16x16x32_bf16 v[160:163], v[192:195], v[104:107], v[160:163]
v_exp_f32 v147, v147
v_mfma_f32_16x16x32_bf16 v[168:171], v[192:195], v[108:111], v[168:171]
s_waitcnt lgkmcnt(3)
ds_read_b64_tr_b16 v[204:205], v234 offset:24576
v_cvt_pk_bf16_f32 v176, v144, v145
v_cvt_pk_bf16_f32 v177, v146, v147
v_mfma_f32_16x16x32_bf16 v[172:175], v[196:199], v[108:111], v[172:175]
v_exp_f32 v148, v148
)"
R"(v_mfma_f32_16x16x32_bf16 v[164:167], v[196:199], v[104:107], v[164:167]
s_waitcnt lgkmcnt(3)
ds_read_b128 v[192:195], v233 offset:43136
ds_read_b128 v[196:199], v232 offset:37120
v_mfma_f32_16x16x32_bf16 v[160:163], v[200:203], v[112:115], v[160:163]
v_exp_f32 v149, v149
v_mfma_f32_16x16x32_bf16 v[168:171], v[200:203], v[116:119], v[168:171]
s_waitcnt lgkmcnt(4)
ds_read_b64_tr_b16 v[206:207], v234 offset:30720
v_exp_f32 v150, v150
v_mfma_f32_16x16x32_bf16 v[172:175], v[184:187], v[116:119], v[172:175]
v_exp_f32 v151, v151
v_mfma_f32_16x16x32_bf16 v[164:167], v[184:187], v[112:115], v[164:167]
s_waitcnt lgkmcnt(4)
ds_read_b128 v[200:203], v232 offset:43264
ds_read_b128 v[184:187], v233 offset:37120
)"
R"(v_mfma_f32_16x16x32_bf16 v[160:163], v[188:191], v[120:123], v[160:163]
v_exp_f32 v152, v152
v_mfma_f32_16x16x32_bf16 v[168:171], v[188:191], v[124:127], v[168:171]
s_waitcnt lgkmcnt(4)
ds_read_b64_tr_b16 v[208:209], v235 offset:24576
v_cvt_pk_bf16_f32 v178, v148, v149
v_cvt_pk_bf16_f32 v179, v150, v151
v_mfma_f32_16x16x32_bf16 v[172:175], v[192:195], v[124:127], v[172:175]
v_exp_f32 v153, v153
v_mfma_f32_16x16x32_bf16 v[164:167], v[192:195], v[120:123], v[164:167]
s_waitcnt lgkmcnt(4)
ds_read_b128 v[188:191], v233 offset:43264
ds_read_b64_tr_b16 v[210:211], v235 offset:30720
v_mfma_f32_16x16x32_bf16 v[160:163], v[196:199], v[128:131], v[160:163]
v_exp_f32 v154, v154
v_mfma_f32_16x16x32_bf16 v[168:171], v[196:199], v[132:135], v[168:171]
)"
R"(s_waitcnt lgkmcnt(4)
ds_read_b64_tr_b16 v[212:213], v236 offset:24576
v_exp_f32 v155, v155
v_mfma_f32_16x16x32_bf16 v[172:175], v[200:203], v[132:135], v[172:175]
v_exp_f32 v156, v156
v_mfma_f32_16x16x32_bf16 v[164:167], v[200:203], v[128:131], v[164:167]
s_waitcnt lgkmcnt(4)
ds_read_b64_tr_b16 v[214:215], v236 offset:30720
v_cvt_pk_bf16_f32 v180, v152, v153
v_cvt_pk_bf16_f32 v181, v154, v155
v_mfma_f32_16x16x32_bf16 v[160:163], v[184:187], v[136:139], v[160:163]
v_exp_f32 v157, v157
v_mfma_f32_16x16x32_bf16 v[168:171], v[184:187], v[140:143], v[168:171]
s_waitcnt lgkmcnt(3)
ds_read_b64_tr_b16 v[216:217], v237 offset:24576
v_exp_f32 v158, v158
)"
R"(v_mfma_f32_16x16x32_bf16 v[172:175], v[188:191], v[140:143], v[172:175]
v_exp_f32 v159, v159
v_mfma_f32_16x16x32_bf16 v[164:167], v[188:191], v[136:139], v[164:167]
ds_read_b64_tr_b16 v[218:219], v237 offset:30720
v_cvt_pk_bf16_f32 v182, v156, v157
v_cvt_pk_bf16_f32 v183, v158, v159
v_mfma_f32_16x16x32_bf16 v[0:3], v[204:207], v[176:179], v[0:3]
ds_read_b64_tr_b16 v[220:221], v234 offset:24704
ds_read_b64_tr_b16 v[222:223], v234 offset:30848
v_mfma_f32_16x16x32_bf16 v[4:7], v[204:207], v[180:183], v[4:7]
s_waitcnt lgkmcnt(6)
ds_read_b64_tr_b16 v[184:185], v235 offset:24704
ds_read_b64_tr_b16 v[186:187], v235 offset:30848
v_mfma_f32_16x16x32_bf16 v[12:15], v[208:211], v[180:183], v[12:15]
ds_read_b64_tr_b16 v[188:189], v236 offset:24704
v_add_f32 v238, v238, v144
)"
R"(v_mfma_f32_16x16x32_bf16 v[8:11], v[208:211], v[176:179], v[8:11]
s_waitcnt lgkmcnt(7)
ds_read_b64_tr_b16 v[190:191], v236 offset:30848
v_add_f32 v240, v240, v152
v_mfma_f32_16x16x32_bf16 v[16:19], v[212:215], v[176:179], v[16:19]
ds_read_b64_tr_b16 v[192:193], v237 offset:24704
v_max3_f32 v242, v160, v161, v162
v_mfma_f32_16x16x32_bf16 v[20:23], v[212:215], v[180:183], v[20:23]
s_waitcnt lgkmcnt(7)
ds_read_b64_tr_b16 v[194:195], v237 offset:30848
v_max3_f32 v243, v167, v168, v169
v_mfma_f32_16x16x32_bf16 v[28:31], v[216:219], v[180:183], v[28:31]
ds_read_b64_tr_b16 v[196:197], v234 offset:24832
v_max3_f32 v242, v242, v163, v164
v_mfma_f32_16x16x32_bf16 v[24:27], v[216:219], v[176:179], v[24:27]
s_waitcnt lgkmcnt(7)
)"
R"(ds_read_b64_tr_b16 v[198:199], v234 offset:30976
ds_read_b64_tr_b16 v[200:201], v235 offset:24832
v_mfma_f32_16x16x32_bf16 v[32:35], v[220:223], v[176:179], v[32:35]
ds_read_b64_tr_b16 v[202:203], v235 offset:30976
ds_read_b64_tr_b16 v[204:205], v236 offset:24832
v_mfma_f32_16x16x32_bf16 v[36:39], v[220:223], v[180:183], v[36:39]
s_waitcnt lgkmcnt(9)
ds_read_b64_tr_b16 v[206:207], v236 offset:30976
ds_read_b64_tr_b16 v[208:209], v237 offset:24832
v_mfma_f32_16x16x32_bf16 v[44:47], v[184:187], v[180:183], v[44:47]
ds_read_b64_tr_b16 v[210:211], v237 offset:30976
v_max3_f32 v243, v243, v170, v171
v_mfma_f32_16x16x32_bf16 v[40:43], v[184:187], v[176:179], v[40:43]
s_waitcnt lgkmcnt(10)
v_max3_f32 v242, v242, v165, v166
v_max3_f32 v243, v243, v172, v173
)"
R"(v_mfma_f32_16x16x32_bf16 v[48:51], v[188:191], v[176:179], v[48:51]
v_max3_f32 v242, v242, v174, v175
v_add_f32 v239, v239, v145
v_mfma_f32_16x16x32_bf16 v[52:55], v[188:191], v[180:183], v[52:55]
s_waitcnt lgkmcnt(8)
v_max_f32 v242, v242, v243
v_add_f32 v241, v241, v153
v_mfma_f32_16x16x32_bf16 v[60:63], v[192:195], v[180:183], v[60:63]
v_cmp_lt_f32 vcc, 0x41000000, v242
v_add_f32 v238, v238, v146
v_mfma_f32_16x16x32_bf16 v[56:59], v[192:195], v[176:179], v[56:59]
s_waitcnt lgkmcnt(0)
v_add_f32 v240, v240, v154
v_add_f32 v239, v239, v147
s_barrier
v_mfma_f32_16x16x32_bf16 v[64:67], v[196:199], v[176:179], v[64:67]
)"
R"(ds_read_b128 v[184:187], v232 offset:0
ds_read_b128 v[188:191], v232 offset:6144
v_mfma_f32_16x16x32_bf16 v[68:71], v[196:199], v[180:183], v[68:71]
ds_read_b128 v[192:195], v233 offset:0
v_add_f32 v241, v241, v155
v_mfma_f32_16x16x32_bf16 v[76:79], v[200:203], v[180:183], v[76:79]
ds_read_b128 v[196:199], v233 offset:6144
v_add_f32 v238, v238, v148
v_mfma_f32_16x16x32_bf16 v[72:75], v[200:203], v[176:179], v[72:75]
ds_read_b128 v[200:203], v232 offset:128
v_add_f32 v240, v240, v156
v_mfma_f32_16x16x32_bf16 v[80:83], v[204:207], v[176:179], v[80:83]
v_add_f32 v239, v239, v149
v_add_f32 v241, v241, v157
v_mfma_f32_16x16x32_bf16 v[84:87], v[204:207], v[180:183], v[84:87]
v_add_f32 v238, v238, v150
)"
R"(v_add_f32 v240, v240, v158
v_mfma_f32_16x16x32_bf16 v[92:95], v[208:211], v[180:183], v[92:95]
v_add_f32 v239, v239, v151
v_add_f32 v241, v241, v159
v_mfma_f32_16x16x32_bf16 v[88:91], v[208:211], v[176:179], v[88:91]
s_waitcnt lgkmcnt(4)
s_cbranch_vccz LkB11_2
s_getpc_b64 s[80:81]
s_branch Lrare_1
LkB11_2:
s_cmp_eq_u32 s59, 4
s_cbranch_scc0 Lw4_8
s_mov_b64 exec, 0xf
s_waitcnt vmcnt(0)
buffer_store_dword v251, v250, s[40:43], 0 offen offset:128 sc1
s_mov_b64 exec, -1
)"
R"(Lw4_8:
v_mfma_f32_16x16x32_bf16 v[144:147], v[184:187], v[96:99], v[224:227]
v_exp_f32 v160, v160
v_mfma_f32_16x16x32_bf16 v[152:155], v[184:187], v[100:103], v[228:231]
s_waitcnt lgkmcnt(3)
v_exp_f32 v161, v161
v_mfma_f32_16x16x32_bf16 v[156:159], v[188:191], v[100:103], v[228:231]
v_exp_f32 v162, v162
v_mfma_f32_16x16x32_bf16 v[148:151], v[188:191], v[96:99], v[224:227]
s_waitcnt lgkmcnt(2)
ds_read_b128 v[184:187], v232 offset:6272
ds_read_b128 v[188:191], v233 offset:128
v_mfma_f32_16x16x32_bf16 v[144:147], v[192:195], v[104:107], v[144:147]
v_exp_f32 v163, v163
v_mfma_f32_16x16x32_bf16 v[152:155], v[192:195], v[108:111], v[152:155]
s_waitcnt lgkmcnt(3)
)"
R"(ds_read_b64_tr_b16 v[204:205], v234 offset:36864
v_cvt_pk_bf16_f32 v176, v160, v161
v_cvt_pk_bf16_f32 v177, v162, v163
v_mfma_f32_16x16x32_bf16 v[156:159], v[196:199], v[108:111], v[156:159]
v_exp_f32 v164, v164
v_mfma_f32_16x16x32_bf16 v[148:151], v[196:199], v[104:107], v[148:151]
s_waitcnt lgkmcnt(3)
ds_read_b128 v[192:195], v233 offset:6272
ds_read_b128 v[196:199], v232 offset:256
v_mfma_f32_16x16x32_bf16 v[144:147], v[200:203], v[112:115], v[144:147]
v_exp_f32 v165, v165
v_mfma_f32_16x16x32_bf16 v[152:155], v[200:203], v[116:119], v[152:155]
s_waitcnt lgkmcnt(4)
ds_read_b64_tr_b16 v[206:207], v234 offset:43008
v_exp_f32 v166, v166
v_mfma_f32_16x16x32_bf16 v[156:159], v[184:187], v[116:119], v[156:159]
)"
R"(v_exp_f32 v167, v167
v_mfma_f32_16x16x32_bf16 v[148:151], v[184:187], v[112:115], v[148:151]
s_waitcnt lgkmcnt(4)
ds_read_b128 v[200:203], v232 offset:6400
ds_read_b128 v[184:187], v233 offset:256
v_mfma_f32_16x16x32_bf16 v[144:147], v[188:191], v[120:123], v[144:147]
v_exp_f32 v168, v168
v_mfma_f32_16x16x32_bf16 v[152:155], v[188:191], v[124:127], v[152:155]
s_waitcnt lgkmcnt(4)
ds_read_b64_tr_b16 v[208:209], v235 offset:36864
v_cvt_pk_bf16_f32 v178, v164, v165
v_cvt_pk_bf16_f32 v179, v166, v167
v_mfma_f32_16x16x32_bf16 v[156:159], v[192:195], v[124:127], v[156:159]
v_exp_f32 v169, v169
v_mfma_f32_16x16x32_bf16 v[148:151], v[192:195], v[120:123], v[148:151]
s_waitcnt lgkmcnt(4)
)"
R"(ds_read_b128 v[188:191], v233 offset:6400
ds_read_b64_tr_b16 v[210:211], v235 offset:43008
v_mfma_f32_16x16x32_bf16 v[144:147], v[196:199], v[128:131], v[144:147]
v_exp_f32 v170, v170
v_mfma_f32_16x16x32_bf16 v[152:155], v[196:199], v[132:135], v[152:155]
s_waitcnt lgkmcnt(4)
ds_read_b64_tr_b16 v[212:213], v236 offset:36864
v_exp_f32 v171, v171
v_mfma_f32_16x16x32_bf16 v[156:159], v[200:203], v[132:135], v[156:159]
v_exp_f32 v172, v172
v_mfma_f32_16x16x32_bf16 v[148:151], v[200:203], v[128:131], v[148:151]
s_waitcnt lgkmcnt(4)
ds_read_b64_tr_b16 v[214:215], v236 offset:43008
v_cvt_pk_bf16_f32 v180, v168, v169
v_cvt_pk_bf16_f32 v181, v170, v171
v_mfma_f32_16x16x32_bf16 v[144:147], v[184:187], v[136:139], v[144:147]
)"
R"(v_exp_f32 v173, v173
v_mfma_f32_16x16x32_bf16 v[152:155], v[184:187], v[140:143], v[152:155]
s_waitcnt lgkmcnt(3)
ds_read_b64_tr_b16 v[216:217], v237 offset:36864
v_exp_f32 v174, v174
v_mfma_f32_16x16x32_bf16 v[156:159], v[188:191], v[140:143], v[156:159]
v_exp_f32 v175, v175
v_mfma_f32_16x16x32_bf16 v[148:151], v[188:191], v[136:139], v[148:151]
ds_read_b64_tr_b16 v[218:219], v237 offset:43008
v_cvt_pk_bf16_f32 v182, v172, v173
v_cvt_pk_bf16_f32 v183, v174, v175
v_mfma_f32_16x16x32_bf16 v[0:3], v[204:207], v[176:179], v[0:3]
ds_read_b64_tr_b16 v[220:221], v234 offset:36992
ds_read_b64_tr_b16 v[222:223], v234 offset:43136
v_mfma_f32_16x16x32_bf16 v[4:7], v[204:207], v[180:183], v[4:7]
s_waitcnt lgkmcnt(6)
)"
R"(ds_read_b64_tr_b16 v[184:185], v235 offset:36992
ds_read_b64_tr_b16 v[186:187], v235 offset:43136
v_mfma_f32_16x16x32_bf16 v[12:15], v[208:211], v[180:183], v[12:15]
ds_read_b64_tr_b16 v[188:189], v236 offset:36992
v_add_f32 v238, v238, v160
v_mfma_f32_16x16x32_bf16 v[8:11], v[208:211], v[176:179], v[8:11]
s_waitcnt lgkmcnt(7)
ds_read_b64_tr_b16 v[190:191], v236 offset:43136
v_add_f32 v240, v240, v168
v_mfma_f32_16x16x32_bf16 v[16:19], v[212:215], v[176:179], v[16:19]
ds_read_b64_tr_b16 v[192:193], v237 offset:36992
v_max3_f32 v242, v144, v145, v146
v_mfma_f32_16x16x32_bf16 v[20:23], v[212:215], v[180:183], v[20:23]
s_waitcnt lgkmcnt(7)
ds_read_b64_tr_b16 v[194:195], v237 offset:43136
v_max3_f32 v243, v151, v152, v153
)"
R"(v_mfma_f32_16x16x32_bf16 v[28:31], v[216:219], v[180:183], v[28:31]
ds_read_b64_tr_b16 v[196:197], v234 offset:37120
v_max3_f32 v242, v242, v147, v148
v_mfma_f32_16x16x32_bf16 v[24:27], v[216:219], v[176:179], v[24:27]
s_waitcnt lgkmcnt(7)
ds_read_b64_tr_b16 v[198:199], v234 offset:43264
ds_read_b64_tr_b16 v[200:201], v235 offset:37120
v_mfma_f32_16x16x32_bf16 v[32:35], v[220:223], v[176:179], v[32:35]
ds_read_b64_tr_b16 v[202:203], v235 offset:43264
ds_read_b64_tr_b16 v[204:205], v236 offset:37120
v_mfma_f32_16x16x32_bf16 v[36:39], v[220:223], v[180:183], v[36:39]
s_waitcnt lgkmcnt(9)
ds_read_b64_tr_b16 v[206:207], v236 offset:43264
ds_read_b64_tr_b16 v[208:209], v237 offset:37120
v_mfma_f32_16x16x32_bf16 v[44:47], v[184:187], v[180:183], v[44:47]
ds_read_b64_tr_b16 v[210:211], v237 offset:43264
)"
R"(v_max3_f32 v243, v243, v154, v155
v_mfma_f32_16x16x32_bf16 v[40:43], v[184:187], v[176:179], v[40:43]
s_waitcnt lgkmcnt(10)
v_max3_f32 v242, v242, v149, v150
v_max3_f32 v243, v243, v156, v157
v_mfma_f32_16x16x32_bf16 v[48:51], v[188:191], v[176:179], v[48:51]
v_max3_f32 v242, v242, v158, v159
v_add_f32 v239, v239, v161
v_mfma_f32_16x16x32_bf16 v[52:55], v[188:191], v[180:183], v[52:55]
s_waitcnt lgkmcnt(8)
v_max_f32 v242, v242, v243
v_add_f32 v241, v241, v169
v_mfma_f32_16x16x32_bf16 v[60:63], v[192:195], v[180:183], v[60:63]
v_cmp_lt_f32 vcc, 0x41000000, v242
v_add_f32 v238, v238, v162
v_mfma_f32_16x16x32_bf16 v[56:59], v[192:195], v[176:179], v[56:59]
)"
R"(s_waitcnt lgkmcnt(0)
v_add_f32 v240, v240, v170
v_add_f32 v239, v239, v163
s_mov_b32 s58, 5
Lloop_B12:
s_barrier
v_mfma_f32_16x16x32_bf16 v[64:67], v[196:199], v[176:179], v[64:67]
ds_read_b128 v[184:187], v232 offset:12288
ds_read_b128 v[188:191], v232 offset:18432
v_mfma_f32_16x16x32_bf16 v[68:71], v[196:199], v[180:183], v[68:71]
ds_read_b128 v[192:195], v233 offset:12288
v_add_f32 v241, v241, v171
v_mfma_f32_16x16x32_bf16 v[76:79], v[200:203], v[180:183], v[76:79]
ds_read_b128 v[196:199], v233 offset:18432
v_add_f32 v238, v238, v164
v_mfma_f32_16x16x32_bf16 v[72:75], v[200:203], v[176:179], v[72:75]
)"
R"(ds_read_b128 v[200:203], v232 offset:12416
v_add_f32 v240, v240, v172
v_mfma_f32_16x16x32_bf16 v[80:83], v[204:207], v[176:179], v[80:83]
v_add_f32 v239, v239, v165
v_add_f32 v241, v241, v173
v_mfma_f32_16x16x32_bf16 v[84:87], v[204:207], v[180:183], v[84:87]
v_add_f32 v238, v238, v166
v_add_f32 v240, v240, v174
v_mfma_f32_16x16x32_bf16 v[92:95], v[208:211], v[180:183], v[92:95]
v_add_f32 v239, v239, v167
v_add_f32 v241, v241, v175
v_mfma_f32_16x16x32_bf16 v[88:91], v[208:211], v[176:179], v[88:91]
s_waitcnt lgkmcnt(4)
s_cbranch_vccz LkB12_3
s_getpc_b64 s[80:81]
s_branch Lrare_0
)"
R"(LkB12_3:
v_mfma_f32_16x16x32_bf16 v[160:163], v[184:187], v[96:99], v[224:227]
v_exp_f32 v144, v144
v_mfma_f32_16x16x32_bf16 v[168:171], v[184:187], v[100:103], v[228:231]
s_waitcnt lgkmcnt(3)
v_exp_f32 v145, v145
v_mfma_f32_16x16x32_bf16 v[172:175], v[188:191], v[100:103], v[228:231]
v_exp_f32 v146, v146
v_mfma_f32_16x16x32_bf16 v[164:167], v[188:191], v[96:99], v[224:227]
s_waitcnt lgkmcnt(2)
ds_read_b128 v[184:187], v232 offset:18560
ds_read_b128 v[188:191], v233 offset:12416
v_mfma_f32_16x16x32_bf16 v[160:163], v[192:195], v[104:107], v[160:163]
v_exp_f32 v147, v147
v_mfma_f32_16x16x32_bf16 v[168:171], v[192:195], v[108:111], v[168:171]
s_waitcnt lgkmcnt(3)
)"
R"(ds_read_b64_tr_b16 v[204:205], v234 offset:0
v_cvt_pk_bf16_f32 v176, v144, v145
v_cvt_pk_bf16_f32 v177, v146, v147
v_mfma_f32_16x16x32_bf16 v[172:175], v[196:199], v[108:111], v[172:175]
v_exp_f32 v148, v148
v_mfma_f32_16x16x32_bf16 v[164:167], v[196:199], v[104:107], v[164:167]
s_waitcnt lgkmcnt(3)
ds_read_b128 v[192:195], v233 offset:18560
ds_read_b128 v[196:199], v232 offset:12544
v_mfma_f32_16x16x32_bf16 v[160:163], v[200:203], v[112:115], v[160:163]
v_exp_f32 v149, v149
v_mfma_f32_16x16x32_bf16 v[168:171], v[200:203], v[116:119], v[168:171]
s_waitcnt lgkmcnt(4)
ds_read_b64_tr_b16 v[206:207], v234 offset:6144
v_exp_f32 v150, v150
v_mfma_f32_16x16x32_bf16 v[172:175], v[184:187], v[116:119], v[172:175]
)"
R"(v_exp_f32 v151, v151
v_mfma_f32_16x16x32_bf16 v[164:167], v[184:187], v[112:115], v[164:167]
s_waitcnt lgkmcnt(4)
ds_read_b128 v[200:203], v232 offset:18688
ds_read_b128 v[184:187], v233 offset:12544
v_mfma_f32_16x16x32_bf16 v[160:163], v[188:191], v[120:123], v[160:163]
v_exp_f32 v152, v152
v_mfma_f32_16x16x32_bf16 v[168:171], v[188:191], v[124:127], v[168:171]
s_waitcnt lgkmcnt(4)
ds_read_b64_tr_b16 v[208:209], v235 offset:0
v_cvt_pk_bf16_f32 v178, v148, v149
v_cvt_pk_bf16_f32 v179, v150, v151
v_mfma_f32_16x16x32_bf16 v[172:175], v[192:195], v[124:127], v[172:175]
v_exp_f32 v153, v153
v_mfma_f32_16x16x32_bf16 v[164:167], v[192:195], v[120:123], v[164:167]
s_waitcnt lgkmcnt(4)
)"
R"(ds_read_b128 v[188:191], v233 offset:18688
ds_read_b64_tr_b16 v[210:211], v235 offset:6144
v_mfma_f32_16x16x32_bf16 v[160:163], v[196:199], v[128:131], v[160:163]
v_exp_f32 v154, v154
v_mfma_f32_16x16x32_bf16 v[168:171], v[196:199], v[132:135], v[168:171]
s_waitcnt lgkmcnt(4)
ds_read_b64_tr_b16 v[212:213], v236 offset:0
v_exp_f32 v155, v155
v_mfma_f32_16x16x32_bf16 v[172:175], v[200:203], v[132:135], v[172:175]
v_exp_f32 v156, v156
v_mfma_f32_16x16x32_bf16 v[164:167], v[200:203], v[128:131], v[164:167]
s_waitcnt lgkmcnt(4)
ds_read_b64_tr_b16 v[214:215], v236 offset:6144
v_cvt_pk_bf16_f32 v180, v152, v153
v_cvt_pk_bf16_f32 v181, v154, v155
v_mfma_f32_16x16x32_bf16 v[160:163], v[184:187], v[136:139], v[160:163]
)"
R"(v_exp_f32 v157, v157
v_mfma_f32_16x16x32_bf16 v[168:171], v[184:187], v[140:143], v[168:171]
s_waitcnt lgkmcnt(3)
ds_read_b64_tr_b16 v[216:217], v237 offset:0
v_exp_f32 v158, v158
v_mfma_f32_16x16x32_bf16 v[172:175], v[188:191], v[140:143], v[172:175]
v_exp_f32 v159, v159
v_mfma_f32_16x16x32_bf16 v[164:167], v[188:191], v[136:139], v[164:167]
ds_read_b64_tr_b16 v[218:219], v237 offset:6144
v_cvt_pk_bf16_f32 v182, v156, v157
v_cvt_pk_bf16_f32 v183, v158, v159
v_mfma_f32_16x16x32_bf16 v[0:3], v[204:207], v[176:179], v[0:3]
ds_read_b64_tr_b16 v[220:221], v234 offset:128
ds_read_b64_tr_b16 v[222:223], v234 offset:6272
v_mfma_f32_16x16x32_bf16 v[4:7], v[204:207], v[180:183], v[4:7]
s_waitcnt lgkmcnt(6)
)"
R"(ds_read_b64_tr_b16 v[184:185], v235 offset:128
ds_read_b64_tr_b16 v[186:187], v235 offset:6272
v_mfma_f32_16x16x32_bf16 v[12:15], v[208:211], v[180:183], v[12:15]
ds_read_b64_tr_b16 v[188:189], v236 offset:128
v_add_f32 v238, v238, v144
v_mfma_f32_16x16x32_bf16 v[8:11], v[208:211], v[176:179], v[8:11]
s_waitcnt lgkmcnt(7)
ds_read_b64_tr_b16 v[190:191], v236 offset:6272
v_add_f32 v240, v240, v152
v_mfma_f32_16x16x32_bf16 v[16:19], v[212:215], v[176:179], v[16:19]
ds_read_b64_tr_b16 v[192:193], v237 offset:128
v_max3_f32 v242, v160, v161, v162
v_mfma_f32_16x16x32_bf16 v[20:23], v[212:215], v[180:183], v[20:23]
s_waitcnt lgkmcnt(7)
ds_read_b64_tr_b16 v[194:195], v237 offset:6272
v_max3_f32 v243, v167, v168, v169
)"
R"(v_mfma_f32_16x16x32_bf16 v[28:31], v[216:219], v[180:183], v[28:31]
ds_read_b64_tr_b16 v[196:197], v234 offset:256
v_max3_f32 v242, v242, v163, v164
v_mfma_f32_16x16x32_bf16 v[24:27], v[216:219], v[176:179], v[24:27]
s_waitcnt lgkmcnt(7)
ds_read_b64_tr_b16 v[198:199], v234 offset:6400
ds_read_b64_tr_b16 v[200:201], v235 offset:256
v_mfma_f32_16x16x32_bf16 v[32:35], v[220:223], v[176:179], v[32:35]
ds_read_b64_tr_b16 v[202:203], v235 offset:6400
ds_read_b64_tr_b16 v[204:205], v236 offset:256
v_mfma_f32_16x16x32_bf16 v[36:39], v[220:223], v[180:183], v[36:39]
s_waitcnt lgkmcnt(9)
ds_read_b64_tr_b16 v[206:207], v236 offset:6400
ds_read_b64_tr_b16 v[208:209], v237 offset:256
v_mfma_f32_16x16x32_bf16 v[44:47], v[184:187], v[180:183], v[44:47]
ds_read_b64_tr_b16 v[210:211], v237 offset:6400
)"
R"(v_max3_f32 v243, v243, v170, v171
v_mfma_f32_16x16x32_bf16 v[40:43], v[184:187], v[176:179], v[40:43]
s_waitcnt lgkmcnt(10)
v_max3_f32 v242, v242, v165, v166
v_max3_f32 v243, v243, v172, v173
v_mfma_f32_16x16x32_bf16 v[48:51], v[188:191], v[176:179], v[48:51]
v_max3_f32 v242, v242, v174, v175
v_add_f32 v239, v239, v145
v_mfma_f32_16x16x32_bf16 v[52:55], v[188:191], v[180:183], v[52:55]
s_waitcnt lgkmcnt(8)
v_max_f32 v242, v242, v243
v_add_f32 v241, v241, v153
v_mfma_f32_16x16x32_bf16 v[60:63], v[192:195], v[180:183], v[60:63]
v_cmp_lt_f32 vcc, 0x41000000, v242
v_add_f32 v238, v238, v146
v_mfma_f32_16x16x32_bf16 v[56:59], v[192:195], v[176:179], v[56:59]
)"
R"(s_waitcnt lgkmcnt(0)
v_add_f32 v240, v240, v154
v_add_f32 v239, v239, v147
s_barrier
v_mfma_f32_16x16x32_bf16 v[64:67], v[196:199], v[176:179], v[64:67]
ds_read_b128 v[184:187], v232 offset:24576
ds_read_b128 v[188:191], v232 offset:30720
v_mfma_f32_16x16x32_bf16 v[68:71], v[196:199], v[180:183], v[68:71]
ds_read_b128 v[192:195], v233 offset:24576
v_add_f32 v241, v241, v155
v_mfma_f32_16x16x32_bf16 v[76:79], v[200:203], v[180:183], v[76:79]
ds_read_b128 v[196:199], v233 offset:30720
v_add_f32 v238, v238, v148
v_mfma_f32_16x16x32_bf16 v[72:75], v[200:203], v[176:179], v[72:75]
ds_read_b128 v[200:203], v232 offset:24704
v_add_f32 v240, v240, v156
)"
R"(v_mfma_f32_16x16x32_bf16 v[80:83], v[204:207], v[176:179], v[80:83]
v_add_f32 v239, v239, v149
v_add_f32 v241, v241, v157
v_mfma_f32_16x16x32_bf16 v[84:87], v[204:207], v[180:183], v[84:87]
v_add_f32 v238, v238, v150
v_add_f32 v240, v240, v158
v_mfma_f32_16x16x32_bf16 v[92:95], v[208:211], v[180:183], v[92:95]
v_add_f32 v239, v239, v151
v_add_f32 v241, v241, v159
v_mfma_f32_16x16x32_bf16 v[88:91], v[208:211], v[176:179], v[88:91]
s_waitcnt lgkmcnt(4)
s_cbranch_vccz LkB12_0
s_getpc_b64 s[80:81]
s_branch Lrare_1
LkB12_0:
v_mfma_f32_16x16x32_bf16 v[144:147], v[184:187], v[96:99], v[224:227]
)"
R"(v_exp_f32 v160, v160
v_mfma_f32_16x16x32_bf16 v[152:155], v[184:187], v[100:103], v[228:231]
s_waitcnt lgkmcnt(3)
v_exp_f32 v161, v161
v_mfma_f32_16x16x32_bf16 v[156:159], v[188:191], v[100:103], v[228:231]
v_exp_f32 v162, v162
v_mfma_f32_16x16x32_bf16 v[148:151], v[188:191], v[96:99], v[224:227]
s_waitcnt lgkmcnt(2)
ds_read_b128 v[184:187], v232 offset:30848
ds_read_b128 v[188:191], v233 offset:24704
v_mfma_f32_16x16x32_bf16 v[144:147], v[192:195], v[104:107], v[144:147]
v_exp_f32 v163, v163
v_mfma_f32_16x16x32_bf16 v[152:155], v[192:195], v[108:111], v[152:155]
s_waitcnt lgkmcnt(3)
ds_read_b64_tr_b16 v[204:205], v234 offset:12288
v_cvt_pk_bf16_f32 v176, v160, v161
)"
R"(v_cvt_pk_bf16_f32 v177, v162, v163
v_mfma_f32_16x16x32_bf16 v[156:159], v[196:199], v[108:111], v[156:159]
v_exp_f32 v164, v164
v_mfma_f32_16x16x32_bf16 v[148:151], v[196:199], v[104:107], v[148:151]
s_waitcnt lgkmcnt(3)
ds_read_b128 v[192:195], v233 offset:30848
ds_read_b128 v[196:199], v232 offset:24832
v_mfma_f32_16x16x32_bf16 v[144:147], v[200:203], v[112:115], v[144:147]
v_exp_f32 v165, v165
v_mfma_f32_16x16x32_bf16 v[152:155], v[200:203], v[116:119], v[152:155]
s_waitcnt lgkmcnt(4)
ds_read_b64_tr_b16 v[206:207], v234 offset:18432
v_exp_f32 v166, v166
v_mfma_f32_16x16x32_bf16 v[156:159], v[184:187], v[116:119], v[156:159]
v_exp_f32 v167, v167
v_mfma_f32_16x16x32_bf16 v[148:151], v[184:187], v[112:115], v[148:151]
)"
R"(s_waitcnt lgkmcnt(4)
ds_read_b128 v[200:203], v232 offset:30976
ds_read_b128 v[184:187], v233 offset:24832
v_mfma_f32_16x16x32_bf16 v[144:147], v[188:191], v[120:123], v[144:147]
v_exp_f32 v168, v168
v_mfma_f32_16x16x32_bf16 v[152:155], v[188:191], v[124:127], v[152:155]
s_waitcnt lgkmcnt(4)
ds_read_b64_tr_b16 v[208:209], v235 offset:12288
v_cvt_pk_bf16_f32 v178, v164, v165
v_cvt_pk_bf16_f32 v179, v166, v167
v_mfma_f32_16x16x32_bf16 v[156:159], v[192:195], v[124:127], v[156:159]
v_exp_f32 v169, v169
v_mfma_f32_16x16x32_bf16 v[148:151], v[192:195], v[120:123], v[148:151]
s_waitcnt lgkmcnt(4)
ds_read_b128 v[188:191], v233 offset:30976
ds_read_b64_tr_b16 v[210:211], v235 offset:18432
)"
R"(v_mfma_f32_16x16x32_bf16 v[144:147], v[196:199], v[128:131], v[144:147]
v_exp_f32 v170, v170
v_mfma_f32_16x16x32_bf16 v[152:155], v[196:199], v[132:135], v[152:155]
s_waitcnt lgkmcnt(4)
ds_read_b64_tr_b16 v[212:213], v236 offset:12288
v_exp_f32 v171, v171
v_mfma_f32_16x16x32_bf16 v[156:159], v[200:203], v[132:135], v[156:159]
v_exp_f32 v172, v172
v_mfma_f32_16x16x32_bf16 v[148:151], v[200:203], v[128:131], v[148:151]
s_waitcnt lgkmcnt(4)
ds_read_b64_tr_b16 v[214:215], v236 offset:18432
v_cvt_pk_bf16_f32 v180, v168, v169
v_cvt_pk_bf16_f32 v181, v170, v171
v_mfma_f32_16x16x32_bf16 v[144:147], v[184:187], v[136:139], v[144:147]
v_exp_f32 v173, v173
v_mfma_f32_16x16x32_bf16 v[152:155], v[184:187], v[140:143], v[152:155]
)"
R"(s_waitcnt lgkmcnt(3)
ds_read_b64_tr_b16 v[216:217], v237 offset:12288
v_exp_f32 v174, v174
v_mfma_f32_16x16x32_bf16 v[156:159], v[188:191], v[140:143], v[156:159]
v_exp_f32 v175, v175
v_mfma_f32_16x16x32_bf16 v[148:151], v[188:191], v[136:139], v[148:151]
ds_read_b64_tr_b16 v[218:219], v237 offset:18432
v_cvt_pk_bf16_f32 v182, v172, v173
v_cvt_pk_bf16_f32 v183, v174, v175
v_mfma_f32_16x16x32_bf16 v[0:3], v[204:207], v[176:179], v[0:3]
ds_read_b64_tr_b16 v[220:221], v234 offset:12416
ds_read_b64_tr_b16 v[222:223], v234 offset:18560
v_mfma_f32_16x16x32_bf16 v[4:7], v[204:207], v[180:183], v[4:7]
s_waitcnt lgkmcnt(6)
ds_read_b64_tr_b16 v[184:185], v235 offset:12416
ds_read_b64_tr_b16 v[186:187], v235 offset:18560
)"
R"(v_mfma_f32_16x16x32_bf16 v[12:15], v[208:211], v[180:183], v[12:15]
ds_read_b64_tr_b16 v[188:189], v236 offset:12416
v_add_f32 v238, v238, v160
v_mfma_f32_16x16x32_bf16 v[8:11], v[208:211], v[176:179], v[8:11]
s_waitcnt lgkmcnt(7)
ds_read_b64_tr_b16 v[190:191], v236 offset:18560
v_add_f32 v240, v240, v168
v_mfma_f32_16x16x32_bf16 v[16:19], v[212:215], v[176:179], v[16:19]
ds_read_b64_tr_b16 v[192:193], v237 offset:12416
v_max3_f32 v242, v144, v145, v146
v_mfma_f32_16x16x32_bf16 v[20:23], v[212:215], v[180:183], v[20:23]
s_waitcnt lgkmcnt(7)
ds_read_b64_tr_b16 v[194:195], v237 offset:18560
v_max3_f32 v243, v151, v152, v153
v_mfma_f32_16x16x32_bf16 v[28:31], v[216:219], v[180:183], v[28:31]
ds_read_b64_tr_b16 v[196:197], v234 offset:12544
)"
R"(v_max3_f32 v242, v242, v147, v148
v_mfma_f32_16x16x32_bf16 v[24:27], v[216:219], v[176:179], v[24:27]
s_waitcnt lgkmcnt(7)
ds_read_b64_tr_b16 v[198:199], v234 offset:18688
ds_read_b64_tr_b16 v[200:201], v235 offset:12544
v_mfma_f32_16x16x32_bf16 v[32:35], v[220:223], v[176:179], v[32:35]
ds_read_b64_tr_b16 v[202:203], v235 offset:18688
ds_read_b64_tr_b16 v[204:205], v236 offset:12544
v_mfma_f32_16x16x32_bf16 v[36:39], v[220:223], v[180:183], v[36:39]
s_waitcnt lgkmcnt(9)
ds_read_b64_tr_b16 v[206:207], v236 offset:18688
ds_read_b64_tr_b16 v[208:209], v237 offset:12544
v_mfma_f32_16x16x32_bf16 v[44:47], v[184:187], v[180:183], v[44:47]
ds_read_b64_tr_b16 v[210:211], v237 offset:18688
v_max3_f32 v243, v243, v154, v155
v_mfma_f32_16x16x32_bf16 v[40:43], v[184:187], v[176:179], v[40:43]
)"
R"(s_waitcnt lgkmcnt(10)
v_max3_f32 v242, v242, v149, v150
v_max3_f32 v243, v243, v156, v157
v_mfma_f32_16x16x32_bf16 v[48:51], v[188:191], v[176:179], v[48:51]
v_max3_f32 v242, v242, v158, v159
v_add_f32 v239, v239, v161
v_mfma_f32_16x16x32_bf16 v[52:55], v[188:191], v[180:183], v[52:55]
s_waitcnt lgkmcnt(8)
v_max_f32 v242, v242, v243
v_add_f32 v241, v241, v169
v_mfma_f32_16x16x32_bf16 v[60:63], v[192:195], v[180:183], v[60:63]
v_cmp_lt_f32 vcc, 0x41000000, v242
v_add_f32 v238, v238, v162
v_mfma_f32_16x16x32_bf16 v[56:59], v[192:195], v[176:179], v[56:59]
s_waitcnt lgkmcnt(0)
v_add_f32 v240, v240, v170
)"
R"(v_add_f32 v239, v239, v163
s_barrier
v_mfma_f32_16x16x32_bf16 v[64:67], v[196:199], v[176:179], v[64:67]
ds_read_b128 v[184:187], v232 offset:36864
ds_read_b128 v[188:191], v232 offset:43008
v_mfma_f32_16x16x32_bf16 v[68:71], v[196:199], v[180:183], v[68:71]
ds_read_b128 v[192:195], v233 offset:36864
v_add_f32 v241, v241, v171
v_mfma_f32_16x16x32_bf16 v[76:79], v[200:203], v[180:183], v[76:79]
ds_read_b128 v[196:199], v233 offset:43008
v_add_f32 v238, v238, v164
v_mfma_f32_16x16x32_bf16 v[72:75], v[200:203], v[176:179], v[72:75]
ds_read_b128 v[200:203], v232 offset:36992
v_add_f32 v240, v240, v172
v_mfma_f32_16x16x32_bf16 v[80:83], v[204:207], v[176:179], v[80:83]
v_add_f32 v239, v239, v165
)"
R"(v_add_f32 v241, v241, v173
v_mfma_f32_16x16x32_bf16 v[84:87], v[204:207], v[180:183], v[84:87]
v_add_f32 v238, v238, v166
v_add_f32 v240, v240, v174
v_mfma_f32_16x16x32_bf16 v[92:95], v[208:211], v[180:183], v[92:95]
v_add_f32 v239, v239, v167
v_add_f32 v241, v241, v175
v_mfma_f32_16x16x32_bf16 v[88:91], v[208:211], v[176:179], v[88:91]
s_waitcnt lgkmcnt(4)
s_cbranch_vccz LkB12_1
s_getpc_b64 s[80:81]
s_branch Lrare_0
LkB12_1:
v_mfma_f32_16x16x32_bf16 v[160:163], v[184:187], v[96:99], v[224:227]
v_exp_f32 v144, v144
v_mfma_f32_16x16x32_bf16 v[168:171], v[184:187], v[100:103], v[228:231]
)"
R"(s_waitcnt lgkmcnt(3)
v_exp_f32 v145, v145
v_mfma_f32_16x16x32_bf16 v[172:175], v[188:191], v[100:103], v[228:231]
v_exp_f32 v146, v146
v_mfma_f32_16x16x32_bf16 v[164:167], v[188:191], v[96:99], v[224:227]
s_waitcnt lgkmcnt(2)
ds_read_b128 v[184:187], v232 offset:43136
ds_read_b128 v[188:191], v233 offset:36992
v_mfma_f32_16x16x32_bf16 v[160:163], v[192:195], v[104:107], v[160:163]
v_exp_f32 v147, v147
v_mfma_f32_16x16x32_bf16 v[168:171], v[192:195], v[108:111], v[168:171]
s_waitcnt lgkmcnt(3)
ds_read_b64_tr_b16 v[204:205], v234 offset:24576
v_cvt_pk_bf16_f32 v176, v144, v145
v_cvt_pk_bf16_f32 v177, v146, v147
v_mfma_f32_16x16x32_bf16 v[172:175], v[196:199], v[108:111], v[172:175]
)"
R"(v_exp_f32 v148, v148
v_mfma_f32_16x16x32_bf16 v[164:167], v[196:199], v[104:107], v[164:167]
s_waitcnt lgkmcnt(3)
ds_read_b128 v[192:195], v233 offset:43136
ds_read_b128 v[196:199], v232 offset:37120
v_mfma_f32_16x16x32_bf16 v[160:163], v[200:203], v[112:115], v[160:163]
v_exp_f32 v149, v149
v_mfma_f32_16x16x32_bf16 v[168:171], v[200:203], v[116:119], v[168:171]
s_waitcnt lgkmcnt(4)
ds_read_b64_tr_b16 v[206:207], v234 offset:30720
v_exp_f32 v150, v150
v_mfma_f32_16x16x32_bf16 v[172:175], v[184:187], v[116:119], v[172:175]
v_exp_f32 v151, v151
v_mfma_f32_16x16x32_bf16 v[164:167], v[184:187], v[112:115], v[164:167]
s_waitcnt lgkmcnt(4)
ds_read_b128 v[200:203], v232 offset:43264
)"
R"(ds_read_b128 v[184:187], v233 offset:37120
v_mfma_f32_16x16x32_bf16 v[160:163], v[188:191], v[120:123], v[160:163]
v_exp_f32 v152, v152
v_mfma_f32_16x16x32_bf16 v[168:171], v[188:191], v[124:127], v[168:171]
s_waitcnt lgkmcnt(4)
ds_read_b64_tr_b16 v[208:209], v235 offset:24576
v_cvt_pk_bf16_f32 v178, v148, v149
v_cvt_pk_bf16_f32 v179, v150, v151
v_mfma_f32_16x16x32_bf16 v[172:175], v[192:195], v[124:127], v[172:175]
v_exp_f32 v153, v153
v_mfma_f32_16x16x32_bf16 v[164:167], v[192:195], v[120:123], v[164:167]
s_waitcnt lgkmcnt(4)
ds_read_b128 v[188:191], v233 offset:43264
ds_read_b64_tr_b16 v[210:211], v235 offset:30720
v_mfma_f32_16x16x32_bf16 v[160:163], v[196:199], v[128:131], v[160:163]
v_exp_f32 v154, v154
)"
R"(v_mfma_f32_16x16x32_bf16 v[168:171], v[196:199], v[132:135], v[168:171]
s_waitcnt lgkmcnt(4)
ds_read_b64_tr_b16 v[212:213], v236 offset:24576
v_exp_f32 v155, v155
v_mfma_f32_16x16x32_bf16 v[172:175], v[200:203], v[132:135], v[172:175]
v_exp_f32 v156, v156
v_mfma_f32_16x16x32_bf16 v[164:167], v[200:203], v[128:131], v[164:167]
s_waitcnt lgkmcnt(4)
ds_read_b64_tr_b16 v[214:215], v236 offset:30720
v_cvt_pk_bf16_f32 v180, v152, v153
v_cvt_pk_bf16_f32 v181, v154, v155
v_mfma_f32_16x16x32_bf16 v[160:163], v[184:187], v[136:139], v[160:163]
v_exp_f32 v157, v157
v_mfma_f32_16x16x32_bf16 v[168:171], v[184:187], v[140:143], v[168:171]
s_waitcnt lgkmcnt(3)
ds_read_b64_tr_b16 v[216:217], v237 offset:24576
)"
R"(v_exp_f32 v158, v158
v_mfma_f32_16x16x32_bf16 v[172:175], v[188:191], v[140:143], v[172:175]
v_exp_f32 v159, v159
v_mfma_f32_16x16x32_bf16 v[164:167], v[188:191], v[136:139], v[164:167]
ds_read_b64_tr_b16 v[218:219], v237 offset:30720
v_cvt_pk_bf16_f32 v182, v156, v157
v_cvt_pk_bf16_f32 v183, v158, v159
v_mfma_f32_16x16x32_bf16 v[0:3], v[204:207], v[176:179], v[0:3]
ds_read_b64_tr_b16 v[220:221], v234 offset:24704
ds_read_b64_tr_b16 v[222:223], v234 offset:30848
v_mfma_f32_16x16x32_bf16 v[4:7], v[204:207], v[180:183], v[4:7]
s_waitcnt lgkmcnt(6)
ds_read_b64_tr_b16 v[184:185], v235 offset:24704
ds_read_b64_tr_b16 v[186:187], v235 offset:30848
v_mfma_f32_16x16x32_bf16 v[12:15], v[208:211], v[180:183], v[12:15]
ds_read_b64_tr_b16 v[188:189], v236 offset:24704
)"
R"(v_add_f32 v238, v238, v144
v_mfma_f32_16x16x32_bf16 v[8:11], v[208:211], v[176:179], v[8:11]
s_waitcnt lgkmcnt(7)
ds_read_b64_tr_b16 v[190:191], v236 offset:30848
v_add_f32 v240, v240, v152
v_mfma_f32_16x16x32_bf16 v[16:19], v[212:215], v[176:179], v[16:19]
ds_read_b64_tr_b16 v[192:193], v237 offset:24704
v_max3_f32 v242, v160, v161, v162
v_mfma_f32_16x16x32_bf16 v[20:23], v[212:215], v[180:183], v[20:23]
s_waitcnt lgkmcnt(7)
ds_read_b64_tr_b16 v[194:195], v237 offset:30848
v_max3_f32 v243, v167, v168, v169
v_mfma_f32_16x16x32_bf16 v[28:31], v[216:219], v[180:183], v[28:31]
ds_read_b64_tr_b16 v[196:197], v234 offset:24832
v_max3_f32 v242, v242, v163, v164
v_mfma_f32_16x16x32_bf16 v[24:27], v[216:219], v[176:179], v[24:27]
)"
R"(s_waitcnt lgkmcnt(7)
ds_read_b64_tr_b16 v[198:199], v234 offset:30976
ds_read_b64_tr_b16 v[200:201], v235 offset:24832
v_mfma_f32_16x16x32_bf16 v[32:35], v[220:223], v[176:179], v[32:35]
ds_read_b64_tr_b16 v[202:203], v235 offset:30976
ds_read_b64_tr_b16 v[204:205], v236 offset:24832
v_mfma_f32_16x16x32_bf16 v[36:39], v[220:223], v[180:183], v[36:39]
s_waitcnt lgkmcnt(9)
ds_read_b64_tr_b16 v[206:207], v236 offset:30976
ds_read_b64_tr_b16 v[208:209], v237 offset:24832
v_mfma_f32_16x16x32_bf16 v[44:47], v[184:187], v[180:183], v[44:47]
ds_read_b64_tr_b16 v[210:211], v237 offset:30976
v_max3_f32 v243, v243, v170, v171
v_mfma_f32_16x16x32_bf16 v[40:43], v[184:187], v[176:179], v[40:43]
s_waitcnt lgkmcnt(10)
v_max3_f32 v242, v242, v165, v166
)"
R"(v_max3_f32 v243, v243, v172, v173
v_mfma_f32_16x16x32_bf16 v[48:51], v[188:191], v[176:179], v[48:51]
v_max3_f32 v242, v242, v174, v175
v_add_f32 v239, v239, v145
v_mfma_f32_16x16x32_bf16 v[52:55], v[188:191], v[180:183], v[52:55]
s_waitcnt lgkmcnt(8)
v_max_f32 v242, v242, v243
v_add_f32 v241, v241, v153
v_mfma_f32_16x16x32_bf16 v[60:63], v[192:195], v[180:183], v[60:63]
v_cmp_lt_f32 vcc, 0x41000000, v242
v_add_f32 v238, v238, v146
v_mfma_f32_16x16x32_bf16 v[56:59], v[192:195], v[176:179], v[56:59]
s_waitcnt lgkmcnt(0)
v_add_f32 v240, v240, v154
v_add_f32 v239, v239, v147
s_barrier
)"
R"(v_mfma_f32_16x16x32_bf16 v[64:67], v[196:199], v[176:179], v[64:67]
ds_read_b128 v[184:187], v232 offset:0
ds_read_b128 v[188:191], v232 offset:6144
v_mfma_f32_16x16x32_bf16 v[68:71], v[196:199], v[180:183], v[68:71]
ds_read_b128 v[192:195], v233 offset:0
v_add_f32 v241, v241, v155
v_mfma_f32_16x16x32_bf16 v[76:79], v[200:203], v[180:183], v[76:79]
ds_read_b128 v[196:199], v233 offset:6144
v_add_f32 v238, v238, v148
v_mfma_f32_16x16x32_bf16 v[72:75], v[200:203], v[176:179], v[72:75]
ds_read_b128 v[200:203], v232 offset:128
v_add_f32 v240, v240, v156
v_mfma_f32_16x16x32_bf16 v[80:83], v[204:207], v[176:179], v[80:83]
v_add_f32 v239, v239, v149
v_add_f32 v241, v241, v157
v_mfma_f32_16x16x32_bf16 v[84:87], v[204:207], v[180:183], v[84:87]
)"
R"(v_add_f32 v238, v238, v150
v_add_f32 v240, v240, v158
v_mfma_f32_16x16x32_bf16 v[92:95], v[208:211], v[180:183], v[92:95]
v_add_f32 v239, v239, v151
v_add_f32 v241, v241, v159
v_mfma_f32_16x16x32_bf16 v[88:91], v[208:211], v[176:179], v[88:91]
s_waitcnt lgkmcnt(4)
s_cbranch_vccz LkB12_2
s_getpc_b64 s[80:81]
s_branch Lrare_1
LkB12_2:
v_mfma_f32_16x16x32_bf16 v[144:147], v[184:187], v[96:99], v[224:227]
v_exp_f32 v160, v160
v_mfma_f32_16x16x32_bf16 v[152:155], v[184:187], v[100:103], v[228:231]
s_waitcnt lgkmcnt(3)
v_exp_f32 v161, v161
)"
R"(v_mfma_f32_16x16x32_bf16 v[156:159], v[188:191], v[100:103], v[228:231]
v_exp_f32 v162, v162
v_mfma_f32_16x16x32_bf16 v[148:151], v[188:191], v[96:99], v[224:227]
s_waitcnt lgkmcnt(2)
ds_read_b128 v[184:187], v232 offset:6272
ds_read_b128 v[188:191], v233 offset:128
v_mfma_f32_16x16x32_bf16 v[144:147], v[192:195], v[104:107], v[144:147]
v_exp_f32 v163, v163
v_mfma_f32_16x16x32_bf16 v[152:155], v[192:195], v[108:111], v[152:155]
s_waitcnt lgkmcnt(3)
ds_read_b64_tr_b16 v[204:205], v234 offset:36864
v_cvt_pk_bf16_f32 v176, v160, v161
v_cvt_pk_bf16_f32 v177, v162, v163
v_mfma_f32_16x16x32_bf16 v[156:159], v[196:199], v[108:111], v[156:159]
v_exp_f32 v164, v164
v_mfma_f32_16x16x32_bf16 v[148:151], v[196:199], v[104:107], v[148:151]
)"
R"(s_waitcnt lgkmcnt(3)
ds_read_b128 v[192:195], v233 offset:6272
ds_read_b128 v[196:199], v232 offset:256
v_mfma_f32_16x16x32_bf16 v[144:147], v[200:203], v[112:115], v[144:147]
v_exp_f32 v165, v165
v_mfma_f32_16x16x32_bf16 v[152:155], v[200:203], v[116:119], v[152:155]
s_waitcnt lgkmcnt(4)
ds_read_b64_tr_b16 v[206:207], v234 offset:43008
v_exp_f32 v166, v166
v_mfma_f32_16x16x32_bf16 v[156:159], v[184:187], v[116:119], v[156:159]
v_exp_f32 v167, v167
v_mfma_f32_16x16x32_bf16 v[148:151], v[184:187], v[112:115], v[148:151]
s_waitcnt lgkmcnt(4)
ds_read_b128 v[200:203], v232 offset:6400
ds_read_b128 v[184:187], v233 offset:256
v_mfma_f32_16x16x32_bf16 v[144:147], v[188:191], v[120:123], v[144:147]
)"
R"(v_exp_f32 v168, v168
v_mfma_f32_16x16x32_bf16 v[152:155], v[188:191], v[124:127], v[152:155]
s_waitcnt lgkmcnt(4)
ds_read_b64_tr_b16 v[208:209], v235 offset:36864
v_cvt_pk_bf16_f32 v178, v164, v165
v_cvt_pk_bf16_f32 v179, v166, v167
v_mfma_f32_16x16x32_bf16 v[156:159], v[192:195], v[124:127], v[156:159]
v_exp_f32 v169, v169
v_mfma_f32_16x16x32_bf16 v[148:151], v[192:195], v[120:123], v[148:151]
s_waitcnt lgkmcnt(4)
ds_read_b128 v[188:191], v233 offset:6400
ds_read_b64_tr_b16 v[210:211], v235 offset:43008
v_mfma_f32_16x16x32_bf16 v[144:147], v[196:199], v[128:131], v[144:147]
v_exp_f32 v170, v170
v_mfma_f32_16x16x32_bf16 v[152:155], v[196:199], v[132:135], v[152:155]
s_waitcnt lgkmcnt(4)
)"
R"(ds_read_b64_tr_b16 v[212:213], v236 offset:36864
v_exp_f32 v171, v171
v_mfma_f32_16x16x32_bf16 v[156:159], v[200:203], v[132:135], v[156:159]
v_exp_f32 v172, v172
v_mfma_f32_16x16x32_bf16 v[148:151], v[200:203], v[128:131], v[148:151]
s_waitcnt lgkmcnt(4)
ds_read_b64_tr_b16 v[214:215], v236 offset:43008
v_cvt_pk_bf16_f32 v180, v168, v169
v_cvt_pk_bf16_f32 v181, v170, v171
v_mfma_f32_16x16x32_bf16 v[144:147], v[184:187], v[136:139], v[144:147]
v_exp_f32 v173, v173
v_mfma_f32_16x16x32_bf16 v[152:155], v[184:187], v[140:143], v[152:155]
s_waitcnt lgkmcnt(3)
ds_read_b64_tr_b16 v[216:217], v237 offset:36864
v_exp_f32 v174, v174
v_mfma_f32_16x16x32_bf16 v[156:159], v[188:191], v[140:143], v[156:159]
)"
R"(v_exp_f32 v175, v175
v_mfma_f32_16x16x32_bf16 v[148:151], v[188:191], v[136:139], v[148:151]
ds_read_b64_tr_b16 v[218:219], v237 offset:43008
v_cvt_pk_bf16_f32 v182, v172, v173
v_cvt_pk_bf16_f32 v183, v174, v175
v_mfma_f32_16x16x32_bf16 v[0:3], v[204:207], v[176:179], v[0:3]
ds_read_b64_tr_b16 v[220:221], v234 offset:36992
ds_read_b64_tr_b16 v[222:223], v234 offset:43136
v_mfma_f32_16x16x32_bf16 v[4:7], v[204:207], v[180:183], v[4:7]
s_waitcnt lgkmcnt(6)
ds_read_b64_tr_b16 v[184:185], v235 offset:36992
ds_read_b64_tr_b16 v[186:187], v235 offset:43136
v_mfma_f32_16x16x32_bf16 v[12:15], v[208:211], v[180:183], v[12:15]
ds_read_b64_tr_b16 v[188:189], v236 offset:36992
v_add_f32 v238, v238, v160
v_mfma_f32_16x16x32_bf16 v[8:11], v[208:211], v[176:179], v[8:11]
)"
R"(s_waitcnt lgkmcnt(7)
ds_read_b64_tr_b16 v[190:191], v236 offset:43136
v_add_f32 v240, v240, v168
v_mfma_f32_16x16x32_bf16 v[16:19], v[212:215], v[176:179], v[16:19]
ds_read_b64_tr_b16 v[192:193], v237 offset:36992
v_max3_f32 v242, v144, v145, v146
v_mfma_f32_16x16x32_bf16 v[20:23], v[212:215], v[180:183], v[20:23]
s_waitcnt lgkmcnt(7)
ds_read_b64_tr_b16 v[194:195], v237 offset:43136
v_max3_f32 v243, v151, v152, v153
v_mfma_f32_16x16x32_bf16 v[28:31], v[216:219], v[180:183], v[28:31]
ds_read_b64_tr_b16 v[196:197], v234 offset:37120
v_max3_f32 v242, v242, v147, v148
v_mfma_f32_16x16x32_bf16 v[24:27], v[216:219], v[176:179], v[24:27]
s_waitcnt lgkmcnt(7)
ds_read_b64_tr_b16 v[198:199], v234 offset:43264
)"
R"(ds_read_b64_tr_b16 v[200:201], v235 offset:37120
v_mfma_f32_16x16x32_bf16 v[32:35], v[220:223], v[176:179], v[32:35]
ds_read_b64_tr_b16 v[202:203], v235 offset:43264
ds_read_b64_tr_b16 v[204:205], v236 offset:37120
v_mfma_f32_16x16x32_bf16 v[36:39], v[220:223], v[180:183], v[36:39]
s_waitcnt lgkmcnt(9)
ds_read_b64_tr_b16 v[206:207], v236 offset:43264
ds_read_b64_tr_b16 v[208:209], v237 offset:37120
v_mfma_f32_16x16x32_bf16 v[44:47], v[184:187], v[180:183], v[44:47]
ds_read_b64_tr_b16 v[210:211], v237 offset:43264
v_max3_f32 v243, v243, v154, v155
v_mfma_f32_16x16x32_bf16 v[40:43], v[184:187], v[176:179], v[40:43]
s_waitcnt lgkmcnt(10)
v_max3_f32 v242, v242, v149, v150
v_max3_f32 v243, v243, v156, v157
v_mfma_f32_16x16x32_bf16 v[48:51], v[188:191], v[176:179], v[48:51]
)"
R"(v_max3_f32 v242, v242, v158, v159
v_add_f32 v239, v239, v161
v_mfma_f32_16x16x32_bf16 v[52:55], v[188:191], v[180:183], v[52:55]
s_waitcnt lgkmcnt(8)
v_max_f32 v242, v242, v243
v_add_f32 v241, v241, v169
v_mfma_f32_16x16x32_bf16 v[60:63], v[192:195], v[180:183], v[60:63]
v_cmp_lt_f32 vcc, 0x41000000, v242
v_add_f32 v238, v238, v162
v_mfma_f32_16x16x32_bf16 v[56:59], v[192:195], v[176:179], v[56:59]
s_waitcnt lgkmcnt(0)
v_add_f32 v240, v240, v170
v_add_f32 v239, v239, v163
s_sub_u32 s58, s58, 1
s_cmp_lg_u32 s58, 0
s_cbranch_scc1 Lloop_B12
)"
R"(s_barrier
v_mfma_f32_16x16x32_bf16 v[64:67], v[196:199], v[176:179], v[64:67]
ds_read_b128 v[184:187], v232 offset:12288
ds_read_b128 v[188:191], v232 offset:18432
v_mfma_f32_16x16x32_bf16 v[68:71], v[196:199], v[180:183], v[68:71]
ds_read_b128 v[192:195], v233 offset:12288
v_add_f32 v241, v241, v171
v_mfma_f32_16x16x32_bf16 v[76:79], v[200:203], v[180:183], v[76:79]
ds_read_b128 v[196:199], v233 offset:18432
v_add_f32 v238, v238, v164
v_mfma_f32_16x16x32_bf16 v[72:75], v[200:203], v[176:179], v[72:75]
ds_read_b128 v[200:203], v232 offset:12416
v_add_f32 v240, v240, v172
v_mfma_f32_16x16x32_bf16 v[80:83], v[204:207], v[176:179], v[80:83]
v_add_f32 v239, v239, v165
v_add_f32 v241, v241, v173
)"
R"(v_mfma_f32_16x16x32_bf16 v[84:87], v[204:207], v[180:183], v[84:87]
v_add_f32 v238, v238, v166
v_add_f32 v240, v240, v174
v_mfma_f32_16x16x32_bf16 v[92:95], v[208:211], v[180:183], v[92:95]
v_add_f32 v239, v239, v167
v_add_f32 v241, v241, v175
v_mfma_f32_16x16x32_bf16 v[88:91], v[208:211], v[176:179], v[88:91]
s_waitcnt lgkmcnt(4)
s_cbranch_vccz LkB13_3
s_getpc_b64 s[80:81]
s_branch Lrare_0
LkB13_3:
v_mfma_f32_16x16x32_bf16 v[160:163], v[184:187], v[96:99], v[224:227]
v_exp_f32 v144, v144
v_mfma_f32_16x16x32_bf16 v[168:171], v[184:187], v[100:103], v[228:231]
s_waitcnt lgkmcnt(3)
)"
R"(v_exp_f32 v145, v145
v_mfma_f32_16x16x32_bf16 v[172:175], v[188:191], v[100:103], v[228:231]
v_exp_f32 v146, v146
v_mfma_f32_16x16x32_bf16 v[164:167], v[188:191], v[96:99], v[224:227]
s_waitcnt lgkmcnt(2)
ds_read_b128 v[184:187], v232 offset:18560
ds_read_b128 v[188:191], v233 offset:12416
v_mfma_f32_16x16x32_bf16 v[160:163], v[192:195], v[104:107], v[160:163]
v_exp_f32 v147, v147
v_mfma_f32_16x16x32_bf16 v[168:171], v[192:195], v[108:111], v[168:171]
s_waitcnt lgkmcnt(3)
ds_read_b64_tr_b16 v[204:205], v234 offset:0
v_cvt_pk_bf16_f32 v176, v144, v145
v_cvt_pk_bf16_f32 v177, v146, v147
v_mfma_f32_16x16x32_bf16 v[172:175], v[196:199], v[108:111], v[172:175]
v_exp_f32 v148, v148
)"
R"(v_mfma_f32_16x16x32_bf16 v[164:167], v[196:199], v[104:107], v[164:167]
s_waitcnt lgkmcnt(3)
ds_read_b128 v[192:195], v233 offset:18560
ds_read_b128 v[196:199], v232 offset:12544
v_mfma_f32_16x16x32_bf16 v[160:163], v[200:203], v[112:115], v[160:163]
v_exp_f32 v149, v149
v_mfma_f32_16x16x32_bf16 v[168:171], v[200:203], v[116:119], v[168:171]
s_waitcnt lgkmcnt(4)
ds_read_b64_tr_b16 v[206:207], v234 offset:6144
v_exp_f32 v150, v150
v_mfma_f32_16x16x32_bf16 v[172:175], v[184:187], v[116:119], v[172:175]
v_exp_f32 v151, v151
v_mfma_f32_16x16x32_bf16 v[164:167], v[184:187], v[112:115], v[164:167]
s_waitcnt lgkmcnt(4)
ds_read_b128 v[200:203], v232 offset:18688
ds_read_b128 v[184:187], v233 offset:12544
)"
R"(v_mfma_f32_16x16x32_bf16 v[160:163], v[188:191], v[120:123], v[160:163]
v_exp_f32 v152, v152
v_mfma_f32_16x16x32_bf16 v[168:171], v[188:191], v[124:127], v[168:171]
s_waitcnt lgkmcnt(4)
ds_read_b64_tr_b16 v[208:209], v235 offset:0
v_cvt_pk_bf16_f32 v178, v148, v149
v_cvt_pk_bf16_f32 v179, v150, v151
v_mfma_f32_16x16x32_bf16 v[172:175], v[192:195], v[124:127], v[172:175]
v_exp_f32 v153, v153
v_mfma_f32_16x16x32_bf16 v[164:167], v[192:195], v[120:123], v[164:167]
s_waitcnt lgkmcnt(4)
ds_read_b128 v[188:191], v233 offset:18688
ds_read_b64_tr_b16 v[210:211], v235 offset:6144
v_mfma_f32_16x16x32_bf16 v[160:163], v[196:199], v[128:131], v[160:163]
v_exp_f32 v154, v154
v_mfma_f32_16x16x32_bf16 v[168:171], v[196:199], v[132:135], v[168:171]
)"
R"(s_waitcnt lgkmcnt(4)
ds_read_b64_tr_b16 v[212:213], v236 offset:0
v_exp_f32 v155, v155
v_mfma_f32_16x16x32_bf16 v[172:175], v[200:203], v[132:135], v[172:175]
v_exp_f32 v156, v156
v_mfma_f32_16x16x32_bf16 v[164:167], v[200:203], v[128:131], v[164:167]
s_waitcnt lgkmcnt(4)
ds_read_b64_tr_b16 v[214:215], v236 offset:6144
v_cvt_pk_bf16_f32 v180, v152, v153
v_cvt_pk_bf16_f32 v181, v154, v155
v_mfma_f32_16x16x32_bf16 v[160:163], v[184:187], v[136:139], v[160:163]
v_exp_f32 v157, v157
v_mfma_f32_16x16x32_bf16 v[168:171], v[184:187], v[140:143], v[168:171]
s_waitcnt lgkmcnt(3)
ds_read_b64_tr_b16 v[216:217], v237 offset:0
v_exp_f32 v158, v158
)"
R"(v_mfma_f32_16x16x32_bf16 v[172:175], v[188:191], v[140:143], v[172:175]
v_exp_f32 v159, v159
v_mfma_f32_16x16x32_bf16 v[164:167], v[188:191], v[136:139], v[164:167]
ds_read_b64_tr_b16 v[218:219], v237 offset:6144
v_cvt_pk_bf16_f32 v182, v156, v157
v_cvt_pk_bf16_f32 v183, v158, v159
v_mfma_f32_16x16x32_bf16 v[0:3], v[204:207], v[176:179], v[0:3]
ds_read_b64_tr_b16 v[220:221], v234 offset:128
ds_read_b64_tr_b16 v[222:223], v234 offset:6272
v_mfma_f32_16x16x32_bf16 v[4:7], v[204:207], v[180:183], v[4:7]
s_waitcnt lgkmcnt(6)
ds_read_b64_tr_b16 v[184:185], v235 offset:128
ds_read_b64_tr_b16 v[186:187], v235 offset:6272
v_mfma_f32_16x16x32_bf16 v[12:15], v[208:211], v[180:183], v[12:15]
ds_read_b64_tr_b16 v[188:189], v236 offset:128
v_add_f32 v238, v238, v144
)"
R"(v_mfma_f32_16x16x32_bf16 v[8:11], v[208:211], v[176:179], v[8:11]
s_waitcnt lgkmcnt(7)
ds_read_b64_tr_b16 v[190:191], v236 offset:6272
v_add_f32 v240, v240, v152
v_mfma_f32_16x16x32_bf16 v[16:19], v[212:215], v[176:179], v[16:19]
ds_read_b64_tr_b16 v[192:193], v237 offset:128
v_max3_f32 v242, v160, v161, v162
v_mfma_f32_16x16x32_bf16 v[20:23], v[212:215], v[180:183], v[20:23]
s_waitcnt lgkmcnt(7)
ds_read_b64_tr_b16 v[194:195], v237 offset:6272
v_max3_f32 v243, v167, v168, v169
v_mfma_f32_16x16x32_bf16 v[28:31], v[216:219], v[180:183], v[28:31]
ds_read_b64_tr_b16 v[196:197], v234 offset:256
v_max3_f32 v242, v242, v163, v164
v_mfma_f32_16x16x32_bf16 v[24:27], v[216:219], v[176:179], v[24:27]
s_waitcnt lgkmcnt(7)
)"
R"(ds_read_b64_tr_b16 v[198:199], v234 offset:6400
ds_read_b64_tr_b16 v[200:201], v235 offset:256
v_mfma_f32_16x16x32_bf16 v[32:35], v[220:223], v[176:179], v[32:35]
ds_read_b64_tr_b16 v[202:203], v235 offset:6400
ds_read_b64_tr_b16 v[204:205], v236 offset:256
v_mfma_f32_16x16x32_bf16 v[36:39], v[220:223], v[180:183], v[36:39]
s_waitcnt lgkmcnt(9)
ds_read_b64_tr_b16 v[206:207], v236 offset:6400
ds_read_b64_tr_b16 v[208:209], v237 offset:256
v_mfma_f32_16x16x32_bf16 v[44:47], v[184:187], v[180:183], v[44:47]
ds_read_b64_tr_b16 v[210:211], v237 offset:6400
v_max3_f32 v243, v243, v170, v171
v_mfma_f32_16x16x32_bf16 v[40:43], v[184:187], v[176:179], v[40:43]
s_waitcnt lgkmcnt(10)
v_max3_f32 v242, v242, v165, v166
v_max3_f32 v243, v243, v172, v173
)"
R"(v_mfma_f32_16x16x32_bf16 v[48:51], v[188:191], v[176:179], v[48:51]
v_max3_f32 v242, v242, v174, v175
v_add_f32 v239, v239, v145
v_mfma_f32_16x16x32_bf16 v[52:55], v[188:191], v[180:183], v[52:55]
s_waitcnt lgkmcnt(8)
v_max_f32 v242, v242, v243
v_add_f32 v241, v241, v153
v_mfma_f32_16x16x32_bf16 v[60:63], v[192:195], v[180:183], v[60:63]
v_cmp_lt_f32 vcc, 0x41000000, v242
v_add_f32 v238, v238, v146
v_mfma_f32_16x16x32_bf16 v[56:59], v[192:195], v[176:179], v[56:59]
s_waitcnt lgkmcnt(0)
v_add_f32 v240, v240, v154
v_add_f32 v239, v239, v147
s_barrier
v_mfma_f32_16x16x32_bf16 v[64:67], v[196:199], v[176:179], v[64:67]
)"
R"(ds_read_b128 v[184:187], v232 offset:24576
ds_read_b128 v[188:191], v232 offset:30720
v_mfma_f32_16x16x32_bf16 v[68:71], v[196:199], v[180:183], v[68:71]
ds_read_b128 v[192:195], v233 offset:24576
v_add_f32 v241, v241, v155
v_mfma_f32_16x16x32_bf16 v[76:79], v[200:203], v[180:183], v[76:79]
ds_read_b128 v[196:199], v233 offset:30720
v_add_f32 v238, v238, v148
v_mfma_f32_16x16x32_bf16 v[72:75], v[200:203], v[176:179], v[72:75]
ds_read_b128 v[200:203], v232 offset:24704
v_add_f32 v240, v240, v156
v_mfma_f32_16x16x32_bf16 v[80:83], v[204:207], v[176:179], v[80:83]
v_add_f32 v239, v239, v149
v_add_f32 v241, v241, v157
v_mfma_f32_16x16x32_bf16 v[84:87], v[204:207], v[180:183], v[84:87]
v_add_f32 v238, v238, v150
)"
R"(v_add_f32 v240, v240, v158
v_mfma_f32_16x16x32_bf16 v[92:95], v[208:211], v[180:183], v[92:95]
v_add_f32 v239, v239, v151
v_add_f32 v241, v241, v159
v_mfma_f32_16x16x32_bf16 v[88:91], v[208:211], v[176:179], v[88:91]
s_waitcnt lgkmcnt(4)
s_cbranch_vccz LkB13_0
s_getpc_b64 s[80:81]
s_branch Lrare_1
LkB13_0:
v_mfma_f32_16x16x32_bf16 v[144:147], v[184:187], v[96:99], v[224:227]
v_exp_f32 v160, v160
v_mfma_f32_16x16x32_bf16 v[152:155], v[184:187], v[100:103], v[228:231]
s_waitcnt lgkmcnt(3)
v_exp_f32 v161, v161
v_mfma_f32_16x16x32_bf16 v[156:159], v[188:191], v[100:103], v[228:231]
)"
R"(v_exp_f32 v162, v162
v_mfma_f32_16x16x32_bf16 v[148:151], v[188:191], v[96:99], v[224:227]
s_waitcnt lgkmcnt(2)
ds_read_b128 v[184:187], v232 offset:30848
ds_read_b128 v[188:191], v233 offset:24704
v_mfma_f32_16x16x32_bf16 v[144:147], v[192:195], v[104:107], v[144:147]
v_exp_f32 v163, v163
v_mfma_f32_16x16x32_bf16 v[152:155], v[192:195], v[108:111], v[152:155]
s_waitcnt lgkmcnt(3)
ds_read_b64_tr_b16 v[204:205], v234 offset:12288
v_cvt_pk_bf16_f32 v176, v160, v161
v_cvt_pk_bf16_f32 v177, v162, v163
v_mfma_f32_16x16x32_bf16 v[156:159], v[196:199], v[108:111], v[156:159]
v_exp_f32 v164, v164
v_mfma_f32_16x16x32_bf16 v[148:151], v[196:199], v[104:107], v[148:151]
s_waitcnt lgkmcnt(3)
)"
R"(ds_read_b128 v[192:195], v233 offset:30848
ds_read_b128 v[196:199], v232 offset:24832
v_mfma_f32_16x16x32_bf16 v[144:147], v[200:203], v[112:115], v[144:147]
v_exp_f32 v165, v165
v_mfma_f32_16x16x32_bf16 v[152:155], v[200:203], v[116:119], v[152:155]
s_waitcnt lgkmcnt(4)
ds_read_b64_tr_b16 v[206:207], v234 offset:18432
v_exp_f32 v166, v166
v_mfma_f32_16x16x32_bf16 v[156:159], v[184:187], v[116:119], v[156:159]
v_exp_f32 v167, v167
v_mfma_f32_16x16x32_bf16 v[148:151], v[184:187], v[112:115], v[148:151]
s_waitcnt lgkmcnt(4)
ds_read_b128 v[200:203], v232 offset:30976
ds_read_b128 v[184:187], v233 offset:24832
v_mfma_f32_16x16x32_bf16 v[144:147], v[188:191], v[120:123], v[144:147]
v_exp_f32 v168, v168
)"
R"(v_mfma_f32_16x16x32_bf16 v[152:155], v[188:191], v[124:127], v[152:155]
s_waitcnt lgkmcnt(4)
ds_read_b64_tr_b16 v[208:209], v235 offset:12288
v_cvt_pk_bf16_f32 v178, v164, v165
v_cvt_pk_bf16_f32 v179, v166, v167
v_mfma_f32_16x16x32_bf16 v[156:159], v[192:195], v[124:127], v[156:159]
v_exp_f32 v169, v169
v_mfma_f32_16x16x32_bf16 v[148:151], v[192:195], v[120:123], v[148:151]
s_waitcnt lgkmcnt(4)
ds_read_b128 v[188:191], v233 offset:30976
ds_read_b64_tr_b16 v[210:211], v235 offset:18432
v_mfma_f32_16x16x32_bf16 v[144:147], v[196:199], v[128:131], v[144:147]
v_exp_f32 v170, v170
v_mfma_f32_16x16x32_bf16 v[152:155], v[196:199], v[132:135], v[152:155]
s_waitcnt lgkmcnt(4)
ds_read_b64_tr_b16 v[212:213], v236 offset:12288
)"
R"(v_exp_f32 v171, v171
v_mfma_f32_16x16x32_bf16 v[156:159], v[200:203], v[132:135], v[156:159]
v_exp_f32 v172, v172
v_mfma_f32_16x16x32_bf16 v[148:151], v[200:203], v[128:131], v[148:151]
s_waitcnt lgkmcnt(4)
ds_read_b64_tr_b16 v[214:215], v236 offset:18432
v_cvt_pk_bf16_f32 v180, v168, v169
v_cvt_pk_bf16_f32 v181, v170, v171
v_mfma_f32_16x16x32_bf16 v[144:147], v[184:187], v[136:139], v[144:147]
v_exp_f32 v173, v173
v_mfma_f32_16x16x32_bf16 v[152:155], v[184:187], v[140:143], v[152:155]
s_waitcnt lgkmcnt(3)
ds_read_b64_tr_b16 v[216:217], v237 offset:12288
v_exp_f32 v174, v174
v_mfma_f32_16x16x32_bf16 v[156:159], v[188:191], v[140:143], v[156:159]
v_exp_f32 v175, v175
)"
R"(v_mfma_f32_16x16x32_bf16 v[148:151], v[188:191], v[136:139], v[148:151]
ds_read_b64_tr_b16 v[218:219], v237 offset:18432
v_cvt_pk_bf16_f32 v182, v172, v173
v_cvt_pk_bf16_f32 v183, v174, v175
v_mfma_f32_16x16x32_bf16 v[0:3], v[204:207], v[176:179], v[0:3]
ds_read_b64_tr_b16 v[220:221], v234 offset:12416
ds_read_b64_tr_b16 v[222:223], v234 offset:18560
v_mfma_f32_16x16x32_bf16 v[4:7], v[204:207], v[180:183], v[4:7]
s_waitcnt lgkmcnt(6)
ds_read_b64_tr_b16 v[184:185], v235 offset:12416
ds_read_b64_tr_b16 v[186:187], v235 offset:18560
v_mfma_f32_16x16x32_bf16 v[12:15], v[208:211], v[180:183], v[12:15]
ds_read_b64_tr_b16 v[188:189], v236 offset:12416
v_add_f32 v238, v238, v160
v_mfma_f32_16x16x32_bf16 v[8:11], v[208:211], v[176:179], v[8:11]
s_waitcnt lgkmcnt(7)
)"
R"(ds_read_b64_tr_b16 v[190:191], v236 offset:18560
v_add_f32 v240, v240, v168
v_mfma_f32_16x16x32_bf16 v[16:19], v[212:215], v[176:179], v[16:19]
ds_read_b64_tr_b16 v[192:193], v237 offset:12416
v_max3_f32 v242, v144, v145, v146
v_mfma_f32_16x16x32_bf16 v[20:23], v[212:215], v[180:183], v[20:23]
s_waitcnt lgkmcnt(7)
ds_read_b64_tr_b16 v[194:195], v237 offset:18560
v_max3_f32 v243, v151, v152, v153
v_mfma_f32_16x16x32_bf16 v[28:31], v[216:219], v[180:183], v[28:31]
ds_read_b64_tr_b16 v[196:197], v234 offset:12544
v_max3_f32 v242, v242, v147, v148
v_mfma_f32_16x16x32_bf16 v[24:27], v[216:219], v[176:179], v[24:27]
s_waitcnt lgkmcnt(7)
ds_read_b64_tr_b16 v[198:199], v234 offset:18688
ds_read_b64_tr_b16 v[200:201], v235 offset:12544
)"
R"(v_mfma_f32_16x16x32_bf16 v[32:35], v[220:223], v[176:179], v[32:35]
ds_read_b64_tr_b16 v[202:203], v235 offset:18688
ds_read_b64_tr_b16 v[204:205], v236 offset:12544
v_mfma_f32_16x16x32_bf16 v[36:39], v[220:223], v[180:183], v[36:39]
s_waitcnt lgkmcnt(9)
ds_read_b64_tr_b16 v[206:207], v236 offset:18688
ds_read_b64_tr_b16 v[208:209], v237 offset:12544
v_mfma_f32_16x16x32_bf16 v[44:47], v[184:187], v[180:183], v[44:47]
ds_read_b64_tr_b16 v[210:211], v237 offset:18688
v_max3_f32 v243, v243, v154, v155
v_mfma_f32_16x16x32_bf16 v[40:43], v[184:187], v[176:179], v[40:43]
s_waitcnt lgkmcnt(10)
v_max3_f32 v242, v242, v149, v150
v_max3_f32 v243, v243, v156, v157
v_mfma_f32_16x16x32_bf16 v[48:51], v[188:191], v[176:179], v[48:51]
v_max3_f32 v242, v242, v158, v159
)"
R"(v_add_f32 v239, v239, v161
v_mfma_f32_16x16x32_bf16 v[52:55], v[188:191], v[180:183], v[52:55]
s_waitcnt lgkmcnt(8)
v_max_f32 v242, v242, v243
v_add_f32 v241, v241, v169
v_mfma_f32_16x16x32_bf16 v[60:63], v[192:195], v[180:183], v[60:63]
v_cmp_lt_f32 vcc, 0x41000000, v242
v_add_f32 v238, v238, v162
v_mfma_f32_16x16x32_bf16 v[56:59], v[192:195], v[176:179], v[56:59]
s_waitcnt lgkmcnt(0)
v_add_f32 v240, v240, v170
v_add_f32 v239, v239, v163
s_barrier
v_mfma_f32_16x16x32_bf16 v[64:67], v[196:199], v[176:179], v[64:67]
ds_read_b128 v[184:187], v232 offset:36864
ds_read_b128 v[188:191], v232 offset:43008
)"
R"(v_mfma_f32_16x16x32_bf16 v[68:71], v[196:199], v[180:183], v[68:71]
ds_read_b128 v[192:195], v233 offset:36864
v_add_f32 v241, v241, v171
v_mfma_f32_16x16x32_bf16 v[76:79], v[200:203], v[180:183], v[76:79]
ds_read_b128 v[196:199], v233 offset:43008
v_add_f32 v238, v238, v164
v_mfma_f32_16x16x32_bf16 v[72:75], v[200:203], v[176:179], v[72:75]
ds_read_b128 v[200:203], v232 offset:36992
v_add_f32 v240, v240, v172
v_mfma_f32_16x16x32_bf16 v[80:83], v[204:207], v[176:179], v[80:83]
v_add_f32 v239, v239, v165
v_add_f32 v241, v241, v173
v_mfma_f32_16x16x32_bf16 v[84:87], v[204:207], v[180:183], v[84:87]
v_add_f32 v238, v238, v166
v_add_f32 v240, v240, v174
v_mfma_f32_16x16x32_bf16 v[92:95], v[208:211], v[180:183], v[92:95]
)"
R"(v_add_f32 v239, v239, v167
v_add_f32 v241, v241, v175
v_mfma_f32_16x16x32_bf16 v[88:91], v[208:211], v[176:179], v[88:91]
s_waitcnt lgkmcnt(4)
s_cbranch_vccz LkB13_1
s_getpc_b64 s[80:81]
s_branch Lrare_0
LkB13_1:
v_mfma_f32_16x16x32_bf16 v[160:163], v[184:187], v[96:99], v[224:227]
v_exp_f32 v144, v144
v_mfma_f32_16x16x32_bf16 v[168:171], v[184:187], v[100:103], v[228:231]
s_waitcnt lgkmcnt(3)
v_exp_f32 v145, v145
v_mfma_f32_16x16x32_bf16 v[172:175], v[188:191], v[100:103], v[228:231]
v_exp_f32 v146, v146
v_mfma_f32_16x16x32_bf16 v[164:167], v[188:191], v[96:99], v[224:227]
)"
R"(s_waitcnt lgkmcnt(2)
ds_read_b128 v[184:187], v232 offset:43136
ds_read_b128 v[188:191], v233 offset:36992
v_mfma_f32_16x16x32_bf16 v[160:163], v[192:195], v[104:107], v[160:163]
v_exp_f32 v147, v147
v_mfma_f32_16x16x32_bf16 v[168:171], v[192:195], v[108:111], v[168:171]
s_waitcnt lgkmcnt(3)
ds_read_b64_tr_b16 v[204:205], v234 offset:24576
v_cvt_pk_bf16_f32 v176, v144, v145
v_cvt_pk_bf16_f32 v177, v146, v147
v_mfma_f32_16x16x32_bf16 v[172:175], v[196:199], v[108:111], v[172:175]
v_exp_f32 v148, v148
v_mfma_f32_16x16x32_bf16 v[164:167], v[196:199], v[104:107], v[164:167]
s_waitcnt lgkmcnt(3)
ds_read_b128 v[192:195], v233 offset:43136
ds_read_b128 v[196:199], v232 offset:37120
)"
R"(v_mfma_f32_16x16x32_bf16 v[160:163], v[200:203], v[112:115], v[160:163]
v_exp_f32 v149, v149
v_mfma_f32_16x16x32_bf16 v[168:171], v[200:203], v[116:119], v[168:171]
s_waitcnt lgkmcnt(4)
ds_read_b64_tr_b16 v[206:207], v234 offset:30720
v_exp_f32 v150, v150
v_mfma_f32_16x16x32_bf16 v[172:175], v[184:187], v[116:119], v[172:175]
v_exp_f32 v151, v151
v_mfma_f32_16x16x32_bf16 v[164:167], v[184:187], v[112:115], v[164:167]
s_waitcnt lgkmcnt(4)
ds_read_b128 v[200:203], v232 offset:43264
ds_read_b128 v[184:187], v233 offset:37120
v_mfma_f32_16x16x32_bf16 v[160:163], v[188:191], v[120:123], v[160:163]
v_exp_f32 v152, v152
v_mfma_f32_16x16x32_bf16 v[168:171], v[188:191], v[124:127], v[168:171]
s_waitcnt lgkmcnt(4)
)"
R"(ds_read_b64_tr_b16 v[208:209], v235 offset:24576
v_cvt_pk_bf16_f32 v178, v148, v149
v_cvt_pk_bf16_f32 v179, v150, v151
v_mfma_f32_16x16x32_bf16 v[172:175], v[192:195], v[124:127], v[172:175]
v_exp_f32 v153, v153
v_mfma_f32_16x16x32_bf16 v[164:167], v[192:195], v[120:123], v[164:167]
s_waitcnt lgkmcnt(4)
ds_read_b128 v[188:191], v233 offset:43264
ds_read_b64_tr_b16 v[210:211], v235 offset:30720
v_mfma_f32_16x16x32_bf16 v[160:163], v[196:199], v[128:131], v[160:163]
v_exp_f32 v154, v154
v_mfma_f32_16x16x32_bf16 v[168:171], v[196:199], v[132:135], v[168:171]
s_waitcnt lgkmcnt(4)
ds_read_b64_tr_b16 v[212:213], v236 offset:24576
v_exp_f32 v155, v155
v_mfma_f32_16x16x32_bf16 v[172:175], v[200:203], v[132:135], v[172:175]
)"
R"(v_exp_f32 v156, v156
v_mfma_f32_16x16x32_bf16 v[164:167], v[200:203], v[128:131], v[164:167]
s_waitcnt lgkmcnt(4)
ds_read_b64_tr_b16 v[214:215], v236 offset:30720
v_cvt_pk_bf16_f32 v180, v152, v153
v_cvt_pk_bf16_f32 v181, v154, v155
v_mfma_f32_16x16x32_bf16 v[160:163], v[184:187], v[136:139], v[160:163]
v_exp_f32 v157, v157
v_mfma_f32_16x16x32_bf16 v[168:171], v[184:187], v[140:143], v[168:171]
s_waitcnt lgkmcnt(3)
ds_read_b64_tr_b16 v[216:217], v237 offset:24576
v_exp_f32 v158, v158
v_mfma_f32_16x16x32_bf16 v[172:175], v[188:191], v[140:143], v[172:175]
v_exp_f32 v159, v159
v_mfma_f32_16x16x32_bf16 v[164:167], v[188:191], v[136:139], v[164:167]
ds_read_b64_tr_b16 v[218:219], v237 offset:30720
)"
R"(v_cvt_pk_bf16_f32 v182, v156, v157
v_cvt_pk_bf16_f32 v183, v158, v159
v_mfma_f32_16x16x32_bf16 v[0:3], v[204:207], v[176:179], v[0:3]
ds_read_b64_tr_b16 v[220:221], v234 offset:24704
ds_read_b64_tr_b16 v[222:223], v234 offset:30848
v_mfma_f32_16x16x32_bf16 v[4:7], v[204:207], v[180:183], v[4:7]
s_waitcnt lgkmcnt(6)
ds_read_b64_tr_b16 v[184:185], v235 offset:24704
ds_read_b64_tr_b16 v[186:187], v235 offset:30848
v_mfma_f32_16x16x32_bf16 v[12:15], v[208:211], v[180:183], v[12:15]
ds_read_b64_tr_b16 v[188:189], v236 offset:24704
v_add_f32 v238, v238, v144
v_mfma_f32_16x16x32_bf16 v[8:11], v[208:211], v[176:179], v[8:11]
s_waitcnt lgkmcnt(7)
ds_read_b64_tr_b16 v[190:191], v236 offset:30848
v_add_f32 v240, v240, v152
)"
R"(v_mfma_f32_16x16x32_bf16 v[16:19], v[212:215], v[176:179], v[16:19]
ds_read_b64_tr_b16 v[192:193], v237 offset:24704
v_max3_f32 v242, v160, v161, v162
v_mfma_f32_16x16x32_bf16 v[20:23], v[212:215], v[180:183], v[20:23]
s_waitcnt lgkmcnt(7)
ds_read_b64_tr_b16 v[194:195], v237 offset:30848
v_max3_f32 v243, v167, v168, v169
v_mfma_f32_16x16x32_bf16 v[28:31], v[216:219], v[180:183], v[28:31]
ds_read_b64_tr_b16 v[196:197], v234 offset:24832
v_max3_f32 v242, v242, v163, v164
v_mfma_f32_16x16x32_bf16 v[24:27], v[216:219], v[176:179], v[24:27]
s_waitcnt lgkmcnt(7)
ds_read_b64_tr_b16 v[198:199], v234 offset:30976
ds_read_b64_tr_b16 v[200:201], v235 offset:24832
v_mfma_f32_16x16x32_bf16 v[32:35], v[220:223], v[176:179], v[32:35]
ds_read_b64_tr_b16 v[202:203], v235 offset:30976
)"
R"(ds_read_b64_tr_b16 v[204:205], v236 offset:24832
v_mfma_f32_16x16x32_bf16 v[36:39], v[220:223], v[180:183], v[36:39]
s_waitcnt lgkmcnt(9)
ds_read_b64_tr_b16 v[206:207], v236 offset:30976
ds_read_b64_tr_b16 v[208:209], v237 offset:24832
v_mfma_f32_16x16x32_bf16 v[44:47], v[184:187], v[180:183], v[44:47]
ds_read_b64_tr_b16 v[210:211], v237 offset:30976
v_max3_f32 v243, v243, v170, v171
v_mfma_f32_16x16x32_bf16 v[40:43], v[184:187], v[176:179], v[40:43]
s_waitcnt lgkmcnt(10)
v_max3_f32 v242, v242, v165, v166
v_max3_f32 v243, v243, v172, v173
v_mfma_f32_16x16x32_bf16 v[48:51], v[188:191], v[176:179], v[48:51]
v_max3_f32 v242, v242, v174, v175
v_add_f32 v239, v239, v145
v_mfma_f32_16x16x32_bf16 v[52:55], v[188:191], v[180:183], v[52:55]
)"
R"(s_waitcnt lgkmcnt(8)
v_max_f32 v242, v242, v243
v_add_f32 v241, v241, v153
v_mfma_f32_16x16x32_bf16 v[60:63], v[192:195], v[180:183], v[60:63]
v_cmp_lt_f32 vcc, 0x41000000, v242
v_add_f32 v238, v238, v146
v_mfma_f32_16x16x32_bf16 v[56:59], v[192:195], v[176:179], v[56:59]
s_waitcnt lgkmcnt(0)
v_add_f32 v240, v240, v154
v_add_f32 v239, v239, v147
s_barrier
v_mfma_f32_16x16x32_bf16 v[64:67], v[196:199], v[176:179], v[64:67]
v_mfma_f32_16x16x32_bf16 v[68:71], v[196:199], v[180:183], v[68:71]
v_add_f32 v241, v241, v155
v_mfma_f32_16x16x32_bf16 v[76:79], v[200:203], v[180:183], v[76:79]
v_add_f32 v238, v238, v148
)"
R"(v_mfma_f32_16x16x32_bf16 v[72:75], v[200:203], v[176:179], v[72:75]
v_add_f32 v240, v240, v156
v_mfma_f32_16x16x32_bf16 v[80:83], v[204:207], v[176:179], v[80:83]
v_add_f32 v239, v239, v149
v_add_f32 v241, v241, v157
v_mfma_f32_16x16x32_bf16 v[84:87], v[204:207], v[180:183], v[84:87]
v_add_f32 v238, v238, v150
v_add_f32 v240, v240, v158
v_mfma_f32_16x16x32_bf16 v[92:95], v[208:211], v[180:183], v[92:95]
v_add_f32 v239, v239, v151
v_add_f32 v241, v241, v159
v_mfma_f32_16x16x32_bf16 v[88:91], v[208:211], v[176:179], v[88:91]
s_cbranch_vccz LkB13_2
s_getpc_b64 s[80:81]
s_branch Lrare_1
LkB13_2:
)"
R"(ds_read_b64_tr_b16 v[204:205], v234 offset:36864
ds_read_b64_tr_b16 v[206:207], v234 offset:43008
ds_read_b64_tr_b16 v[208:209], v235 offset:36864
ds_read_b64_tr_b16 v[210:211], v235 offset:43008
ds_read_b64_tr_b16 v[212:213], v236 offset:36864
ds_read_b64_tr_b16 v[214:215], v236 offset:43008
ds_read_b64_tr_b16 v[216:217], v237 offset:36864
ds_read_b64_tr_b16 v[218:219], v237 offset:43008
ds_read_b64_tr_b16 v[220:221], v234 offset:36992
ds_read_b64_tr_b16 v[222:223], v234 offset:43136
ds_read_b64_tr_b16 v[184:185], v235 offset:36992
ds_read_b64_tr_b16 v[186:187], v235 offset:43136
v_exp_f32 v160, v160
v_exp_f32 v161, v161
v_exp_f32 v162, v162
v_exp_f32 v163, v163
)"
R"(v_exp_f32 v164, v164
v_exp_f32 v165, v165
v_exp_f32 v166, v166
v_exp_f32 v167, v167
v_exp_f32 v168, v168
v_exp_f32 v169, v169
v_exp_f32 v170, v170
v_exp_f32 v171, v171
v_exp_f32 v172, v172
v_exp_f32 v173, v173
v_exp_f32 v174, v174
v_exp_f32 v175, v175
v_cvt_pk_bf16_f32 v176, v160, v161
v_cvt_pk_bf16_f32 v177, v162, v163
v_cvt_pk_bf16_f32 v178, v164, v165
v_cvt_pk_bf16_f32 v179, v166, v167
)"
R"(v_cvt_pk_bf16_f32 v180, v168, v169
v_cvt_pk_bf16_f32 v181, v170, v171
v_cvt_pk_bf16_f32 v182, v172, v173
v_cvt_pk_bf16_f32 v183, v174, v175
s_waitcnt lgkmcnt(10)
v_mfma_f32_16x16x32_bf16 v[0:3], v[204:207], v[176:179], v[0:3]
ds_read_b64_tr_b16 v[188:189], v236 offset:36992
ds_read_b64_tr_b16 v[190:191], v236 offset:43136
v_mfma_f32_16x16x32_bf16 v[4:7], v[204:207], v[180:183], v[4:7]
v_add_f32 v238, v238, v160
v_add_f32 v240, v240, v168
s_waitcnt lgkmcnt(10)
v_mfma_f32_16x16x32_bf16 v[12:15], v[208:211], v[180:183], v[12:15]
ds_read_b64_tr_b16 v[192:193], v237 offset:36992
ds_read_b64_tr_b16 v[194:195], v237 offset:43136
v_mfma_f32_16x16x32_bf16 v[8:11], v[208:211], v[176:179], v[8:11]
)"
R"(v_add_f32 v239, v239, v161
v_add_f32 v241, v241, v169
s_waitcnt lgkmcnt(10)
v_mfma_f32_16x16x32_bf16 v[16:19], v[212:215], v[176:179], v[16:19]
ds_read_b64_tr_b16 v[196:197], v234 offset:37120
ds_read_b64_tr_b16 v[198:199], v234 offset:43264
v_mfma_f32_16x16x32_bf16 v[20:23], v[212:215], v[180:183], v[20:23]
v_add_f32 v238, v238, v162
v_add_f32 v240, v240, v170
s_waitcnt lgkmcnt(10)
v_mfma_f32_16x16x32_bf16 v[28:31], v[216:219], v[180:183], v[28:31]
ds_read_b64_tr_b16 v[200:201], v235 offset:37120
ds_read_b64_tr_b16 v[202:203], v235 offset:43264
v_mfma_f32_16x16x32_bf16 v[24:27], v[216:219], v[176:179], v[24:27]
v_add_f32 v239, v239, v163
v_add_f32 v241, v241, v171
)"
R"(s_waitcnt lgkmcnt(10)
v_mfma_f32_16x16x32_bf16 v[32:35], v[220:223], v[176:179], v[32:35]
ds_read_b64_tr_b16 v[204:205], v236 offset:37120
ds_read_b64_tr_b16 v[206:207], v236 offset:43264
v_mfma_f32_16x16x32_bf16 v[36:39], v[220:223], v[180:183], v[36:39]
v_add_f32 v238, v238, v164
v_add_f32 v240, v240, v172
s_waitcnt lgkmcnt(10)
v_mfma_f32_16x16x32_bf16 v[44:47], v[184:187], v[180:183], v[44:47]
ds_read_b64_tr_b16 v[208:209], v237 offset:37120
ds_read_b64_tr_b16 v[210:211], v237 offset:43264
v_mfma_f32_16x16x32_bf16 v[40:43], v[184:187], v[176:179], v[40:43]
v_add_f32 v239, v239, v165
v_add_f32 v241, v241, v173
s_waitcnt lgkmcnt(10)
v_mfma_f32_16x16x32_bf16 v[48:51], v[188:191], v[176:179], v[48:51]
)"
R"(v_add_f32 v238, v238, v166
v_add_f32 v240, v240, v174
v_mfma_f32_16x16x32_bf16 v[52:55], v[188:191], v[180:183], v[52:55]
v_add_f32 v239, v239, v167
v_add_f32 v241, v241, v175
s_waitcnt lgkmcnt(8)
v_mfma_f32_16x16x32_bf16 v[60:63], v[192:195], v[180:183], v[60:63]
v_mfma_f32_16x16x32_bf16 v[56:59], v[192:195], v[176:179], v[56:59]
s_waitcnt lgkmcnt(6)
v_mfma_f32_16x16x32_bf16 v[64:67], v[196:199], v[176:179], v[64:67]
v_mfma_f32_16x16x32_bf16 v[68:71], v[196:199], v[180:183], v[68:71]
s_waitcnt lgkmcnt(4)
v_mfma_f32_16x16x32_bf16 v[76:79], v[200:203], v[180:183], v[76:79]
v_mfma_f32_16x16x32_bf16 v[72:75], v[200:203], v[176:179], v[72:75]
s_waitcnt lgkmcnt(2)
v_mfma_f32_16x16x32_bf16 v[80:83], v[204:207], v[176:179], v[80:83]
)"
R"(v_mfma_f32_16x16x32_bf16 v[84:87], v[204:207], v[180:183], v[84:87]
s_waitcnt lgkmcnt(0)
v_mfma_f32_16x16x32_bf16 v[92:95], v[208:211], v[180:183], v[92:95]
v_mfma_f32_16x16x32_bf16 v[88:91], v[208:211], v[176:179], v[88:91]
s_setprio 2
s_add_u32 s86, s83, 4096
s_add_u32 s87, s83, 8192
v_add_f32 v238, v238, v239
v_add_f32 v240, v240, v241
v_mov_b32 v242, v238
v_mov_b32 v243, v240
s_nop 0
v_permlane32_swap_b32 v238, v242
v_permlane32_swap_b32 v240, v243
v_add_f32 v238, v238, v242
v_add_f32 v240, v240, v243
)"
R"(v_mov_b32 v242, v238
v_mov_b32 v243, v240
s_nop 0
v_permlane16_swap_b32 v238, v242
v_permlane16_swap_b32 v240, v243
v_add_f32 v238, v238, v242
v_add_f32 v240, v240, v243
v_rcp_f32 v180, v238
v_rcp_f32 v182, v240
s_mov_b32 s68, 122880
s_cmp_eq_u32 s60, 1
s_cselect_b32 s68, 73728, s68
s_cmp_eq_u32 s60, 2
s_cselect_b32 s68, 36864, s68
s_cmp_eq_u32 s60, 3
s_cselect_b32 s68, 110592, s68
)"
R"(v_add_u32 v242, s68, v247
v_pk_mul_f32 v[0:1], v[0:1], v[180:181] op_sel_hi:[1,0]
v_pk_mul_f32 v[2:3], v[2:3], v[180:181] op_sel_hi:[1,0]
v_pk_mul_f32 v[8:9], v[8:9], v[180:181] op_sel_hi:[1,0]
v_pk_mul_f32 v[10:11], v[10:11], v[180:181] op_sel_hi:[1,0]
v_cvt_pk_bf16_f32 v96, v0, v1
v_cvt_pk_bf16_f32 v97, v2, v3
v_cvt_pk_bf16_f32 v98, v8, v9
v_cvt_pk_bf16_f32 v99, v10, v11
ds_write_b128 v242, v[96:99] offset:0
buffer_store_dwordx4 v[96:99], v247, s[40:43], s83 offen sc1
v_pk_mul_f32 v[4:5], v[4:5], v[182:183] op_sel_hi:[1,0]
v_pk_mul_f32 v[6:7], v[6:7], v[182:183] op_sel_hi:[1,0]
v_pk_mul_f32 v[12:13], v[12:13], v[182:183] op_sel_hi:[1,0]
v_pk_mul_f32 v[14:15], v[14:15], v[182:183] op_sel_hi:[1,0]
v_cvt_pk_bf16_f32 v100, v4, v5
)"
R"(v_cvt_pk_bf16_f32 v101, v6, v7
v_cvt_pk_bf16_f32 v102, v12, v13
v_cvt_pk_bf16_f32 v103, v14, v15
ds_write_b128 v242, v[100:103] offset:1024
buffer_store_dwordx4 v[100:103], v247, s[40:43], s83 offen offset:1024 sc1
v_pk_mul_f32 v[16:17], v[16:17], v[180:181] op_sel_hi:[1,0]
v_pk_mul_f32 v[18:19], v[18:19], v[180:181] op_sel_hi:[1,0]
v_pk_mul_f32 v[24:25], v[24:25], v[180:181] op_sel_hi:[1,0]
v_pk_mul_f32 v[26:27], v[26:27], v[180:181] op_sel_hi:[1,0]
v_cvt_pk_bf16_f32 v104, v16, v17
v_cvt_pk_bf16_f32 v105, v18, v19
v_cvt_pk_bf16_f32 v106, v24, v25
v_cvt_pk_bf16_f32 v107, v26, v27
ds_write_b128 v242, v[104:107] offset:2048
buffer_store_dwordx4 v[104:107], v247, s[40:43], s83 offen offset:2048 sc1
v_pk_mul_f32 v[20:21], v[20:21], v[182:183] op_sel_hi:[1,0]
)"
R"(v_pk_mul_f32 v[22:23], v[22:23], v[182:183] op_sel_hi:[1,0]
v_pk_mul_f32 v[28:29], v[28:29], v[182:183] op_sel_hi:[1,0]
v_pk_mul_f32 v[30:31], v[30:31], v[182:183] op_sel_hi:[1,0]
v_cvt_pk_bf16_f32 v108, v20, v21
v_cvt_pk_bf16_f32 v109, v22, v23
v_cvt_pk_bf16_f32 v110, v28, v29
v_cvt_pk_bf16_f32 v111, v30, v31
ds_write_b128 v242, v[108:111] offset:3072
buffer_store_dwordx4 v[108:111], v247, s[40:43], s83 offen offset:3072 sc1
v_pk_mul_f32 v[32:33], v[32:33], v[180:181] op_sel_hi:[1,0]
v_pk_mul_f32 v[34:35], v[34:35], v[180:181] op_sel_hi:[1,0]
v_pk_mul_f32 v[40:41], v[40:41], v[180:181] op_sel_hi:[1,0]
v_pk_mul_f32 v[42:43], v[42:43], v[180:181] op_sel_hi:[1,0]
v_cvt_pk_bf16_f32 v112, v32, v33
v_cvt_pk_bf16_f32 v113, v34, v35
v_cvt_pk_bf16_f32 v114, v40, v41
)"
R"(v_cvt_pk_bf16_f32 v115, v42, v43
ds_write_b128 v242, v[112:115] offset:4096
buffer_store_dwordx4 v[112:115], v247, s[40:43], s86 offen sc1
v_pk_mul_f32 v[36:37], v[36:37], v[182:183] op_sel_hi:[1,0]
v_pk_mul_f32 v[38:39], v[38:39], v[182:183] op_sel_hi:[1,0]
v_pk_mul_f32 v[44:45], v[44:45], v[182:183] op_sel_hi:[1,0]
v_pk_mul_f32 v[46:47], v[46:47], v[182:183] op_sel_hi:[1,0]
v_cvt_pk_bf16_f32 v116, v36, v37
v_cvt_pk_bf16_f32 v117, v38, v39
v_cvt_pk_bf16_f32 v118, v44, v45
v_cvt_pk_bf16_f32 v119, v46, v47
ds_write_b128 v242, v[116:119] offset:5120
buffer_store_dwordx4 v[116:119], v247, s[40:43], s86 offen offset:1024 sc1
v_pk_mul_f32 v[48:49], v[48:49], v[180:181] op_sel_hi:[1,0]
v_pk_mul_f32 v[50:51], v[50:51], v[180:181] op_sel_hi:[1,0]
v_pk_mul_f32 v[56:57], v[56:57], v[180:181] op_sel_hi:[1,0]
)"
R"(v_pk_mul_f32 v[58:59], v[58:59], v[180:181] op_sel_hi:[1,0]
v_cvt_pk_bf16_f32 v120, v48, v49
v_cvt_pk_bf16_f32 v121, v50, v51
v_cvt_pk_bf16_f32 v122, v56, v57
v_cvt_pk_bf16_f32 v123, v58, v59
ds_write_b128 v242, v[120:123] offset:6144
buffer_store_dwordx4 v[120:123], v247, s[40:43], s86 offen offset:2048 sc1
v_pk_mul_f32 v[52:53], v[52:53], v[182:183] op_sel_hi:[1,0]
v_pk_mul_f32 v[54:55], v[54:55], v[182:183] op_sel_hi:[1,0]
v_pk_mul_f32 v[60:61], v[60:61], v[182:183] op_sel_hi:[1,0]
v_pk_mul_f32 v[62:63], v[62:63], v[182:183] op_sel_hi:[1,0]
v_cvt_pk_bf16_f32 v124, v52, v53
v_cvt_pk_bf16_f32 v125, v54, v55
v_cvt_pk_bf16_f32 v126, v60, v61
v_cvt_pk_bf16_f32 v127, v62, v63
ds_write_b128 v242, v[124:127] offset:7168
)"
R"(buffer_store_dwordx4 v[124:127], v247, s[40:43], s86 offen offset:3072 sc1
v_pk_mul_f32 v[64:65], v[64:65], v[180:181] op_sel_hi:[1,0]
v_pk_mul_f32 v[66:67], v[66:67], v[180:181] op_sel_hi:[1,0]
v_pk_mul_f32 v[72:73], v[72:73], v[180:181] op_sel_hi:[1,0]
v_pk_mul_f32 v[74:75], v[74:75], v[180:181] op_sel_hi:[1,0]
v_cvt_pk_bf16_f32 v128, v64, v65
v_cvt_pk_bf16_f32 v129, v66, v67
v_cvt_pk_bf16_f32 v130, v72, v73
v_cvt_pk_bf16_f32 v131, v74, v75
ds_write_b128 v242, v[128:131] offset:8192
buffer_store_dwordx4 v[128:131], v247, s[40:43], s87 offen sc1
v_pk_mul_f32 v[68:69], v[68:69], v[182:183] op_sel_hi:[1,0]
v_pk_mul_f32 v[70:71], v[70:71], v[182:183] op_sel_hi:[1,0]
v_pk_mul_f32 v[76:77], v[76:77], v[182:183] op_sel_hi:[1,0]
v_pk_mul_f32 v[78:79], v[78:79], v[182:183] op_sel_hi:[1,0]
v_cvt_pk_bf16_f32 v132, v68, v69
)"
R"(v_cvt_pk_bf16_f32 v133, v70, v71
v_cvt_pk_bf16_f32 v134, v76, v77
v_cvt_pk_bf16_f32 v135, v78, v79
ds_write_b128 v242, v[132:135] offset:9216
buffer_store_dwordx4 v[132:135], v247, s[40:43], s87 offen offset:1024 sc1
v_pk_mul_f32 v[80:81], v[80:81], v[180:181] op_sel_hi:[1,0]
v_pk_mul_f32 v[82:83], v[82:83], v[180:181] op_sel_hi:[1,0]
v_pk_mul_f32 v[88:89], v[88:89], v[180:181] op_sel_hi:[1,0]
v_pk_mul_f32 v[90:91], v[90:91], v[180:181] op_sel_hi:[1,0]
v_cvt_pk_bf16_f32 v136, v80, v81
v_cvt_pk_bf16_f32 v137, v82, v83
v_cvt_pk_bf16_f32 v138, v88, v89
v_cvt_pk_bf16_f32 v139, v90, v91
ds_write_b128 v242, v[136:139] offset:10240
buffer_store_dwordx4 v[136:139], v247, s[40:43], s87 offen offset:2048 sc1
v_pk_mul_f32 v[84:85], v[84:85], v[182:183] op_sel_hi:[1,0]
)"
R"(v_pk_mul_f32 v[86:87], v[86:87], v[182:183] op_sel_hi:[1,0]
v_pk_mul_f32 v[92:93], v[92:93], v[182:183] op_sel_hi:[1,0]
v_pk_mul_f32 v[94:95], v[94:95], v[182:183] op_sel_hi:[1,0]
v_cvt_pk_bf16_f32 v140, v84, v85
v_cvt_pk_bf16_f32 v141, v86, v87
v_cvt_pk_bf16_f32 v142, v92, v93
v_cvt_pk_bf16_f32 v143, v94, v95
ds_write_b128 v242, v[140:143] offset:11264
buffer_store_dwordx4 v[140:143], v247, s[40:43], s87 offen offset:3072 sc1
s_mul_i32 s68, s64, 1966080
s_mul_i32 s69, s63, 320
s_add_u32 s52, s68, s69
s_mov_b32 s90, s63
s_cmp_ge_u32 0, s63
s_cselect_b32 s91, 1, 0
s_cmp_ge_u32 1, s63
)"
R"(s_cselect_b32 s92, 2, 1
s_cmp_ge_u32 2, s63
s_cselect_b32 s93, 3, 2
s_mov_b32 s96, s63
s_add_u32 s97, s63, 1
s_and_b32 s97, s97, 3
s_add_u32 s98, s63, 2
s_and_b32 s98, s98, 3
s_add_u32 s99, s63, 3
s_and_b32 s99, s99, 3
s_waitcnt lgkmcnt(0)
s_setprio 0
s_barrier
s_mul_i32 s50, s97, 245760
s_add_u32 s50, s50, s52
s_add_u32 s50, s50, 1024000
)"
R"(s_add_u32 m0, s56, 86016
s_nop 0
buffer_load_dwordx4 v244, s[32:35], s50 offen lds
buffer_load_dwordx4 v245, s[32:35], s50 offen offset:1024 lds
buffer_load_dwordx4 v246, s[32:35], s50 offen offset:2048 lds
s_add_u32 s50, s50, 40960
s_add_u32 m0, s56, 135168
s_nop 0
buffer_load_dwordx4 v244, s[32:35], s50 offen lds
buffer_load_dwordx4 v245, s[32:35], s50 offen offset:1024 lds
buffer_load_dwordx4 v246, s[32:35], s50 offen offset:2048 lds
s_add_u32 s50, s50, 40960
s_waitcnt vmcnt(6)
s_barrier
s_cmp_eq_u32 s59, 4
s_cbranch_scc0 Lw4_9
)"
R"(s_mov_b64 exec, 0xf0
buffer_store_dword v251, v250, s[40:43], 0 offen offset:128 sc1
s_mov_b64 exec, -1
Lw4_9:
s_add_u32 m0, s56, 122880
s_nop 0
buffer_load_dwordx4 v244, s[32:35], s50 offen lds
buffer_load_dwordx4 v245, s[32:35], s50 offen offset:1024 lds
buffer_load_dwordx4 v246, s[32:35], s50 offen offset:2048 lds
s_add_u32 s50, s50, 40960
s_add_u32 m0, s56, 73728
s_nop 0
buffer_load_dwordx4 v244, s[32:35], s50 offen lds
buffer_load_dwordx4 v245, s[32:35], s50 offen offset:1024 lds
buffer_load_dwordx4 v246, s[32:35], s50 offen offset:2048 lds
s_add_u32 s50, s50, 40960
)"
R"(s_add_u32 m0, s56, 36864
s_nop 0
buffer_load_dwordx4 v244, s[32:35], s50 offen lds
buffer_load_dwordx4 v245, s[32:35], s50 offen offset:1024 lds
buffer_load_dwordx4 v246, s[32:35], s50 offen offset:2048 lds
s_add_u32 s50, s50, 40960
s_mul_i32 s50, s98, 245760
s_add_u32 s50, s50, s52
s_add_u32 s50, s50, 983040
s_add_u32 m0, s56, 110592
s_nop 0
buffer_load_dwordx4 v244, s[32:35], s50 offen lds
buffer_load_dwordx4 v245, s[32:35], s50 offen offset:1024 lds
buffer_load_dwordx4 v246, s[32:35], s50 offen offset:2048 lds
s_add_u32 s50, s50, 40960
s_add_u32 m0, s56, 147456
)"
R"(s_nop 0
buffer_load_dwordx4 v244, s[32:35], s50 offen lds
buffer_load_dwordx4 v245, s[32:35], s50 offen offset:1024 lds
buffer_load_dwordx4 v246, s[32:35], s50 offen offset:2048 lds
s_add_u32 s50, s50, 40960
s_add_u32 m0, s56, 0
s_nop 0
buffer_load_dwordx4 v244, s[32:35], s50 offen lds
buffer_load_dwordx4 v245, s[32:35], s50 offen offset:1024 lds
buffer_load_dwordx4 v246, s[32:35], s50 offen offset:2048 lds
s_add_u32 s50, s50, 40960
s_cmp_eq_u32 s59, 4
s_cbranch_scc0 Lw4_10
s_mov_b64 exec, 0xf0
s_mov_b32 s82, 65536
buffer_load_dword v252, v249, s[40:43], 0 offen offset:128 sc1
)"
R"(s_sleep 2
buffer_load_dword v253, v249, s[40:43], 0 offen offset:128 sc1
s_sleep 2
buffer_load_dword v254, v249, s[40:43], 0 offen offset:128 sc1
Lp_11:
s_waitcnt vmcnt(2)
v_cmp_ne_u32 vcc, v252, v248
s_nop 1
s_cbranch_vccz Lpd_11
buffer_load_dword v252, v249, s[40:43], 0 offen offset:128 sc1
s_waitcnt vmcnt(2)
v_cmp_ne_u32 vcc, v253, v248
s_nop 1
s_cbranch_vccz Lpd_11
buffer_load_dword v253, v249, s[40:43], 0 offen offset:128 sc1
s_waitcnt vmcnt(2)
)"
R"(v_cmp_ne_u32 vcc, v254, v248
s_nop 1
s_cbranch_vccz Lpd_11
buffer_load_dword v254, v249, s[40:43], 0 offen offset:128 sc1
s_sub_u32 s82, s82, 1
s_cmp_lg_u32 s82, 0
s_cbranch_scc1 Lp_11
Lpd_11:
s_mov_b64 exec, -1
Lw4_10:
s_barrier
s_waitcnt vmcnt(15)
s_barrier
s_add_u32 m0, s56, 12288
s_nop 0
buffer_load_dwordx4 v244, s[32:35], s50 offen lds
)"
R"(buffer_load_dwordx4 v245, s[32:35], s50 offen offset:1024 lds
buffer_load_dwordx4 v246, s[32:35], s50 offen offset:2048 lds
s_add_u32 s50, s50, 40960
s_add_u32 m0, s56, 49152
s_nop 0
buffer_load_dwordx4 v244, s[32:35], s50 offen lds
buffer_load_dwordx4 v245, s[32:35], s50 offen offset:1024 lds
buffer_load_dwordx4 v246, s[32:35], s50 offen offset:2048 lds
s_add_u32 s50, s50, 40960
s_waitcnt vmcnt(12)
s_barrier
s_add_u32 m0, s56, 98304
s_nop 0
buffer_load_dwordx4 v244, s[32:35], s50 offen lds
buffer_load_dwordx4 v245, s[32:35], s50 offen offset:1024 lds
buffer_load_dwordx4 v246, s[32:35], s50 offen offset:2048 lds
)"
R"(s_add_u32 s50, s50, 40960
s_mul_i32 s50, s99, 245760
s_add_u32 s50, s50, s52
s_add_u32 s50, s50, 983040
s_add_u32 m0, s56, 24576
s_nop 0
buffer_load_dwordx4 v244, s[32:35], s50 offen lds
buffer_load_dwordx4 v245, s[32:35], s50 offen offset:1024 lds
buffer_load_dwordx4 v246, s[32:35], s50 offen offset:2048 lds
s_add_u32 s50, s50, 40960
s_waitcnt vmcnt(9)
s_barrier
s_add_u32 m0, s56, 61440
s_nop 0
buffer_load_dwordx4 v244, s[32:35], s50 offen lds
buffer_load_dwordx4 v245, s[32:35], s50 offen offset:1024 lds
)"
R"(buffer_load_dwordx4 v246, s[32:35], s50 offen offset:2048 lds
s_add_u32 s50, s50, 40960
s_add_u32 m0, s56, 86016
s_nop 0
buffer_load_dwordx4 v244, s[32:35], s50 offen lds
buffer_load_dwordx4 v245, s[32:35], s50 offen offset:1024 lds
buffer_load_dwordx4 v246, s[32:35], s50 offen offset:2048 lds
s_add_u32 s50, s50, 40960
s_add_u32 m0, s56, 135168
s_nop 0
buffer_load_dwordx4 v244, s[32:35], s50 offen lds
buffer_load_dwordx4 v245, s[32:35], s50 offen offset:1024 lds
buffer_load_dwordx4 v246, s[32:35], s50 offen offset:2048 lds
s_add_u32 s50, s50, 40960
s_waitcnt vmcnt(9)
s_barrier
)"
R"(s_add_u32 m0, s56, 122880
s_nop 0
buffer_load_dwordx4 v244, s[32:35], s50 offen lds
buffer_load_dwordx4 v245, s[32:35], s50 offen offset:1024 lds
buffer_load_dwordx4 v246, s[32:35], s50 offen offset:2048 lds
s_add_u32 s50, s50, 40960
s_add_u32 m0, s56, 73728
s_nop 0
buffer_load_dwordx4 v244, s[32:35], s50 offen lds
buffer_load_dwordx4 v245, s[32:35], s50 offen offset:1024 lds
buffer_load_dwordx4 v246, s[32:35], s50 offen offset:2048 lds
s_add_u32 s50, s50, 40960
s_waitcnt vmcnt(6)
s_barrier
s_waitcnt vmcnt(0)
s_barrier
)"
R"(s_cmp_eq_u32 s59, 4
s_cbranch_scc0 Lw4_12
s_mov_b64 exec, 0xfff
buffer_load_dword v251, v249, s[40:43], 0 offen sc1
s_waitcnt vmcnt(0)
s_mov_b64 exec, -1
Lw4_12:
s_barrier
s_cmp_eq_u32 s59, 4
s_cbranch_scc0 Lw4_13
s_mov_b64 exec, 0xf00
buffer_store_dword v251, v250, s[40:43], 0 offen offset:128 sc1
s_mov_b32 s82, 65536
buffer_load_dword v252, v249, s[40:43], 0 offen offset:128 sc1
s_sleep 2
buffer_load_dword v253, v249, s[40:43], 0 offen offset:128 sc1
)"
R"(s_sleep 2
buffer_load_dword v254, v249, s[40:43], 0 offen offset:128 sc1
Lp_14:
s_waitcnt vmcnt(2)
v_cmp_ne_u32 vcc, v252, v248
s_nop 1
s_cbranch_vccz Lpd_14
buffer_load_dword v252, v249, s[40:43], 0 offen offset:128 sc1
s_waitcnt vmcnt(2)
v_cmp_ne_u32 vcc, v253, v248
s_nop 1
s_cbranch_vccz Lpd_14
buffer_load_dword v253, v249, s[40:43], 0 offen offset:128 sc1
s_waitcnt vmcnt(2)
v_cmp_ne_u32 vcc, v254, v248
s_nop 1
)"
R"(s_cbranch_vccz Lpd_14
buffer_load_dword v254, v249, s[40:43], 0 offen offset:128 sc1
s_sub_u32 s82, s82, 1
s_cmp_lg_u32 s82, 0
s_cbranch_scc1 Lp_14
Lpd_14:
s_mov_b64 exec, -1
Lw4_13:
s_barrier
s_cmp_eq_u32 s59, 4
s_cbranch_scc0 Lw4_15
s_mov_b64 exec, 0xf00
buffer_load_dword v251, v249, s[40:43], 0 offen sc1
s_waitcnt vmcnt(0)
buffer_store_dword v251, v250, s[40:43], 0 offen offset:128 sc1
s_mov_b64 exec, -1
)"
R"(Lw4_15:
s_endpgm
Lrare_0:
s_nop 9
v_max3_f32 v242, v144, v145, v146
v_max3_f32 v242, v242, v147, v148
v_max3_f32 v242, v242, v149, v150
v_max_f32 v242, v242, v151
v_max3_f32 v243, v152, v153, v154
v_max3_f32 v243, v243, v155, v156
v_max3_f32 v243, v243, v157, v158
v_max_f32 v243, v243, v159
v_mov_b32 v212, v242
v_mov_b32 v213, v243
s_nop 0
v_permlane32_swap_b32 v242, v212
)"
R"(v_permlane32_swap_b32 v243, v213
v_max_f32 v242, v242, v212
v_max_f32 v243, v243, v213
v_mov_b32 v212, v242
v_mov_b32 v213, v243
s_nop 0
v_permlane16_swap_b32 v242, v212
v_permlane16_swap_b32 v243, v213
v_max_f32 v242, v242, v212
v_max_f32 v243, v243, v213
v_max_f32 v242, 0, v242
v_sub_f32 v224, v224, v242
v_sub_f32 v225, v225, v242
v_sub_f32 v226, v226, v242
v_sub_f32 v227, v227, v242
v_exp_f32_e64 v212, -v242
)"
R"(v_max_f32 v243, 0, v243
v_sub_f32 v228, v228, v243
v_sub_f32 v229, v229, v243
v_sub_f32 v230, v230, v243
v_sub_f32 v231, v231, v243
v_exp_f32_e64 v214, -v243
v_sub_f32 v144, v144, v242
v_sub_f32 v145, v145, v242
v_sub_f32 v146, v146, v242
v_sub_f32 v147, v147, v242
v_sub_f32 v148, v148, v242
v_sub_f32 v149, v149, v242
v_sub_f32 v150, v150, v242
v_sub_f32 v151, v151, v242
v_sub_f32 v152, v152, v243
v_sub_f32 v153, v153, v243
)"
R"(v_sub_f32 v154, v154, v243
v_sub_f32 v155, v155, v243
v_sub_f32 v156, v156, v243
v_sub_f32 v157, v157, v243
v_sub_f32 v158, v158, v243
v_sub_f32 v159, v159, v243
v_pk_mul_f32 v[238:239], v[238:239], v[212:213] op_sel_hi:[1,0]
v_pk_mul_f32 v[240:241], v[240:241], v[214:215] op_sel_hi:[1,0]
v_pk_mul_f32 v[0:1], v[0:1], v[212:213] op_sel_hi:[1,0]
v_pk_mul_f32 v[2:3], v[2:3], v[212:213] op_sel_hi:[1,0]
v_pk_mul_f32 v[4:5], v[4:5], v[214:215] op_sel_hi:[1,0]
v_pk_mul_f32 v[6:7], v[6:7], v[214:215] op_sel_hi:[1,0]
v_pk_mul_f32 v[8:9], v[8:9], v[212:213] op_sel_hi:[1,0]
v_pk_mul_f32 v[10:11], v[10:11], v[212:213] op_sel_hi:[1,0]
v_pk_mul_f32 v[12:13], v[12:13], v[214:215] op_sel_hi:[1,0]
v_pk_mul_f32 v[14:15], v[14:15], v[214:215] op_sel_hi:[1,0]
)"
R"(v_pk_mul_f32 v[16:17], v[16:17], v[212:213] op_sel_hi:[1,0]
v_pk_mul_f32 v[18:19], v[18:19], v[212:213] op_sel_hi:[1,0]
v_pk_mul_f32 v[20:21], v[20:21], v[214:215] op_sel_hi:[1,0]
v_pk_mul_f32 v[22:23], v[22:23], v[214:215] op_sel_hi:[1,0]
v_pk_mul_f32 v[24:25], v[24:25], v[212:213] op_sel_hi:[1,0]
v_pk_mul_f32 v[26:27], v[26:27], v[212:213] op_sel_hi:[1,0]
v_pk_mul_f32 v[28:29], v[28:29], v[214:215] op_sel_hi:[1,0]
v_pk_mul_f32 v[30:31], v[30:31], v[214:215] op_sel_hi:[1,0]
v_pk_mul_f32 v[32:33], v[32:33], v[212:213] op_sel_hi:[1,0]
v_pk_mul_f32 v[34:35], v[34:35], v[212:213] op_sel_hi:[1,0]
v_pk_mul_f32 v[36:37], v[36:37], v[214:215] op_sel_hi:[1,0]
v_pk_mul_f32 v[38:39], v[38:39], v[214:215] op_sel_hi:[1,0]
v_pk_mul_f32 v[40:41], v[40:41], v[212:213] op_sel_hi:[1,0]
v_pk_mul_f32 v[42:43], v[42:43], v[212:213] op_sel_hi:[1,0]
v_pk_mul_f32 v[44:45], v[44:45], v[214:215] op_sel_hi:[1,0]
v_pk_mul_f32 v[46:47], v[46:47], v[214:215] op_sel_hi:[1,0]
)"
R"(v_pk_mul_f32 v[48:49], v[48:49], v[212:213] op_sel_hi:[1,0]
v_pk_mul_f32 v[50:51], v[50:51], v[212:213] op_sel_hi:[1,0]
v_pk_mul_f32 v[52:53], v[52:53], v[214:215] op_sel_hi:[1,0]
v_pk_mul_f32 v[54:55], v[54:55], v[214:215] op_sel_hi:[1,0]
v_pk_mul_f32 v[56:57], v[56:57], v[212:213] op_sel_hi:[1,0]
v_pk_mul_f32 v[58:59], v[58:59], v[212:213] op_sel_hi:[1,0]
v_pk_mul_f32 v[60:61], v[60:61], v[214:215] op_sel_hi:[1,0]
v_pk_mul_f32 v[62:63], v[62:63], v[214:215] op_sel_hi:[1,0]
v_pk_mul_f32 v[64:65], v[64:65], v[212:213] op_sel_hi:[1,0]
v_pk_mul_f32 v[66:67], v[66:67], v[212:213] op_sel_hi:[1,0]
v_pk_mul_f32 v[68:69], v[68:69], v[214:215] op_sel_hi:[1,0]
v_pk_mul_f32 v[70:71], v[70:71], v[214:215] op_sel_hi:[1,0]
v_pk_mul_f32 v[72:73], v[72:73], v[212:213] op_sel_hi:[1,0]
v_pk_mul_f32 v[74:75], v[74:75], v[212:213] op_sel_hi:[1,0]
v_pk_mul_f32 v[76:77], v[76:77], v[214:215] op_sel_hi:[1,0]
v_pk_mul_f32 v[78:79], v[78:79], v[214:215] op_sel_hi:[1,0]
)"
R"(v_pk_mul_f32 v[80:81], v[80:81], v[212:213] op_sel_hi:[1,0]
v_pk_mul_f32 v[82:83], v[82:83], v[212:213] op_sel_hi:[1,0]
v_pk_mul_f32 v[84:85], v[84:85], v[214:215] op_sel_hi:[1,0]
v_pk_mul_f32 v[86:87], v[86:87], v[214:215] op_sel_hi:[1,0]
v_pk_mul_f32 v[88:89], v[88:89], v[212:213] op_sel_hi:[1,0]
v_pk_mul_f32 v[90:91], v[90:91], v[212:213] op_sel_hi:[1,0]
v_pk_mul_f32 v[92:93], v[92:93], v[214:215] op_sel_hi:[1,0]
v_pk_mul_f32 v[94:95], v[94:95], v[214:215] op_sel_hi:[1,0]
s_add_u32 s80, s80, 4
s_addc_u32 s81, s81, 0
s_nop 1
s_setpc_b64 s[80:81]
Lrare_1:
s_nop 9
v_max3_f32 v242, v160, v161, v162
v_max3_f32 v242, v242, v163, v164
)"
R"(v_max3_f32 v242, v242, v165, v166
v_max_f32 v242, v242, v167
v_max3_f32 v243, v168, v169, v170
v_max3_f32 v243, v243, v171, v172
v_max3_f32 v243, v243, v173, v174
v_max_f32 v243, v243, v175
v_mov_b32 v212, v242
v_mov_b32 v213, v243
s_nop 0
v_permlane32_swap_b32 v242, v212
v_permlane32_swap_b32 v243, v213
v_max_f32 v242, v242, v212
v_max_f32 v243, v243, v213
v_mov_b32 v212, v242
v_mov_b32 v213, v243
s_nop 0
)"
R"(v_permlane16_swap_b32 v242, v212
v_permlane16_swap_b32 v243, v213
v_max_f32 v242, v242, v212
v_max_f32 v243, v243, v213
v_max_f32 v242, 0, v242
v_sub_f32 v224, v224, v242
v_sub_f32 v225, v225, v242
v_sub_f32 v226, v226, v242
v_sub_f32 v227, v227, v242
v_exp_f32_e64 v212, -v242
v_max_f32 v243, 0, v243
v_sub_f32 v228, v228, v243
v_sub_f32 v229, v229, v243
v_sub_f32 v230, v230, v243
v_sub_f32 v231, v231, v243
v_exp_f32_e64 v214, -v243
)"
R"(v_sub_f32 v160, v160, v242
v_sub_f32 v161, v161, v242
v_sub_f32 v162, v162, v242
v_sub_f32 v163, v163, v242
v_sub_f32 v164, v164, v242
v_sub_f32 v165, v165, v242
v_sub_f32 v166, v166, v242
v_sub_f32 v167, v167, v242
v_sub_f32 v168, v168, v243
v_sub_f32 v169, v169, v243
v_sub_f32 v170, v170, v243
v_sub_f32 v171, v171, v243
v_sub_f32 v172, v172, v243
v_sub_f32 v173, v173, v243
v_sub_f32 v174, v174, v243
v_sub_f32 v175, v175, v243
)"
R"(v_pk_mul_f32 v[238:239], v[238:239], v[212:213] op_sel_hi:[1,0]
v_pk_mul_f32 v[240:241], v[240:241], v[214:215] op_sel_hi:[1,0]
v_pk_mul_f32 v[0:1], v[0:1], v[212:213] op_sel_hi:[1,0]
v_pk_mul_f32 v[2:3], v[2:3], v[212:213] op_sel_hi:[1,0]
v_pk_mul_f32 v[4:5], v[4:5], v[214:215] op_sel_hi:[1,0]
v_pk_mul_f32 v[6:7], v[6:7], v[214:215] op_sel_hi:[1,0]
v_pk_mul_f32 v[8:9], v[8:9], v[212:213] op_sel_hi:[1,0]
v_pk_mul_f32 v[10:11], v[10:11], v[212:213] op_sel_hi:[1,0]
v_pk_mul_f32 v[12:13], v[12:13], v[214:215] op_sel_hi:[1,0]
v_pk_mul_f32 v[14:15], v[14:15], v[214:215] op_sel_hi:[1,0]
v_pk_mul_f32 v[16:17], v[16:17], v[212:213] op_sel_hi:[1,0]
v_pk_mul_f32 v[18:19], v[18:19], v[212:213] op_sel_hi:[1,0]
v_pk_mul_f32 v[20:21], v[20:21], v[214:215] op_sel_hi:[1,0]
v_pk_mul_f32 v[22:23], v[22:23], v[214:215] op_sel_hi:[1,0]
v_pk_mul_f32 v[24:25], v[24:25], v[212:213] op_sel_hi:[1,0]
v_pk_mul_f32 v[26:27], v[26:27], v[212:213] op_sel_hi:[1,0]
)"
R"(v_pk_mul_f32 v[28:29], v[28:29], v[214:215] op_sel_hi:[1,0]
v_pk_mul_f32 v[30:31], v[30:31], v[214:215] op_sel_hi:[1,0]
v_pk_mul_f32 v[32:33], v[32:33], v[212:213] op_sel_hi:[1,0]
v_pk_mul_f32 v[34:35], v[34:35], v[212:213] op_sel_hi:[1,0]
v_pk_mul_f32 v[36:37], v[36:37], v[214:215] op_sel_hi:[1,0]
v_pk_mul_f32 v[38:39], v[38:39], v[214:215] op_sel_hi:[1,0]
v_pk_mul_f32 v[40:41], v[40:41], v[212:213] op_sel_hi:[1,0]
v_pk_mul_f32 v[42:43], v[42:43], v[212:213] op_sel_hi:[1,0]
v_pk_mul_f32 v[44:45], v[44:45], v[214:215] op_sel_hi:[1,0]
v_pk_mul_f32 v[46:47], v[46:47], v[214:215] op_sel_hi:[1,0]
v_pk_mul_f32 v[48:49], v[48:49], v[212:213] op_sel_hi:[1,0]
v_pk_mul_f32 v[50:51], v[50:51], v[212:213] op_sel_hi:[1,0]
v_pk_mul_f32 v[52:53], v[52:53], v[214:215] op_sel_hi:[1,0]
v_pk_mul_f32 v[54:55], v[54:55], v[214:215] op_sel_hi:[1,0]
v_pk_mul_f32 v[56:57], v[56:57], v[212:213] op_sel_hi:[1,0]
v_pk_mul_f32 v[58:59], v[58:59], v[212:213] op_sel_hi:[1,0]
)"
R"(v_pk_mul_f32 v[60:61], v[60:61], v[214:215] op_sel_hi:[1,0]
v_pk_mul_f32 v[62:63], v[62:63], v[214:215] op_sel_hi:[1,0]
v_pk_mul_f32 v[64:65], v[64:65], v[212:213] op_sel_hi:[1,0]
v_pk_mul_f32 v[66:67], v[66:67], v[212:213] op_sel_hi:[1,0]
v_pk_mul_f32 v[68:69], v[68:69], v[214:215] op_sel_hi:[1,0]
v_pk_mul_f32 v[70:71], v[70:71], v[214:215] op_sel_hi:[1,0]
v_pk_mul_f32 v[72:73], v[72:73], v[212:213] op_sel_hi:[1,0]
v_pk_mul_f32 v[74:75], v[74:75], v[212:213] op_sel_hi:[1,0]
v_pk_mul_f32 v[76:77], v[76:77], v[214:215] op_sel_hi:[1,0]
v_pk_mul_f32 v[78:79], v[78:79], v[214:215] op_sel_hi:[1,0]
v_pk_mul_f32 v[80:81], v[80:81], v[212:213] op_sel_hi:[1,0]
v_pk_mul_f32 v[82:83], v[82:83], v[212:213] op_sel_hi:[1,0]
v_pk_mul_f32 v[84:85], v[84:85], v[214:215] op_sel_hi:[1,0]
v_pk_mul_f32 v[86:87], v[86:87], v[214:215] op_sel_hi:[1,0]
v_pk_mul_f32 v[88:89], v[88:89], v[212:213] op_sel_hi:[1,0]
v_pk_mul_f32 v[90:91], v[90:91], v[212:213] op_sel_hi:[1,0]
)"
R"(v_pk_mul_f32 v[92:93], v[92:93], v[214:215] op_sel_hi:[1,0]
v_pk_mul_f32 v[94:95], v[94:95], v[214:215] op_sel_hi:[1,0]
s_add_u32 s80, s80, 4
s_addc_u32 s81, s81, 0
s_nop 1
s_setpc_b64 s[80:81]
Lcode1:
)"
               :
               : "s"(__builtin_amdgcn_kernarg_segment_ptr()), "s"(blockIdx.x), "v"(threadIdx.x), "s"(lb)
               : "v1", "v2", "v3", "v4", "v5", "v6", "v7", "v8", "v9", "v10", "v11", "v12", "v13", "v14", "v15", "v16", "v17", "v18", "v19", "v20", "v21", "v22", "v23", "v24", "v25", "v26", "v27", "v28", "v29", "v30", "v31", "v32", "v33", "v34", "v35", "v36", "v37", "v38", "v39", "v40", "v41", "v42", "v43", "v44", "v45", "v46", "v47", "v48", "v49", "v50", "v51", "v52", "v53", "v54", "v55", "v56", "v57", "v58", "v59", "v60", "v61", "v62", "v63", "v64", "v65", "v66", "v67", "v68", "v69", "v70", "v71", "v72", "v73", "v74", "v75", "v76", "v77", "v78", "v79", "v80", "v81", "v82", "v83", "v84", "v85", "v86", "v87", "v88", "v89", "v90", "v91", "v92", "v93", "v94", "v95", "v96", "v97", "v98", "v99", "v100", "v101", "v102", "v103", "v104", "v105", "v106", "v107", "v108", "v109", "v110", "v111", "v112", "v113", "v114", "v115", "v116", "v117", "v118", "v119", "v120", "v121", "v122", "v123", "v124", "v125", "v126", "v127", "v128", "v129", "v130", "v131", "v132", "v133", "v134", "v135", "v136", "v137", "v138", "v139", "v140", "v141", "v142", "v143", "v144", "v145", "v146", "v147", "v148", "v149", "v150", "v151", "v152", "v153", "v154", "v155", "v156", "v157", "v158", "v159", "v160", "v161", "v162", "v163", "v164", "v165", "v166", "v167", "v168", "v169", "v170", "v171", "v172", "v173", "v174", "v175", "v176", "v177", "v178", "v179", "v180", "v181", "v182", "v183", "v184", "v185", "v186", "v187", "v188", "v189", "v190", "v191", "v192", "v193", "v194", "v195", "v196", "v197", "v198", "v199", "v200", "v201", "v202", "v203", "v204", "v205", "v206", "v207", "v208", "v209", "v210", "v211", "v212", "v213", "v214", "v215", "v216", "v217", "v218", "v219", "v220", "v221", "v222", "v223", "v224", "v225", "v226", "v227", "v228", "v229", "v230", "v231", "v232", "v233", "v234", "v235", "v236", "v237", "v238", "v239", "v240", "v241", "v242", "v243", "v244", "v245", "v246", "v247", "v248", "v249", "v250", "v251", "v252", "v253", "v254", "v255", "s20", "s21", "s22", "s23", "s24", "s25", "s26", "s27", "s28", "s29", "s30", "s31", "s33", "s34", "s35", "s36", "s37", "s38", "s39", "s40", "s41", "s42", "s43", "s44", "s45", "s46", "s47", "s48", "s49", "s50", "s51", "s52", "s53", "s54", "s55", "s56", "s57", "s58", "s59", "s60", "s61", "s62", "s63", "s64", "s65", "s66", "s67", "s68", "s69", "s70", "s71", "s72", "s73", "s74", "s75", "s76", "s77", "s78", "s79", "s80", "s81", "s82", "s83", "s84", "s85", "s86", "s87", "s88", "s89", "s90", "s91", "s92", "s93", "s94", "s95", "s96", "s97", "s98", "s99", "vcc", "scc", "memory");
}

extern "C" void kernel_launch(void* const* d_in, const int* in_sizes, int n_in, void* d_out, int out_size, void* d_ws,
                              size_t ws_size, hipStream_t stream) {
  mha_pair_fused<<<256, 512, 0, stream>>>(d_in[0], d_in[1], d_in[2], d_in[3], d_in[4], d_in[5], d_in[6], d_in[7], d_in[8],
                                          d_in[9], d_out, d_ws);
}
```
